```python
import torch

def _softmax(x, axis=-1):
    return torch.softmax(x, dim=axis)

def _gelu(x):
    return torch.nn.functional.gelu(x, approximate='tanh')

def _silu(x):
    return torch.nn.functional.silu(x)

def _sigmoid(x):
    return torch.sigmoid(x)

def _layernorm(x, axis=-1, eps=1e-05):
    m = x.mean(dim=axis, keepdim=True)
    v = x.var(dim=axis, keepdim=True, unbiased=False)
    return (x - m) / torch.sqrt(v + eps)

def _scatter_add(tgt, idx, src, axis=0):
    return tgt.clone().index_add_(axis, idx, src)

def _segment_sum(src, seg, n):
    out = torch.zeros((n,) + src.shape[1:], dtype=src.dtype)
    return out.index_add_(0, seg, src)

def _pad(x, pw):
    flat = tuple((v for pair in reversed(pw) for v in pair))
    return torch.nn.functional.pad(x, flat)
kernel_name = 'standalone__kv_cache_quant_pre_rope_vs_post_rope__sampling'

def setup_inputs():

    _w = lambda a: (lambda t: t.to(torch.bfloat16) if t.is_floating_point() else t)(torch.as_tensor(a))
    B, Sq, Skv = (8, 1, 2048)
    H, Dh0 = (32, 192)
    G = -1 if -1 > 0 else Dh0
    G = max(2, G // 2 * 2)
    Dh = max(G, Dh0 // G * G)
    Skv = max(Skv, Sq)
    ng = Dh // G
    bits = 4 if 4 > 1 else 4
    bits = max(2, min(8, bits))
    qmax = (1 << bits) - 1
    half = Dh // 2
    inv = 1.0 / 10000.0 ** (torch.arange(half) / max(1, half))
    ang_k = torch.arange(Skv)[:, None] * inv[None, :]
    ang_q = torch.arange(Skv - Sq, Skv)[:, None] * inv[None, :]
    kc = torch.cat([torch.cos(ang_k), torch.cos(ang_k)], dim=-1)
    ks = torch.cat([torch.sin(ang_k), torch.sin(ang_k)], dim=-1)
    qc = torch.cat([torch.cos(ang_q), torch.cos(ang_q)], dim=-1)
    qs = torch.cat([torch.sin(ang_q), torch.sin(ang_q)], dim=-1)
    qk_i = torch.randint(0, qmax + 1, (B, H, Skv, Dh)).to(torch.int32)
    qv_i = torch.randint(0, qmax + 1, (B, H, Skv, Dh)).to(torch.int32)
    ksc = torch.abs((lambda *s: torch.randn(*s, dtype=torch.bfloat16) * 0.2)(B, H, Skv, ng)) * 0.1 + 0.01
    kzp = torch.full((B, H, Skv, ng), qmax / 2.0)
    vsc = torch.abs((lambda *s: torch.randn(*s, dtype=torch.bfloat16) * 0.2)(B, H, Skv, ng)) * 0.1 + 0.01
    vzp = torch.full((B, H, Skv, ng), qmax / 2.0)
    return {'q': _w((lambda *s: torch.randn(*s, dtype=torch.bfloat16) * 6.4)(B, H, Sq, Dh) * 0.5), 'qk': _w(qk_i), 'k_scales': _w(ksc), 'k_zeros': _w(kzp), 'qv': _w(qv_i), 'v_scales': _w(vsc), 'v_zeros': _w(vzp), 'q_cos': _w(qc), 'q_sin': _w(qs), 'k_cos': _w(kc), 'k_sin': _w(ks)}

def reference(q, qk, k_scales, k_zeros, qv, v_scales, v_zeros, q_cos, q_sin, k_cos, k_sin):
    B, H, Sq, Dh = q.shape
    ng = k_scales.shape[-1]
    G = Dh // ng
    half = Dh // 2
    ks = (lambda x, n, axis: torch.repeat_interleave(x, n, dim=axis))(k_scales, G, axis=-1)
    kz = (lambda x, n, axis: torch.repeat_interleave(x, n, dim=axis))(k_zeros, G, axis=-1)
    k = ((lambda x, dt: x.to(dt))(qk, q.dtype) - kz) * ks
    vs = (lambda x, n, axis: torch.repeat_interleave(x, n, dim=axis))(v_scales, G, axis=-1)
    vz = (lambda x, n, axis: torch.repeat_interleave(x, n, dim=axis))(v_zeros, G, axis=-1)
    v = ((lambda x, dt: x.to(dt))(qv, q.dtype) - vz) * vs
    k1 = k[..., :half].clone()
    k2 = k[..., half:].clone()
    krh = torch.concatenate([-k2, k1], axis=-1)
    kr = k * k_cos[None, None, :, :] + krh * k_sin[None, None, :, :]
    q1 = q[..., :half].clone()
    q2 = q[..., half:].clone()
    qrh = torch.concatenate([-q2, q1], axis=-1)
    qr = q * q_cos[None, None, :, :] + qrh * q_sin[None, None, :, :]
    logits = torch.einsum('bhqd,bhkd->bhqk', qr, kr) * float(Dh) ** (-0.5)
    attn = _softmax(logits, axis=-1)
    out = torch.einsum('bhqk,bhkd->bhqd', attn, v)
    return out
```

```cpp
#include <hip/hip_runtime.h>
#include <hip/hip_bf16.h>
#include <stdint.h>

#define DH 192
#define HALF 96
#define SKV 2048
#define NH 256
#define NSW 8
#define NWAVES (NSW + 4)
#define NTHREADS (64 * NWAVES)
#define BROWS 128
#define NSPLIT 16
#define GSZ 16
#define WQ 10
#define NU (NH / GSZ + 3)
#define NPC 1024
#define NRR (NH + 3 * GSZ)
#define NIN 8
#define NOUT 8
#define WALL (15 * 144 + 4 * 48)
#define SC1 16
#define NRND 48
#define RST 4352
#define TK 2
#define NMIX 3
#define TUNIT 311
#define T01 50
#define TMIX 7
#define TOFF 50
#define PSF 100
#define TGS 68
#define PSW (NPC / 2 + 28)
#define TG0 8
#define TG1 3
#define TWO -75
#define PSG (NPC / 2 + 60)
#define TH0 3
#define TH1 3
#define EARLY 10
#define LEADP 24
#define DPH 2
#define TA 210
#define TA2 70
#define CSH 2
#define NPRB 3
#define TMAP 520
#define MINV 96
#define VW 4
#define MASTER 203
#define TRES 190
#define TB1 1200
#define PER 300
#define RDD 100
#define LEAD 70
#define LHI 85
#define LOK 74
#define PSL0 124
#define PUBP -8
#define LJ 210
#define LOK2 75
#define NTRY 3
#define TFO 6
#define SL0 42.f
#define PST0 200
#define KC 4
#define TC0 1000
#define TCS 9
#define PRE0 3

#define WS_EPOCH 0
#define WS_POST0 8192
#define WS_POST (WS_POST0 + ((unsigned)E1 & 15) * 2048)
#define WS_NEWS (49152 + 256)
#define WS_HELLO 59904
#define WS_BOARD 65536
#define WS_PART (2u << 20)
#define WS_SWAP (WS_PART + NRR * NSPLIT * 512)

typedef unsigned int v2u __attribute__((ext_vector_type(2)));
typedef unsigned int v16u __attribute__((ext_vector_type(16)));
typedef unsigned int v4u __attribute__((ext_vector_type(4)));
typedef int v4i __attribute__((ext_vector_type(4)));
typedef float v2f __attribute__((ext_vector_type(2)));
typedef float v4f __attribute__((ext_vector_type(4)));
typedef unsigned long long u64;

__device__ __forceinline__ float bflo(unsigned u) { return __uint_as_float(u << 16); }
__device__ __forceinline__ float bfhi(unsigned u) { return __uint_as_float(u & 0xffff0000u); }
__device__ __forceinline__ float bfr(float x) {
    __bf16 b = (__bf16)x;
    return __uint_as_float(((unsigned)__builtin_bit_cast(unsigned short, b)) << 16);
}

#define SLEEP(n) SLEEP_(n)
#define SLEEP_(n) asm volatile("s_sleep " #n "\n\tv_add_co_u32 %0, vcc, 1, %0\n\ts_cbranch_vccz 1f\n\ts_endpgm\n1:" : "+v"(spins) :: "vcc", "memory");
#define SPIN0 0xf0000000u
#define CBAR() asm volatile("" ::: "memory")

__device__ __forceinline__ void wave_sync() {
    __builtin_amdgcn_fence(__ATOMIC_RELEASE, "wavefront");
    __builtin_amdgcn_wave_barrier();
    __builtin_amdgcn_fence(__ATOMIC_ACQUIRE, "wavefront");
}
template <int CTRL>
__device__ __forceinline__ float dpp_add(float x) {
    int y = __builtin_amdgcn_update_dpp(0, __float_as_int(x), CTRL, 0xf, 0xf, true);
    return x + __int_as_float(y);
}
template <int CTRL>
__device__ __forceinline__ float dpp_max(float x) {
    int y = __builtin_amdgcn_update_dpp(__float_as_int(x), __float_as_int(x), CTRL, 0xf, 0xf, false);
    return fmaxf(x, __int_as_float(y));
}
__device__ __forceinline__ float row_sum(float x) { x = dpp_add<0x128>(x); x = dpp_add<0x124>(x); x = dpp_add<0x122>(x); return dpp_add<0x121>(x); }
__device__ __forceinline__ float row_max(float m) { m = dpp_max<0x128>(m); m = dpp_max<0x124>(m); m = dpp_max<0x122>(m); return dpp_max<0x121>(m); }
__device__ __forceinline__ float rdlane(float x, int l) { return __int_as_float(__builtin_amdgcn_readlane(__float_as_int(x), l)); }
__device__ __forceinline__ float wave_max(float m) { m = row_max(m); return fmaxf(fmaxf(rdlane(m, 0), rdlane(m, 16)), fmaxf(rdlane(m, 32), rdlane(m, 48))); }
__device__ __forceinline__ float wave_maxv(float m) {
    m = row_max(m);
    const auto a = __builtin_amdgcn_permlane16_swap(__float_as_uint(m), __float_as_uint(m), false, false);
    m = fmaxf(__uint_as_float(a[0]), __uint_as_float(a[1]));
    const auto b = __builtin_amdgcn_permlane32_swap(__float_as_uint(m), __float_as_uint(m), false, false);
    return fmaxf(__uint_as_float(b[0]), __uint_as_float(b[1]));
}
__device__ __forceinline__ float wave_sum(float x) { x = row_sum(x); return (rdlane(x, 0) + rdlane(x, 16)) + (rdlane(x, 32) + rdlane(x, 48)); }
__device__ __forceinline__ unsigned lds_ld(const unsigned* p) { return __builtin_amdgcn_readfirstlane(__hip_atomic_load(p, __ATOMIC_RELAXED, __HIP_MEMORY_SCOPE_WORKGROUP)); }
__device__ __forceinline__ unsigned lds_addr(const void* p) { return (unsigned)(size_t)(const __attribute__((address_space(3))) void*)p; }
#define DMA16(SRC, LDS) asm volatile("s_mov_b32 %0, m0\n\ts_mov_b32 m0, %2\n\ts_nop 0\n\tglobal_load_lds_dwordx4 %1, off\n\ts_mov_b32 m0, %0" : "=&s"(m0keep) : "v"(SRC), "s"(LDS) : "memory")
__device__ __forceinline__ void lds_st(unsigned* p, unsigned v) { __hip_atomic_store(p, v, __ATOMIC_RELAXED, __HIP_MEMORY_SCOPE_WORKGROUP); }
#define NOW() ((unsigned)__builtin_amdgcn_s_memrealtime())
__device__ __forceinline__ unsigned tdue(unsigned g) {
    return (g * TUNIT + min(g, 480u) * TK + min(g, 48u) * T01 + (min(g, 32u * NMIX) + min(max((int)g - NPC / 2, 0), 32 * NMIX)) * TMIX + (g >= PSF ? TGS * 32 : 0)) >> 5;
}

__device__ __forceinline__ unsigned unit_of(unsigned g) { const unsigned p = g & 511; return (g >> 9) * NU + (p >> 5) + min(max((int)p - 32 * WQ, 0) >> 3, 3); }
__device__ __forceinline__ unsigned first_of(unsigned e) { return 32 * e - 24 * min(max((int)e - WQ, 0), 4); }
#define QUARTER(E) ((E) - WQ < 4)
__device__ __forceinline__ unsigned went(unsigned e) { return 144 * e - 96 * min(max((int)e - WQ, 0), 4); }
__device__ __forceinline__ unsigned wunit(unsigned x) { x += 24 * NMIX; return x < 24 * WQ ? x / 24 : x < 24 * WQ + 32 ? WQ + (x - 24 * WQ) / 8 : (x + 64) / 24; }

#define ROTT 0xed8d2c3d2c35ed84ull, 0xba85aa165b03ac82ull, 0xee7efa489a38af71ull, 0xe932da1b0bab51d8ull, 0xfa96112d5fee971bull, 0x2c7e9ea5a544f071ull, 0x3d86093d5f0e810bull, 0xe932da1b0bab51d8ull, 0xfa96112d5fee971bull, 0xd834e62c5fa723e9ull, 0x1d7d1a3d2c061d88ull, 0xe932da1b0bab51d8ull, 0xfa96112d5fee971bull, 0xc834e12c0aaa4ec7ull, 0x3d86093d5f0e810bull, 0xe932da1b0bab51d8ull
static __device__ const u64 ROT[16] = {ROTT};

struct __attribute__((aligned(16))) Smem {
    unsigned cs[NSW][4][768];
    double acc[NOUT][DH];
    float ent[NOUT][BROWS];
    unsigned short wv[WALL];
    unsigned short wo[WALL];
    unsigned q[NIN][HALF];
    float2 kz[NIN][BROWS];
    float2 vz[NIN][BROWS];
    unsigned utab[64];
    unsigned line, gap;
    unsigned late, grab;
    unsigned rdy[NIN], cnt[NOUT];
    unsigned tabs, vrdy, swp;
    unsigned aline;
    unsigned tag_rdy, tag_lo, tag_hi, merged, cdone, qdone;
    unsigned slow;
    unsigned chead;
};

#define LDS_ADD(P, V) { u64 ex_; asm volatile("s_mov_b64 %0, exec\n\ts_mov_b64 exec, 1\n\tds_add_u32 %1, %2\n\ts_mov_b64 exec, %0" : "=&s"(ex_) : "v"(lds_addr(P)), "v"(V) : "memory"); }
#define LDS_OR(P, V) { u64 ex_; asm volatile("s_mov_b64 %0, exec\n\ts_mov_b64 exec, 1\n\tds_or_b32 %1, %2\n\ts_mov_b64 exec, %0" : "=&s"(ex_) : "v"(lds_addr(P)), "v"(V) : "memory"); }

struct Args { const void* p[13]; };
#define GP(T, I) ((T*)(__attribute__((address_space(1))) T*)(size_t)args.p[I])
__global__ __launch_bounds__(NTHREADS, 3) void attn_kernel(const Args args)
{
    const unsigned short* __restrict__ q = GP(const unsigned short, 0); const int* __restrict__ qk = GP(const int, 1);
    const unsigned short* __restrict__ k_scales = GP(const unsigned short, 2); const unsigned short* __restrict__ k_zeros = GP(const unsigned short, 3);
    const int* __restrict__ qv = GP(const int, 4);
    const unsigned short* __restrict__ v_scales = GP(const unsigned short, 5); const unsigned short* __restrict__ v_zeros = GP(const unsigned short, 6);
    const unsigned short* __restrict__ q_cos = GP(const unsigned short, 7); const unsigned short* __restrict__ q_sin = GP(const unsigned short, 8);
    const unsigned short* __restrict__ k_cos = GP(const unsigned short, 9); const unsigned short* __restrict__ k_sin = GP(const unsigned short, 10);
    __hip_bfloat16* __restrict__ out = GP(__hip_bfloat16, 11); unsigned char* __restrict__ ws = GP(unsigned char, 12);
    __shared__ Smem sm;
    const int tid = threadIdx.x;
    unsigned spins = SPIN0;
    const int lane = tid & 63;
    const int hw = __builtin_amdgcn_readfirstlane(tid >> 6);
    u64 t00; unsigned ka;
    asm volatile("s_memrealtime %0\n\ts_load_dword %1, %2, 0x40" : "=&s"(t00), "=&s"(ka) : "s"(__builtin_amdgcn_kernarg_segment_ptr()));
    if (hw == 0) {
        const char* pc0; asm volatile("s_getpc_b64 %0" : "=s"(pc0));
        asm volatile("global_load_dword a6, %0, %1" :: "v"(((blockIdx.x & ~7u) + (lane & 7)) * 128), "s"(pc0) : "a6", "memory");
    }
    const int wave = hw >= 4 ? hw - 4 : hw + NSW;
    const unsigned split = (blockIdx.x & 1) + (blockIdx.x & 2) * 4 + ((blockIdx.x >> 3) & 3) * 2;
    const unsigned side = (split ^ (blockIdx.x >> 2)) & 1, rank = blockIdx.x >> 5;
    const unsigned head0 = 2 * rank + side;
    const unsigned rf0 = (8 * split + 1) / 3, r3 = 8 * split + 1 - 3 * rf0;
    const unsigned koff = ((lane >> 5) * DH + 3 * (lane & 31)) * 4, koffb = koff + 384;
#define AR(S) (16 + (S) * 8)
#define GLD(A, OFF, BASE, IMM) asm volatile("global_load_dwordx3 a[%c0:%c1], %2, %3 offset:%4 nt" :: "i"(A), "i"((A) + 2), "v"(OFF), "s"(BASE), "i"(IMM))
#define ISSUE(S, J, BASE, OA, OB) { GLD(AR(S), OA, BASE, (J) * 1536); GLD(AR(S) + 4, OB, BASE, (J) * 1536); }
#define HEAD2 ((GSZ + 1 - 2 * side) * (SKV * DH * 4))
    const unsigned wo = (hw - 2) & 7;
    const bool pre = hw >= 4 && wo < PRE0;
    if (pre) {
        const char* a = (const char*)qk + (head0 * 512 + side * 256 + rf0 + wave + (wave >= 6 ? 80 : 0)) * 3072; const char* b = a + HEAD2;
        ISSUE(0, 0, a, koff, koffb) ISSUE(1, 1, a, koff, koffb) ISSUE(2, 0, b, koff, koffb) ISSUE(3, 1, b, koff, koffb)
    }
    asm volatile("s_waitcnt lgkmcnt(0)" : "+s"(t00), "+s"(ka));
    if (hw == 0) {
        sm.utab[lane] = ~0u;
        if (lane < 32) (&sm.line)[lane] = lane == 3 ? 64u : (lane & 29) == 0 ? (unsigned)t00 + TOFF : 0u;
        asm volatile("s_waitcnt lgkmcnt(0)" ::: "memory");
    }
    __builtin_amdgcn_s_barrier();
    CBAR();
    const u64 rtab = r3 == 1 ? 0x5756552c2b020100ull : r3 == 0 ? 0x5756552c2b2a0100ull : 0x57562d2c2b020100ull;
#define R8(I) (rf0 + ((unsigned)(rtab >> (8 * (I))) & 255u))

    if (__builtin_expect(wave < NSW, 1)) {
        const int cst = 2 * side;
        const unsigned r8w = R8((unsigned)wave);
        const char* cbase[3];
#pragma unroll
        for (int j = 0; j < 3; j++) { const int x = j * 64 + lane; cbase[j] = (x < 96 ? (const char*)k_cos + x * 16 : (const char*)k_sin + (x - 96) * 16) + r8w * (4 * DH * 2); }
        unsigned m0keep;
#define CS_DMA(W, C) { _Pragma("unroll") for (int j = 0; j < 3; j++) DMA16(cbase[j] + (C) * (512 * DH * 2), lds_addr(&sm.cs[0][0][0]) + ((W) * 4 + (C)) * 3072 + j * 1024); }
#define REPACK(W, C) { \
        unsigned* cw = &sm.cs[W][C][0]; \
        const unsigned* l = cw + rsel * (DH / 2) + qoff; \
        unsigned r[2][6]; \
        _Pragma("unroll") \
        for (int j = 0; j < 2; j++) { \
            unsigned e[4][2]; \
            _Pragma("unroll") \
            for (int k = 0; k < 4; k++) { \
                const unsigned* a = l + j * DH + (k & 1) * 48 + (k >> 1) * 384;       \
                unsigned x0 = a[0], x1 = a[1]; \
                e[k][0] = __builtin_amdgcn_alignbit(x1, x0, sh); \
                e[k][1] = x1 >> sh; \
            } \
            r[j][0] = (e[0][0] & 0xffffu) | (e[3][0] << 16); \
            r[j][1] = (e[0][0] >> 16) | (e[3][0] & 0xffff0000u); \
            r[j][2] = (e[0][1] & 0xffffu) | (e[3][1] << 16); \
            r[j][3] = (e[1][0] & 0xffffu) | (e[2][0] << 16); \
            r[j][4] = (e[1][0] >> 16) | (e[2][0] & 0xffff0000u); \
            r[j][5] = (e[1][1] & 0xffffu) | (e[2][1] << 16); \
        } \
        wave_sync(); \
        _Pragma("unroll") \
        for (int j = 0; j < 2; j++) { *(v4u*)&cw[j * 256 + lane * 4] = (v4u){r[j][0], r[j][1], r[j][2], r[j][3]}; *(v2u*)&cw[512 + j * 128 + lane * 2] = (v2u){r[j][4], r[j][5]}; } \
        wave_sync(); }
        const int sub = lane & 31;
        const int rsel = lane >> 5;
        const unsigned sh = (sub & 1) * 16;
        const int qoff = 3 * (sub >> 1) + (sub & 1);
        const unsigned voff = 3 * lane * 4;
        asm volatile("" ::: "a16", "a17", "a18", "a19", "a20", "a21", "a22", "a23", "a24", "a25", "a26", "a27", "a28", "a29", "a30", "a31",
                            "a32", "a33", "a34", "a35", "a36", "a37", "a38", "a39", "a40", "a41", "a42", "a43", "a44", "a45", "a46", "a47");
        asm volatile("" ::: "a48", "a49", "a50", "a51", "a52", "a53", "a54", "a55", "a56", "a57", "a58", "a59", "a60", "a61", "a62", "a63",
                            "a64", "a65", "a66", "a67", "a68", "a69", "a70", "a71", "a72", "a73", "a74", "a75", "a76", "a77", "a78", "a79");
        int ta[4][3], tb[4][3];
#define WAITT(P, S, N) asm volatile("s_waitcnt vmcnt(%6)\n\tv_accvgpr_read_b32 %0, a[%c7]\n\tv_accvgpr_read_b32 %1, a[%c8]\n\tv_accvgpr_read_b32 %2, a[%c9]\n\t" \
                              "v_accvgpr_read_b32 %3, a[%c10]\n\tv_accvgpr_read_b32 %4, a[%c11]\n\tv_accvgpr_read_b32 %5, a[%c12]" \
                              : "=v"(ta[P][0]), "=v"(ta[P][1]), "=v"(ta[P][2]), "=v"(tb[P][0]), "=v"(tb[P][1]), "=v"(tb[P][2]) \
                              : "i"(N), "i"(AR(S)), "i"(AR(S) + 1), "i"(AR(S) + 2), "i"(AR(S) + 4), "i"(AR(S) + 5), "i"(AR(S) + 6))
        const unsigned voffb = voff + 768;
        u64 ln = 0;
#define TDUE(G) (tdue(G) + ((G) >= PSW ? __builtin_amdgcn_readfirstlane((unsigned)(ln >> 32)) >> ((G) >= PSG ? 16 : 0) & 0xffffu : 0u))
#define GATE(DUE) { int d_ = (int)((DUE) - NOW()); if (d_ < 3000) while (d_ > 0) { __builtin_amdgcn_s_sleep(1); d_ = (int)((DUE) - NOW()); } }
#define REPORT() { const unsigned t_ = now - tdc; if (lane == 0) lds_st(&sm.late, t_); }
#define LINE() ln = LDV((u64*)&sm.line);
#define LDV(P) __hip_atomic_load(P, __ATOMIC_RELAXED, __HIP_MEMORY_SCOPE_WORKGROUP)
        unsigned u = 0, rg = cst * 8 + wave, np = 0;
        CS_DMA(wave, cst)

        const char* kb0 = (const char*)qk + (size_t)(head0 * 512 + r8w) * 3072;
#define KB(I) (kb0 + ((cst + (I)) & 3) * (128 * 3072))
        const char* kb; const char* kb2;
#define SISSUE(I) { kb = KB(I); kb2 = kb + HEAD2; const unsigned du_ = lds_ld(&sm.line) + tdue(16 * (I) + 2 * wo); GATE(du_) \
            ISSUE(((I) & 1) * 4, 0, kb, koff, koffb) ISSUE(((I) & 1) * 4 + 1, 1, kb, koff, koffb) ISSUE(((I) & 1) * 4 + 2, 0, kb2, koff, koffb) ISSUE(((I) & 1) * 4 + 3, 1, kb2, koff, koffb) }
        if (!pre) SISSUE(0)
        CS_DMA(wave, (cst + 1) & 3)
        if (pre) asm volatile("s_waitcnt vmcnt(3)" ::: "memory");
        asm volatile("s_waitcnt vmcnt(11)" ::: "memory");
        if (!pre) LDS_ADD(&sm.slow, NOW() - lds_ld(&sm.line) - tdue(2 * wo))
        REPACK(wave, cst)
        SISSUE(1)
#define TABDONE(I) LDS_OR(&sm.tabs, 1u << ((((cst + (I)) & 3) * 8) + wave))
        TABDONE(0)
        const float scale = 0.07216878364870322f;
        double acc[3] = {0.0, 0.0, 0.0};
        bool first = true, chg, none = false;
        unsigned Qp[3], Qn[3], Rp[3], Rn[3];
        float kz4[4], ksc, kres[4];
        v4u ca[4]; v2u cb[4];
        v2f qe[4][3], A[4];
        unsigned vut, nrg;
        const unsigned vr = ((lane >> 3) & 3) * 128 + R8((unsigned)lane & 7);
        unsigned g, mg;
        const unsigned grab_adr = lds_addr(&sm.grab);
        asm volatile("v_accvgpr_write_b32 a13, 1\n\tv_accvgpr_write_b32 a14, 2" ::: "a13", "a14", "a15");
#define GRAB(N) { u64 ex_; asm volatile("s_mov_b64 %0, exec\n\ts_mov_b64 exec, 1\n\tds_add_rtn_u32 a15, %1, a%c2\n\ts_mov_b64 exec, %0" : "=&s"(ex_) : "v"(grab_adr), "i"(12 + (N)) : "memory"); }
#define GRABBED() { unsigned t_; asm volatile("s_waitcnt lgkmcnt(0)\n\tv_accvgpr_read_b32 %1, a15\n\ts_nop 0\n\tv_readfirstlane_b32 %0, %1\n\ts_nop 1" : "=s"(g), "=v"(t_)); }
        unsigned oa, ob, un, tdn, tiss, tdc = 0;
#define PLAN() { \
            GRABBED() un = unit_of(g); chg = un != u; none = g >= NPC; \
            const bool nv = g >= NPC / 2; \
            unsigned hd = __builtin_amdgcn_readlane(vut, un); \
            while (hd == ~0u) { SLEEP(1) hd = lds_ld(&sm.utab[un]); } \
            nrg = (g + (hd >> 8)) & 31; hd &= 255; \
            unsigned boff, boff2; asm("s_mul_i32 %0, %1, 0xc00" : "=s"(boff) : "s"(hd * 512 + __builtin_amdgcn_readlane(vr, nrg))); \
            asm("s_mul_i32 %0, %1, 0xc00" : "=s"(boff2) : "s"(hd * 512 + __builtin_amdgcn_readlane(vr, nrg + 1))); \
            const char* b_ = nv ? (const char*)qv : (const char*)qk; \
            kb = b_ + boff; kb2 = b_ + boff2; \
            oa = nv ? voff : koff; ob = nv ? voffb : koffb; \
            tdn = TDUE(g); }
#define FIRE() { ISSUE(0, 0, kb, oa, ob) ISSUE(1, 1, kb, oa, ob) ISSUE(2, 0, kb2, oa, ob) ISSUE(3, 1, kb2, oa, ob) }
#define LAUNCH() { \
            LINE() \
            const unsigned du_ = __builtin_amdgcn_readfirstlane((unsigned)ln) + tdn; \
            GATE(du_) \
            ISSUE(0, 0, kb, oa, ob) ISSUE(1, 1, kb, oa, ob) \
            GATE(du_ + 10) \
            ISSUE(2, 0, kb2, oa, ob) ISSUE(3, 1, kb2, oa, ob) tiss = NOW(); }
#define ARRIVED() \
            const unsigned now = NOW(); \
            const bool stall = now - tiss > LJ && g - PST0 < NPC - 48 - PST0; \
            const bool late = !none && !stall && (int)(now + EARLY - __builtin_amdgcn_readfirstlane((unsigned)ln) - tdn) >= 0; \
            if (late) { FIRE() tiss = now; } \
            if (stall && lane == 0) __hip_atomic_fetch_max(&sm.line, (unsigned)ln + (now - tiss) - LOK2, __ATOMIC_RELAXED, __HIP_MEMORY_SCOPE_WORKGROUP); \
            REPORT()
#define ADVANCE(ISV) { \
            if (chg) FLUSH(ISV) \
            if (!none && !late) LAUNCH() \
            first = chg; u = un; rg = nrg; tdc = tdn + 10; }
#define K_TAB(MODE) { _Pragma("unroll") for (int p = 0; p < ((MODE) ? 2 : 4); p++) { \
            const unsigned* cw = &sm.cs[(rg & 7) + (p >> 1)][rg >> 3][0]; \
            ca[p] = *(const v4u*)&cw[(p & 1) * 256 + lane * 4]; cb[p] = *(const v2u*)&cw[512 + (p & 1) * 128 + lane * 2]; } }
#define K_PRE(MODE, P) { \
        float e0, e1, e2, f0, f1, f2; \
        const unsigned* qp_ = ((MODE) && (P) >= 2) ? Rp : Qp; const unsigned* qn_ = ((MODE) && (P) >= 2) ? Rn : Qn; \
        const int tp_ = (MODE) ? (P) & 1 : (P); \
        asm("v_dot2_f32_bf16 %0, %6, %12, 0\n\tv_dot2_f32_bf16 %3, %9, %15, 0\n\tv_dot2_f32_bf16 %1, %7, %13, 0\n\tv_dot2_f32_bf16 %4, %10, %16, 0\n\t" \
            "v_dot2_f32_bf16 %2, %8, %14, 0\n\tv_dot2_f32_bf16 %5, %11, %17, 0\n\ts_nop 2" \
            : "=&v"(e0), "=&v"(e1), "=&v"(e2), "=&v"(f0), "=&v"(f1), "=&v"(f2) \
            : "v"(qp_[0]), "v"(qp_[1]), "v"(qp_[2]), "v"(qn_[0]), "v"(qn_[1]), "v"(qn_[2]), \
              "v"(ca[tp_].x), "v"(ca[tp_].y), "v"(ca[tp_].z), "v"(ca[tp_].w), "v"(cb[tp_].x), "v"(cb[tp_].y)); \
        qe[P][0] = (v2f){e0, f0}; qe[P][1] = (v2f){e1, f1}; qe[P][2] = (v2f){e2, f2}; \
        A[P] = (qe[P][0] + qe[P][1] + qe[P][2]) * (v2f){-kz4[P], -kz4[P]}; }
#define K_WAIT(MODE, P) { \
        if ((MODE) == 0) { WAITT(P, P, 6 - 2 * (P)); } \
        else { WAITT(P, (((MODE) - 1) & 1) * 4 + (P), ((MODE) < 3 ? 20 : (MODE) == 3 ? 17 : 6) - 2 * (P)); } }
#define K_FMA(P) { \
        _Pragma("unroll") \
        for (int i = 0; i < 3; i++) { v2f x = {(float)ta[P][i], (float)tb[P][i]}; A[P] = qe[P][i] * x + A[P]; } \
        kres[P] = A[P].x + A[P].y; }
#define K_RED(MODE) { \
        const bool o1 = lane & 1, o2 = lane & 2; \
        float s01 = (o1 ? kres[1] : kres[0]) + __int_as_float(__builtin_amdgcn_update_dpp(0, __float_as_int(o1 ? kres[0] : kres[1]), 0xb1, 0xf, 0xf, true)); \
        float s23 = (o1 ? kres[3] : kres[2]) + __int_as_float(__builtin_amdgcn_update_dpp(0, __float_as_int(o1 ? kres[2] : kres[3]), 0xb1, 0xf, 0xf, true)); \
        float sx = (o2 ? s23 : s01) + __int_as_float(__builtin_amdgcn_update_dpp(0, __float_as_int(o2 ? s01 : s23), 0x4e, 0xf, 0xf, true)); \
        sx = dpp_add<0x124>(sx); sx = dpp_add<0x128>(sx); \
        const auto sw = __builtin_amdgcn_permlane16_swap(__float_as_uint(sx), __float_as_uint(sx), false, false); \
        sx = __uint_as_float(sw[0]) + __uint_as_float(sw[1]); \
        if ((lane & 28) == 0) { if (MODE) sm.ent[(lane >> 1) & 1][rg * 4 + 2 * (lane & 1) + rsel] = sx * ksc; else sm.ent[u % NOUT][rg * 4 + 2 * (lane & 3) + rsel] = sx * ksc; } }
#define K_PRE4(MODE) { K_PRE(MODE, 0) K_PRE(MODE, 1) K_PRE(MODE, 2) K_PRE(MODE, 3) asm volatile("" :: "v"(A[0]), "v"(A[1]), "v"(A[2]), "v"(A[3])); }
#define K_BODY(MODE) { K_PRE4(MODE) K_WAIT(MODE, 0) K_FMA(0) K_WAIT(MODE, 1) K_FMA(1) K_WAIT(MODE, 2) K_FMA(2) K_WAIT(MODE, 3) K_FMA(3) K_RED(MODE) }
#define V_FMA(J, W0, W1) { \
        _Pragma("unroll") \
        for (int i = 0; i < 3; i++) { \
            acc[i] = fma(W0, (double)ta[J][i], acc[i]); \
            acc[i] = fma(W1, (double)tb[J][i], acc[i]); \
        } }
#define V_PIN() asm volatile("" :: "v"(acc[0]), "v"(acc[1]), "v"(acc[2]));
#define RDYWAIT(SL, SEQ) { while (lds_ld(&sm.rdy[SL]) != (SEQ)) SLEEP(1); CBAR(); }
#define LOADZ() { _Pragma("unroll") for (int p = 0; p < 4; p++) kz4[p] = sm.kz[u % NIN][rg * 4 + 2 * p + rsel].x; ksc = sm.kz[u % NIN][rg * 4 + 2 * (lane & 3) + rsel].y * scale; }
#define LOADQ() { _Pragma("unroll") for (int i = 0; i < 3; i++) { Qp[i] = sm.q[u % NIN][3 * sub + i]; Qn[i] = __builtin_amdgcn_alignbit(Qp[i], Qp[i], 16) ^ 0x80000000u; } }
#define FLUSH(ISV) { \
            mg = __builtin_amdgcn_readfirstlane(mg); \
            while (u >= mg + NOUT) { SLEEP(1); mg = lds_ld(&sm.merged); } \
            CBAR(); \
            if (ISV) { const unsigned aa = lds_addr(&sm.acc[u % NOUT][lane]); \
                asm volatile("ds_add_f64 %0, %1\n\tds_add_f64 %0, %2 offset:512\n\tds_add_f64 %0, %3 offset:1024" :: "v"(aa), "v"(acc[0]), "v"(acc[1]), "v"(acc[2]) : "memory"); \
                acc[0] = 0.0; acc[1] = 0.0; acc[2] = 0.0; } \
            CBAR(); \
            LDS_ADD(&sm.cnt[u % NOUT], np) \
            CBAR(); \
            np = 0; }

#define LOADZ2() { _Pragma("unroll") for (int p = 0; p < 4; p++) kz4[p] = sm.kz[p >> 1][rg * 4 + 2 * (p & 1) + rsel].x; ksc = sm.kz[(lane >> 1) & 1][rg * 4 + 2 * (lane & 1) + rsel].y * scale; }
#define STATIC_STEP(I) { \
            rg = ((cst + (I)) & 3) * 8 + wave; \
            LOADZ2() \
            K_TAB((I) + 1) K_BODY((I) + 1) }
        CS_DMA(wave, (cst + 2) & 3)
        RDYWAIT(0, 1u) RDYWAIT(1, 2u)
        LOADQ()
#pragma unroll
        for (int i = 0; i < 3; i++) { Rp[i] = sm.q[1][3 * sub + i]; Rn[i] = __builtin_amdgcn_alignbit(Rp[i], Rp[i], 16) ^ 0x80000000u; }
        STATIC_STEP(0)
        asm volatile("s_waitcnt vmcnt(11)" ::: "memory");
        REPACK(wave, (cst + 1) & 3)
        TABDONE(1)
        SISSUE(2)
        CS_DMA(wave, (cst + 3) & 3)
        STATIC_STEP(1)
        asm volatile("s_waitcnt vmcnt(11)" ::: "memory");
        REPACK(wave, (cst + 2) & 3)
        TABDONE(2)
        SISSUE(3)
        STATIC_STEP(2)
        asm volatile("s_waitcnt vmcnt(8)" ::: "memory");
        REPACK(wave, (cst + 3) & 3)
        TABDONE(3)
        GRAB(2)
        vut = LDV(&sm.utab[lane]);
        STATIC_STEP(3)
        CBAR();
        LDS_ADD(&sm.cnt[0], 4u) LDS_ADD(&sm.cnt[1], 4u)
        PLAN()
        LAUNCH()
        u = un; rg = nrg; tdc = tdn + 10;
        bool tabok = false;
        __builtin_amdgcn_s_setprio(2);
#pragma unroll 1
        while (u < NU) {
            GRAB(2)
            mg = LDV(&sm.merged);
            vut = LDV(&sm.utab[lane]);
            LINE()
            if (!tabok) {
                for (;;) { const unsigned tb_ = lds_ld(&sm.tabs); tabok = tb_ == ~0u; if (((tb_ >> rg) & 3u) == 3u) break; SLEEP(1) }
                CBAR();
            }
            K_TAB(0)
            if (first) {
                unsigned fl = LDV(&sm.rdy[u % NIN]);
                CBAR();
                LOADQ() LOADZ()
                if (__builtin_amdgcn_readfirstlane(fl) != u + 1) { RDYWAIT(u % NIN, u + 1) LOADQ() LOADZ() }
            } else LOADZ()
            K_PRE4(0)
            PLAN()
            K_WAIT(0, 0) K_FMA(0) K_WAIT(0, 1) K_FMA(1)
            asm volatile("" :: "v"(kres[0]), "v"(kres[1]));
            K_WAIT(0, 2) K_WAIT(0, 3)
            ARRIVED()
            K_FMA(2) K_FMA(3) K_RED(0)
            np += 2;
            ADVANCE(false)
        }
#pragma unroll 1
        while (u < 2 * NU) {
            GRAB(2)
            mg = LDV(&sm.merged);
            LINE()
            const unsigned vr_ = LDV(&sm.vrdy);
            CBAR();
            const unsigned short* wp = &sm.wv[went(u - NU) + (QUARTER(u - NU) ? rg & 7 : rg) * 4];
            v4i pw = *(const v4i*)wp;
            if (first && !((__builtin_amdgcn_readfirstlane(vr_) >> (u - NU)) & 1)) { while (!((lds_ld(&sm.vrdy) >> (u - NU)) & 1)) SLEEP(1); CBAR(); pw = *(const v4i*)wp; }
            const double w0 = (short)pw.x, w1 = pw.x >> 16, w2 = (short)pw.y, w3 = pw.y >> 16, w4 = (short)pw.z, w5 = pw.z >> 16, w6 = (short)pw.w, w7 = pw.w >> 16;
            asm volatile("" :: "v"(w0), "v"(w1), "v"(w2), "v"(w3), "v"(w4), "v"(w5), "v"(w6), "v"(w7));
            PLAN()
            WAITT(0, 0, 6); V_FMA(0, w0, w1) V_PIN()
            WAITT(1, 1, 4); V_FMA(1, w2, w3) V_PIN()
            WAITT(2, 2, 2); V_FMA(2, w4, w5) V_PIN()
            WAITT(3, 3, 0);
            np += 2;
            if (none) { V_FMA(3, w6, w7) FLUSH(true) break; }
            ARRIVED()
            V_FMA(3, w6, w7)
            ADVANCE(true)
        }
        __builtin_amdgcn_s_setprio(0);
        {
            const int head = lds_ld(&sm.chead);
            const unsigned char* rb = ws + WS_PART + head * (NSPLIT * 512) + wave * 1024;
            const unsigned t15 = lds_ld(&sm.tag_lo) & 0x7fffu;
            bool got; v2u f[2];
#define POLL(S) asm volatile("global_load_dwordx2 a[%c0:%c0+1], %1, %2 sc1\n\tglobal_load_dwordx2 a[%c0+2:%c0+3], %1, %2 offset:512 sc1" :: "i"(AR(S)), "v"(lane * 8), "s"(rb) : "memory");
#define TAKE(S, N) { v2u r_[2]; \
                asm volatile("s_waitcnt vmcnt(%c5)\n\tv_accvgpr_read_b32 %0, a[%c4]\n\tv_accvgpr_read_b32 %1, a[%c4+1]\n\tv_accvgpr_read_b32 %2, a[%c4+2]\n\tv_accvgpr_read_b32 %3, a[%c4+3]" \
                             : "=v"(r_[0].x), "=v"(r_[0].y), "=v"(r_[1].x), "=v"(r_[1].y) : "i"(AR(S)), "i"(N)); \
                f[0] = r_[0]; f[1] = r_[1]; \
                got = __builtin_amdgcn_ballot_w64(((r_[0].y >> 17 ^ t15) | (r_[1].y >> 17 ^ t15)) == 0) == ~0ull; }
            if (head < NH - GSZ) while (lds_ld(&sm.cnt[(2 * NU - 1) % NOUT]) < 32 && lds_ld(&sm.merged) < 2 * NU) SLEEP(1)
            POLL(0)
            TAKE(0, 0)
            if (!got) {
                POLL(0) SLEEP(3) POLL(1)
#pragma unroll 1
                for (;;) {
                    TAKE(0, 2) if (got) break; SLEEP(1) POLL(0)
                    TAKE(1, 2) if (got) break; SLEEP(1) POLL(1)
                }
            }
            const u64 hb[2] = {__builtin_amdgcn_ballot_w64((f[0].y & 0x10000u) != 0), __builtin_amdgcn_ballot_w64((f[1].y & 0x10000u) != 0)};
            const float L0 = __uint_as_float((unsigned)hb[0]), L1 = __uint_as_float((unsigned)hb[1]), M = fmaxf(L0, L1);
            const float w0 = __builtin_amdgcn_exp2f(L0 - M), w1 = __builtin_amdgcn_exp2f(L1 - M);
            const float c0 = ldexpf(w0, (int)((unsigned)(hb[0] >> 32) & 255u) - 141), c1 = ldexpf(w1, (int)((unsigned)(hb[1] >> 32) & 255u) - 141);
            v4f* pb = (v4f*)((char*)&sm.cs[0][0][0] + 65536);
            float* pm = (float*)(pb + NSW * 64);
            pb[wave * 64 + lane] = (v4f){fmaf(c1, (float)(short)f[1].x, c0 * (float)(short)f[0].x), fmaf(c1, (float)((int)f[1].x >> 16), c0 * (float)((int)f[0].x >> 16)),
                                         fmaf(c1, (float)(short)f[1].y, c0 * (float)(short)f[0].y), w0 + w1};
            if (lane == 0) pm[wave] = M;
            CBAR();
            { u64 ex_; asm volatile("s_mov_b64 %0, exec\n\ts_mov_b64 exec, 1\n\tds_add_rtn_u32 a15, %1, a13\n\ts_mov_b64 exec, %0" : "=&s"(ex_) : "v"(lds_addr(&sm.cdone)) : "memory"); }
            const v4f ma = *(const v4f*)pm, mb = *(const v4f*)(pm + 4);
            v4f ps[NSW];
#pragma unroll
            for (int w = 0; w < NSW; w++) ps[w] = pb[w * 64 + lane];
            GRABBED()
            if (g == NSW - 1) {
                const float mw[NSW] = {ma.x, ma.y, ma.z, ma.w, mb.x, mb.y, mb.z, mb.w};
                float Mx = mw[0];
#pragma unroll
                for (int w = 1; w < NSW; w++) Mx = fmaxf(Mx, mw[w]);
                v4f sum = {0.f, 0.f, 0.f, 0.f};
#pragma unroll
                for (int w = 0; w < NSW; w++) sum += ps[w] * __builtin_amdgcn_exp2f(mw[w] - Mx);
                if (head >> 4 == WQ) {
                    while (lds_ld(&sm.qdone) != 3) SLEEP(0)
                    CBAR();
                    const v4f mq = *(const v4f*)(pm + NSW), q0 = pb[(NSW + 1) * 64 + lane], q1 = pb[(NSW + 2) * 64 + lane], q2 = pb[(NSW + 3) * 64 + lane];
                    const float Mn = fmaxf(fmaxf(Mx, mq.x), fmaxf(mq.y, mq.z));
                    sum = sum * __builtin_amdgcn_exp2f(Mx - Mn) + q0 * __builtin_amdgcn_exp2f(mq.x - Mn) + q1 * __builtin_amdgcn_exp2f(mq.y - Mn) + q2 * __builtin_amdgcn_exp2f(mq.z - Mn);
                }
                const float inv = __builtin_amdgcn_rcpf(sum.w);
                __hip_bfloat16* o = out + (size_t)head * DH + 3 * lane;
                o[0] = __float2bfloat16(sum.x * inv); o[1] = __float2bfloat16(sum.y * inv); o[2] = __float2bfloat16(sum.z * inv);
            }
        }
        return;
    }

    u64 E1;
    if (wave == NSW) {
        unsigned ref;
        const unsigned sidx = (blockIdx.x & 7) | ((blockIdx.x >> 7) << 3);
#define SAMPLE(K) ((((blockIdx.x >> 3) - (blockIdx.x & 7) - (K) * 5) & 15) == 0)
#define BTAG(K) ((btag + (K)) & 0xfffffu)
#define POST(K, C) { if (lane == 0) __hip_atomic_store((u64*)(ws + (bd + (K) * RST + (sidx >> 3) * 256 + (sidx & 7) * 8)), (u64)(C) | ((u64)(((C) - ref) | BTAG(K) << 12) << 32), __ATOMIC_RELAXED, __HIP_MEMORY_SCOPE_AGENT); }
        const unsigned own = lds_ld(&sm.line);
        if (SAMPLE(0) && lane < NTRY) __hip_atomic_store((unsigned*)(ws + WS_HELLO + lane * 768) + sidx, own, __ATOMIC_RELAXED, __HIP_MEMORY_SCOPE_AGENT);
        const u64 E = __hip_atomic_load((const u64*)(ws + WS_EPOCH), __ATOMIC_RELAXED, __HIP_MEMORY_SCOPE_AGENT);
        E1 = E + 1;
        if (lane == 0) { sm.tag_lo = (unsigned)E1; sm.tag_hi = (unsigned)(E1 >> 32); CBAR(); lds_st(&sm.tag_rdy, 1u); }
        const unsigned btag = __builtin_amdgcn_readfirstlane((unsigned)E1) * 61u, bd = WS_BOARD + (btag & 7) * (NRND * RST);
        ref = own;
        int off = 0;
        {
            unsigned tries = 0, cur = own, tl = own;
            bool news = false;
#pragma unroll 1
            while (!news || cur != ref) {
                const unsigned now = NOW();
                if (tries < NTRY && (int)(now - own) >= (int)(TA + tries * TA2)) {
                    v16u x;
                    asm volatile("s_load_dwordx16 %0, %1, %2\n\ts_waitcnt lgkmcnt(0)" : "=&s"(x) : "s"(ws), "s"(__builtin_amdgcn_readfirstlane(WS_HELLO + tries * 768)) : "memory");
                    unsigned bad = 0; int sum = 0, dif = 0;
#pragma unroll
                    for (int i = 0; i < 16; i++) { bad |= x[i] - own + 512; sum += x[i] - x[0]; dif += i & 4 ? x[i] : -x[i]; }
                    tries++;
                    if (bad < 1024) { const unsigned com = x[0] + (sum >> 4) + abs(dif) * (8 - KC) / 128; ref = x[0] + ((sum + (blockIdx.x & 4 ? dif : -dif)) >> 4); off = ref - com;
                        tries = NTRY; tl = NOW(); if (lane == 0) lds_st(&sm.aline, com); }
                    continue;
                }
                if (!news && lds_ld(&sm.utab[31]) != ~0u) { news = true; CBAR(); if (cur == ref) tl = now; ref = lds_ld(&sm.aline) + off; tries = NTRY; }
                const int d = (now - tl) >> CSH;
                if (cur != ref && d) { tl += d << CSH; cur += min(max((int)(ref - cur), -d), d); if (lane == 0) lds_st(&sm.line, cur); }
                SLEEP(2)
            }
        }
        ref -= off;
        const unsigned ref0 = ref;
        const unsigned lead = (blockIdx.x >> 3) == 9 ? 0 : LEAD;
        unsigned posted = ref, lagf = ref, tpost = ref + TB1, tpoll = tpost + RDD + lead, kw = 0, kr = 0, cur = ref + off, khold = 0;
        int o = off; bool held = false;
#pragma unroll 1
        for (;;) {
            const unsigned p = lds_ld(&sm.grab);
            if (p >= NPC) break;
            lagf += (int)(lds_ld(&sm.late) - lagf) >> 1;
            { const unsigned l = lds_ld(&sm.line); if (l != cur) { cur = l; khold = kw; held = true; } }
            const unsigned now = NOW();
            if (o) { o = off * min(max((int)(ref0 + TC0 + (1 << TCS) - now), 0), 1 << TCS) >> TCS; if (!held && cur != ref + o) { cur = ref + o; if (lane == 0) lds_st(&sm.line, cur); } }
            if (kw < NRND && (int)(now - tpost) >= 0) {
                const unsigned c = ref + min((p >= PSL0 && (int)(lagf - cur) > LHI ? lagf - LOK : cur) - o - ref, 4000u);
                if (SAMPLE(kw) && (c != ref || (ref != posted && (sidx & 7) == (kw & 7)))) POST(kw, c)
                posted = ref;
                tpost += PER; kw++;
            }
            if (kr < NRND && (int)(now - tpoll) >= 0) {
                v16u xa, xb;
                asm volatile("s_load_dwordx16 %0, %2, %3\n\ts_load_dwordx16 %1, %2, %4\n\ts_waitcnt lgkmcnt(0)" : "=&s"(xa), "=&s"(xb)
                             : "s"(ws), "s"(__builtin_amdgcn_readfirstlane(bd + kr * RST)), "s"(__builtin_amdgcn_readfirstlane(bd + kr * RST + 256)) : "memory");
                int nb = 0, sg = 0;
#pragma unroll
                for (int i = 0; i < 16; i++) {
                    const unsigned lo = i < 8 ? xa[2 * i & 15] : xb[2 * i & 15], hi = i < 8 ? xa[(2 * i + 1) & 15] : xb[(2 * i + 1) & 15];
                    if (hi >> 12 == BTAG(kr) && lo - ref0 < 8000) { nb = max(nb, (int)(lo - (hi & 4095u) - ref)); sg += hi & 4095u; }
                }
                ref += nb + (sg >> 4);
                if (cur != ref + o && (kr >= khold || (int)(ref + o - cur) > 0)) { cur = ref + o; held = false; if (lane == 0) lds_st(&sm.line, cur); }
                tpoll += PER; kr++;
            }
            SLEEP(2)
        }
    } else if (wave == NSW + 1 || wave == NSW + 2) {
        const int ql = lane < 48 ? lane : 47;
        unsigned c1, c2, s1, s2;
#define QT_LOADS() { int qo_ = ql; asm volatile("" : "+v"(qo_)); \
                     c1 = ((const unsigned*)q_cos)[qo_]; c2 = ((const unsigned*)q_cos)[48 + qo_]; s1 = ((const unsigned*)q_sin)[qo_]; s2 = ((const unsigned*)q_sin)[48 + qo_]; }
        unsigned q1, q2, lkz, lks, lvz, lvs;
        const unsigned sofs = R8(((unsigned)lane >> 1) & 7) * 2 + (lane & 1);
#define UNIT_LOADS(HEAD, GRP) { \
        q1 = ((const unsigned*)(q + (size_t)(HEAD) * DH))[ql]; q2 = ((const unsigned*)(q + (size_t)(HEAD) * DH))[48 + ql]; \
        const size_t so = (size_t)(HEAD) * (SKV / 2) + (GRP) * 256 + sofs; \
        lkz = *((const unsigned*)k_zeros + so); lks = *((const unsigned*)k_scales + so); \
        lvz = *((const unsigned*)v_zeros + so); lvs = *((const unsigned*)v_scales + so); }
#define UNIT_FILL(SL, U) { \
            v4f qq; \
            qq.x = bfr(bfr(bflo(q1) * bflo(c1)) + bfr(-bflo(q2) * bflo(s1))); \
            qq.y = bfr(bfr(bflo(q2) * bflo(c2)) + bfr(bflo(q1) * bflo(s2))); \
            qq.z = bfr(bfr(bfhi(q1) * bfhi(c1)) + bfr(-bfhi(q2) * bfhi(s1))); \
            qq.w = bfr(bfr(bfhi(q2) * bfhi(c2)) + bfr(bfhi(q1) * bfhi(s2))); \
            *(v2u*)&sm.q[SL][2 * ql] = (v2u){(__float_as_uint(qq.x) >> 16) | __float_as_uint(qq.y), (__float_as_uint(qq.z) >> 16) | __float_as_uint(qq.w)}; \
            *(v4f*)&sm.kz[SL][2 * lane] = (v4f){bflo(lkz), bflo(lks), bfhi(lkz), bfhi(lks)}; \
            *(v4f*)&sm.vz[SL][2 * lane] = (v4f){bflo(lvz), bflo(lvs), bfhi(lvz), bfhi(lvs)}; \
            CBAR(); \
            if (lane == 0) lds_st(&sm.rdy[SL], (U) + 1); \
            CBAR(); }
        const unsigned dw = NSW + 2 - wave;
#define TOUCH() { const unsigned x_ = ((blockIdx.x >> 3) - 18) * 64 + lane; \
            const char* p_ = x_ < 384 ? (const char*)(x_ < 192 ? qk : qv) + (size_t)(x_ < 192 ? x_ : x_ - 192) * (2u << 20) + (x_ * 8 + (blockIdx.x & 7)) * 640 : (const char*)out + (x_ - 384) * 4096; \
            if (x_ < 408) asm volatile("global_load_dword a0, %0, off nt" :: "v"(p_) : "a0"); }
        QT_LOADS()
        UNIT_LOADS(head0 + dw * (GSZ + 1 - 2 * side), lane >> 4)
        if (dw) TOUCH()
#pragma unroll 1
        for (unsigned u = dw;;) {
            while (u >= lds_ld(&sm.merged) + NIN) SLEEP(4);
            CBAR();
            UNIT_FILL(u % NIN, u)
            u += 2;
            if (u >= NU) break;
            if (u == 4) {
                const unsigned tpf = tdue(PSF - 1) + TFO;
                { int d; while ((d = (int)(lds_ld(&sm.line) + tpf - NOW())) > 0 && d < 3000) SLEEP(1) }
#define PF3(P) asm volatile("global_load_dwordx4 a[0:3], %0, %1\n\tglobal_load_dwordx4 a[0:3], %0, %1 offset:1024\n\tglobal_load_dwordx4 a[0:3], %0, %1 offset:2048" \
                            :: "v"(64 * SKV * 2 + blockIdx.x * 3072 + lane * 16), "s"(P) : "a0", "a1", "a2", "a3")
                PF3(k_zeros); PF3(k_scales); PF3(v_zeros); PF3(v_scales);
                asm volatile("global_load_dword a0, %0, %1" :: "v"(blockIdx.x * (DH * 2) + ql * 8), "s"(q) : "a0");
            }
            const unsigned tin = tdue(first_of(u) - (QUARTER(u) ? 24 : 0) - LEADP + (split & 7) * DPH);
            { int d; while ((d = (int)(lds_ld(&sm.line) + tin - NOW())) > 0 && d < 3000) SLEEP(4) }
            unsigned hd;
            while ((hd = lds_ld(&sm.utab[u])) == ~0u) SLEEP(2)
            UNIT_LOADS(hd & 255, QUARTER(u) ? rank & 3 : lane >> 4)
        }
        while (lds_ld(&sm.tag_rdy) == 0) SLEEP(1);
        CBAR();
        E1 = (u64)lds_ld(&sm.tag_lo) | ((u64)lds_ld(&sm.tag_hi) << 32);
        if (wave == NSW + 1 && blockIdx.x == 0) {
            for (;;) {
                bool ok = true;
#pragma unroll
                for (int i = 0; i < NH / 64; i++) ok &= __hip_atomic_load((const u64*)(ws + WS_POST) + i * 64 + lane, __ATOMIC_RELAXED, __HIP_MEMORY_SCOPE_AGENT) >> 24 == (E1 << 24) >> 24;
                if (__builtin_amdgcn_ballot_w64(ok) == ~0ull) break;
                SLEEP(64);
            }
            if (lane == 0) __hip_atomic_store((u64*)(ws + WS_EPOCH), E1, __ATOMIC_RELAXED, __HIP_MEMORY_SCOPE_AGENT);
        }
        if (wave == NSW + 2) {
            const unsigned t16 = (unsigned)E1 & 0xffffu, t16b = ((unsigned)E1 + ((unsigned)E1 >> 16)) & 0xffffu;
            const __amdgpu_buffer_rsrc_t rsrc = __builtin_amdgcn_make_buffer_rsrc(ws + WS_SWAP + (blockIdx.x ^ 4) * 5120, 0, 5120, 0x00020000);
            const unsigned swp = lds_ld(&sm.swp);
            if (swp) {
                unsigned use = 0;
#pragma unroll
                for (int i = 0; i < 5; i++) use |= ((swp >> wunit(i * 64 + lane)) & 1) << i;
                const unsigned tg = tdue(PSG - 1) + (lds_ld(&sm.gap) & 0xffffu);
                while ((int)(NOW() - lds_ld(&sm.line) - tg) < 0) SLEEP(2)
                for (;;) {
                    v4u d[5]; bool ok = true;
#pragma unroll
                    for (int i = 0; i < 5; i++) d[i] = __builtin_amdgcn_raw_buffer_load_b128(rsrc, (use >> i) & 1 ? (i * 64 + lane) * 16 : 0x40000000, 0, SC1 | 2);
#pragma unroll
                    for (int i = 0; i < 5; i++) if ((use >> i) & 1) {
                        ok &= d[i].y >> 16 == t16 && d[i].w >> 16 == t16b;
                        unsigned* w = (unsigned*)&sm.wv[144 * NMIX + (i * 64 + lane) * 6];
                        w[0] = d[i].x; w[1] = (d[i].y & 0xffffu) | (d[i].z << 16); w[2] = (d[i].z >> 16) | (d[i].w << 16);
                    }
                    if (__builtin_amdgcn_ballot_w64(ok) == ~0ull) break;
                    SLEEP(32)
                }
                CBAR();
                LDS_OR(&sm.vrdy, swp)
            }
        }
    } else {
        const unsigned eu = lane >= NU ? lane - NU : lane, win = min(first_of(eu) >> 5, 15u), pair = QUARTER(eu) ? 2 * (eu - WQ) + (rank >> 2) : rank;
        const unsigned rot2 = ((const unsigned char*)ROT)[win * 8 + pair];
#define UENT(BIT) (win * 16 + 2 * pair + ((BIT) ^ side) | (QUARTER(eu) ? (rank - (eu - WQ)) * 8 & 31 : (rot2 >> ((BIT) ^ side) * 4 & 15) * 2) << 8)
        unsigned far = 0;
        const unsigned hp = (blockIdx.x & 1) * NH + (blockIdx.x >> 1) * 4 + 2;
        if (blockIdx.x < 144) {
            const char* pb = (blockIdx.x < 128 ? (const char*)(hp >= NH ? qv : qk) + (size_t)(hp & (NH - 1)) * (SKV * DH * 4) + SKV * DH * 2
                                               : (const char*)ws + WS_PART + (4u << 20) + (blockIdx.x & 15) * 65536) + ((NOW() * 0x9e3779b1u) >> 26) * 128;
            unsigned mn[2] = {~0u, ~0u};
#pragma unroll 1
            for (unsigned i = 1; i < 2 * NPRB + 2; i++) {
                u64 t0, t1; unsigned dm;
                asm volatile("s_memtime %0\n\ts_waitcnt lgkmcnt(0)\n\ts_load_dword %2, %3, 0x0\n\ts_waitcnt lgkmcnt(0)\n\ts_memtime %1\n\ts_waitcnt lgkmcnt(0)"
                             : "=&s"(t0), "=&s"(t1), "=&s"(dm) : "s"(pb + i * 8192) : "memory");
                const unsigned t = (unsigned)(t1 - t0) < 330 || i == 1 ? ~0u : (unsigned)(t1 - t0);
                if (i & 1) mn[1] = min(mn[1], t); else mn[0] = min(mn[0], t);
            }
            far = mn[0] > mn[1];
        }
        if (lane < NMIX) lds_st(&sm.utab[lane], UENT(lane & 1));
        while (lds_ld(&sm.tag_rdy) == 0) SLEEP(1);
        CBAR();
        E1 = (u64)lds_ld(&sm.tag_lo) | ((u64)lds_ld(&sm.tag_hi) << 32);
        {
            const unsigned own = lds_ld(&sm.line), tg = (unsigned)E1;
            if (lane == 0) __hip_atomic_store((u64*)(ws + WS_POST) + blockIdx.x, E1 << 24 | min(lds_ld(&sm.slow), 4000u) << 1 | ((far ^ (blockIdx.x >> 2) ^ (blockIdx.x < 128 ? 0x96 >> (hp & 7) : 0)) & 1), __ATOMIC_RELAXED, __HIP_MEMORY_SCOPE_AGENT);
            const __amdgpu_buffer_rsrc_t nrs = __builtin_amdgcn_make_buffer_rsrc(ws + WS_NEWS, 0, 8 * 1280, 0x00020000);
            if (blockIdx.x == MASTER) {
                while ((int)(NOW() - own) < TMAP) SLEEP(2)
                const unsigned a = tg << 24, b = (unsigned)(E1 >> 8);
                v4u x; v2u z; u64 v0, v1;
#pragma unroll 1
                for (int i = 0; i < 8; i++) {
                    x = __builtin_amdgcn_raw_buffer_load_b128(__builtin_amdgcn_make_buffer_rsrc(ws + WS_POST, 0, 1024, 0x00020000), lane * 16, 0, SC1);
                    z = __builtin_amdgcn_raw_buffer_load_b64(__builtin_amdgcn_make_buffer_rsrc(ws + WS_POST + 1024, 0, 128, 0x00020000), lane * 8, 0, SC1);
                    v0 = __builtin_amdgcn_ballot_w64(((x.x ^ a) >> 24 | (x.y ^ b)) == 0); v1 = __builtin_amdgcn_ballot_w64(((x.z ^ a) >> 24 | (x.w ^ b)) == 0);
                    if (__builtin_popcountll(v0) + __builtin_popcountll(v1) >= MINV) break;
                    SLEEP(8)
                }
                const u64 m0 = __builtin_amdgcn_ballot_w64(x.x & 1) & v0, m1 = __builtin_amdgcn_ballot_w64(x.z & 1) & v1;
                const int s0 = lane < 43 ? min(max(lane - VW, 4 * NMIX), 42 - 2 * VW) : min(max(lane - VW, 43), 63 - 2 * VW);
#define PC(M) __builtin_popcount((unsigned)((M) >> s0) & ((2u << 2 * VW) - 1))
                const u64 g0 = __builtin_amdgcn_ballot_w64(2 * PC(m0) > PC(v0)), g1 = __builtin_amdgcn_ballot_w64(2 * PC(m1) > PC(v1));
                const float sl = wave_sum(((v0 >> lane & 1) ? (float)(x.x >> 1 & 4095u) : 0.f) + ((v1 >> lane & 1) ? (float)(x.z >> 1 & 4095u) : 0.f)) / ((8.f - PRE0) * (__builtin_popcountll(v0) + __builtin_popcountll(v1)) + 1.f);
                const unsigned al = lds_ld(&sm.aline), mean = (al ? al : own) + (int)fmaxf(sl - SL0, 0.f);
                const bool vz = lane < 16 && ((z.x ^ a) >> 24 | (z.y ^ b)) == 0;
                const unsigned gw = 2 * __builtin_popcountll(__builtin_amdgcn_ballot_w64(vz && (z.x & 1))) > __builtin_popcountll(__builtin_amdgcn_ballot_w64(vz));
                const unsigned k = lane & 3;
                if (lane < 32 && k < 3) __builtin_amdgcn_raw_buffer_store_b128((v4u){k == 0 ? (unsigned)g0 : k == 1 ? (unsigned)g1 : mean, tg, k == 0 ? (unsigned)(g0 >> 32) : k == 1 ? (unsigned)(g1 >> 32) : gw, tg},
                                                                              nrs, (lane >> 2) * 1280 + k * 16, 0, SC1);
            }
            while ((int)(NOW() - own) < TMAP + TRES) SLEEP(2)
            v4u y;
            for (;;) {
                y = __builtin_amdgcn_raw_buffer_load_b128(nrs, lane < 3 ? (blockIdx.x & 7) * 1280 + lane * 16 : 0x40000000, 0, SC1);
                if (((unsigned)__builtin_amdgcn_ballot_w64(y.y == tg && y.w == tg) & 7u) == 7u) break;
                SLEEP(8)
            }
            const u64 f0 = (unsigned)__builtin_amdgcn_readlane(y.x, 0) | (u64)(unsigned)__builtin_amdgcn_readlane(y.z, 0) << 32, f1 = (unsigned)__builtin_amdgcn_readlane(y.x, 1) | (u64)(unsigned)__builtin_amdgcn_readlane(y.z, 1) << 32;
            if (lane == 2) { sm.aline = y.x - own + 500 < 1000 ? y.x : own; sm.chead = (blockIdx.x & ~5u) | ((blockIdx.x >> 2 ^ y.z) & 1) | (blockIdx.x & 1) << 2; }
            unsigned bit = ((unsigned)((lane >= NU ? f1 : f0) >> (4 * win + (pair >> 1))) ^ (0x66 >> pair)) & 1;
            if (eu < NMIX) bit = eu & 1;
            const u64 bm = __builtin_amdgcn_ballot_w64(bit);
            const unsigned nsw = __builtin_popcountll((f0 ^ f1) >> (4 * NMIX));
            if (lane == 0) { sm.swp = (unsigned)(bm ^ (bm >> NU)) & ((1u << NU) - 1); lds_st(&sm.gap, nsw ? (TG0 + TG1 * nsw / 8) * 0x10001u + ((TH0 + TH1 * nsw / 8) << 16) : 0u); }
            CBAR();
            lds_st(&sm.utab[lane], UENT(bit));
        }
        const unsigned swp = lds_ld(&sm.swp);
        const unsigned T = lds_ld(&sm.tag_lo);
        const unsigned t15 = T & 0x7fffu;
        const __amdgpu_buffer_rsrc_t rsrc = __builtin_amdgcn_make_buffer_rsrc(ws + WS_PART + split * 512, 0, NRR * NSPLIT * 512 - split * 512, 0x00020000);
        v2u* recs = (v2u*)&sm.cs[0][0][0];
#pragma unroll
        for (int i = 0; i < NOUT * 3; i++) (&sm.acc[0][0])[i * 64 + lane] = 0.0;
        CBAR();
        unsigned vut = sm.utab[NU + (lane & 31)] & 255;
        if (QUARTER((unsigned)lane) && (rank & 3)) vut = NH - 16 + (rank & 3) * 16 + ((vut & 15) ^ 1);
        __builtin_amdgcn_s_setprio(3);
        bool direct = false, last = false;
#define PUBLISH(N) { \
        _Pragma("unroll 1") for (unsigned i = 0; i < 20; i += 4) { \
            const unsigned j = i < 10 ? i + WQ : i - 12; \
            v2u r[4]; \
            _Pragma("unroll") for (int k = 0; k < 4; k++) r[k] = recs[(j + k < 20 ? j + k : j + k - 12) * 64 + lane]; \
            _Pragma("unroll") for (int k = 0; k < 4; k++) { const unsigned x = j + k < 20 ? j + k : j + k - 12; \
                if (x < (N)) __builtin_amdgcn_raw_buffer_store_b64(r[k], rsrc, lane * 8, __builtin_amdgcn_readlane(vut, x) * (NSPLIT * 512), SC1); } } }
#pragma unroll 1
        for (unsigned u = 0; u < 2 * NU; u++) {
            const unsigned e = u % NU, so = u % NOUT;
            if (u >= NU) while (!((lds_ld(&sm.vrdy) >> e) & 1)) SLEEP(4)
            CBAR();
            const v4f h = *(const v4f*)&sm.wv[went(e) + (QUARTER(e) ? 32 : 128)];
            const unsigned row = __builtin_amdgcn_readlane(vut, e) * (NSPLIT * 512);
            double a0, a1, a2;
            for (;;) {
                if (!direct && (lds_ld(&sm.grab) >= NPC - PUBP || last)) { direct = true; wave_sync(); PUBLISH(u - NU) }
                const unsigned cn = __hip_atomic_load(&sm.cnt[so], __ATOMIC_RELAXED, __HIP_MEMORY_SCOPE_WORKGROUP);
                CBAR();
                if (direct) { a0 = sm.acc[so][lane]; a1 = sm.acc[so][64 + lane]; a2 = sm.acc[so][128 + lane]; }
                if (__builtin_amdgcn_readfirstlane(cn) == (QUARTER(e) ? 8 : 32)) { if (direct || u != 2 * NU - 1) break; last = true; }
                if (direct) SLEEP(0) else SLEEP(1)
            }
            CBAR();
            if (!direct) { a0 = sm.acc[so][lane]; a1 = sm.acc[so][64 + lane]; a2 = sm.acc[so][128 + lane]; }
            if (u < NU) {
                float2 lg = *(const float2*)&sm.ent[so][2 * lane];
                if (QUARTER(e) && lane >> 4 != (rank & 3)) lg = make_float2(-INFINITY, -INFINITY);
                const v4f zs = *(const v4f*)&sm.vz[u % NIN][2 * lane];
                const float m = wave_max(fmaxf(lg.x, lg.y));
                const float p0 = __expf(lg.x - m), p1 = __expf(lg.y - m);
                const float x0 = p0 * zs.y, x1 = p1 * zs.w;
                const unsigned eb = max(__float_as_uint(wave_max(fmaxf(fabsf(x0), fabsf(x1)))) >> 23, 14u);
                const float f = __uint_as_float((268u - eb) << 23);
                const float w0 = fminf(fmaxf(rintf(x0 * f), -32767.f), 32767.f), w1 = fminf(fmaxf(rintf(x1 * f), -32767.f), 32767.f);
                const float den = wave_sum(p0 + p1);
                const float bz = wave_sum(fmaf(w0, zs.x, w1 * zs.z));
                const float L = fmaf(m, 1.4426950408889634f, __builtin_amdgcn_logf(den));
                unsigned* wd = (unsigned*)(((swp >> u) & 1 ? sm.wo : sm.wv) + went(u));
                if (!QUARTER(u) || lane >> 4 == (rank & 3)) wd[QUARTER(u) ? lane & 15 : lane] = ((unsigned)(int)w0 & 0xffffu) | ((unsigned)(int)w1 << 16);
                if (lane == 0) *(v4f*)&wd[QUARTER(u) ? 16 : 64] = (v4f){ldexpf(1.f / den, (int)eb - 141), bz, L, 0.f};
                CBAR();
                if (!((swp >> u) & 1)) LDS_OR(&sm.vrdy, 1u << u)
            } else {
                const float n0 = (float)(a0 - (double)h.y) * h.x, n1 = (float)(a1 - (double)h.y) * h.x, n2 = (float)(a2 - (double)h.y) * h.x;
                const unsigned e2 = max(__float_as_uint(wave_maxv(fmaxf(fmaxf(fabsf(n0), fabsf(n1)), fabsf(n2)))) >> 23, 14u);
                const float f2 = __uint_as_float((268u - e2) << 23);
                const unsigned m01 = __builtin_bit_cast(unsigned, __builtin_amdgcn_cvt_pk_i16((int)rintf(n0 * f2), (int)rintf(n1 * f2)));
                const unsigned m2 = __builtin_bit_cast(unsigned, __builtin_amdgcn_cvt_pk_i16((int)rintf(n2 * f2), 0));
                const unsigned bit = ((lane < 32 ? __float_as_uint(h.z) : e2) >> (lane & 31)) & 1u;
                const v2u r = {m01, m2 | (bit << 16) | (t15 << 17)};
                if (direct) __builtin_amdgcn_raw_buffer_store_b64(r, rsrc, lane * 8, row, SC1);
                else recs[e * 64 + lane] = r;
                CBAR();
                sm.acc[so][lane] = 0.0; sm.acc[so][64 + lane] = 0.0; sm.acc[so][128 + lane] = 0.0;
            }
            CBAR();
            if (lane == 0) { lds_st(&sm.cnt[so], 0u); CBAR(); lds_st(&sm.merged, u + 1); }
            if (u == NU - 1 && swp) {
                wave_sync();
                const __amdgpu_buffer_rsrc_t srs = __builtin_amdgcn_make_buffer_rsrc(ws + WS_SWAP + blockIdx.x * 5120, 0, 5120, 0x00020000);
                v4u d[5]; unsigned o[5];
#pragma unroll
                for (int i = 0; i < 5; i++) {
                    const unsigned* w = (const unsigned*)&sm.wo[144 * NMIX + (i * 64 + lane) * 6];
                    const unsigned d0 = w[0], d1 = w[1], d2 = w[2];
                    d[i] = (v4u){d0, (d1 & 0xffffu) | (T << 16), (d1 >> 16) | (d2 << 16), (d2 >> 16) | ((T + (T >> 16)) << 16)};
                    o[i] = (swp >> wunit(i * 64 + lane)) & 1 ? (i * 64 + lane) * 16 : 0x40000000;
                }
                while ((int)(NOW() - lds_ld(&sm.line)) < (int)tdue(PSW) + TWO) SLEEP(1)
#pragma unroll
                for (int i = 0; i < 5; i++) __builtin_amdgcn_raw_buffer_store_b128(d[i], srs, o[i], 0, SC1 | 2);
            }
        }
        return;
    }
    if (blockIdx.x >> 4 == WQ) {
        while (lds_ld(&sm.grab) < NPC + 2 * NSW) SLEEP(2)
        const __amdgpu_buffer_rsrc_t rq = __builtin_amdgcn_make_buffer_rsrc(ws + WS_PART + (size_t)(NH + (wave - NSW) * 16 + ((lds_ld(&sm.chead) & 15) ^ 1)) * (NSPLIT * 512), 0, NSPLIT * 512, 0x00020000);
        const unsigned t15 = lds_ld(&sm.tag_lo) & 0x7fffu;
        v2u x[NSPLIT];
        unsigned vl = 0, ve = 0;
        for (;;) {
            bool ok = true;
#pragma unroll
            for (int k = 0; k < NSPLIT; k++) x[k] = __builtin_amdgcn_raw_buffer_load_b64(rq, lane * 8 + k * 512, 0, SC1);
#pragma unroll
            for (int k = 0; k < NSPLIT; k++) {
                ok &= (x[k].y >> 17) == t15;
                const u64 bits = __builtin_amdgcn_ballot_w64((x[k].y & 0x10000u) != 0);
                if ((lane & 15) == k) { vl = (unsigned)bits; ve = (unsigned)(bits >> 32); }
            }
            if (__builtin_amdgcn_ballot_w64(ok) == ~0ull) break;
            SLEEP(4)
        }
        const float M = rdlane(row_max(__uint_as_float(vl)), 0), w = __builtin_amdgcn_exp2f(__uint_as_float(vl) - M);
        const int ex = (int)(ve & 255u), em = (int)rdlane(row_max((float)ex), 0);
        const float sc = ldexpf(w, ex - em);
        v4f sum = {0.f, 0.f, 0.f, rdlane(row_sum(w), 0)};
#pragma unroll
        for (int k = 0; k < NSPLIT; k++) {
            const float c = rdlane(sc, k);
            sum.x = fmaf(c, (float)(short)(x[k].x & 0xffffu), sum.x); sum.y = fmaf(c, (float)((int)x[k].x >> 16), sum.y); sum.z = fmaf(c, (float)(short)(x[k].y & 0xffffu), sum.z);
        }
        sum.x = ldexpf(sum.x, em - 141); sum.y = ldexpf(sum.y, em - 141); sum.z = ldexpf(sum.z, em - 141);
        v4f* pb = (v4f*)((char*)&sm.cs[0][0][0] + 65536);
        pb[(wave + 1) * 64 + lane] = sum;
        if (lane == 0) ((float*)(pb + NSW * 64))[wave] = M;
        CBAR();
        LDS_ADD(&sm.qdone, 1u)
    }
}

extern "C" void kernel_launch(void* const* d_in, const int* in_sizes, int n_in,
                              void* d_out, int out_size, void* d_ws, size_t ws_size,
                              hipStream_t stream) {
    Args a;
    for (int i = 0; i < 11; i++) a.p[i] = d_in[i];
    a.p[11] = d_out; a.p[12] = d_ws;
    attn_kernel<<<NSPLIT * GSZ, NTHREADS, 0, stream>>>(a);
}
```

```cpp
#define DIAG 0
#include <hip/hip_runtime.h>
#include <hip/hip_bf16.h>
#include <stdint.h>

#define DH 192
#define HALF 96
#define SKV 2048
#define NH 256
#define NSW 8
#define NWAVES (NSW + 4)
#define NTHREADS (64 * NWAVES)
#define BROWS 128
#define NSPLIT 16
#define GSZ 16
#define WQ 10
#define NU (NH / GSZ + 3)
#define NPC 1024
#define NRR (NH + 3 * GSZ)
#define NIN 8
#define NOUT 8
#define WALL (15 * 144 + 4 * 48)
#define SC1 16
#define NRND 48
#define RST 4352
#define TK 2
#define NMIX 3
#define TUNIT 311
#define T01 50
#define TMIX 7
#define TOFF 50
#define PSF 100
#define TGS 68
#define PSW (NPC / 2 + 28)
#define TG0 8
#define TG1 3
#define TWO -75
#define PSG (NPC / 2 + 60)
#define TH0 3
#define TH1 3
#define EARLY 10
#define LEADP 24
#define DPH 2
#define TA 210
#define TA2 70
#define CSH 2
#define NPRB 3
#define TMAP 520
#define MINV 96
#define VW 4
#define MASTER 203
#define TRES 190
#define TB1 1200
#define PER 300
#define RDD 100
#define LEAD 70
#define LHI 85
#define LOK 74
#define PSL0 124
#define PUBP -8
#define LJ 210
#define LOK2 75
#define NTRY 6
#define TFO 6
#define SL0 42.f
#define PST0 200
#define KC 4
#define TC0 1000
#define TCS 9
#define PRE0 3

#define WS_EPOCH 0
#define WS_POST0 8192
#define WS_POST (WS_POST0 + ((unsigned)E1 & 15) * 2048)
#define WS_NEWS (49152 + 256)
#define WS_HELLO 59904
#define WS_BOARD 65536
#define WS_PART (2u << 20)
#define WS_SWAP (WS_PART + NRR * NSPLIT * 512)

typedef unsigned int v2u __attribute__((ext_vector_type(2)));
typedef unsigned int v16u __attribute__((ext_vector_type(16)));
typedef unsigned int v4u __attribute__((ext_vector_type(4)));
typedef int v4i __attribute__((ext_vector_type(4)));
typedef float v2f __attribute__((ext_vector_type(2)));
typedef float v4f __attribute__((ext_vector_type(4)));
typedef unsigned long long u64;

__device__ __forceinline__ float bflo(unsigned u) { return __uint_as_float(u << 16); }
__device__ __forceinline__ float bfhi(unsigned u) { return __uint_as_float(u & 0xffff0000u); }
__device__ __forceinline__ float bfr(float x) {
    __bf16 b = (__bf16)x;
    return __uint_as_float(((unsigned)__builtin_bit_cast(unsigned short, b)) << 16);
}

#define SLEEP(n) SLEEP_(n)
#define SLEEP_(n) asm volatile("s_sleep " #n "\n\tv_add_co_u32 %0, vcc, 1, %0\n\ts_cbranch_vccz 1f\n\ts_endpgm\n1:" : "+v"(spins) :: "vcc", "memory");
#define SPIN0 0xf0000000u
#define CBAR() asm volatile("" ::: "memory")

__device__ __forceinline__ void wave_sync() {
    __builtin_amdgcn_fence(__ATOMIC_RELEASE, "wavefront");
    __builtin_amdgcn_wave_barrier();
    __builtin_amdgcn_fence(__ATOMIC_ACQUIRE, "wavefront");
}
template <int CTRL>
__device__ __forceinline__ float dpp_add(float x) {
    int y = __builtin_amdgcn_update_dpp(0, __float_as_int(x), CTRL, 0xf, 0xf, true);
    return x + __int_as_float(y);
}
template <int CTRL>
__device__ __forceinline__ float dpp_max(float x) {
    int y = __builtin_amdgcn_update_dpp(__float_as_int(x), __float_as_int(x), CTRL, 0xf, 0xf, false);
    return fmaxf(x, __int_as_float(y));
}
__device__ __forceinline__ float row_sum(float x) { x = dpp_add<0x128>(x); x = dpp_add<0x124>(x); x = dpp_add<0x122>(x); return dpp_add<0x121>(x); }
__device__ __forceinline__ float row_max(float m) { m = dpp_max<0x128>(m); m = dpp_max<0x124>(m); m = dpp_max<0x122>(m); return dpp_max<0x121>(m); }
__device__ __forceinline__ float rdlane(float x, int l) { return __int_as_float(__builtin_amdgcn_readlane(__float_as_int(x), l)); }
__device__ __forceinline__ float wave_max(float m) { m = row_max(m); return fmaxf(fmaxf(rdlane(m, 0), rdlane(m, 16)), fmaxf(rdlane(m, 32), rdlane(m, 48))); }
__device__ __forceinline__ float wave_maxv(float m) {
    m = row_max(m);
    const auto a = __builtin_amdgcn_permlane16_swap(__float_as_uint(m), __float_as_uint(m), false, false);
    m = fmaxf(__uint_as_float(a[0]), __uint_as_float(a[1]));
    const auto b = __builtin_amdgcn_permlane32_swap(__float_as_uint(m), __float_as_uint(m), false, false);
    return fmaxf(__uint_as_float(b[0]), __uint_as_float(b[1]));
}
__device__ __forceinline__ float wave_sum(float x) { x = row_sum(x); return (rdlane(x, 0) + rdlane(x, 16)) + (rdlane(x, 32) + rdlane(x, 48)); }
__device__ __forceinline__ unsigned lds_ld(const unsigned* p) { return __builtin_amdgcn_readfirstlane(__hip_atomic_load(p, __ATOMIC_RELAXED, __HIP_MEMORY_SCOPE_WORKGROUP)); }
__device__ __forceinline__ unsigned lds_addr(const void* p) { return (unsigned)(size_t)(const __attribute__((address_space(3))) void*)p; }
#define DMA16(SRC, LDS) asm volatile("s_mov_b32 %0, m0\n\ts_mov_b32 m0, %2\n\ts_nop 0\n\tglobal_load_lds_dwordx4 %1, off\n\ts_mov_b32 m0, %0" : "=&s"(m0keep) : "v"(SRC), "s"(LDS) : "memory")
__device__ __forceinline__ void lds_st(unsigned* p, unsigned v) { __hip_atomic_store(p, v, __ATOMIC_RELAXED, __HIP_MEMORY_SCOPE_WORKGROUP); }
#define NOW() ((unsigned)__builtin_amdgcn_s_memrealtime())
__device__ __forceinline__ unsigned tdue(unsigned g) {
    return (g * TUNIT + min(g, 480u) * TK + min(g, 48u) * T01 + (min(g, 32u * NMIX) + min(max((int)g - NPC / 2, 0), 32 * NMIX)) * TMIX + (g >= PSF ? TGS * 32 : 0)) >> 5;
}

__device__ __forceinline__ unsigned unit_of(unsigned g) { const unsigned p = g & 511; return (g >> 9) * NU + (p >> 5) + min(max((int)p - 32 * WQ, 0) >> 3, 3); }
__device__ __forceinline__ unsigned first_of(unsigned e) { return 32 * e - 24 * min(max((int)e - WQ, 0), 4); }
#define QUARTER(E) ((E) - WQ < 4)
__device__ __forceinline__ unsigned went(unsigned e) { return 144 * e - 96 * min(max((int)e - WQ, 0), 4); }
__device__ __forceinline__ unsigned wunit(unsigned x) { x += 24 * NMIX; return x < 24 * WQ ? x / 24 : x < 24 * WQ + 32 ? WQ + (x - 24 * WQ) / 8 : (x + 64) / 24; }

#define ROTT 0xed8d2c3d2c35ed84ull, 0xba85aa165b03ac82ull, 0xee7efa489a38af71ull, 0xe932da1b0bab51d8ull, 0xfa96112d5fee971bull, 0x2c7e9ea5a544f071ull, 0x3d86093d5f0e810bull, 0xe932da1b0bab51d8ull, 0xfa96112d5fee971bull, 0xd834e62c5fa723e9ull, 0x1d7d1a3d2c061d88ull, 0xe932da1b0bab51d8ull, 0xfa96112d5fee971bull, 0xc834e12c0aaa4ec7ull, 0x3d86093d5f0e810bull, 0xe932da1b0bab51d8ull
static __device__ const u64 ROT[16] = {ROTT};

struct __attribute__((aligned(16))) Smem {
    unsigned cs[NSW][4][768];
    double acc[NOUT][DH];
    float ent[NOUT][BROWS];
    unsigned short wv[WALL];
    unsigned short wo[WALL];
    unsigned q[NIN][HALF];
    float2 kz[NIN][BROWS];
    float2 vz[NIN][BROWS];
    unsigned utab[64];
    unsigned line, gap;
    unsigned late, grab;
    unsigned rdy[NIN], cnt[NOUT];
    unsigned tabs, vrdy, swp;
    unsigned aline;
    unsigned tag_rdy, tag_lo, tag_hi, merged, cdone, qdone;
    unsigned slow;
    unsigned chead, dg;
};

#define LDS_ADD(P, V) { u64 ex_; asm volatile("s_mov_b64 %0, exec\n\ts_mov_b64 exec, 1\n\tds_add_u32 %1, %2\n\ts_mov_b64 exec, %0" : "=&s"(ex_) : "v"(lds_addr(P)), "v"(V) : "memory"); }
#define LDS_OR(P, V) { u64 ex_; asm volatile("s_mov_b64 %0, exec\n\ts_mov_b64 exec, 1\n\tds_or_b32 %1, %2\n\ts_mov_b64 exec, %0" : "=&s"(ex_) : "v"(lds_addr(P)), "v"(V) : "memory"); }

__global__ __launch_bounds__(NTHREADS, 3) void attn_kernel(
    const unsigned short* __restrict__ q, const int* __restrict__ qk,
    const unsigned short* __restrict__ k_scales, const unsigned short* __restrict__ k_zeros,
    const int* __restrict__ qv,
    const unsigned short* __restrict__ v_scales, const unsigned short* __restrict__ v_zeros,
    const unsigned short* __restrict__ q_cos, const unsigned short* __restrict__ q_sin,
    const unsigned short* __restrict__ k_cos, const unsigned short* __restrict__ k_sin,
    __hip_bfloat16* __restrict__ out, unsigned char* __restrict__ ws)
{
    __shared__ Smem sm;
    const int tid = threadIdx.x;
    unsigned spins = SPIN0;
    const int lane = tid & 63;
    const int hw = __builtin_amdgcn_readfirstlane(tid >> 6);
    u64 t00; unsigned ka;
    asm volatile("s_memrealtime %0\n\ts_load_dword %1, %2, 0x40" : "=&s"(t00), "=&s"(ka) : "s"(__builtin_amdgcn_kernarg_segment_ptr()));
    if (hw == 0) {
        const char* pc0; asm volatile("s_getpc_b64 %0" : "=s"(pc0));
        asm volatile("global_load_dword a6, %0, %1" :: "v"(((blockIdx.x & ~7u) + (lane & 7)) * 128), "s"(pc0) : "a6", "memory");
    }
    const int wave = hw >= 4 ? hw - 4 : hw + NSW;
    const unsigned split = (blockIdx.x & 1) + (blockIdx.x & 2) * 4 + ((blockIdx.x >> 3) & 3) * 2;
    const unsigned side = (split ^ (blockIdx.x >> 2)) & 1, rank = blockIdx.x >> 5;
    const unsigned head0 = 2 * rank + side;
    const unsigned rf0 = (8 * split + 1) / 3, r3 = 8 * split + 1 - 3 * rf0;
    const unsigned koff = ((lane >> 5) * DH + 3 * (lane & 31)) * 4, koffb = koff + 384;
#define AR(S) (16 + (S) * 8)
#define GLD(A, OFF, BASE, IMM) asm volatile("global_load_dwordx3 a[%c0:%c1], %2, %3 offset:%4 nt" :: "i"(A), "i"((A) + 2), "v"(OFF), "s"(BASE), "i"(IMM))
#define ISSUE(S, J, BASE, OA, OB) { GLD(AR(S), OA, BASE, (J) * 1536); GLD(AR(S) + 4, OB, BASE, (J) * 1536); }
#define HEAD2 ((GSZ + 1 - 2 * side) * (SKV * DH * 4))
    const unsigned wo = (hw - 2) & 7;
    const bool pre = hw >= 4 && wo < PRE0;
    if (pre) {
        const char* a = (const char*)qk + (head0 * 512 + side * 256 + rf0 + wave + (wave >= 6 ? 80 : 0)) * 3072; const char* b = a + HEAD2;
        ISSUE(0, 0, a, koff, koffb) ISSUE(1, 1, a, koff, koffb) ISSUE(2, 0, b, koff, koffb) ISSUE(3, 1, b, koff, koffb)
    }
    asm volatile("s_waitcnt lgkmcnt(0)" : "+s"(t00), "+s"(ka));
    if (hw == 0) {
        if (DIAG == 30) sm.dg = NOW() - (unsigned)t00;
        sm.utab[lane] = ~0u;
        if (lane < 32) (&sm.line)[lane] = lane == 3 ? 64u : (lane & 29) == 0 ? (unsigned)t00 + TOFF : 0u;
        asm volatile("s_waitcnt lgkmcnt(0)" ::: "memory");
    }
    __builtin_amdgcn_s_barrier();
    CBAR();
    const u64 rtab = r3 == 1 ? 0x5756552c2b020100ull : r3 == 0 ? 0x5756552c2b2a0100ull : 0x57562d2c2b020100ull;
#define R8(I) (rf0 + ((unsigned)(rtab >> (8 * (I))) & 255u))

    if (__builtin_expect(wave < NSW, 1)) {
        const int cst = 2 * side;
        const unsigned r8w = R8((unsigned)wave);
        const char* cbase[3];
#pragma unroll
        for (int j = 0; j < 3; j++) { const int x = j * 64 + lane; cbase[j] = (x < 96 ? (const char*)k_cos + x * 16 : (const char*)k_sin + (x - 96) * 16) + r8w * (4 * DH * 2); }
        unsigned m0keep;
#define CS_DMA(W, C) { _Pragma("unroll") for (int j = 0; j < 3; j++) DMA16(cbase[j] + (C) * (512 * DH * 2), lds_addr(&sm.cs[0][0][0]) + ((W) * 4 + (C)) * 3072 + j * 1024); }
#define REPACK(W, C) { \
        unsigned* cw = &sm.cs[W][C][0]; \
        const unsigned* l = cw + rsel * (DH / 2) + qoff; \
        unsigned r[2][6]; \
        _Pragma("unroll") \
        for (int j = 0; j < 2; j++) { \
            unsigned e[4][2]; \
            _Pragma("unroll") \
            for (int k = 0; k < 4; k++) { \
                const unsigned* a = l + j * DH + (k & 1) * 48 + (k >> 1) * 384;       \
                unsigned x0 = a[0], x1 = a[1]; \
                e[k][0] = __builtin_amdgcn_alignbit(x1, x0, sh); \
                e[k][1] = x1 >> sh; \
            } \
            r[j][0] = (e[0][0] & 0xffffu) | (e[3][0] << 16); \
            r[j][1] = (e[0][0] >> 16) | (e[3][0] & 0xffff0000u); \
            r[j][2] = (e[0][1] & 0xffffu) | (e[3][1] << 16); \
            r[j][3] = (e[1][0] & 0xffffu) | (e[2][0] << 16); \
            r[j][4] = (e[1][0] >> 16) | (e[2][0] & 0xffff0000u); \
            r[j][5] = (e[1][1] & 0xffffu) | (e[2][1] << 16); \
        } \
        wave_sync(); \
        _Pragma("unroll") \
        for (int j = 0; j < 2; j++) { *(v4u*)&cw[j * 256 + lane * 4] = (v4u){r[j][0], r[j][1], r[j][2], r[j][3]}; *(v2u*)&cw[512 + j * 128 + lane * 2] = (v2u){r[j][4], r[j][5]}; } \
        wave_sync(); }
        const int sub = lane & 31;
        const int rsel = lane >> 5;
        const unsigned sh = (sub & 1) * 16;
        const int qoff = 3 * (sub >> 1) + (sub & 1);
        const unsigned voff = 3 * lane * 4;
        asm volatile("" ::: "a16", "a17", "a18", "a19", "a20", "a21", "a22", "a23", "a24", "a25", "a26", "a27", "a28", "a29", "a30", "a31",
                            "a32", "a33", "a34", "a35", "a36", "a37", "a38", "a39", "a40", "a41", "a42", "a43", "a44", "a45", "a46", "a47");
        asm volatile("" ::: "a48", "a49", "a50", "a51", "a52", "a53", "a54", "a55", "a56", "a57", "a58", "a59", "a60", "a61", "a62", "a63",
                            "a64", "a65", "a66", "a67", "a68", "a69", "a70", "a71", "a72", "a73", "a74", "a75", "a76", "a77", "a78", "a79");
        int ta[4][3], tb[4][3];
#define WAITT(P, S, N) asm volatile("s_waitcnt vmcnt(%6)\n\tv_accvgpr_read_b32 %0, a[%c7]\n\tv_accvgpr_read_b32 %1, a[%c8]\n\tv_accvgpr_read_b32 %2, a[%c9]\n\t" \
                              "v_accvgpr_read_b32 %3, a[%c10]\n\tv_accvgpr_read_b32 %4, a[%c11]\n\tv_accvgpr_read_b32 %5, a[%c12]" \
                              : "=v"(ta[P][0]), "=v"(ta[P][1]), "=v"(ta[P][2]), "=v"(tb[P][0]), "=v"(tb[P][1]), "=v"(tb[P][2]) \
                              : "i"(N), "i"(AR(S)), "i"(AR(S) + 1), "i"(AR(S) + 2), "i"(AR(S) + 4), "i"(AR(S) + 5), "i"(AR(S) + 6))
        const unsigned voffb = voff + 768;
        u64 ln = 0;
#define TDUE(G) (tdue(G) + ((G) >= PSW ? __builtin_amdgcn_readfirstlane((unsigned)(ln >> 32)) >> ((G) >= PSG ? 16 : 0) & 0xffffu : 0u))
#define GATE(DUE) { int d_ = (int)((DUE) - NOW()); if (d_ < 3000) while (d_ > 0) { __builtin_amdgcn_s_sleep(1); d_ = (int)((DUE) - NOW()); } }
#define REPORT() { const unsigned t_ = now - tdc; if (lane == 0) lds_st(&sm.late, t_); }
#define LINE() ln = LDV((u64*)&sm.line);
#define LDV(P) __hip_atomic_load(P, __ATOMIC_RELAXED, __HIP_MEMORY_SCOPE_WORKGROUP)
        unsigned u = 0, rg = cst * 8 + wave, np = 0;
        CS_DMA(wave, cst)

        const char* kb0 = (const char*)qk + (size_t)(head0 * 512 + r8w) * 3072;
#define KB(I) (kb0 + ((cst + (I)) & 3) * (128 * 3072))
        const char* kb; const char* kb2;
#define SISSUE(I) { kb = KB(I); kb2 = kb + HEAD2; const unsigned du_ = lds_ld(&sm.line) + tdue(16 * (I) + 2 * wo); GATE(du_) \
            ISSUE(((I) & 1) * 4, 0, kb, koff, koffb) ISSUE(((I) & 1) * 4 + 1, 1, kb, koff, koffb) ISSUE(((I) & 1) * 4 + 2, 0, kb2, koff, koffb) ISSUE(((I) & 1) * 4 + 3, 1, kb2, koff, koffb) }
        if (!pre) SISSUE(0)
        CS_DMA(wave, (cst + 1) & 3)
        if (pre) asm volatile("s_waitcnt vmcnt(3)" ::: "memory");
        if (DIAG == 32 && wave == 0 && lane == 0) sm.dg = NOW() - (unsigned)t00;
        if (DIAG == 33 && wave == 1 && lane == 0) sm.dg = NOW() - (unsigned)t00;
        asm volatile("s_waitcnt vmcnt(11)" ::: "memory");
        if (!pre) LDS_ADD(&sm.slow, NOW() - lds_ld(&sm.line) - tdue(2 * wo))
        REPACK(wave, cst)
        SISSUE(1)
#define TABDONE(I) LDS_OR(&sm.tabs, 1u << ((((cst + (I)) & 3) * 8) + wave))
        TABDONE(0)
        const float scale = 0.07216878364870322f;
        double acc[3] = {0.0, 0.0, 0.0};
        bool first = true, chg, none = false;
        unsigned Qp[3], Qn[3], Rp[3], Rn[3];
        float kz4[4], ksc, kres[4];
        v4u ca[4]; v2u cb[4];
        v2f qe[4][3], A[4];
        unsigned vut, nrg;
        const unsigned vr = ((lane >> 3) & 3) * 128 + R8((unsigned)lane & 7);
        unsigned g, mg;
        const unsigned grab_adr = lds_addr(&sm.grab);
        asm volatile("v_accvgpr_write_b32 a13, 1\n\tv_accvgpr_write_b32 a14, 2" ::: "a13", "a14", "a15");
#define GRAB(N) { u64 ex_; asm volatile("s_mov_b64 %0, exec\n\ts_mov_b64 exec, 1\n\tds_add_rtn_u32 a15, %1, a%c2\n\ts_mov_b64 exec, %0" : "=&s"(ex_) : "v"(grab_adr), "i"(12 + (N)) : "memory"); }
#define GRABBED() { unsigned t_; asm volatile("s_waitcnt lgkmcnt(0)\n\tv_accvgpr_read_b32 %1, a15\n\ts_nop 0\n\tv_readfirstlane_b32 %0, %1\n\ts_nop 1" : "=s"(g), "=v"(t_)); }
        unsigned oa, ob, un, tdn, tiss, tdc = 0;
#define PLAN() { \
            GRABBED() un = unit_of(g); chg = un != u; none = g >= NPC; \
            const bool nv = g >= NPC / 2; \
            unsigned hd = __builtin_amdgcn_readlane(vut, un); \
            while (hd == ~0u) { SLEEP(1) hd = lds_ld(&sm.utab[un]); } \
            nrg = (g + (hd >> 8)) & 31; hd &= 255; \
            unsigned boff, boff2; asm("s_mul_i32 %0, %1, 0xc00" : "=s"(boff) : "s"(hd * 512 + __builtin_amdgcn_readlane(vr, nrg))); \
            asm("s_mul_i32 %0, %1, 0xc00" : "=s"(boff2) : "s"(hd * 512 + __builtin_amdgcn_readlane(vr, nrg + 1))); \
            const char* b_ = nv ? (const char*)qv : (const char*)qk; \
            kb = b_ + boff; kb2 = b_ + boff2; \
            oa = nv ? voff : koff; ob = nv ? voffb : koffb; \
            tdn = TDUE(g); }
#define FIRE() { ISSUE(0, 0, kb, oa, ob) ISSUE(1, 1, kb, oa, ob) ISSUE(2, 0, kb2, oa, ob) ISSUE(3, 1, kb2, oa, ob) }
#define LAUNCH() { \
            LINE() \
            const unsigned du_ = __builtin_amdgcn_readfirstlane((unsigned)ln) + tdn; \
            GATE(du_) \
            ISSUE(0, 0, kb, oa, ob) ISSUE(1, 1, kb, oa, ob) \
            GATE(du_ + 10) \
            ISSUE(2, 0, kb2, oa, ob) ISSUE(3, 1, kb2, oa, ob) tiss = NOW(); }
#define ARRIVED() \
            const unsigned now = NOW(); \
            const bool stall = now - tiss > LJ && g - PST0 < NPC - 48 - PST0; \
            const bool late = !none && !stall && (int)(now + EARLY - __builtin_amdgcn_readfirstlane((unsigned)ln) - tdn) >= 0; \
            if (late) { FIRE() tiss = now; } \
            if (stall && lane == 0) __hip_atomic_fetch_max(&sm.line, (unsigned)ln + (now - tiss) - LOK2, __ATOMIC_RELAXED, __HIP_MEMORY_SCOPE_WORKGROUP); \
            REPORT()
#define ADVANCE(ISV) { \
            if (chg) FLUSH(ISV) \
            if (!none && !late) LAUNCH() \
            first = chg; u = un; rg = nrg; tdc = tdn + 10; }
#define K_TAB(MODE) { _Pragma("unroll") for (int p = 0; p < ((MODE) ? 2 : 4); p++) { \
            const unsigned* cw = &sm.cs[(rg & 7) + (p >> 1)][rg >> 3][0]; \
            ca[p] = *(const v4u*)&cw[(p & 1) * 256 + lane * 4]; cb[p] = *(const v2u*)&cw[512 + (p & 1) * 128 + lane * 2]; } }
#define K_PRE(MODE, P) { \
        float e0, e1, e2, f0, f1, f2; \
        const unsigned* qp_ = ((MODE) && (P) >= 2) ? Rp : Qp; const unsigned* qn_ = ((MODE) && (P) >= 2) ? Rn : Qn; \
        const int tp_ = (MODE) ? (P) & 1 : (P); \
        asm("v_dot2_f32_bf16 %0, %6, %12, 0\n\tv_dot2_f32_bf16 %3, %9, %15, 0\n\tv_dot2_f32_bf16 %1, %7, %13, 0\n\tv_dot2_f32_bf16 %4, %10, %16, 0\n\t" \
            "v_dot2_f32_bf16 %2, %8, %14, 0\n\tv_dot2_f32_bf16 %5, %11, %17, 0\n\ts_nop 2" \
            : "=&v"(e0), "=&v"(e1), "=&v"(e2), "=&v"(f0), "=&v"(f1), "=&v"(f2) \
            : "v"(qp_[0]), "v"(qp_[1]), "v"(qp_[2]), "v"(qn_[0]), "v"(qn_[1]), "v"(qn_[2]), \
              "v"(ca[tp_].x), "v"(ca[tp_].y), "v"(ca[tp_].z), "v"(ca[tp_].w), "v"(cb[tp_].x), "v"(cb[tp_].y)); \
        qe[P][0] = (v2f){e0, f0}; qe[P][1] = (v2f){e1, f1}; qe[P][2] = (v2f){e2, f2}; \
        A[P] = (qe[P][0] + qe[P][1] + qe[P][2]) * (v2f){-kz4[P], -kz4[P]}; }
#define K_WAIT(MODE, P) { \
        if ((MODE) == 0) { WAITT(P, P, 6 - 2 * (P)); } \
        else { WAITT(P, (((MODE) - 1) & 1) * 4 + (P), ((MODE) < 3 ? 20 : (MODE) == 3 ? 17 : 6) - 2 * (P)); } }
#define K_FMA(P) { \
        _Pragma("unroll") \
        for (int i = 0; i < 3; i++) { v2f x = {(float)ta[P][i], (float)tb[P][i]}; A[P] = qe[P][i] * x + A[P]; } \
        kres[P] = A[P].x + A[P].y; }
#define K_RED(MODE) { \
        const bool o1 = lane & 1, o2 = lane & 2; \
        float s01 = (o1 ? kres[1] : kres[0]) + __int_as_float(__builtin_amdgcn_update_dpp(0, __float_as_int(o1 ? kres[0] : kres[1]), 0xb1, 0xf, 0xf, true)); \
        float s23 = (o1 ? kres[3] : kres[2]) + __int_as_float(__builtin_amdgcn_update_dpp(0, __float_as_int(o1 ? kres[2] : kres[3]), 0xb1, 0xf, 0xf, true)); \
        float sx = (o2 ? s23 : s01) + __int_as_float(__builtin_amdgcn_update_dpp(0, __float_as_int(o2 ? s01 : s23), 0x4e, 0xf, 0xf, true)); \
        sx = dpp_add<0x124>(sx); sx = dpp_add<0x128>(sx); \
        const auto sw = __builtin_amdgcn_permlane16_swap(__float_as_uint(sx), __float_as_uint(sx), false, false); \
        sx = __uint_as_float(sw[0]) + __uint_as_float(sw[1]); \
        if ((lane & 28) == 0) { if (MODE) sm.ent[(lane >> 1) & 1][rg * 4 + 2 * (lane & 1) + rsel] = sx * ksc; else sm.ent[u % NOUT][rg * 4 + 2 * (lane & 3) + rsel] = sx * ksc; } }
#define K_PRE4(MODE) { K_PRE(MODE, 0) K_PRE(MODE, 1) K_PRE(MODE, 2) K_PRE(MODE, 3) asm volatile("" :: "v"(A[0]), "v"(A[1]), "v"(A[2]), "v"(A[3])); }
#define K_BODY(MODE) { K_PRE4(MODE) K_WAIT(MODE, 0) K_FMA(0) K_WAIT(MODE, 1) K_FMA(1) K_WAIT(MODE, 2) K_FMA(2) K_WAIT(MODE, 3) K_FMA(3) K_RED(MODE) }
#define V_FMA(J, W0, W1) { \
        _Pragma("unroll") \
        for (int i = 0; i < 3; i++) { \
            acc[i] = fma(W0, (double)ta[J][i], acc[i]); \
            acc[i] = fma(W1, (double)tb[J][i], acc[i]); \
        } }
#define V_PIN() asm volatile("" :: "v"(acc[0]), "v"(acc[1]), "v"(acc[2]));
#define RDYWAIT(SL, SEQ) { while (lds_ld(&sm.rdy[SL]) != (SEQ)) SLEEP(1); CBAR(); }
#define LOADZ() { _Pragma("unroll") for (int p = 0; p < 4; p++) kz4[p] = sm.kz[u % NIN][rg * 4 + 2 * p + rsel].x; ksc = sm.kz[u % NIN][rg * 4 + 2 * (lane & 3) + rsel].y * scale; }
#define LOADQ() { _Pragma("unroll") for (int i = 0; i < 3; i++) { Qp[i] = sm.q[u % NIN][3 * sub + i]; Qn[i] = __builtin_amdgcn_alignbit(Qp[i], Qp[i], 16) ^ 0x80000000u; } }
#define FLUSH(ISV) { \
            mg = __builtin_amdgcn_readfirstlane(mg); \
            while (u >= mg + NOUT) { SLEEP(1); mg = lds_ld(&sm.merged); } \
            CBAR(); \
            if (ISV) { const unsigned aa = lds_addr(&sm.acc[u % NOUT][lane]); \
                asm volatile("ds_add_f64 %0, %1\n\tds_add_f64 %0, %2 offset:512\n\tds_add_f64 %0, %3 offset:1024" :: "v"(aa), "v"(acc[0]), "v"(acc[1]), "v"(acc[2]) : "memory"); \
                acc[0] = 0.0; acc[1] = 0.0; acc[2] = 0.0; } \
            CBAR(); \
            LDS_ADD(&sm.cnt[u % NOUT], np) \
            CBAR(); \
            np = 0; }

#define LOADZ2() { _Pragma("unroll") for (int p = 0; p < 4; p++) kz4[p] = sm.kz[p >> 1][rg * 4 + 2 * (p & 1) + rsel].x; ksc = sm.kz[(lane >> 1) & 1][rg * 4 + 2 * (lane & 1) + rsel].y * scale; }
#define STATIC_STEP(I) { \
            rg = ((cst + (I)) & 3) * 8 + wave; \
            LOADZ2() \
            K_TAB((I) + 1) K_BODY((I) + 1) }
        CS_DMA(wave, (cst + 2) & 3)
        RDYWAIT(0, 1u) RDYWAIT(1, 2u)
        LOADQ()
#pragma unroll
        for (int i = 0; i < 3; i++) { Rp[i] = sm.q[1][3 * sub + i]; Rn[i] = __builtin_amdgcn_alignbit(Rp[i], Rp[i], 16) ^ 0x80000000u; }
        STATIC_STEP(0)
        asm volatile("s_waitcnt vmcnt(11)" ::: "memory");
        REPACK(wave, (cst + 1) & 3)
        TABDONE(1)
        SISSUE(2)
        CS_DMA(wave, (cst + 3) & 3)
        STATIC_STEP(1)
        asm volatile("s_waitcnt vmcnt(11)" ::: "memory");
        REPACK(wave, (cst + 2) & 3)
        TABDONE(2)
        SISSUE(3)
        STATIC_STEP(2)
        asm volatile("s_waitcnt vmcnt(8)" ::: "memory");
        REPACK(wave, (cst + 3) & 3)
        TABDONE(3)
        GRAB(2)
        vut = LDV(&sm.utab[lane]);
        STATIC_STEP(3)
        CBAR();
        LDS_ADD(&sm.cnt[0], 4u) LDS_ADD(&sm.cnt[1], 4u)
        PLAN()
        LAUNCH()
        u = un; rg = nrg; tdc = tdn + 10;
        bool tabok = false;
        __builtin_amdgcn_s_setprio(2);
#pragma unroll 1
        while (u < NU) {
            GRAB(2)
            mg = LDV(&sm.merged);
            vut = LDV(&sm.utab[lane]);
            LINE()
            if (!tabok) {
                for (;;) { const unsigned tb_ = lds_ld(&sm.tabs); tabok = tb_ == ~0u; if (((tb_ >> rg) & 3u) == 3u) break; SLEEP(1) }
                CBAR();
            }
            K_TAB(0)
            if (first) {
                unsigned fl = LDV(&sm.rdy[u % NIN]);
                CBAR();
                LOADQ() LOADZ()
                if (__builtin_amdgcn_readfirstlane(fl) != u + 1) { RDYWAIT(u % NIN, u + 1) LOADQ() LOADZ() }
            } else LOADZ()
            K_PRE4(0)
            PLAN()
            K_WAIT(0, 0) K_FMA(0) K_WAIT(0, 1) K_FMA(1)
            asm volatile("" :: "v"(kres[0]), "v"(kres[1]));
            K_WAIT(0, 2) K_WAIT(0, 3)
            ARRIVED()
            K_FMA(2) K_FMA(3) K_RED(0)
            np += 2;
            ADVANCE(false)
        }
#pragma unroll 1
        while (u < 2 * NU) {
            GRAB(2)
            mg = LDV(&sm.merged);
            LINE()
            const unsigned vr_ = LDV(&sm.vrdy);
            CBAR();
            const unsigned short* wp = &sm.wv[went(u - NU) + (QUARTER(u - NU) ? rg & 7 : rg) * 4];
            v4i pw = *(const v4i*)wp;
            if (first && !((__builtin_amdgcn_readfirstlane(vr_) >> (u - NU)) & 1)) { while (!((lds_ld(&sm.vrdy) >> (u - NU)) & 1)) SLEEP(1); CBAR(); pw = *(const v4i*)wp; }
            const double w0 = (short)pw.x, w1 = pw.x >> 16, w2 = (short)pw.y, w3 = pw.y >> 16, w4 = (short)pw.z, w5 = pw.z >> 16, w6 = (short)pw.w, w7 = pw.w >> 16;
            asm volatile("" :: "v"(w0), "v"(w1), "v"(w2), "v"(w3), "v"(w4), "v"(w5), "v"(w6), "v"(w7));
            PLAN()
            WAITT(0, 0, 6); V_FMA(0, w0, w1) V_PIN()
            WAITT(1, 1, 4); V_FMA(1, w2, w3) V_PIN()
            WAITT(2, 2, 2); V_FMA(2, w4, w5) V_PIN()
            WAITT(3, 3, 0);
            np += 2;
            if (none) { V_FMA(3, w6, w7) FLUSH(true) break; }
            ARRIVED()
            V_FMA(3, w6, w7)
            ADVANCE(true)
        }
        __builtin_amdgcn_s_setprio(0);
        {
            const int head = lds_ld(&sm.chead);
            const unsigned char* rb = ws + WS_PART + head * (NSPLIT * 512) + wave * 1024;
            const unsigned t15 = lds_ld(&sm.tag_lo) & 0x7fffu;
            bool got; v2u f[2];
#define POLL(S) asm volatile("global_load_dwordx2 a[%c0:%c0+1], %1, %2 sc1\n\tglobal_load_dwordx2 a[%c0+2:%c0+3], %1, %2 offset:512 sc1" :: "i"(AR(S)), "v"(lane * 8), "s"(rb) : "memory");
#define TAKE(S, N) { v2u r_[2]; \
                asm volatile("s_waitcnt vmcnt(%c5)\n\tv_accvgpr_read_b32 %0, a[%c4]\n\tv_accvgpr_read_b32 %1, a[%c4+1]\n\tv_accvgpr_read_b32 %2, a[%c4+2]\n\tv_accvgpr_read_b32 %3, a[%c4+3]" \
                             : "=v"(r_[0].x), "=v"(r_[0].y), "=v"(r_[1].x), "=v"(r_[1].y) : "i"(AR(S)), "i"(N)); \
                f[0] = r_[0]; f[1] = r_[1]; \
                got = __builtin_amdgcn_ballot_w64(((r_[0].y >> 17 ^ t15) | (r_[1].y >> 17 ^ t15)) == 0) == ~0ull; }
            if (head < NH - GSZ) while (lds_ld(&sm.cnt[(2 * NU - 1) % NOUT]) < 32 && lds_ld(&sm.merged) < 2 * NU) SLEEP(1)
            POLL(0)
            TAKE(0, 0)
            if (!got) {
                POLL(0) SLEEP(3) POLL(1)
#pragma unroll 1
                for (;;) {
                    TAKE(0, 2) if (got) break; SLEEP(1) POLL(0)
                    TAKE(1, 2) if (got) break; SLEEP(1) POLL(1)
                }
            }
            const u64 hb[2] = {__builtin_amdgcn_ballot_w64((f[0].y & 0x10000u) != 0), __builtin_amdgcn_ballot_w64((f[1].y & 0x10000u) != 0)};
            const float L0 = __uint_as_float((unsigned)hb[0]), L1 = __uint_as_float((unsigned)hb[1]), M = fmaxf(L0, L1);
            const float w0 = __builtin_amdgcn_exp2f(L0 - M), w1 = __builtin_amdgcn_exp2f(L1 - M);
            const float c0 = ldexpf(w0, (int)((unsigned)(hb[0] >> 32) & 255u) - 141), c1 = ldexpf(w1, (int)((unsigned)(hb[1] >> 32) & 255u) - 141);
            v4f* pb = (v4f*)((char*)&sm.cs[0][0][0] + 65536);
            float* pm = (float*)(pb + NSW * 64);
            pb[wave * 64 + lane] = (v4f){fmaf(c1, (float)(short)f[1].x, c0 * (float)(short)f[0].x), fmaf(c1, (float)((int)f[1].x >> 16), c0 * (float)((int)f[0].x >> 16)),
                                         fmaf(c1, (float)(short)f[1].y, c0 * (float)(short)f[0].y), w0 + w1};
            if (lane == 0) pm[wave] = M;
            CBAR();
            { u64 ex_; asm volatile("s_mov_b64 %0, exec\n\ts_mov_b64 exec, 1\n\tds_add_rtn_u32 a15, %1, a13\n\ts_mov_b64 exec, %0" : "=&s"(ex_) : "v"(lds_addr(&sm.cdone)) : "memory"); }
            const v4f ma = *(const v4f*)pm, mb = *(const v4f*)(pm + 4);
            v4f ps[NSW];
#pragma unroll
            for (int w = 0; w < NSW; w++) ps[w] = pb[w * 64 + lane];
            GRABBED()
            if (g == NSW - 1) {
                const float mw[NSW] = {ma.x, ma.y, ma.z, ma.w, mb.x, mb.y, mb.z, mb.w};
                float Mx = mw[0];
#pragma unroll
                for (int w = 1; w < NSW; w++) Mx = fmaxf(Mx, mw[w]);
                v4f sum = {0.f, 0.f, 0.f, 0.f};
#pragma unroll
                for (int w = 0; w < NSW; w++) sum += ps[w] * __builtin_amdgcn_exp2f(mw[w] - Mx);
                if (head >> 4 == WQ) {
                    while (lds_ld(&sm.qdone) != 3) SLEEP(0)
                    CBAR();
                    const v4f mq = *(const v4f*)(pm + NSW), q0 = pb[(NSW + 1) * 64 + lane], q1 = pb[(NSW + 2) * 64 + lane], q2 = pb[(NSW + 3) * 64 + lane];
                    const float Mn = fmaxf(fmaxf(Mx, mq.x), fmaxf(mq.y, mq.z));
                    sum = sum * __builtin_amdgcn_exp2f(Mx - Mn) + q0 * __builtin_amdgcn_exp2f(mq.x - Mn) + q1 * __builtin_amdgcn_exp2f(mq.y - Mn) + q2 * __builtin_amdgcn_exp2f(mq.z - Mn);
                }
                const float inv = __builtin_amdgcn_rcpf(sum.w);
                __hip_bfloat16* o = out + (size_t)head * DH + 3 * lane;
                o[0] = __float2bfloat16(sum.x * inv); o[1] = __float2bfloat16(sum.y * inv); o[2] = __float2bfloat16(sum.z * inv);
            }
        }
        return;
    }

    u64 E1;
    if (wave == NSW) {
        unsigned ref;
        const unsigned sidx = (blockIdx.x & 7) | ((blockIdx.x >> 7) << 3);
#define SAMPLE(K) ((((blockIdx.x >> 3) - (blockIdx.x & 7) - (K) * 5) & 15) == 0)
#define BTAG(K) ((btag + (K)) & 0xfffffu)
#define POST(K, C) { if (lane == 0) __hip_atomic_store((u64*)(ws + (bd + (K) * RST + (sidx >> 3) * 256 + (sidx & 7) * 8)), (u64)(C) | ((u64)(((C) - ref) | BTAG(K) << 12) << 32), __ATOMIC_RELAXED, __HIP_MEMORY_SCOPE_AGENT); }
        const unsigned own = lds_ld(&sm.line);
        if (SAMPLE(0) && lane < NTRY) __hip_atomic_store((unsigned*)(ws + WS_HELLO + lane * 768) + sidx, own, __ATOMIC_RELAXED, __HIP_MEMORY_SCOPE_AGENT);
        const u64 E = __hip_atomic_load((const u64*)(ws + WS_EPOCH), __ATOMIC_RELAXED, __HIP_MEMORY_SCOPE_AGENT);
        E1 = E + 1;
        if (lane == 0) { sm.tag_lo = (unsigned)E1; sm.tag_hi = (unsigned)(E1 >> 32); CBAR(); lds_st(&sm.tag_rdy, 1u); }
        const unsigned btag = __builtin_amdgcn_readfirstlane((unsigned)E1) * 61u, bd = WS_BOARD + (btag & 7) * (NRND * RST);
        ref = own; unsigned hmin = own, tnews = 0, ntry = 9; unsigned hmean = own; int nl = 20, iod = 0, hmx = 500;
        int off = 0;
        {
            unsigned tries = 0, cur = own, tl = own;
            bool news = false;
#pragma unroll 1
            while (!news || cur != ref) {
                const unsigned now = NOW();
                if (tries < NTRY && (int)(now - own) >= (int)(TA + tries * TA2)) {
                    v16u x;
                    asm volatile("s_load_dwordx16 %0, %1, %2\n\ts_waitcnt lgkmcnt(0)" : "=&s"(x) : "s"(ws), "s"(__builtin_amdgcn_readfirstlane(WS_HELLO + tries * 768)) : "memory");
                    unsigned bad = 0; int sum = 0, dif = 0;
#pragma unroll
                    for (int i = 0; i < 16; i++) { bad |= x[i] - own + 512; sum += x[i] - x[0]; dif += i & 4 ? x[i] : -x[i]; }
                    tries++;
                    if (bad < 1024) { ntry = tries; hmean = x[0] + (sum >> 4); for (int i = 0; i < 16; i++) if ((int)(x[i] - hmin) < 0) hmin = x[i]; nl = 0; hmx = 0; for (int i = 0; i < 16; i++) { nl += x[i] - hmin > 80; iod += (i & 4) ? (int)(x[i] - hmin) : -(int)(x[i] - hmin); hmx = max(hmx, (int)(x[i] - hmin)); } const unsigned com = x[0] + (sum >> 4) + abs(dif) * (8 - KC) / 128; ref = x[0] + ((sum + (blockIdx.x & 4 ? dif : -dif)) >> 4); off = ref - com;
                        tries = NTRY; tl = NOW(); if (lane == 0) lds_st(&sm.aline, com); }
                    continue;
                }
                if (!news && lds_ld(&sm.utab[31]) != ~0u) { news = true; tnews = now; CBAR(); if (cur == ref) tl = now; ref = lds_ld(&sm.aline) + off; tries = NTRY; }
                const int d = (now - tl) >> CSH;
                if (cur != ref && d) { tl += d << CSH; cur += min(max((int)(ref - cur), -d), d); if (lane == 0) lds_st(&sm.line, cur); }
                SLEEP(2)
            }
        }
        ref -= off;
        const unsigned ref0 = ref;
        unsigned dg[2] = {0, 0}; int mxj = 0, nsh = 0; unsigned lsum = 0, lcnt = 0;
        const unsigned lead = (blockIdx.x >> 3) == 9 ? 0 : LEAD;
        unsigned posted = ref, lagf = ref, tpost = ref + TB1, tpoll = tpost + RDD + lead, kw = 0, kr = 0, cur = ref + off, khold = 0;
        int o = off; bool held = false;
#pragma unroll 1
        for (;;) {
            const unsigned p = lds_ld(&sm.grab);
            if (p >= NPC) break;
            if (p < 260) dg[0] = ref; if (p < NPC / 2) dg[1] = ref;
            lagf += (int)(lds_ld(&sm.late) - lagf) >> 1;
            { const unsigned l = lds_ld(&sm.line); if (l != cur) { cur = l; khold = kw; held = true; } }
            const unsigned now = NOW();
            if (o) { o = off * min(max((int)(ref0 + TC0 + (1 << TCS) - now), 0), 1 << TCS) >> TCS; if (!held && cur != ref + o) { cur = ref + o; if (lane == 0) lds_st(&sm.line, cur); } }
            if (kw < NRND && (int)(now - tpost) >= 0) {
                const unsigned c = ref + min((p >= PSL0 && (int)(lagf - cur) > LHI ? lagf - LOK : cur) - o - ref, 4000u);
                if (SAMPLE(kw) && (c != ref || (ref != posted && (sidx & 7) == (kw & 7)))) POST(kw, c)
                posted = ref;
                tpost += PER; kw++;
            }
            if (kr < NRND && (int)(now - tpoll) >= 0) {
                v16u xa, xb;
                asm volatile("s_load_dwordx16 %0, %2, %3\n\ts_load_dwordx16 %1, %2, %4\n\ts_waitcnt lgkmcnt(0)" : "=&s"(xa), "=&s"(xb)
                             : "s"(ws), "s"(__builtin_amdgcn_readfirstlane(bd + kr * RST)), "s"(__builtin_amdgcn_readfirstlane(bd + kr * RST + 256)) : "memory");
                int nb = 0, sg = 0;
#pragma unroll
                for (int i = 0; i < 16; i++) {
                    const unsigned lo = i < 8 ? xa[2 * i & 15] : xb[2 * i & 15], hi = i < 8 ? xa[(2 * i + 1) & 15] : xb[(2 * i + 1) & 15];
                    if (hi >> 12 == BTAG(kr) && lo - ref0 < 8000) { nb = max(nb, (int)(lo - (hi & 4095u) - ref)); sg += hi & 4095u; }
                }
                ref += nb + (sg >> 4); mxj = max(mxj, nb + (sg >> 4)); nsh += (nb + (sg >> 4)) > 0;
                if (cur != ref + o && (kr >= khold || (int)(ref + o - cur) > 0)) { cur = ref + o; held = false; if (lane == 0) lds_st(&sm.line, cur); }
                tpoll += PER; kr++;
            }
            SLEEP(2)
        }
        if (blockIdx.x == 0) {
            unsigned X = 0;
            if (DIAG == 2) X = (ref - ref0) * 8;
            if (DIAG == 6) X = (dg[1] - ref0) * 8;
            if (DIAG == 8) X = (dg[0] - ref0) * 8;
            if (DIAG == 17) X = hmx * 8;
            if (DIAG == 22) X = (ref0 - hmean) * 8;
            if (DIAG == 24) X = (hmean - hmin) * 8;
            if (DIAG == 25) X = (ref - hmin) * 4;
            if (DIAG == 27) X = __builtin_popcount(lds_ld(&sm.swp)) * 100;
            if (DIAG == 20) X = lds_ld(&sm.slow) * 2;
            if (DIAG >= 30 && DIAG < 40) X = lds_ld(&sm.dg) * 8;
            if (DIAG == 11) X = mxj * 8;
            X = min(X, 6000u);
            while (NOW() - hmin < 15000 + X) SLEEP(1)
        }
    } else if (wave == NSW + 1 || wave == NSW + 2) {
        const int ql = lane < 48 ? lane : 47;
        unsigned c1, c2, s1, s2;
#define QT_LOADS() { int qo_ = ql; asm volatile("" : "+v"(qo_)); \
                     c1 = ((const unsigned*)q_cos)[qo_]; c2 = ((const unsigned*)q_cos)[48 + qo_]; s1 = ((const unsigned*)q_sin)[qo_]; s2 = ((const unsigned*)q_sin)[48 + qo_]; }
        unsigned q1, q2, lkz, lks, lvz, lvs;
        const unsigned sofs = R8(((unsigned)lane >> 1) & 7) * 2 + (lane & 1);
#define UNIT_LOADS(HEAD, GRP) { \
        q1 = ((const unsigned*)(q + (size_t)(HEAD) * DH))[ql]; q2 = ((const unsigned*)(q + (size_t)(HEAD) * DH))[48 + ql]; \
        const size_t so = (size_t)(HEAD) * (SKV / 2) + (GRP) * 256 + sofs; \
        lkz = *((const unsigned*)k_zeros + so); lks = *((const unsigned*)k_scales + so); \
        lvz = *((const unsigned*)v_zeros + so); lvs = *((const unsigned*)v_scales + so); }
#define UNIT_FILL(SL, U) { \
            v4f qq; \
            qq.x = bfr(bfr(bflo(q1) * bflo(c1)) + bfr(-bflo(q2) * bflo(s1))); \
            qq.y = bfr(bfr(bflo(q2) * bflo(c2)) + bfr(bflo(q1) * bflo(s2))); \
            qq.z = bfr(bfr(bfhi(q1) * bfhi(c1)) + bfr(-bfhi(q2) * bfhi(s1))); \
            qq.w = bfr(bfr(bfhi(q2) * bfhi(c2)) + bfr(bfhi(q1) * bfhi(s2))); \
            *(v2u*)&sm.q[SL][2 * ql] = (v2u){(__float_as_uint(qq.x) >> 16) | __float_as_uint(qq.y), (__float_as_uint(qq.z) >> 16) | __float_as_uint(qq.w)}; \
            *(v4f*)&sm.kz[SL][2 * lane] = (v4f){bflo(lkz), bflo(lks), bfhi(lkz), bfhi(lks)}; \
            *(v4f*)&sm.vz[SL][2 * lane] = (v4f){bflo(lvz), bflo(lvs), bfhi(lvz), bfhi(lvs)}; \
            CBAR(); \
            if (lane == 0) lds_st(&sm.rdy[SL], (U) + 1); \
            CBAR(); }
        const unsigned dw = NSW + 2 - wave;
#define TOUCH() { const unsigned x_ = ((blockIdx.x >> 3) - 18) * 64 + lane; \
            const char* p_ = x_ < 384 ? (const char*)(x_ < 192 ? qk : qv) + (size_t)(x_ < 192 ? x_ : x_ - 192) * (2u << 20) + (x_ * 8 + (blockIdx.x & 7)) * 640 : (const char*)out + (x_ - 384) * 4096; \
            if (x_ < 408) asm volatile("global_load_dword a0, %0, off nt" :: "v"(p_) : "a0"); }
        QT_LOADS()
        UNIT_LOADS(head0 + dw * (GSZ + 1 - 2 * side), lane >> 4)
        if (dw) TOUCH()
#pragma unroll 1
        for (unsigned u = dw;;) {
            while (u >= lds_ld(&sm.merged) + NIN) SLEEP(4);
            CBAR();
            UNIT_FILL(u % NIN, u)
            u += 2;
            if (u >= NU) break;
            if (u == 4) {
                const unsigned tpf = tdue(PSF - 1) + TFO;
                { int d; while ((d = (int)(lds_ld(&sm.line) + tpf - NOW())) > 0 && d < 3000) SLEEP(1) }
#define PF3(P) asm volatile("global_load_dwordx4 a[0:3], %0, %1\n\tglobal_load_dwordx4 a[0:3], %0, %1 offset:1024\n\tglobal_load_dwordx4 a[0:3], %0, %1 offset:2048" \
                            :: "v"(64 * SKV * 2 + blockIdx.x * 3072 + lane * 16), "s"(P) : "a0", "a1", "a2", "a3")
                PF3(k_zeros); PF3(k_scales); PF3(v_zeros); PF3(v_scales);
                asm volatile("global_load_dword a0, %0, %1" :: "v"(blockIdx.x * (DH * 2) + ql * 8), "s"(q) : "a0");
            }
            const unsigned tin = tdue(first_of(u) - (QUARTER(u) ? 24 : 0) - LEADP + (split & 7) * DPH);
            { int d; while ((d = (int)(lds_ld(&sm.line) + tin - NOW())) > 0 && d < 3000) SLEEP(4) }
            unsigned hd;
            while ((hd = lds_ld(&sm.utab[u])) == ~0u) SLEEP(2)
            UNIT_LOADS(hd & 255, QUARTER(u) ? rank & 3 : lane >> 4)
        }
        while (lds_ld(&sm.tag_rdy) == 0) SLEEP(1);
        CBAR();
        E1 = (u64)lds_ld(&sm.tag_lo) | ((u64)lds_ld(&sm.tag_hi) << 32);
        if (wave == NSW + 1 && blockIdx.x == 0) {
            for (;;) {
                bool ok = true;
#pragma unroll
                for (int i = 0; i < NH / 64; i++) ok &= __hip_atomic_load((const u64*)(ws + WS_POST) + i * 64 + lane, __ATOMIC_RELAXED, __HIP_MEMORY_SCOPE_AGENT) >> 24 == (E1 << 24) >> 24;
                if (__builtin_amdgcn_ballot_w64(ok) == ~0ull) break;
                SLEEP(64);
            }
            if (lane == 0) __hip_atomic_store((u64*)(ws + WS_EPOCH), E1, __ATOMIC_RELAXED, __HIP_MEMORY_SCOPE_AGENT);
        }
        if (wave == NSW + 2) {
            const unsigned t16 = (unsigned)E1 & 0xffffu, t16b = ((unsigned)E1 + ((unsigned)E1 >> 16)) & 0xffffu;
            const __amdgpu_buffer_rsrc_t rsrc = __builtin_amdgcn_make_buffer_rsrc(ws + WS_SWAP + (blockIdx.x ^ 4) * 5120, 0, 5120, 0x00020000);
            const unsigned swp = lds_ld(&sm.swp);
            if (swp) {
                unsigned use = 0;
#pragma unroll
                for (int i = 0; i < 5; i++) use |= ((swp >> wunit(i * 64 + lane)) & 1) << i;
                const unsigned tg = tdue(PSG - 1) + (lds_ld(&sm.gap) & 0xffffu);
                while ((int)(NOW() - lds_ld(&sm.line) - tg) < 0) SLEEP(2)
                for (;;) {
                    v4u d[5]; bool ok = true;
#pragma unroll
                    for (int i = 0; i < 5; i++) d[i] = __builtin_amdgcn_raw_buffer_load_b128(rsrc, (use >> i) & 1 ? (i * 64 + lane) * 16 : 0x40000000, 0, SC1 | 2);
#pragma unroll
                    for (int i = 0; i < 5; i++) if ((use >> i) & 1) {
                        ok &= d[i].y >> 16 == t16 && d[i].w >> 16 == t16b;
                        unsigned* w = (unsigned*)&sm.wv[144 * NMIX + (i * 64 + lane) * 6];
                        w[0] = d[i].x; w[1] = (d[i].y & 0xffffu) | (d[i].z << 16); w[2] = (d[i].z >> 16) | (d[i].w << 16);
                    }
                    if (__builtin_amdgcn_ballot_w64(ok) == ~0ull) break;
                    SLEEP(32)
                }
                CBAR();
                LDS_OR(&sm.vrdy, swp)
            }
        }
    } else {
        const unsigned eu = lane >= NU ? lane - NU : lane, win = min(first_of(eu) >> 5, 15u), pair = QUARTER(eu) ? 2 * (eu - WQ) + (rank >> 2) : rank;
        const unsigned rot2 = ((const unsigned char*)ROT)[win * 8 + pair];
#define UENT(BIT) (win * 16 + 2 * pair + ((BIT) ^ side) | (QUARTER(eu) ? (rank - (eu - WQ)) * 8 & 31 : (rot2 >> ((BIT) ^ side) * 4 & 15) * 2) << 8)
        unsigned far = 0;
        const unsigned hp = (blockIdx.x & 1) * NH + (blockIdx.x >> 1) * 4 + 2;
        if (blockIdx.x < 144) {
            const char* pb = (blockIdx.x < 128 ? (const char*)(hp >= NH ? qv : qk) + (size_t)(hp & (NH - 1)) * (SKV * DH * 4) + SKV * DH * 2
                                               : (const char*)ws + WS_PART + (4u << 20) + (blockIdx.x & 15) * 65536) + ((NOW() * 0x9e3779b1u) >> 26) * 128;
            unsigned mn[2] = {~0u, ~0u};
#pragma unroll 1
            for (unsigned i = 1; i < 2 * NPRB + 2; i++) {
                u64 t0, t1; unsigned dm;
                asm volatile("s_memtime %0\n\ts_waitcnt lgkmcnt(0)\n\ts_load_dword %2, %3, 0x0\n\ts_waitcnt lgkmcnt(0)\n\ts_memtime %1\n\ts_waitcnt lgkmcnt(0)"
                             : "=&s"(t0), "=&s"(t1), "=&s"(dm) : "s"(pb + i * 8192) : "memory");
                const unsigned t = (unsigned)(t1 - t0) < 330 || i == 1 ? ~0u : (unsigned)(t1 - t0);
                if (i & 1) mn[1] = min(mn[1], t); else mn[0] = min(mn[0], t);
            }
            far = mn[0] > mn[1];
        }
        if (lane < NMIX) lds_st(&sm.utab[lane], UENT(lane & 1));
        while (lds_ld(&sm.tag_rdy) == 0) SLEEP(1);
        CBAR();
        E1 = (u64)lds_ld(&sm.tag_lo) | ((u64)lds_ld(&sm.tag_hi) << 32);
        {
            const unsigned own = lds_ld(&sm.line), tg = (unsigned)E1;
            if (lane == 0) __hip_atomic_store((u64*)(ws + WS_POST) + blockIdx.x, E1 << 24 | min(lds_ld(&sm.slow), 4000u) << 1 | ((far ^ (blockIdx.x >> 2) ^ (blockIdx.x < 128 ? 0x96 >> (hp & 7) : 0)) & 1), __ATOMIC_RELAXED, __HIP_MEMORY_SCOPE_AGENT);
            const __amdgpu_buffer_rsrc_t nrs = __builtin_amdgcn_make_buffer_rsrc(ws + WS_NEWS, 0, 8 * 1280, 0x00020000);
            if (blockIdx.x == MASTER) {
                while ((int)(NOW() - own) < TMAP) SLEEP(2)
                const unsigned a = tg << 24, b = (unsigned)(E1 >> 8);
                v4u x; v2u z; u64 v0, v1;
#pragma unroll 1
                for (int i = 0; i < 8; i++) {
                    x = __builtin_amdgcn_raw_buffer_load_b128(__builtin_amdgcn_make_buffer_rsrc(ws + WS_POST, 0, 1024, 0x00020000), lane * 16, 0, SC1);
                    z = __builtin_amdgcn_raw_buffer_load_b64(__builtin_amdgcn_make_buffer_rsrc(ws + WS_POST + 1024, 0, 128, 0x00020000), lane * 8, 0, SC1);
                    v0 = __builtin_amdgcn_ballot_w64(((x.x ^ a) >> 24 | (x.y ^ b)) == 0); v1 = __builtin_amdgcn_ballot_w64(((x.z ^ a) >> 24 | (x.w ^ b)) == 0);
                    if (__builtin_popcountll(v0) + __builtin_popcountll(v1) >= MINV) break;
                    SLEEP(8)
                }
                const u64 m0 = __builtin_amdgcn_ballot_w64(x.x & 1) & v0, m1 = __builtin_amdgcn_ballot_w64(x.z & 1) & v1;
                const int s0 = lane < 43 ? min(max(lane - VW, 4 * NMIX), 42 - 2 * VW) : min(max(lane - VW, 43), 63 - 2 * VW);
#define PC(M) __builtin_popcount((unsigned)((M) >> s0) & ((2u << 2 * VW) - 1))
                const u64 g0 = __builtin_amdgcn_ballot_w64(2 * PC(m0) > PC(v0)), g1 = __builtin_amdgcn_ballot_w64(2 * PC(m1) > PC(v1));
                const float sl = wave_sum(((v0 >> lane & 1) ? (float)(x.x >> 1 & 4095u) : 0.f) + ((v1 >> lane & 1) ? (float)(x.z >> 1 & 4095u) : 0.f)) / ((8.f - PRE0) * (__builtin_popcountll(v0) + __builtin_popcountll(v1)) + 1.f);
                const unsigned al = lds_ld(&sm.aline), mean = (al ? al : own) + (int)fmaxf(sl - SL0, 0.f);
                const bool vz = lane < 16 && ((z.x ^ a) >> 24 | (z.y ^ b)) == 0;
                const unsigned gw = 2 * __builtin_popcountll(__builtin_amdgcn_ballot_w64(vz && (z.x & 1))) > __builtin_popcountll(__builtin_amdgcn_ballot_w64(vz));
                const unsigned k = lane & 3;
                if (lane < 32 && k < 3) __builtin_amdgcn_raw_buffer_store_b128((v4u){k == 0 ? (unsigned)g0 : k == 1 ? (unsigned)g1 : mean, tg, k == 0 ? (unsigned)(g0 >> 32) : k == 1 ? (unsigned)(g1 >> 32) : gw, tg},
                                                                              nrs, (lane >> 2) * 1280 + k * 16, 0, SC1);
            }
            while ((int)(NOW() - own) < TMAP + TRES) SLEEP(2)
            v4u y;
            for (;;) {
                y = __builtin_amdgcn_raw_buffer_load_b128(nrs, lane < 3 ? (blockIdx.x & 7) * 1280 + lane * 16 : 0x40000000, 0, SC1);
                if (((unsigned)__builtin_amdgcn_ballot_w64(y.y == tg && y.w == tg) & 7u) == 7u) break;
                SLEEP(8)
            }
            const u64 f0 = (unsigned)__builtin_amdgcn_readlane(y.x, 0) | (u64)(unsigned)__builtin_amdgcn_readlane(y.z, 0) << 32, f1 = (unsigned)__builtin_amdgcn_readlane(y.x, 1) | (u64)(unsigned)__builtin_amdgcn_readlane(y.z, 1) << 32;
            if (lane == 2) { sm.aline = y.x - own + 500 < 1000 ? y.x : own; sm.chead = (blockIdx.x & ~5u) | ((blockIdx.x >> 2 ^ y.z) & 1) | (blockIdx.x & 1) << 2; }
            unsigned bit = ((unsigned)((lane >= NU ? f1 : f0) >> (4 * win + (pair >> 1))) ^ (0x66 >> pair)) & 1;
            if (eu < NMIX) bit = eu & 1;
            const u64 bm = __builtin_amdgcn_ballot_w64(bit);
            const unsigned nsw = __builtin_popcountll((f0 ^ f1) >> (4 * NMIX));
            if (lane == 0) { sm.swp = (unsigned)(bm ^ (bm >> NU)) & ((1u << NU) - 1); lds_st(&sm.gap, nsw ? (TG0 + TG1 * nsw / 8) * 0x10001u + ((TH0 + TH1 * nsw / 8) << 16) : 0u); }
            CBAR();
            lds_st(&sm.utab[lane], UENT(bit));
        }
        const unsigned swp = lds_ld(&sm.swp);
        const unsigned T = lds_ld(&sm.tag_lo);
        const unsigned t15 = T & 0x7fffu;
        const __amdgpu_buffer_rsrc_t rsrc = __builtin_amdgcn_make_buffer_rsrc(ws + WS_PART + split * 512, 0, NRR * NSPLIT * 512 - split * 512, 0x00020000);
        v2u* recs = (v2u*)&sm.cs[0][0][0];
#pragma unroll
        for (int i = 0; i < NOUT * 3; i++) (&sm.acc[0][0])[i * 64 + lane] = 0.0;
        CBAR();
        unsigned vut = sm.utab[NU + (lane & 31)] & 255;
        if (QUARTER((unsigned)lane) && (rank & 3)) vut = NH - 16 + (rank & 3) * 16 + ((vut & 15) ^ 1);
        __builtin_amdgcn_s_setprio(3);
        bool direct = false, last = false;
#define PUBLISH(N) { \
        _Pragma("unroll 1") for (unsigned i = 0; i < 20; i += 4) { \
            const unsigned j = i < 10 ? i + WQ : i - 12; \
            v2u r[4]; \
            _Pragma("unroll") for (int k = 0; k < 4; k++) r[k] = recs[(j + k < 20 ? j + k : j + k - 12) * 64 + lane]; \
            _Pragma("unroll") for (int k = 0; k < 4; k++) { const unsigned x = j + k < 20 ? j + k : j + k - 12; \
                if (x < (N)) __builtin_amdgcn_raw_buffer_store_b64(r[k], rsrc, lane * 8, __builtin_amdgcn_readlane(vut, x) * (NSPLIT * 512), SC1); } } }
#pragma unroll 1
        for (unsigned u = 0; u < 2 * NU; u++) {
            const unsigned e = u % NU, so = u % NOUT;
            if (u >= NU) while (!((lds_ld(&sm.vrdy) >> e) & 1)) SLEEP(4)
            CBAR();
            const v4f h = *(const v4f*)&sm.wv[went(e) + (QUARTER(e) ? 32 : 128)];
            const unsigned row = __builtin_amdgcn_readlane(vut, e) * (NSPLIT * 512);
            double a0, a1, a2;
            for (;;) {
                if (!direct && (lds_ld(&sm.grab) >= NPC - PUBP || last)) { direct = true; wave_sync(); PUBLISH(u - NU) }
                const unsigned cn = __hip_atomic_load(&sm.cnt[so], __ATOMIC_RELAXED, __HIP_MEMORY_SCOPE_WORKGROUP);
                CBAR();
                if (direct) { a0 = sm.acc[so][lane]; a1 = sm.acc[so][64 + lane]; a2 = sm.acc[so][128 + lane]; }
                if (__builtin_amdgcn_readfirstlane(cn) == (QUARTER(e) ? 8 : 32)) { if (direct || u != 2 * NU - 1) break; last = true; }
                if (direct) SLEEP(0) else SLEEP(1)
            }
            CBAR();
            if (!direct) { a0 = sm.acc[so][lane]; a1 = sm.acc[so][64 + lane]; a2 = sm.acc[so][128 + lane]; }
            if (u < NU) {
                float2 lg = *(const float2*)&sm.ent[so][2 * lane];
                if (QUARTER(e) && lane >> 4 != (rank & 3)) lg = make_float2(-INFINITY, -INFINITY);
                const v4f zs = *(const v4f*)&sm.vz[u % NIN][2 * lane];
                const float m = wave_max(fmaxf(lg.x, lg.y));
                const float p0 = __expf(lg.x - m), p1 = __expf(lg.y - m);
                const float x0 = p0 * zs.y, x1 = p1 * zs.w;
                const unsigned eb = max(__float_as_uint(wave_max(fmaxf(fabsf(x0), fabsf(x1)))) >> 23, 14u);
                const float f = __uint_as_float((268u - eb) << 23);
                const float w0 = fminf(fmaxf(rintf(x0 * f), -32767.f), 32767.f), w1 = fminf(fmaxf(rintf(x1 * f), -32767.f), 32767.f);
                const float den = wave_sum(p0 + p1);
                const float bz = wave_sum(fmaf(w0, zs.x, w1 * zs.z));
                const float L = fmaf(m, 1.4426950408889634f, __builtin_amdgcn_logf(den));
                unsigned* wd = (unsigned*)(((swp >> u) & 1 ? sm.wo : sm.wv) + went(u));
                if (!QUARTER(u) || lane >> 4 == (rank & 3)) wd[QUARTER(u) ? lane & 15 : lane] = ((unsigned)(int)w0 & 0xffffu) | ((unsigned)(int)w1 << 16);
                if (lane == 0) *(v4f*)&wd[QUARTER(u) ? 16 : 64] = (v4f){ldexpf(1.f / den, (int)eb - 141), bz, L, 0.f};
                CBAR();
                if (!((swp >> u) & 1)) LDS_OR(&sm.vrdy, 1u << u)
            } else {
                const float n0 = (float)(a0 - (double)h.y) * h.x, n1 = (float)(a1 - (double)h.y) * h.x, n2 = (float)(a2 - (double)h.y) * h.x;
                const unsigned e2 = max(__float_as_uint(wave_maxv(fmaxf(fmaxf(fabsf(n0), fabsf(n1)), fabsf(n2)))) >> 23, 14u);
                const float f2 = __uint_as_float((268u - e2) << 23);
                const unsigned m01 = __builtin_bit_cast(unsigned, __builtin_amdgcn_cvt_pk_i16((int)rintf(n0 * f2), (int)rintf(n1 * f2)));
                const unsigned m2 = __builtin_bit_cast(unsigned, __builtin_amdgcn_cvt_pk_i16((int)rintf(n2 * f2), 0));
                const unsigned bit = ((lane < 32 ? __float_as_uint(h.z) : e2) >> (lane & 31)) & 1u;
                const v2u r = {m01, m2 | (bit << 16) | (t15 << 17)};
                if (direct) __builtin_amdgcn_raw_buffer_store_b64(r, rsrc, lane * 8, row, SC1);
                else recs[e * 64 + lane] = r;
                CBAR();
                sm.acc[so][lane] = 0.0; sm.acc[so][64 + lane] = 0.0; sm.acc[so][128 + lane] = 0.0;
            }
            CBAR();
            if (lane == 0) { lds_st(&sm.cnt[so], 0u); CBAR(); lds_st(&sm.merged, u + 1); }
            if (u == NU - 1 && swp) {
                wave_sync();
                const __amdgpu_buffer_rsrc_t srs = __builtin_amdgcn_make_buffer_rsrc(ws + WS_SWAP + blockIdx.x * 5120, 0, 5120, 0x00020000);
                v4u d[5]; unsigned o[5];
#pragma unroll
                for (int i = 0; i < 5; i++) {
                    const unsigned* w = (const unsigned*)&sm.wo[144 * NMIX + (i * 64 + lane) * 6];
                    const unsigned d0 = w[0], d1 = w[1], d2 = w[2];
                    d[i] = (v4u){d0, (d1 & 0xffffu) | (T << 16), (d1 >> 16) | (d2 << 16), (d2 >> 16) | ((T + (T >> 16)) << 16)};
                    o[i] = (swp >> wunit(i * 64 + lane)) & 1 ? (i * 64 + lane) * 16 : 0x40000000;
                }
                while ((int)(NOW() - lds_ld(&sm.line)) < (int)tdue(PSW) + TWO) SLEEP(1)
#pragma unroll
                for (int i = 0; i < 5; i++) __builtin_amdgcn_raw_buffer_store_b128(d[i], srs, o[i], 0, SC1 | 2);
            }
        }
        return;
    }
    if (blockIdx.x >> 4 == WQ) {
        while (lds_ld(&sm.grab) < NPC + 2 * NSW) SLEEP(2)
        const __amdgpu_buffer_rsrc_t rq = __builtin_amdgcn_make_buffer_rsrc(ws + WS_PART + (size_t)(NH + (wave - NSW) * 16 + ((lds_ld(&sm.chead) & 15) ^ 1)) * (NSPLIT * 512), 0, NSPLIT * 512, 0x00020000);
        const unsigned t15 = lds_ld(&sm.tag_lo) & 0x7fffu;
        v2u x[NSPLIT];
        unsigned vl = 0, ve = 0;
        for (;;) {
            bool ok = true;
#pragma unroll
            for (int k = 0; k < NSPLIT; k++) x[k] = __builtin_amdgcn_raw_buffer_load_b64(rq, lane * 8 + k * 512, 0, SC1);
#pragma unroll
            for (int k = 0; k < NSPLIT; k++) {
                ok &= (x[k].y >> 17) == t15;
                const u64 bits = __builtin_amdgcn_ballot_w64((x[k].y & 0x10000u) != 0);
                if ((lane & 15) == k) { vl = (unsigned)bits; ve = (unsigned)(bits >> 32); }
            }
            if (__builtin_amdgcn_ballot_w64(ok) == ~0ull) break;
            SLEEP(4)
        }
        const float M = rdlane(row_max(__uint_as_float(vl)), 0), w = __builtin_amdgcn_exp2f(__uint_as_float(vl) - M);
        const int ex = (int)(ve & 255u), em = (int)rdlane(row_max((float)ex), 0);
        const float sc = ldexpf(w, ex - em);
        v4f sum = {0.f, 0.f, 0.f, rdlane(row_sum(w), 0)};
#pragma unroll
        for (int k = 0; k < NSPLIT; k++) {
            const float c = rdlane(sc, k);
            sum.x = fmaf(c, (float)(short)(x[k].x & 0xffffu), sum.x); sum.y = fmaf(c, (float)((int)x[k].x >> 16), sum.y); sum.z = fmaf(c, (float)(short)(x[k].y & 0xffffu), sum.z);
        }
        sum.x = ldexpf(sum.x, em - 141); sum.y = ldexpf(sum.y, em - 141); sum.z = ldexpf(sum.z, em - 141);
        v4f* pb = (v4f*)((char*)&sm.cs[0][0][0] + 65536);
        pb[(wave + 1) * 64 + lane] = sum;
        if (lane == 0) ((float*)(pb + NSW * 64))[wave] = M;
        CBAR();
        LDS_ADD(&sm.qdone, 1u)
    }
}

extern "C" void kernel_launch(void* const* d_in, const int* in_sizes, int n_in,
                              void* d_out, int out_size, void* d_ws, size_t ws_size,
                              hipStream_t stream) {
    const unsigned short* q = (const unsigned short*)d_in[0];
    const int* qk = (const int*)d_in[1];
    const unsigned short* k_scales = (const unsigned short*)d_in[2];
    const unsigned short* k_zeros = (const unsigned short*)d_in[3];
    const int* qv = (const int*)d_in[4];
    const unsigned short* v_scales = (const unsigned short*)d_in[5];
    const unsigned short* v_zeros = (const unsigned short*)d_in[6];
    const unsigned short* q_cos = (const unsigned short*)d_in[7];
    const unsigned short* q_sin = (const unsigned short*)d_in[8];
    const unsigned short* k_cos = (const unsigned short*)d_in[9];
    const unsigned short* k_sin = (const unsigned short*)d_in[10];
    unsigned char* ws = (unsigned char*)d_ws;
    attn_kernel<<<NSPLIT * GSZ, NTHREADS, 0, stream>>>(q, qk, k_scales, k_zeros, qv, v_scales, v_zeros,
                                                       q_cos, q_sin, k_cos, k_sin, (__hip_bfloat16*)d_out, ws);
}
```

```cpp
#define DIAG 39
#include <hip/hip_runtime.h>
#include <hip/hip_bf16.h>
#include <stdint.h>

#define DH 192
#define HALF 96
#define SKV 2048
#define NH 256
#define NSW 8
#define NWAVES (NSW + 4)
#define NTHREADS (64 * NWAVES)
#define BROWS 128
#define NSPLIT 16
#define GSZ 16
#define WQ 10
#define NU (NH / GSZ + 3)
#define NPC 1024
#define NRR (NH + 3 * GSZ)
#define NIN 8
#define NOUT 8
#define WALL (15 * 144 + 4 * 48)
#define SC1 16
#define NRND 48
#define RST 4352
#define TK 2
#define NMIX 3
#define TUNIT 311
#define T01 50
#define TMIX 7
#define TOFF 50
#define PSF 100
#define TGS 68
#define PSW (NPC / 2 + 28)
#define TG0 8
#define TG1 3
#define TWO -75
#define PSG (NPC / 2 + 60)
#define TH0 3
#define TH1 3
#define EARLY 10
#define LEADP 24
#define DPH 2
#define TA 210
#define TA2 70
#define CSH 2
#define NPRB 3
#define TMAP 520
#define MINV 96
#define VW 4
#define MASTER 203
#define TRES 190
#define TB1 1200
#define PER 300
#define RDD 100
#define LEAD 70
#define LHI 85
#define LOK 74
#define PSL0 124
#define PUBP -8
#define LJ 210
#define LOK2 75
#define NTRY 6
#define TFO 6
#define SL0 42.f
#define PST0 200
#define KC 4
#define TC0 1000
#define TCS 9
#define PRE0 3

#define WS_EPOCH 0
#define WS_POST0 8192
#define WS_POST (WS_POST0 + ((unsigned)E1 & 15) * 2048)
#define WS_NEWS (49152 + 256)
#define WS_HELLO 59904
#define WS_BOARD 65536
#define WS_PART (2u << 20)
#define WS_SWAP (WS_PART + NRR * NSPLIT * 512)

typedef unsigned int v2u __attribute__((ext_vector_type(2)));
typedef unsigned int v16u __attribute__((ext_vector_type(16)));
typedef unsigned int v4u __attribute__((ext_vector_type(4)));
typedef int v4i __attribute__((ext_vector_type(4)));
typedef float v2f __attribute__((ext_vector_type(2)));
typedef float v4f __attribute__((ext_vector_type(4)));
typedef unsigned long long u64;

__device__ __forceinline__ float bflo(unsigned u) { return __uint_as_float(u << 16); }
__device__ __forceinline__ float bfhi(unsigned u) { return __uint_as_float(u & 0xffff0000u); }
__device__ __forceinline__ float bfr(float x) {
    __bf16 b = (__bf16)x;
    return __uint_as_float(((unsigned)__builtin_bit_cast(unsigned short, b)) << 16);
}

#define SLEEP(n) SLEEP_(n)
#define SLEEP_(n) asm volatile("s_sleep " #n "\n\tv_add_co_u32 %0, vcc, 1, %0\n\ts_cbranch_vccz 1f\n\ts_endpgm\n1:" : "+v"(spins) :: "vcc", "memory");
#define SPIN0 0xf0000000u
#define CBAR() asm volatile("" ::: "memory")

__device__ __forceinline__ void wave_sync() {
    __builtin_amdgcn_fence(__ATOMIC_RELEASE, "wavefront");
    __builtin_amdgcn_wave_barrier();
    __builtin_amdgcn_fence(__ATOMIC_ACQUIRE, "wavefront");
}
template <int CTRL>
__device__ __forceinline__ float dpp_add(float x) {
    int y = __builtin_amdgcn_update_dpp(0, __float_as_int(x), CTRL, 0xf, 0xf, true);
    return x + __int_as_float(y);
}
template <int CTRL>
__device__ __forceinline__ float dpp_max(float x) {
    int y = __builtin_amdgcn_update_dpp(__float_as_int(x), __float_as_int(x), CTRL, 0xf, 0xf, false);
    return fmaxf(x, __int_as_float(y));
}
__device__ __forceinline__ float row_sum(float x) { x = dpp_add<0x128>(x); x = dpp_add<0x124>(x); x = dpp_add<0x122>(x); return dpp_add<0x121>(x); }
__device__ __forceinline__ float row_max(float m) { m = dpp_max<0x128>(m); m = dpp_max<0x124>(m); m = dpp_max<0x122>(m); return dpp_max<0x121>(m); }
__device__ __forceinline__ float rdlane(float x, int l) { return __int_as_float(__builtin_amdgcn_readlane(__float_as_int(x), l)); }
__device__ __forceinline__ float wave_max(float m) { m = row_max(m); return fmaxf(fmaxf(rdlane(m, 0), rdlane(m, 16)), fmaxf(rdlane(m, 32), rdlane(m, 48))); }
__device__ __forceinline__ float wave_maxv(float m) {
    m = row_max(m);
    const auto a = __builtin_amdgcn_permlane16_swap(__float_as_uint(m), __float_as_uint(m), false, false);
    m = fmaxf(__uint_as_float(a[0]), __uint_as_float(a[1]));
    const auto b = __builtin_amdgcn_permlane32_swap(__float_as_uint(m), __float_as_uint(m), false, false);
    return fmaxf(__uint_as_float(b[0]), __uint_as_float(b[1]));
}
__device__ __forceinline__ float wave_sum(float x) { x = row_sum(x); return (rdlane(x, 0) + rdlane(x, 16)) + (rdlane(x, 32) + rdlane(x, 48)); }
__device__ __forceinline__ unsigned lds_ld(const unsigned* p) { return __builtin_amdgcn_readfirstlane(__hip_atomic_load(p, __ATOMIC_RELAXED, __HIP_MEMORY_SCOPE_WORKGROUP)); }
__device__ __forceinline__ unsigned lds_addr(const void* p) { return (unsigned)(size_t)(const __attribute__((address_space(3))) void*)p; }
#define DMA16(SRC, LDS) asm volatile("s_mov_b32 %0, m0\n\ts_mov_b32 m0, %2\n\ts_nop 0\n\tglobal_load_lds_dwordx4 %1, off\n\ts_mov_b32 m0, %0" : "=&s"(m0keep) : "v"(SRC), "s"(LDS) : "memory")
__device__ __forceinline__ void lds_st(unsigned* p, unsigned v) { __hip_atomic_store(p, v, __ATOMIC_RELAXED, __HIP_MEMORY_SCOPE_WORKGROUP); }
#define NOW() ((unsigned)__builtin_amdgcn_s_memrealtime())
__device__ __forceinline__ unsigned tdue(unsigned g) {
    return (g * TUNIT + min(g, 480u) * TK + min(g, 48u) * T01 + (min(g, 32u * NMIX) + min(max((int)g - NPC / 2, 0), 32 * NMIX)) * TMIX + (g >= PSF ? TGS * 32 : 0)) >> 5;
}

__device__ __forceinline__ unsigned unit_of(unsigned g) { const unsigned p = g & 511; return (g >> 9) * NU + (p >> 5) + min(max((int)p - 32 * WQ, 0) >> 3, 3); }
__device__ __forceinline__ unsigned first_of(unsigned e) { return 32 * e - 24 * min(max((int)e - WQ, 0), 4); }
#define QUARTER(E) ((E) - WQ < 4)
__device__ __forceinline__ unsigned went(unsigned e) { return 144 * e - 96 * min(max((int)e - WQ, 0), 4); }
__device__ __forceinline__ unsigned wunit(unsigned x) { x += 24 * NMIX; return x < 24 * WQ ? x / 24 : x < 24 * WQ + 32 ? WQ + (x - 24 * WQ) / 8 : (x + 64) / 24; }

#define ROTT 0xed8d2c3d2c35ed84ull, 0xba85aa165b03ac82ull, 0xee7efa489a38af71ull, 0xe932da1b0bab51d8ull, 0xfa96112d5fee971bull, 0x2c7e9ea5a544f071ull, 0x3d86093d5f0e810bull, 0xe932da1b0bab51d8ull, 0xfa96112d5fee971bull, 0xd834e62c5fa723e9ull, 0x1d7d1a3d2c061d88ull, 0xe932da1b0bab51d8ull, 0xfa96112d5fee971bull, 0xc834e12c0aaa4ec7ull, 0x3d86093d5f0e810bull, 0xe932da1b0bab51d8ull
static __device__ const u64 ROT[16] = {ROTT};

struct __attribute__((aligned(16))) Smem {
    unsigned cs[NSW][4][768];
    double acc[NOUT][DH];
    float ent[NOUT][BROWS];
    unsigned short wv[WALL];
    unsigned short wo[WALL];
    unsigned q[NIN][HALF];
    float2 kz[NIN][BROWS];
    float2 vz[NIN][BROWS];
    unsigned utab[64];
    unsigned line, gap;
    unsigned late, grab;
    unsigned rdy[NIN], cnt[NOUT];
    unsigned tabs, vrdy, swp;
    unsigned aline;
    unsigned tag_rdy, tag_lo, tag_hi, merged, cdone, qdone;
    unsigned slow;
    unsigned chead, dg;
};

#define LDS_ADD(P, V) { u64 ex_; asm volatile("s_mov_b64 %0, exec\n\ts_mov_b64 exec, 1\n\tds_add_u32 %1, %2\n\ts_mov_b64 exec, %0" : "=&s"(ex_) : "v"(lds_addr(P)), "v"(V) : "memory"); }
#define LDS_OR(P, V) { u64 ex_; asm volatile("s_mov_b64 %0, exec\n\ts_mov_b64 exec, 1\n\tds_or_b32 %1, %2\n\ts_mov_b64 exec, %0" : "=&s"(ex_) : "v"(lds_addr(P)), "v"(V) : "memory"); }

__global__ __launch_bounds__(NTHREADS, 3) void attn_kernel(
    const unsigned short* __restrict__ q, const int* __restrict__ qk,
    const unsigned short* __restrict__ k_scales, const unsigned short* __restrict__ k_zeros,
    const int* __restrict__ qv,
    const unsigned short* __restrict__ v_scales, const unsigned short* __restrict__ v_zeros,
    const unsigned short* __restrict__ q_cos, const unsigned short* __restrict__ q_sin,
    const unsigned short* __restrict__ k_cos, const unsigned short* __restrict__ k_sin,
    __hip_bfloat16* __restrict__ out, unsigned char* __restrict__ ws)
{
    __shared__ Smem sm;
    const int tid = threadIdx.x;
    unsigned spins = SPIN0;
    const int lane = tid & 63;
    const int hw = __builtin_amdgcn_readfirstlane(tid >> 6);
    u64 t00; unsigned ka;
    asm volatile("s_memrealtime %0\n\ts_load_dword %1, %2, 0x40" : "=&s"(t00), "=&s"(ka) : "s"(__builtin_amdgcn_kernarg_segment_ptr()));
    if (hw == 0) {
        const char* pc0; asm volatile("s_getpc_b64 %0" : "=s"(pc0));
        asm volatile("global_load_dword a6, %0, %1" :: "v"(((blockIdx.x & ~7u) + (lane & 7)) * 128), "s"(pc0) : "a6", "memory");
    }
    const int wave = hw >= 4 ? hw - 4 : hw + NSW;
    const unsigned split = (blockIdx.x & 1) + (blockIdx.x & 2) * 4 + ((blockIdx.x >> 3) & 3) * 2;
    const unsigned side = (split ^ (blockIdx.x >> 2)) & 1, rank = blockIdx.x >> 5;
    const unsigned head0 = 2 * rank + side;
    const unsigned rf0 = (8 * split + 1) / 3, r3 = 8 * split + 1 - 3 * rf0;
    const unsigned koff = ((lane >> 5) * DH + 3 * (lane & 31)) * 4, koffb = koff + 384;
#define AR(S) (16 + (S) * 8)
#define GLD(A, OFF, BASE, IMM) asm volatile("global_load_dwordx3 a[%c0:%c1], %2, %3 offset:%4 nt" :: "i"(A), "i"((A) + 2), "v"(OFF), "s"(BASE), "i"(IMM))
#define ISSUE(S, J, BASE, OA, OB) { GLD(AR(S), OA, BASE, (J) * 1536); GLD(AR(S) + 4, OB, BASE, (J) * 1536); }
#define HEAD2 ((GSZ + 1 - 2 * side) * (SKV * DH * 4))
    const unsigned wo = (hw - 2) & 7;
    const bool pre = hw >= 4 && wo < PRE0;
    if (pre) {
        const char* a = (const char*)qk + (head0 * 512 + side * 256 + rf0 + wave + (wave >= 6 ? 80 : 0)) * 3072; const char* b = a + HEAD2;
        ISSUE(0, 0, a, koff, koffb) ISSUE(1, 1, a, koff, koffb) ISSUE(2, 0, b, koff, koffb) ISSUE(3, 1, b, koff, koffb)
    }
    asm volatile("s_waitcnt lgkmcnt(0)" : "+s"(t00), "+s"(ka));
    if (hw == 0) {
        if (DIAG == 30 || DIAG == 39) sm.dg = NOW() - (unsigned)t00;
        sm.utab[lane] = ~0u;
        if (lane < 32) (&sm.line)[lane] = lane == 3 ? 64u : (lane & 29) == 0 ? (unsigned)t00 + TOFF : 0u;
        asm volatile("s_waitcnt lgkmcnt(0)" ::: "memory");
    }
    __builtin_amdgcn_s_barrier();
    CBAR();
    const u64 rtab = r3 == 1 ? 0x5756552c2b020100ull : r3 == 0 ? 0x5756552c2b2a0100ull : 0x57562d2c2b020100ull;
#define R8(I) (rf0 + ((unsigned)(rtab >> (8 * (I))) & 255u))

    if (__builtin_expect(wave < NSW, 1)) {
        const int cst = 2 * side;
        const unsigned r8w = R8((unsigned)wave);
        const char* cbase[3];
#pragma unroll
        for (int j = 0; j < 3; j++) { const int x = j * 64 + lane; cbase[j] = (x < 96 ? (const char*)k_cos + x * 16 : (const char*)k_sin + (x - 96) * 16) + r8w * (4 * DH * 2); }
        unsigned m0keep;
#define CS_DMA(W, C) { _Pragma("unroll") for (int j = 0; j < 3; j++) DMA16(cbase[j] + (C) * (512 * DH * 2), lds_addr(&sm.cs[0][0][0]) + ((W) * 4 + (C)) * 3072 + j * 1024); }
#define REPACK(W, C) { \
        unsigned* cw = &sm.cs[W][C][0]; \
        const unsigned* l = cw + rsel * (DH / 2) + qoff; \
        unsigned r[2][6]; \
        _Pragma("unroll") \
        for (int j = 0; j < 2; j++) { \
            unsigned e[4][2]; \
            _Pragma("unroll") \
            for (int k = 0; k < 4; k++) { \
                const unsigned* a = l + j * DH + (k & 1) * 48 + (k >> 1) * 384;       \
                unsigned x0 = a[0], x1 = a[1]; \
                e[k][0] = __builtin_amdgcn_alignbit(x1, x0, sh); \
                e[k][1] = x1 >> sh; \
            } \
            r[j][0] = (e[0][0] & 0xffffu) | (e[3][0] << 16); \
            r[j][1] = (e[0][0] >> 16) | (e[3][0] & 0xffff0000u); \
            r[j][2] = (e[0][1] & 0xffffu) | (e[3][1] << 16); \
            r[j][3] = (e[1][0] & 0xffffu) | (e[2][0] << 16); \
            r[j][4] = (e[1][0] >> 16) | (e[2][0] & 0xffff0000u); \
            r[j][5] = (e[1][1] & 0xffffu) | (e[2][1] << 16); \
        } \
        wave_sync(); \
        _Pragma("unroll") \
        for (int j = 0; j < 2; j++) { *(v4u*)&cw[j * 256 + lane * 4] = (v4u){r[j][0], r[j][1], r[j][2], r[j][3]}; *(v2u*)&cw[512 + j * 128 + lane * 2] = (v2u){r[j][4], r[j][5]}; } \
        wave_sync(); }
        const int sub = lane & 31;
        const int rsel = lane >> 5;
        const unsigned sh = (sub & 1) * 16;
        const int qoff = 3 * (sub >> 1) + (sub & 1);
        const unsigned voff = 3 * lane * 4;
        asm volatile("" ::: "a16", "a17", "a18", "a19", "a20", "a21", "a22", "a23", "a24", "a25", "a26", "a27", "a28", "a29", "a30", "a31",
                            "a32", "a33", "a34", "a35", "a36", "a37", "a38", "a39", "a40", "a41", "a42", "a43", "a44", "a45", "a46", "a47");
        asm volatile("" ::: "a48", "a49", "a50", "a51", "a52", "a53", "a54", "a55", "a56", "a57", "a58", "a59", "a60", "a61", "a62", "a63",
                            "a64", "a65", "a66", "a67", "a68", "a69", "a70", "a71", "a72", "a73", "a74", "a75", "a76", "a77", "a78", "a79");
        int ta[4][3], tb[4][3];
#define WAITT(P, S, N) asm volatile("s_waitcnt vmcnt(%6)\n\tv_accvgpr_read_b32 %0, a[%c7]\n\tv_accvgpr_read_b32 %1, a[%c8]\n\tv_accvgpr_read_b32 %2, a[%c9]\n\t" \
                              "v_accvgpr_read_b32 %3, a[%c10]\n\tv_accvgpr_read_b32 %4, a[%c11]\n\tv_accvgpr_read_b32 %5, a[%c12]" \
                              : "=v"(ta[P][0]), "=v"(ta[P][1]), "=v"(ta[P][2]), "=v"(tb[P][0]), "=v"(tb[P][1]), "=v"(tb[P][2]) \
                              : "i"(N), "i"(AR(S)), "i"(AR(S) + 1), "i"(AR(S) + 2), "i"(AR(S) + 4), "i"(AR(S) + 5), "i"(AR(S) + 6))
        const unsigned voffb = voff + 768;
        u64 ln = 0;
#define TDUE(G) (tdue(G) + ((G) >= PSW ? __builtin_amdgcn_readfirstlane((unsigned)(ln >> 32)) >> ((G) >= PSG ? 16 : 0) & 0xffffu : 0u))
#define GATE(DUE) { int d_ = (int)((DUE) - NOW()); if (d_ < 3000) while (d_ > 0) { __builtin_amdgcn_s_sleep(1); d_ = (int)((DUE) - NOW()); } }
#define REPORT() { const unsigned t_ = now - tdc; if (lane == 0) lds_st(&sm.late, t_); }
#define LINE() ln = LDV((u64*)&sm.line);
#define LDV(P) __hip_atomic_load(P, __ATOMIC_RELAXED, __HIP_MEMORY_SCOPE_WORKGROUP)
        unsigned u = 0, rg = cst * 8 + wave, np = 0;
        CS_DMA(wave, cst)

        const char* kb0 = (const char*)qk + (size_t)(head0 * 512 + r8w) * 3072;
#define KB(I) (kb0 + ((cst + (I)) & 3) * (128 * 3072))
        const char* kb; const char* kb2;
#define SISSUE(I) { kb = KB(I); kb2 = kb + HEAD2; const unsigned du_ = lds_ld(&sm.line) + tdue(16 * (I) + 2 * wo); GATE(du_) \
            ISSUE(((I) & 1) * 4, 0, kb, koff, koffb) ISSUE(((I) & 1) * 4 + 1, 1, kb, koff, koffb) ISSUE(((I) & 1) * 4 + 2, 0, kb2, koff, koffb) ISSUE(((I) & 1) * 4 + 3, 1, kb2, koff, koffb) }
        if (!pre) SISSUE(0)
        CS_DMA(wave, (cst + 1) & 3)
        if (pre) asm volatile("s_waitcnt vmcnt(3)" ::: "memory");
        if (DIAG == 32 && wave == 0 && lane == 0) sm.dg = NOW() - (unsigned)t00;
        if (DIAG == 33 && wave == 1 && lane == 0) sm.dg = NOW() - (unsigned)t00;
        asm volatile("s_waitcnt vmcnt(11)" ::: "memory");
        if (!pre) LDS_ADD(&sm.slow, NOW() - lds_ld(&sm.line) - tdue(2 * wo))
        REPACK(wave, cst)
        SISSUE(1)
#define TABDONE(I) LDS_OR(&sm.tabs, 1u << ((((cst + (I)) & 3) * 8) + wave))
        TABDONE(0)
        const float scale = 0.07216878364870322f;
        double acc[3] = {0.0, 0.0, 0.0};
        bool first = true, chg, none = false;
        unsigned Qp[3], Qn[3], Rp[3], Rn[3];
        float kz4[4], ksc, kres[4];
        v4u ca[4]; v2u cb[4];
        v2f qe[4][3], A[4];
        unsigned vut, nrg;
        const unsigned vr = ((lane >> 3) & 3) * 128 + R8((unsigned)lane & 7);
        unsigned g, mg;
        const unsigned grab_adr = lds_addr(&sm.grab);
        asm volatile("v_accvgpr_write_b32 a13, 1\n\tv_accvgpr_write_b32 a14, 2" ::: "a13", "a14", "a15");
#define GRAB(N) { u64 ex_; asm volatile("s_mov_b64 %0, exec\n\ts_mov_b64 exec, 1\n\tds_add_rtn_u32 a15, %1, a%c2\n\ts_mov_b64 exec, %0" : "=&s"(ex_) : "v"(grab_adr), "i"(12 + (N)) : "memory"); }
#define GRABBED() { unsigned t_; asm volatile("s_waitcnt lgkmcnt(0)\n\tv_accvgpr_read_b32 %1, a15\n\ts_nop 0\n\tv_readfirstlane_b32 %0, %1\n\ts_nop 1" : "=s"(g), "=v"(t_)); }
        unsigned oa, ob, un, tdn, tiss, tdc = 0;
#define PLAN() { \
            GRABBED() un = unit_of(g); chg = un != u; none = g >= NPC; \
            const bool nv = g >= NPC / 2; \
            unsigned hd = __builtin_amdgcn_readlane(vut, un); \
            while (hd == ~0u) { SLEEP(1) hd = lds_ld(&sm.utab[un]); } \
            nrg = (g + (hd >> 8)) & 31; hd &= 255; \
            unsigned boff, boff2; asm("s_mul_i32 %0, %1, 0xc00" : "=s"(boff) : "s"(hd * 512 + __builtin_amdgcn_readlane(vr, nrg))); \
            asm("s_mul_i32 %0, %1, 0xc00" : "=s"(boff2) : "s"(hd * 512 + __builtin_amdgcn_readlane(vr, nrg + 1))); \
            const char* b_ = nv ? (const char*)qv : (const char*)qk; \
            kb = b_ + boff; kb2 = b_ + boff2; \
            oa = nv ? voff : koff; ob = nv ? voffb : koffb; \
            tdn = TDUE(g); }
#define FIRE() { ISSUE(0, 0, kb, oa, ob) ISSUE(1, 1, kb, oa, ob) ISSUE(2, 0, kb2, oa, ob) ISSUE(3, 1, kb2, oa, ob) }
#define LAUNCH() { \
            LINE() \
            const unsigned du_ = __builtin_amdgcn_readfirstlane((unsigned)ln) + tdn; \
            GATE(du_) \
            ISSUE(0, 0, kb, oa, ob) ISSUE(1, 1, kb, oa, ob) \
            GATE(du_ + 10) \
            ISSUE(2, 0, kb2, oa, ob) ISSUE(3, 1, kb2, oa, ob) tiss = NOW(); }
#define ARRIVED() \
            const unsigned now = NOW(); \
            const bool stall = now - tiss > LJ && g - PST0 < NPC - 48 - PST0; \
            const bool late = !none && !stall && (int)(now + EARLY - __builtin_amdgcn_readfirstlane((unsigned)ln) - tdn) >= 0; \
            if (late) { FIRE() tiss = now; } \
            if (stall && lane == 0) __hip_atomic_fetch_max(&sm.line, (unsigned)ln + (now - tiss) - LOK2, __ATOMIC_RELAXED, __HIP_MEMORY_SCOPE_WORKGROUP); \
            REPORT()
#define ADVANCE(ISV) { \
            if (chg) FLUSH(ISV) \
            if (!none && !late) LAUNCH() \
            first = chg; u = un; rg = nrg; tdc = tdn + 10; }
#define K_TAB(MODE) { _Pragma("unroll") for (int p = 0; p < ((MODE) ? 2 : 4); p++) { \
            const unsigned* cw = &sm.cs[(rg & 7) + (p >> 1)][rg >> 3][0]; \
            ca[p] = *(const v4u*)&cw[(p & 1) * 256 + lane * 4]; cb[p] = *(const v2u*)&cw[512 + (p & 1) * 128 + lane * 2]; } }
#define K_PRE(MODE, P) { \
        float e0, e1, e2, f0, f1, f2; \
        const unsigned* qp_ = ((MODE) && (P) >= 2) ? Rp : Qp; const unsigned* qn_ = ((MODE) && (P) >= 2) ? Rn : Qn; \
        const int tp_ = (MODE) ? (P) & 1 : (P); \
        asm("v_dot2_f32_bf16 %0, %6, %12, 0\n\tv_dot2_f32_bf16 %3, %9, %15, 0\n\tv_dot2_f32_bf16 %1, %7, %13, 0\n\tv_dot2_f32_bf16 %4, %10, %16, 0\n\t" \
            "v_dot2_f32_bf16 %2, %8, %14, 0\n\tv_dot2_f32_bf16 %5, %11, %17, 0\n\ts_nop 2" \
            : "=&v"(e0), "=&v"(e1), "=&v"(e2), "=&v"(f0), "=&v"(f1), "=&v"(f2) \
            : "v"(qp_[0]), "v"(qp_[1]), "v"(qp_[2]), "v"(qn_[0]), "v"(qn_[1]), "v"(qn_[2]), \
              "v"(ca[tp_].x), "v"(ca[tp_].y), "v"(ca[tp_].z), "v"(ca[tp_].w), "v"(cb[tp_].x), "v"(cb[tp_].y)); \
        qe[P][0] = (v2f){e0, f0}; qe[P][1] = (v2f){e1, f1}; qe[P][2] = (v2f){e2, f2}; \
        A[P] = (qe[P][0] + qe[P][1] + qe[P][2]) * (v2f){-kz4[P], -kz4[P]}; }
#define K_WAIT(MODE, P) { \
        if ((MODE) == 0) { WAITT(P, P, 6 - 2 * (P)); } \
        else { WAITT(P, (((MODE) - 1) & 1) * 4 + (P), ((MODE) < 3 ? 20 : (MODE) == 3 ? 17 : 6) - 2 * (P)); } }
#define K_FMA(P) { \
        _Pragma("unroll") \
        for (int i = 0; i < 3; i++) { v2f x = {(float)ta[P][i], (float)tb[P][i]}; A[P] = qe[P][i] * x + A[P]; } \
        kres[P] = A[P].x + A[P].y; }
#define K_RED(MODE) { \
        const bool o1 = lane & 1, o2 = lane & 2; \
        float s01 = (o1 ? kres[1] : kres[0]) + __int_as_float(__builtin_amdgcn_update_dpp(0, __float_as_int(o1 ? kres[0] : kres[1]), 0xb1, 0xf, 0xf, true)); \
        float s23 = (o1 ? kres[3] : kres[2]) + __int_as_float(__builtin_amdgcn_update_dpp(0, __float_as_int(o1 ? kres[2] : kres[3]), 0xb1, 0xf, 0xf, true)); \
        float sx = (o2 ? s23 : s01) + __int_as_float(__builtin_amdgcn_update_dpp(0, __float_as_int(o2 ? s01 : s23), 0x4e, 0xf, 0xf, true)); \
        sx = dpp_add<0x124>(sx); sx = dpp_add<0x128>(sx); \
        const auto sw = __builtin_amdgcn_permlane16_swap(__float_as_uint(sx), __float_as_uint(sx), false, false); \
        sx = __uint_as_float(sw[0]) + __uint_as_float(sw[1]); \
        if ((lane & 28) == 0) { if (MODE) sm.ent[(lane >> 1) & 1][rg * 4 + 2 * (lane & 1) + rsel] = sx * ksc; else sm.ent[u % NOUT][rg * 4 + 2 * (lane & 3) + rsel] = sx * ksc; } }
#define K_PRE4(MODE) { K_PRE(MODE, 0) K_PRE(MODE, 1) K_PRE(MODE, 2) K_PRE(MODE, 3) asm volatile("" :: "v"(A[0]), "v"(A[1]), "v"(A[2]), "v"(A[3])); }
#define K_BODY(MODE) { K_PRE4(MODE) K_WAIT(MODE, 0) K_FMA(0) K_WAIT(MODE, 1) K_FMA(1) K_WAIT(MODE, 2) K_FMA(2) K_WAIT(MODE, 3) K_FMA(3) K_RED(MODE) }
#define V_FMA(J, W0, W1) { \
        _Pragma("unroll") \
        for (int i = 0; i < 3; i++) { \
            acc[i] = fma(W0, (double)ta[J][i], acc[i]); \
            acc[i] = fma(W1, (double)tb[J][i], acc[i]); \
        } }
#define V_PIN() asm volatile("" :: "v"(acc[0]), "v"(acc[1]), "v"(acc[2]));
#define RDYWAIT(SL, SEQ) { while (lds_ld(&sm.rdy[SL]) != (SEQ)) SLEEP(1); CBAR(); }
#define LOADZ() { _Pragma("unroll") for (int p = 0; p < 4; p++) kz4[p] = sm.kz[u % NIN][rg * 4 + 2 * p + rsel].x; ksc = sm.kz[u % NIN][rg * 4 + 2 * (lane & 3) + rsel].y * scale; }
#define LOADQ() { _Pragma("unroll") for (int i = 0; i < 3; i++) { Qp[i] = sm.q[u % NIN][3 * sub + i]; Qn[i] = __builtin_amdgcn_alignbit(Qp[i], Qp[i], 16) ^ 0x80000000u; } }
#define FLUSH(ISV) { \
            mg = __builtin_amdgcn_readfirstlane(mg); \
            while (u >= mg + NOUT) { SLEEP(1); mg = lds_ld(&sm.merged); } \
            CBAR(); \
            if (ISV) { const unsigned aa = lds_addr(&sm.acc[u % NOUT][lane]); \
                asm volatile("ds_add_f64 %0, %1\n\tds_add_f64 %0, %2 offset:512\n\tds_add_f64 %0, %3 offset:1024" :: "v"(aa), "v"(acc[0]), "v"(acc[1]), "v"(acc[2]) : "memory"); \
                acc[0] = 0.0; acc[1] = 0.0; acc[2] = 0.0; } \
            CBAR(); \
            LDS_ADD(&sm.cnt[u % NOUT], np) \
            CBAR(); \
            np = 0; }

#define LOADZ2() { _Pragma("unroll") for (int p = 0; p < 4; p++) kz4[p] = sm.kz[p >> 1][rg * 4 + 2 * (p & 1) + rsel].x; ksc = sm.kz[(lane >> 1) & 1][rg * 4 + 2 * (lane & 1) + rsel].y * scale; }
#define STATIC_STEP(I) { \
            rg = ((cst + (I)) & 3) * 8 + wave; \
            LOADZ2() \
            K_TAB((I) + 1) K_BODY((I) + 1) }
        CS_DMA(wave, (cst + 2) & 3)
        RDYWAIT(0, 1u) RDYWAIT(1, 2u)
        LOADQ()
#pragma unroll
        for (int i = 0; i < 3; i++) { Rp[i] = sm.q[1][3 * sub + i]; Rn[i] = __builtin_amdgcn_alignbit(Rp[i], Rp[i], 16) ^ 0x80000000u; }
        STATIC_STEP(0)
        asm volatile("s_waitcnt vmcnt(11)" ::: "memory");
        REPACK(wave, (cst + 1) & 3)
        TABDONE(1)
        SISSUE(2)
        CS_DMA(wave, (cst + 3) & 3)
        STATIC_STEP(1)
        asm volatile("s_waitcnt vmcnt(11)" ::: "memory");
        REPACK(wave, (cst + 2) & 3)
        TABDONE(2)
        SISSUE(3)
        STATIC_STEP(2)
        asm volatile("s_waitcnt vmcnt(8)" ::: "memory");
        REPACK(wave, (cst + 3) & 3)
        TABDONE(3)
        GRAB(2)
        vut = LDV(&sm.utab[lane]);
        STATIC_STEP(3)
        CBAR();
        LDS_ADD(&sm.cnt[0], 4u) LDS_ADD(&sm.cnt[1], 4u)
        PLAN()
        LAUNCH()
        u = un; rg = nrg; tdc = tdn + 10;
        bool tabok = false;
        __builtin_amdgcn_s_setprio(2);
#pragma unroll 1
        while (u < NU) {
            GRAB(2)
            mg = LDV(&sm.merged);
            vut = LDV(&sm.utab[lane]);
            LINE()
            if (!tabok) {
                for (;;) { const unsigned tb_ = lds_ld(&sm.tabs); tabok = tb_ == ~0u; if (((tb_ >> rg) & 3u) == 3u) break; SLEEP(1) }
                CBAR();
            }
            K_TAB(0)
            if (first) {
                unsigned fl = LDV(&sm.rdy[u % NIN]);
                CBAR();
                LOADQ() LOADZ()
                if (__builtin_amdgcn_readfirstlane(fl) != u + 1) { RDYWAIT(u % NIN, u + 1) LOADQ() LOADZ() }
            } else LOADZ()
            K_PRE4(0)
            PLAN()
            K_WAIT(0, 0) K_FMA(0) K_WAIT(0, 1) K_FMA(1)
            asm volatile("" :: "v"(kres[0]), "v"(kres[1]));
            K_WAIT(0, 2) K_WAIT(0, 3)
            ARRIVED()
            K_FMA(2) K_FMA(3) K_RED(0)
            np += 2;
            ADVANCE(false)
        }
#pragma unroll 1
        while (u < 2 * NU) {
            GRAB(2)
            mg = LDV(&sm.merged);
            LINE()
            const unsigned vr_ = LDV(&sm.vrdy);
            CBAR();
            const unsigned short* wp = &sm.wv[went(u - NU) + (QUARTER(u - NU) ? rg & 7 : rg) * 4];
            v4i pw = *(const v4i*)wp;
            if (first && !((__builtin_amdgcn_readfirstlane(vr_) >> (u - NU)) & 1)) { while (!((lds_ld(&sm.vrdy) >> (u - NU)) & 1)) SLEEP(1); CBAR(); pw = *(const v4i*)wp; }
            const double w0 = (short)pw.x, w1 = pw.x >> 16, w2 = (short)pw.y, w3 = pw.y >> 16, w4 = (short)pw.z, w5 = pw.z >> 16, w6 = (short)pw.w, w7 = pw.w >> 16;
            asm volatile("" :: "v"(w0), "v"(w1), "v"(w2), "v"(w3), "v"(w4), "v"(w5), "v"(w6), "v"(w7));
            PLAN()
            WAITT(0, 0, 6); V_FMA(0, w0, w1) V_PIN()
            WAITT(1, 1, 4); V_FMA(1, w2, w3) V_PIN()
            WAITT(2, 2, 2); V_FMA(2, w4, w5) V_PIN()
            WAITT(3, 3, 0);
            np += 2;
            if (none) { V_FMA(3, w6, w7) FLUSH(true) break; }
            ARRIVED()
            V_FMA(3, w6, w7)
            ADVANCE(true)
        }
        __builtin_amdgcn_s_setprio(0);
        {
            const int head = lds_ld(&sm.chead);
            const unsigned char* rb = ws + WS_PART + head * (NSPLIT * 512) + wave * 1024;
            const unsigned t15 = lds_ld(&sm.tag_lo) & 0x7fffu;
            bool got; v2u f[2];
#define POLL(S) asm volatile("global_load_dwordx2 a[%c0:%c0+1], %1, %2 sc1\n\tglobal_load_dwordx2 a[%c0+2:%c0+3], %1, %2 offset:512 sc1" :: "i"(AR(S)), "v"(lane * 8), "s"(rb) : "memory");
#define TAKE(S, N) { v2u r_[2]; \
                asm volatile("s_waitcnt vmcnt(%c5)\n\tv_accvgpr_read_b32 %0, a[%c4]\n\tv_accvgpr_read_b32 %1, a[%c4+1]\n\tv_accvgpr_read_b32 %2, a[%c4+2]\n\tv_accvgpr_read_b32 %3, a[%c4+3]" \
                             : "=v"(r_[0].x), "=v"(r_[0].y), "=v"(r_[1].x), "=v"(r_[1].y) : "i"(AR(S)), "i"(N)); \
                f[0] = r_[0]; f[1] = r_[1]; \
                got = __builtin_amdgcn_ballot_w64(((r_[0].y >> 17 ^ t15) | (r_[1].y >> 17 ^ t15)) == 0) == ~0ull; }
            if (head < NH - GSZ) while (lds_ld(&sm.cnt[(2 * NU - 1) % NOUT]) < 32 && lds_ld(&sm.merged) < 2 * NU) SLEEP(1)
            POLL(0)
            TAKE(0, 0)
            if (!got) {
                POLL(0) SLEEP(3) POLL(1)
#pragma unroll 1
                for (;;) {
                    TAKE(0, 2) if (got) break; SLEEP(1) POLL(0)
                    TAKE(1, 2) if (got) break; SLEEP(1) POLL(1)
                }
            }
            const u64 hb[2] = {__builtin_amdgcn_ballot_w64((f[0].y & 0x10000u) != 0), __builtin_amdgcn_ballot_w64((f[1].y & 0x10000u) != 0)};
            const float L0 = __uint_as_float((unsigned)hb[0]), L1 = __uint_as_float((unsigned)hb[1]), M = fmaxf(L0, L1);
            const float w0 = __builtin_amdgcn_exp2f(L0 - M), w1 = __builtin_amdgcn_exp2f(L1 - M);
            const float c0 = ldexpf(w0, (int)((unsigned)(hb[0] >> 32) & 255u) - 141), c1 = ldexpf(w1, (int)((unsigned)(hb[1] >> 32) & 255u) - 141);
            v4f* pb = (v4f*)((char*)&sm.cs[0][0][0] + 65536);
            float* pm = (float*)(pb + NSW * 64);
            pb[wave * 64 + lane] = (v4f){fmaf(c1, (float)(short)f[1].x, c0 * (float)(short)f[0].x), fmaf(c1, (float)((int)f[1].x >> 16), c0 * (float)((int)f[0].x >> 16)),
                                         fmaf(c1, (float)(short)f[1].y, c0 * (float)(short)f[0].y), w0 + w1};
            if (lane == 0) pm[wave] = M;
            CBAR();
            { u64 ex_; asm volatile("s_mov_b64 %0, exec\n\ts_mov_b64 exec, 1\n\tds_add_rtn_u32 a15, %1, a13\n\ts_mov_b64 exec, %0" : "=&s"(ex_) : "v"(lds_addr(&sm.cdone)) : "memory"); }
            const v4f ma = *(const v4f*)pm, mb = *(const v4f*)(pm + 4);
            v4f ps[NSW];
#pragma unroll
            for (int w = 0; w < NSW; w++) ps[w] = pb[w * 64 + lane];
            GRABBED()
            if (g == NSW - 1) {
                const float mw[NSW] = {ma.x, ma.y, ma.z, ma.w, mb.x, mb.y, mb.z, mb.w};
                float Mx = mw[0];
#pragma unroll
                for (int w = 1; w < NSW; w++) Mx = fmaxf(Mx, mw[w]);
                v4f sum = {0.f, 0.f, 0.f, 0.f};
#pragma unroll
                for (int w = 0; w < NSW; w++) sum += ps[w] * __builtin_amdgcn_exp2f(mw[w] - Mx);
                if (head >> 4 == WQ) {
                    while (lds_ld(&sm.qdone) != 3) SLEEP(0)
                    CBAR();
                    const v4f mq = *(const v4f*)(pm + NSW), q0 = pb[(NSW + 1) * 64 + lane], q1 = pb[(NSW + 2) * 64 + lane], q2 = pb[(NSW + 3) * 64 + lane];
                    const float Mn = fmaxf(fmaxf(Mx, mq.x), fmaxf(mq.y, mq.z));
                    sum = sum * __builtin_amdgcn_exp2f(Mx - Mn) + q0 * __builtin_amdgcn_exp2f(mq.x - Mn) + q1 * __builtin_amdgcn_exp2f(mq.y - Mn) + q2 * __builtin_amdgcn_exp2f(mq.z - Mn);
                }
                const float inv = __builtin_amdgcn_rcpf(sum.w);
                __hip_bfloat16* o = out + (size_t)head * DH + 3 * lane;
                o[0] = __float2bfloat16(sum.x * inv); o[1] = __float2bfloat16(sum.y * inv); o[2] = __float2bfloat16(sum.z * inv);
            }
        }
        return;
    }

    u64 E1;
    if (wave == NSW) {
        unsigned ref;
        const unsigned sidx = (blockIdx.x & 7) | ((blockIdx.x >> 7) << 3);
#define SAMPLE(K) ((((blockIdx.x >> 3) - (blockIdx.x & 7) - (K) * 5) & 15) == 0)
#define BTAG(K) ((btag + (K)) & 0xfffffu)
#define POST(K, C) { if (lane == 0) __hip_atomic_store((u64*)(ws + (bd + (K) * RST + (sidx >> 3) * 256 + (sidx & 7) * 8)), (u64)(C) | ((u64)(((C) - ref) | BTAG(K) << 12) << 32), __ATOMIC_RELAXED, __HIP_MEMORY_SCOPE_AGENT); }
        const unsigned own = lds_ld(&sm.line);
        if (SAMPLE(0) && lane < NTRY) __hip_atomic_store((unsigned*)(ws + WS_HELLO + lane * 768) + sidx, own, __ATOMIC_RELAXED, __HIP_MEMORY_SCOPE_AGENT);
        const u64 E = __hip_atomic_load((const u64*)(ws + WS_EPOCH), __ATOMIC_RELAXED, __HIP_MEMORY_SCOPE_AGENT);
        E1 = E + 1;
        if (lane == 0) { sm.tag_lo = (unsigned)E1; sm.tag_hi = (unsigned)(E1 >> 32); CBAR(); lds_st(&sm.tag_rdy, 1u); }
        const unsigned btag = __builtin_amdgcn_readfirstlane((unsigned)E1) * 61u, bd = WS_BOARD + (btag & 7) * (NRND * RST);
        ref = own; unsigned hmin = own, tnews = 0, ntry = 9; unsigned hmean = own; int nl = 20, iod = 0, hmx = 500;
        int off = 0;
        {
            unsigned tries = 0, cur = own, tl = own;
            bool news = false;
#pragma unroll 1
            while (!news || cur != ref) {
                const unsigned now = NOW();
                if (tries < NTRY && (int)(now - own) >= (int)(TA + tries * TA2)) {
                    v16u x;
                    asm volatile("s_load_dwordx16 %0, %1, %2\n\ts_waitcnt lgkmcnt(0)" : "=&s"(x) : "s"(ws), "s"(__builtin_amdgcn_readfirstlane(WS_HELLO + tries * 768)) : "memory");
                    unsigned bad = 0; int sum = 0, dif = 0;
#pragma unroll
                    for (int i = 0; i < 16; i++) { bad |= x[i] - own + 512; sum += x[i] - x[0]; dif += i & 4 ? x[i] : -x[i]; }
                    tries++;
                    if (bad < 1024) { ntry = tries; hmean = x[0] + (sum >> 4); for (int i = 0; i < 16; i++) if ((int)(x[i] - hmin) < 0) hmin = x[i]; nl = 0; hmx = 0; for (int i = 0; i < 16; i++) { nl += x[i] - hmin > 80; iod += (i & 4) ? (int)(x[i] - hmin) : -(int)(x[i] - hmin); hmx = max(hmx, (int)(x[i] - hmin)); } const unsigned com = x[0] + (sum >> 4) + abs(dif) * (8 - KC) / 128; ref = x[0] + ((sum + (blockIdx.x & 4 ? dif : -dif)) >> 4); off = ref - com;
                        tries = NTRY; tl = NOW(); if (lane == 0) lds_st(&sm.aline, com); }
                    continue;
                }
                if (!news && lds_ld(&sm.utab[31]) != ~0u) { news = true; tnews = now; CBAR(); if (cur == ref) tl = now; ref = lds_ld(&sm.aline) + off; tries = NTRY; }
                const int d = (now - tl) >> CSH;
                if (cur != ref && d) { tl += d << CSH; cur += min(max((int)(ref - cur), -d), d); if (lane == 0) lds_st(&sm.line, cur); }
                SLEEP(2)
            }
        }
        ref -= off;
        const unsigned ref0 = ref;
        unsigned dg[2] = {0, 0}; int mxj = 0, nsh = 0; unsigned lsum = 0, lcnt = 0;
        const unsigned lead = (blockIdx.x >> 3) == 9 ? 0 : LEAD;
        unsigned posted = ref, lagf = ref, tpost = ref + TB1, tpoll = tpost + RDD + lead, kw = 0, kr = 0, cur = ref + off, khold = 0;
        int o = off; bool held = false;
#pragma unroll 1
        for (;;) {
            const unsigned p = lds_ld(&sm.grab);
            if (p >= NPC) break;
            if (p < 260) dg[0] = ref; if (p < NPC / 2) dg[1] = ref;
            lagf += (int)(lds_ld(&sm.late) - lagf) >> 1;
            { const unsigned l = lds_ld(&sm.line); if (l != cur) { cur = l; khold = kw; held = true; } }
            const unsigned now = NOW();
            if (o) { o = off * min(max((int)(ref0 + TC0 + (1 << TCS) - now), 0), 1 << TCS) >> TCS; if (!held && cur != ref + o) { cur = ref + o; if (lane == 0) lds_st(&sm.line, cur); } }
            if (kw < NRND && (int)(now - tpost) >= 0) {
                const unsigned c = ref + min((p >= PSL0 && (int)(lagf - cur) > LHI ? lagf - LOK : cur) - o - ref, 4000u);
                if (SAMPLE(kw) && (c != ref || (ref != posted && (sidx & 7) == (kw & 7)))) POST(kw, c)
                posted = ref;
                tpost += PER; kw++;
            }
            if (kr < NRND && (int)(now - tpoll) >= 0) {
                v16u xa, xb;
                asm volatile("s_load_dwordx16 %0, %2, %3\n\ts_load_dwordx16 %1, %2, %4\n\ts_waitcnt lgkmcnt(0)" : "=&s"(xa), "=&s"(xb)
                             : "s"(ws), "s"(__builtin_amdgcn_readfirstlane(bd + kr * RST)), "s"(__builtin_amdgcn_readfirstlane(bd + kr * RST + 256)) : "memory");
                int nb = 0, sg = 0;
#pragma unroll
                for (int i = 0; i < 16; i++) {
                    const unsigned lo = i < 8 ? xa[2 * i & 15] : xb[2 * i & 15], hi = i < 8 ? xa[(2 * i + 1) & 15] : xb[(2 * i + 1) & 15];
                    if (hi >> 12 == BTAG(kr) && lo - ref0 < 8000) { nb = max(nb, (int)(lo - (hi & 4095u) - ref)); sg += hi & 4095u; }
                }
                ref += nb + (sg >> 4); mxj = max(mxj, nb + (sg >> 4)); nsh += (nb + (sg >> 4)) > 0;
                if (cur != ref + o && (kr >= khold || (int)(ref + o - cur) > 0)) { cur = ref + o; held = false; if (lane == 0) lds_st(&sm.line, cur); }
                tpoll += PER; kr++;
            }
            SLEEP(2)
        }
        if (blockIdx.x == 0) {
            unsigned X = 0;
            if (DIAG == 2) X = (ref - ref0) * 8;
            if (DIAG == 6) X = (dg[1] - ref0) * 8;
            if (DIAG == 8) X = (dg[0] - ref0) * 8;
            if (DIAG == 17) X = hmx * 8;
            if (DIAG == 22) X = (ref0 - hmean) * 8;
            if (DIAG == 24) X = (hmean - hmin) * 8;
            if (DIAG == 25) X = (ref - hmin) * 4;
            if (DIAG == 27) X = __builtin_popcount(lds_ld(&sm.swp)) * 100;
            if (DIAG == 20) X = lds_ld(&sm.slow) * 2;
            if (DIAG >= 30 && DIAG < 40) X = lds_ld(&sm.dg) * 8;
            if (DIAG == 11) X = mxj * 8;
            X = min(X, 6000u);
            if (DIAG == 39) { const unsigned f3 = min((ref - hmin) / 40u, 11u), f2 = mxj < 40 ? 0 : mxj < 120 ? 1 : mxj < 220 ? 2 : 3, ka_ = lds_ld(&sm.dg), f1 = ka_ < 50 ? 0 : ka_ < 85 ? 1 : 2, f0 = hmx < 50 ? 0 : hmx < 80 ? 1 : hmx < 110 ? 2 : 3;
                X = (((f3 * 4 + f2) * 3 + f1) * 4 + f0) * 400; }
            while (NOW() - hmin < 15000 + X) SLEEP(1)
        }
    } else if (wave == NSW + 1 || wave == NSW + 2) {
        const int ql = lane < 48 ? lane : 47;
        unsigned c1, c2, s1, s2;
#define QT_LOADS() { int qo_ = ql; asm volatile("" : "+v"(qo_)); \
                     c1 = ((const unsigned*)q_cos)[qo_]; c2 = ((const unsigned*)q_cos)[48 + qo_]; s1 = ((const unsigned*)q_sin)[qo_]; s2 = ((const unsigned*)q_sin)[48 + qo_]; }
        unsigned q1, q2, lkz, lks, lvz, lvs;
        const unsigned sofs = R8(((unsigned)lane >> 1) & 7) * 2 + (lane & 1);
#define UNIT_LOADS(HEAD, GRP) { \
        q1 = ((const unsigned*)(q + (size_t)(HEAD) * DH))[ql]; q2 = ((const unsigned*)(q + (size_t)(HEAD) * DH))[48 + ql]; \
        const size_t so = (size_t)(HEAD) * (SKV / 2) + (GRP) * 256 + sofs; \
        lkz = *((const unsigned*)k_zeros + so); lks = *((const unsigned*)k_scales + so); \
        lvz = *((const unsigned*)v_zeros + so); lvs = *((const unsigned*)v_scales + so); }
#define UNIT_FILL(SL, U) { \
            v4f qq; \
            qq.x = bfr(bfr(bflo(q1) * bflo(c1)) + bfr(-bflo(q2) * bflo(s1))); \
            qq.y = bfr(bfr(bflo(q2) * bflo(c2)) + bfr(bflo(q1) * bflo(s2))); \
            qq.z = bfr(bfr(bfhi(q1) * bfhi(c1)) + bfr(-bfhi(q2) * bfhi(s1))); \
            qq.w = bfr(bfr(bfhi(q2) * bfhi(c2)) + bfr(bfhi(q1) * bfhi(s2))); \
            *(v2u*)&sm.q[SL][2 * ql] = (v2u){(__float_as_uint(qq.x) >> 16) | __float_as_uint(qq.y), (__float_as_uint(qq.z) >> 16) | __float_as_uint(qq.w)}; \
            *(v4f*)&sm.kz[SL][2 * lane] = (v4f){bflo(lkz), bflo(lks), bfhi(lkz), bfhi(lks)}; \
            *(v4f*)&sm.vz[SL][2 * lane] = (v4f){bflo(lvz), bflo(lvs), bfhi(lvz), bfhi(lvs)}; \
            CBAR(); \
            if (lane == 0) lds_st(&sm.rdy[SL], (U) + 1); \
            CBAR(); }
        const unsigned dw = NSW + 2 - wave;
#define TOUCH() { const unsigned x_ = ((blockIdx.x >> 3) - 18) * 64 + lane; \
            const char* p_ = x_ < 384 ? (const char*)(x_ < 192 ? qk : qv) + (size_t)(x_ < 192 ? x_ : x_ - 192) * (2u << 20) + (x_ * 8 + (blockIdx.x & 7)) * 640 : (const char*)out + (x_ - 384) * 4096; \
            if (x_ < 408) asm volatile("global_load_dword a0, %0, off nt" :: "v"(p_) : "a0"); }
        QT_LOADS()
        UNIT_LOADS(head0 + dw * (GSZ + 1 - 2 * side), lane >> 4)
        if (dw) TOUCH()
#pragma unroll 1
        for (unsigned u = dw;;) {
            while (u >= lds_ld(&sm.merged) + NIN) SLEEP(4);
            CBAR();
            UNIT_FILL(u % NIN, u)
            u += 2;
            if (u >= NU) break;
            if (u == 4) {
                const unsigned tpf = tdue(PSF - 1) + TFO;
                { int d; while ((d = (int)(lds_ld(&sm.line) + tpf - NOW())) > 0 && d < 3000) SLEEP(1) }
#define PF3(P) asm volatile("global_load_dwordx4 a[0:3], %0, %1\n\tglobal_load_dwordx4 a[0:3], %0, %1 offset:1024\n\tglobal_load_dwordx4 a[0:3], %0, %1 offset:2048" \
                            :: "v"(64 * SKV * 2 + blockIdx.x * 3072 + lane * 16), "s"(P) : "a0", "a1", "a2", "a3")
                PF3(k_zeros); PF3(k_scales); PF3(v_zeros); PF3(v_scales);
                asm volatile("global_load_dword a0, %0, %1" :: "v"(blockIdx.x * (DH * 2) + ql * 8), "s"(q) : "a0");
            }
            const unsigned tin = tdue(first_of(u) - (QUARTER(u) ? 24 : 0) - LEADP + (split & 7) * DPH);
            { int d; while ((d = (int)(lds_ld(&sm.line) + tin - NOW())) > 0 && d < 3000) SLEEP(4) }
            unsigned hd;
            while ((hd = lds_ld(&sm.utab[u])) == ~0u) SLEEP(2)
            UNIT_LOADS(hd & 255, QUARTER(u) ? rank & 3 : lane >> 4)
        }
        while (lds_ld(&sm.tag_rdy) == 0) SLEEP(1);
        CBAR();
        E1 = (u64)lds_ld(&sm.tag_lo) | ((u64)lds_ld(&sm.tag_hi) << 32);
        if (wave == NSW + 1 && blockIdx.x == 0) {
            for (;;) {
                bool ok = true;
#pragma unroll
                for (int i = 0; i < NH / 64; i++) ok &= __hip_atomic_load((const u64*)(ws + WS_POST) + i * 64 + lane, __ATOMIC_RELAXED, __HIP_MEMORY_SCOPE_AGENT) >> 24 == (E1 << 24) >> 24;
                if (__builtin_amdgcn_ballot_w64(ok) == ~0ull) break;
                SLEEP(64);
            }
            if (lane == 0) __hip_atomic_store((u64*)(ws + WS_EPOCH), E1, __ATOMIC_RELAXED, __HIP_MEMORY_SCOPE_AGENT);
        }
        if (wave == NSW + 2) {
            const unsigned t16 = (unsigned)E1 & 0xffffu, t16b = ((unsigned)E1 + ((unsigned)E1 >> 16)) & 0xffffu;
            const __amdgpu_buffer_rsrc_t rsrc = __builtin_amdgcn_make_buffer_rsrc(ws + WS_SWAP + (blockIdx.x ^ 4) * 5120, 0, 5120, 0x00020000);
            const unsigned swp = lds_ld(&sm.swp);
            if (swp) {
                unsigned use = 0;
#pragma unroll
                for (int i = 0; i < 5; i++) use |= ((swp >> wunit(i * 64 + lane)) & 1) << i;
                const unsigned tg = tdue(PSG - 1) + (lds_ld(&sm.gap) & 0xffffu);
                while ((int)(NOW() - lds_ld(&sm.line) - tg) < 0) SLEEP(2)
                for (;;) {
                    v4u d[5]; bool ok = true;
#pragma unroll
                    for (int i = 0; i < 5; i++) d[i] = __builtin_amdgcn_raw_buffer_load_b128(rsrc, (use >> i) & 1 ? (i * 64 + lane) * 16 : 0x40000000, 0, SC1 | 2);
#pragma unroll
                    for (int i = 0; i < 5; i++) if ((use >> i) & 1) {
                        ok &= d[i].y >> 16 == t16 && d[i].w >> 16 == t16b;
                        unsigned* w = (unsigned*)&sm.wv[144 * NMIX + (i * 64 + lane) * 6];
                        w[0] = d[i].x; w[1] = (d[i].y & 0xffffu) | (d[i].z << 16); w[2] = (d[i].z >> 16) | (d[i].w << 16);
                    }
                    if (__builtin_amdgcn_ballot_w64(ok) == ~0ull) break;
                    SLEEP(32)
                }
                CBAR();
                LDS_OR(&sm.vrdy, swp)
            }
        }
    } else {
        const unsigned eu = lane >= NU ? lane - NU : lane, win = min(first_of(eu) >> 5, 15u), pair = QUARTER(eu) ? 2 * (eu - WQ) + (rank >> 2) : rank;
        const unsigned rot2 = ((const unsigned char*)ROT)[win * 8 + pair];
#define UENT(BIT) (win * 16 + 2 * pair + ((BIT) ^ side) | (QUARTER(eu) ? (rank - (eu - WQ)) * 8 & 31 : (rot2 >> ((BIT) ^ side) * 4 & 15) * 2) << 8)
        unsigned far = 0;
        const unsigned hp = (blockIdx.x & 1) * NH + (blockIdx.x >> 1) * 4 + 2;
        if (blockIdx.x < 144) {
            const char* pb = (blockIdx.x < 128 ? (const char*)(hp >= NH ? qv : qk) + (size_t)(hp & (NH - 1)) * (SKV * DH * 4) + SKV * DH * 2
                                               : (const char*)ws + WS_PART + (4u << 20) + (blockIdx.x & 15) * 65536) + ((NOW() * 0x9e3779b1u) >> 26) * 128;
            unsigned mn[2] = {~0u, ~0u};
#pragma unroll 1
            for (unsigned i = 1; i < 2 * NPRB + 2; i++) {
                u64 t0, t1; unsigned dm;
                asm volatile("s_memtime %0\n\ts_waitcnt lgkmcnt(0)\n\ts_load_dword %2, %3, 0x0\n\ts_waitcnt lgkmcnt(0)\n\ts_memtime %1\n\ts_waitcnt lgkmcnt(0)"
                             : "=&s"(t0), "=&s"(t1), "=&s"(dm) : "s"(pb + i * 8192) : "memory");
                const unsigned t = (unsigned)(t1 - t0) < 330 || i == 1 ? ~0u : (unsigned)(t1 - t0);
                if (i & 1) mn[1] = min(mn[1], t); else mn[0] = min(mn[0], t);
            }
            far = mn[0] > mn[1];
        }
        if (lane < NMIX) lds_st(&sm.utab[lane], UENT(lane & 1));
        while (lds_ld(&sm.tag_rdy) == 0) SLEEP(1);
        CBAR();
        E1 = (u64)lds_ld(&sm.tag_lo) | ((u64)lds_ld(&sm.tag_hi) << 32);
        {
            const unsigned own = lds_ld(&sm.line), tg = (unsigned)E1;
            if (lane == 0) __hip_atomic_store((u64*)(ws + WS_POST) + blockIdx.x, E1 << 24 | min(lds_ld(&sm.slow), 4000u) << 1 | ((far ^ (blockIdx.x >> 2) ^ (blockIdx.x < 128 ? 0x96 >> (hp & 7) : 0)) & 1), __ATOMIC_RELAXED, __HIP_MEMORY_SCOPE_AGENT);
            const __amdgpu_buffer_rsrc_t nrs = __builtin_amdgcn_make_buffer_rsrc(ws + WS_NEWS, 0, 8 * 1280, 0x00020000);
            if (blockIdx.x == MASTER) {
                while ((int)(NOW() - own) < TMAP) SLEEP(2)
                const unsigned a = tg << 24, b = (unsigned)(E1 >> 8);
                v4u x; v2u z; u64 v0, v1;
#pragma unroll 1
                for (int i = 0; i < 8; i++) {
                    x = __builtin_amdgcn_raw_buffer_load_b128(__builtin_amdgcn_make_buffer_rsrc(ws + WS_POST, 0, 1024, 0x00020000), lane * 16, 0, SC1);
                    z = __builtin_amdgcn_raw_buffer_load_b64(__builtin_amdgcn_make_buffer_rsrc(ws + WS_POST + 1024, 0, 128, 0x00020000), lane * 8, 0, SC1);
                    v0 = __builtin_amdgcn_ballot_w64(((x.x ^ a) >> 24 | (x.y ^ b)) == 0); v1 = __builtin_amdgcn_ballot_w64(((x.z ^ a) >> 24 | (x.w ^ b)) == 0);
                    if (__builtin_popcountll(v0) + __builtin_popcountll(v1) >= MINV) break;
                    SLEEP(8)
                }
                const u64 m0 = __builtin_amdgcn_ballot_w64(x.x & 1) & v0, m1 = __builtin_amdgcn_ballot_w64(x.z & 1) & v1;
                const int s0 = lane < 43 ? min(max(lane - VW, 4 * NMIX), 42 - 2 * VW) : min(max(lane - VW, 43), 63 - 2 * VW);
#define PC(M) __builtin_popcount((unsigned)((M) >> s0) & ((2u << 2 * VW) - 1))
                const u64 g0 = __builtin_amdgcn_ballot_w64(2 * PC(m0) > PC(v0)), g1 = __builtin_amdgcn_ballot_w64(2 * PC(m1) > PC(v1));
                const float sl = wave_sum(((v0 >> lane & 1) ? (float)(x.x >> 1 & 4095u) : 0.f) + ((v1 >> lane & 1) ? (float)(x.z >> 1 & 4095u) : 0.f)) / ((8.f - PRE0) * (__builtin_popcountll(v0) + __builtin_popcountll(v1)) + 1.f);
                const unsigned al = lds_ld(&sm.aline), mean = (al ? al : own) + (int)fmaxf(sl - SL0, 0.f);
                const bool vz = lane < 16 && ((z.x ^ a) >> 24 | (z.y ^ b)) == 0;
                const unsigned gw = 2 * __builtin_popcountll(__builtin_amdgcn_ballot_w64(vz && (z.x & 1))) > __builtin_popcountll(__builtin_amdgcn_ballot_w64(vz));
                const unsigned k = lane & 3;
                if (lane < 32 && k < 3) __builtin_amdgcn_raw_buffer_store_b128((v4u){k == 0 ? (unsigned)g0 : k == 1 ? (unsigned)g1 : mean, tg, k == 0 ? (unsigned)(g0 >> 32) : k == 1 ? (unsigned)(g1 >> 32) : gw, tg},
                                                                              nrs, (lane >> 2) * 1280 + k * 16, 0, SC1);
            }
            while ((int)(NOW() - own) < TMAP + TRES) SLEEP(2)
            v4u y;
            for (;;) {
                y = __builtin_amdgcn_raw_buffer_load_b128(nrs, lane < 3 ? (blockIdx.x & 7) * 1280 + lane * 16 : 0x40000000, 0, SC1);
                if (((unsigned)__builtin_amdgcn_ballot_w64(y.y == tg && y.w == tg) & 7u) == 7u) break;
                SLEEP(8)
            }
            const u64 f0 = (unsigned)__builtin_amdgcn_readlane(y.x, 0) | (u64)(unsigned)__builtin_amdgcn_readlane(y.z, 0) << 32, f1 = (unsigned)__builtin_amdgcn_readlane(y.x, 1) | (u64)(unsigned)__builtin_amdgcn_readlane(y.z, 1) << 32;
            if (lane == 2) { sm.aline = y.x - own + 500 < 1000 ? y.x : own; sm.chead = (blockIdx.x & ~5u) | ((blockIdx.x >> 2 ^ y.z) & 1) | (blockIdx.x & 1) << 2; }
            unsigned bit = ((unsigned)((lane >= NU ? f1 : f0) >> (4 * win + (pair >> 1))) ^ (0x66 >> pair)) & 1;
            if (eu < NMIX) bit = eu & 1;
            const u64 bm = __builtin_amdgcn_ballot_w64(bit);
            const unsigned nsw = __builtin_popcountll((f0 ^ f1) >> (4 * NMIX));
            if (lane == 0) { sm.swp = (unsigned)(bm ^ (bm >> NU)) & ((1u << NU) - 1); lds_st(&sm.gap, nsw ? (TG0 + TG1 * nsw / 8) * 0x10001u + ((TH0 + TH1 * nsw / 8) << 16) : 0u); }
            CBAR();
            lds_st(&sm.utab[lane], UENT(bit));
        }
        const unsigned swp = lds_ld(&sm.swp);
        const unsigned T = lds_ld(&sm.tag_lo);
        const unsigned t15 = T & 0x7fffu;
        const __amdgpu_buffer_rsrc_t rsrc = __builtin_amdgcn_make_buffer_rsrc(ws + WS_PART + split * 512, 0, NRR * NSPLIT * 512 - split * 512, 0x00020000);
        v2u* recs = (v2u*)&sm.cs[0][0][0];
#pragma unroll
        for (int i = 0; i < NOUT * 3; i++) (&sm.acc[0][0])[i * 64 + lane] = 0.0;
        CBAR();
        unsigned vut = sm.utab[NU + (lane & 31)] & 255;
        if (QUARTER((unsigned)lane) && (rank & 3)) vut = NH - 16 + (rank & 3) * 16 + ((vut & 15) ^ 1);
        __builtin_amdgcn_s_setprio(3);
        bool direct = false, last = false;
#define PUBLISH(N) { \
        _Pragma("unroll 1") for (unsigned i = 0; i < 20; i += 4) { \
            const unsigned j = i < 10 ? i + WQ : i - 12; \
            v2u r[4]; \
            _Pragma("unroll") for (int k = 0; k < 4; k++) r[k] = recs[(j + k < 20 ? j + k : j + k - 12) * 64 + lane]; \
            _Pragma("unroll") for (int k = 0; k < 4; k++) { const unsigned x = j + k < 20 ? j + k : j + k - 12; \
                if (x < (N)) __builtin_amdgcn_raw_buffer_store_b64(r[k], rsrc, lane * 8, __builtin_amdgcn_readlane(vut, x) * (NSPLIT * 512), SC1); } } }
#pragma unroll 1
        for (unsigned u = 0; u < 2 * NU; u++) {
            const unsigned e = u % NU, so = u % NOUT;
            if (u >= NU) while (!((lds_ld(&sm.vrdy) >> e) & 1)) SLEEP(4)
            CBAR();
            const v4f h = *(const v4f*)&sm.wv[went(e) + (QUARTER(e) ? 32 : 128)];
            const unsigned row = __builtin_amdgcn_readlane(vut, e) * (NSPLIT * 512);
            double a0, a1, a2;
            for (;;) {
                if (!direct && (lds_ld(&sm.grab) >= NPC - PUBP || last)) { direct = true; wave_sync(); PUBLISH(u - NU) }
                const unsigned cn = __hip_atomic_load(&sm.cnt[so], __ATOMIC_RELAXED, __HIP_MEMORY_SCOPE_WORKGROUP);
                CBAR();
                if (direct) { a0 = sm.acc[so][lane]; a1 = sm.acc[so][64 + lane]; a2 = sm.acc[so][128 + lane]; }
                if (__builtin_amdgcn_readfirstlane(cn) == (QUARTER(e) ? 8 : 32)) { if (direct || u != 2 * NU - 1) break; last = true; }
                if (direct) SLEEP(0) else SLEEP(1)
            }
            CBAR();
            if (!direct) { a0 = sm.acc[so][lane]; a1 = sm.acc[so][64 + lane]; a2 = sm.acc[so][128 + lane]; }
            if (u < NU) {
                float2 lg = *(const float2*)&sm.ent[so][2 * lane];
                if (QUARTER(e) && lane >> 4 != (rank & 3)) lg = make_float2(-INFINITY, -INFINITY);
                const v4f zs = *(const v4f*)&sm.vz[u % NIN][2 * lane];
                const float m = wave_max(fmaxf(lg.x, lg.y));
                const float p0 = __expf(lg.x - m), p1 = __expf(lg.y - m);
                const float x0 = p0 * zs.y, x1 = p1 * zs.w;
                const unsigned eb = max(__float_as_uint(wave_max(fmaxf(fabsf(x0), fabsf(x1)))) >> 23, 14u);
                const float f = __uint_as_float((268u - eb) << 23);
                const float w0 = fminf(fmaxf(rintf(x0 * f), -32767.f), 32767.f), w1 = fminf(fmaxf(rintf(x1 * f), -32767.f), 32767.f);
                const float den = wave_sum(p0 + p1);
                const float bz = wave_sum(fmaf(w0, zs.x, w1 * zs.z));
                const float L = fmaf(m, 1.4426950408889634f, __builtin_amdgcn_logf(den));
                unsigned* wd = (unsigned*)(((swp >> u) & 1 ? sm.wo : sm.wv) + went(u));
                if (!QUARTER(u) || lane >> 4 == (rank & 3)) wd[QUARTER(u) ? lane & 15 : lane] = ((unsigned)(int)w0 & 0xffffu) | ((unsigned)(int)w1 << 16);
                if (lane == 0) *(v4f*)&wd[QUARTER(u) ? 16 : 64] = (v4f){ldexpf(1.f / den, (int)eb - 141), bz, L, 0.f};
                CBAR();
                if (!((swp >> u) & 1)) LDS_OR(&sm.vrdy, 1u << u)
            } else {
                const float n0 = (float)(a0 - (double)h.y) * h.x, n1 = (float)(a1 - (double)h.y) * h.x, n2 = (float)(a2 - (double)h.y) * h.x;
                const unsigned e2 = max(__float_as_uint(wave_maxv(fmaxf(fmaxf(fabsf(n0), fabsf(n1)), fabsf(n2)))) >> 23, 14u);
                const float f2 = __uint_as_float((268u - e2) << 23);
                const unsigned m01 = __builtin_bit_cast(unsigned, __builtin_amdgcn_cvt_pk_i16((int)rintf(n0 * f2), (int)rintf(n1 * f2)));
                const unsigned m2 = __builtin_bit_cast(unsigned, __builtin_amdgcn_cvt_pk_i16((int)rintf(n2 * f2), 0));
                const unsigned bit = ((lane < 32 ? __float_as_uint(h.z) : e2) >> (lane & 31)) & 1u;
                const v2u r = {m01, m2 | (bit << 16) | (t15 << 17)};
                if (direct) __builtin_amdgcn_raw_buffer_store_b64(r, rsrc, lane * 8, row, SC1);
                else recs[e * 64 + lane] = r;
                CBAR();
                sm.acc[so][lane] = 0.0; sm.acc[so][64 + lane] = 0.0; sm.acc[so][128 + lane] = 0.0;
            }
            CBAR();
            if (lane == 0) { lds_st(&sm.cnt[so], 0u); CBAR(); lds_st(&sm.merged, u + 1); }
            if (u == NU - 1 && swp) {
                wave_sync();
                const __amdgpu_buffer_rsrc_t srs = __builtin_amdgcn_make_buffer_rsrc(ws + WS_SWAP + blockIdx.x * 5120, 0, 5120, 0x00020000);
                v4u d[5]; unsigned o[5];
#pragma unroll
                for (int i = 0; i < 5; i++) {
                    const unsigned* w = (const unsigned*)&sm.wo[144 * NMIX + (i * 64 + lane) * 6];
                    const unsigned d0 = w[0], d1 = w[1], d2 = w[2];
                    d[i] = (v4u){d0, (d1 & 0xffffu) | (T << 16), (d1 >> 16) | (d2 << 16), (d2 >> 16) | ((T + (T >> 16)) << 16)};
                    o[i] = (swp >> wunit(i * 64 + lane)) & 1 ? (i * 64 + lane) * 16 : 0x40000000;
                }
                while ((int)(NOW() - lds_ld(&sm.line)) < (int)tdue(PSW) + TWO) SLEEP(1)
#pragma unroll
                for (int i = 0; i < 5; i++) __builtin_amdgcn_raw_buffer_store_b128(d[i], srs, o[i], 0, SC1 | 2);
            }
        }
        return;
    }
    if (blockIdx.x >> 4 == WQ) {
        while (lds_ld(&sm.grab) < NPC + 2 * NSW) SLEEP(2)
        const __amdgpu_buffer_rsrc_t rq = __builtin_amdgcn_make_buffer_rsrc(ws + WS_PART + (size_t)(NH + (wave - NSW) * 16 + ((lds_ld(&sm.chead) & 15) ^ 1)) * (NSPLIT * 512), 0, NSPLIT * 512, 0x00020000);
        const unsigned t15 = lds_ld(&sm.tag_lo) & 0x7fffu;
        v2u x[NSPLIT];
        unsigned vl = 0, ve = 0;
        for (;;) {
            bool ok = true;
#pragma unroll
            for (int k = 0; k < NSPLIT; k++) x[k] = __builtin_amdgcn_raw_buffer_load_b64(rq, lane * 8 + k * 512, 0, SC1);
#pragma unroll
            for (int k = 0; k < NSPLIT; k++) {
                ok &= (x[k].y >> 17) == t15;
                const u64 bits = __builtin_amdgcn_ballot_w64((x[k].y & 0x10000u) != 0);
                if ((lane & 15) == k) { vl = (unsigned)bits; ve = (unsigned)(bits >> 32); }
            }
            if (__builtin_amdgcn_ballot_w64(ok) == ~0ull) break;
            SLEEP(4)
        }
        const float M = rdlane(row_max(__uint_as_float(vl)), 0), w = __builtin_amdgcn_exp2f(__uint_as_float(vl) - M);
        const int ex = (int)(ve & 255u), em = (int)rdlane(row_max((float)ex), 0);
        const float sc = ldexpf(w, ex - em);
        v4f sum = {0.f, 0.f, 0.f, rdlane(row_sum(w), 0)};
#pragma unroll
        for (int k = 0; k < NSPLIT; k++) {
            const float c = rdlane(sc, k);
            sum.x = fmaf(c, (float)(short)(x[k].x & 0xffffu), sum.x); sum.y = fmaf(c, (float)((int)x[k].x >> 16), sum.y); sum.z = fmaf(c, (float)(short)(x[k].y & 0xffffu), sum.z);
        }
        sum.x = ldexpf(sum.x, em - 141); sum.y = ldexpf(sum.y, em - 141); sum.z = ldexpf(sum.z, em - 141);
        v4f* pb = (v4f*)((char*)&sm.cs[0][0][0] + 65536);
        pb[(wave + 1) * 64 + lane] = sum;
        if (lane == 0) ((float*)(pb + NSW * 64))[wave] = M;
        CBAR();
        LDS_ADD(&sm.qdone, 1u)
    }
}

extern "C" void kernel_launch(void* const* d_in, const int* in_sizes, int n_in,
                              void* d_out, int out_size, void* d_ws, size_t ws_size,
                              hipStream_t stream) {
    const unsigned short* q = (const unsigned short*)d_in[0];
    const int* qk = (const int*)d_in[1];
    const unsigned short* k_scales = (const unsigned short*)d_in[2];
    const unsigned short* k_zeros = (const unsigned short*)d_in[3];
    const int* qv = (const int*)d_in[4];
    const unsigned short* v_scales = (const unsigned short*)d_in[5];
    const unsigned short* v_zeros = (const unsigned short*)d_in[6];
    const unsigned short* q_cos = (const unsigned short*)d_in[7];
    const unsigned short* q_sin = (const unsigned short*)d_in[8];
    const unsigned short* k_cos = (const unsigned short*)d_in[9];
    const unsigned short* k_sin = (const unsigned short*)d_in[10];
    unsigned char* ws = (unsigned char*)d_ws;
    attn_kernel<<<NSPLIT * GSZ, NTHREADS, 0, stream>>>(q, qk, k_scales, k_zeros, qv, v_scales, v_zeros,
                                                       q_cos, q_sin, k_cos, k_sin, (__hip_bfloat16*)d_out, ws);
}
```

```cpp
#include <hip/hip_runtime.h>
#include <hip/hip_bf16.h>
#include <stdint.h>

#define DH 192
#define HALF 96
#define SKV 2048
#define NH 256
#define NSW 8
#define NWAVES (NSW + 4)
#define NTHREADS (64 * NWAVES)
#define BROWS 128
#define NSPLIT 16
#define GSZ 16
#define WQ 10
#define NU (NH / GSZ + 3)
#define NPC 1024
#define NRR (NH + 3 * GSZ)
#define NIN 8
#define NOUT 8
#define WALL (15 * 144 + 4 * 48)
#define SC1 16
#define NRND 48
#define RST 4352
#define TK 2
#define NMIX 3
#define TUNIT 311
#define T01 50
#define TMIX 7
#define TOFF 50
#define PSF 100
#define TGS 68
#define PSW (NPC / 2 + 28)
#define TG0 8
#define TG1 3
#define TWO -75
#define PSG (NPC / 2 + 60)
#define TH0 3
#define TH1 3
#define EARLY 10
#define LEADP 24
#define DPH 2
#define TA 210
#define TA2 70
#define CSH 2
#define NPRB 3
#define TMAP 520
#define MINV 96
#define VW 4
#define MASTER 203
#define TRES 190
#define TB1 1200
#define PER 300
#define RDD 100
#define LEAD 70
#define LHI 85
#define LOK 74
#define PSL0 124
#define PUBP -12
#define LJ 210
#define LOK2 75
#define NTRY 3
#define TFO 6
#define SL0 42.f
#define PST0 200
#define KC 4
#define TC0 1000
#define TCS 9
#define PRE0 3

#define KREL 12
#define TCH 1
#define TCW 4
#define TWS 0
#define TV2 0
#define PDRY true
#define PDEL 65
#define XPOLL 0
#define NCP 3
#define CPRIO 3
#define NRW (NRR + NCP * GSZ)
#define TRD 115
#define MSLP 0
#define PSLP 1
#define THD 60
#define TRQ 45
#define TCA 145
#define TCD 22
#define CDRY 0
#define WS_EPOCH 0
#define WS_POST0 8192
#define WS_POST (WS_POST0 + ((unsigned)E1 & 15) * 2048)
#define WS_NEWS (49152 + 256)
#define WS_HELLO 59904
#define WS_BOARD 65536
#define WS_PART (2u << 20)
#define WS_SWAP (WS_PART + NRW * NSPLIT * 512)

typedef unsigned int v2u __attribute__((ext_vector_type(2)));
typedef unsigned int v16u __attribute__((ext_vector_type(16)));
typedef unsigned int v4u __attribute__((ext_vector_type(4)));
typedef int v4i __attribute__((ext_vector_type(4)));
typedef float v2f __attribute__((ext_vector_type(2)));
typedef float v4f __attribute__((ext_vector_type(4)));
typedef unsigned long long u64;

__device__ __forceinline__ float bflo(unsigned u) { return __uint_as_float(u << 16); }
__device__ __forceinline__ float bfhi(unsigned u) { return __uint_as_float(u & 0xffff0000u); }
__device__ __forceinline__ float bfr(float x) {
    __bf16 b = (__bf16)x;
    return __uint_as_float(((unsigned)__builtin_bit_cast(unsigned short, b)) << 16);
}

#define SLEEP(n) SLEEP_(n)
#define SLEEP_(n) asm volatile("s_sleep " #n "\n\tv_add_co_u32 %0, vcc, 1, %0\n\ts_cbranch_vccz 1f\n\ts_endpgm\n1:" : "+v"(spins) :: "vcc", "memory");
#define SPIN0 0xf0000000u
#define CBAR() asm volatile("" ::: "memory")

__device__ __forceinline__ void wave_sync() {
    __builtin_amdgcn_fence(__ATOMIC_RELEASE, "wavefront");
    __builtin_amdgcn_wave_barrier();
    __builtin_amdgcn_fence(__ATOMIC_ACQUIRE, "wavefront");
}
template <int CTRL>
__device__ __forceinline__ float dpp_add(float x) {
    int y = __builtin_amdgcn_update_dpp(0, __float_as_int(x), CTRL, 0xf, 0xf, true);
    return x + __int_as_float(y);
}
template <int CTRL>
__device__ __forceinline__ float dpp_max(float x) {
    int y = __builtin_amdgcn_update_dpp(__float_as_int(x), __float_as_int(x), CTRL, 0xf, 0xf, false);
    return fmaxf(x, __int_as_float(y));
}
__device__ __forceinline__ float row_sum(float x) { x = dpp_add<0x128>(x); x = dpp_add<0x124>(x); x = dpp_add<0x122>(x); return dpp_add<0x121>(x); }
__device__ __forceinline__ float row_max(float m) { m = dpp_max<0x128>(m); m = dpp_max<0x124>(m); m = dpp_max<0x122>(m); return dpp_max<0x121>(m); }
__device__ __forceinline__ float rdlane(float x, int l) { return __int_as_float(__builtin_amdgcn_readlane(__float_as_int(x), l)); }
__device__ __forceinline__ float wave_max(float m) { m = row_max(m); return fmaxf(fmaxf(rdlane(m, 0), rdlane(m, 16)), fmaxf(rdlane(m, 32), rdlane(m, 48))); }
__device__ __forceinline__ float wave_maxv(float m) {
    m = row_max(m);
    const auto a = __builtin_amdgcn_permlane16_swap(__float_as_uint(m), __float_as_uint(m), false, false);
    m = fmaxf(__uint_as_float(a[0]), __uint_as_float(a[1]));
    const auto b = __builtin_amdgcn_permlane32_swap(__float_as_uint(m), __float_as_uint(m), false, false);
    return fmaxf(__uint_as_float(b[0]), __uint_as_float(b[1]));
}
__device__ __forceinline__ float wave_sum(float x) { x = row_sum(x); return (rdlane(x, 0) + rdlane(x, 16)) + (rdlane(x, 32) + rdlane(x, 48)); }
__device__ __forceinline__ unsigned lds_ld(const unsigned* p) { return __builtin_amdgcn_readfirstlane(__hip_atomic_load(p, __ATOMIC_RELAXED, __HIP_MEMORY_SCOPE_WORKGROUP)); }
__device__ __forceinline__ unsigned lds_addr(const void* p) { return (unsigned)(size_t)(const __attribute__((address_space(3))) void*)p; }
#define DMA16(SRC, LDS) asm volatile("s_mov_b32 %0, m0\n\ts_mov_b32 m0, %2\n\ts_nop 0\n\tglobal_load_lds_dwordx4 %1, off\n\ts_mov_b32 m0, %0" : "=&s"(m0keep) : "v"(SRC), "s"(LDS) : "memory")
__device__ __forceinline__ void lds_st(unsigned* p, unsigned v) { __hip_atomic_store(p, v, __ATOMIC_RELAXED, __HIP_MEMORY_SCOPE_WORKGROUP); }
#define NOW() ((unsigned)__builtin_amdgcn_s_memrealtime())
__device__ __forceinline__ unsigned tdue(unsigned g) {
    return (g * TUNIT + min(g, 480u) * TK + min(g, 48u) * T01 + (min(g, 32u * NMIX) + min(max((int)g - NPC / 2, 0), 32 * NMIX)) * TMIX + (g >= PSF ? TGS * 32 : 0)) >> 5;
}

__device__ __forceinline__ unsigned unit_of(unsigned g) { const unsigned p = g & 511; return (g >> 9) * NU + (p >> 5) + min(max((int)p - 32 * WQ, 0) >> 3, 3); }
__device__ __forceinline__ unsigned first_of(unsigned e) { return 32 * e - 24 * min(max((int)e - WQ, 0), 4); }
#define QUARTER(E) ((E) - WQ < 4)
__device__ __forceinline__ unsigned went(unsigned e) { return 144 * e - 96 * min(max((int)e - WQ, 0), 4); }
__device__ __forceinline__ unsigned wunit(unsigned x) { x += 24 * NMIX; return x < 24 * WQ ? x / 24 : x < 24 * WQ + 32 ? WQ + (x - 24 * WQ) / 8 : (x + 64) / 24; }

#define ROTT 0xed8d2c3d2c35ed84ull, 0xba85aa165b03ac82ull, 0xee7efa489a38af71ull, 0xe932da1b0bab51d8ull, 0xfa96112d5fee971bull, 0x2c7e9ea5a544f071ull, 0x3d86093d5f0e810bull, 0xe932da1b0bab51d8ull, 0xfa96112d5fee971bull, 0xd834e62c5fa723e9ull, 0x1d7d1a3d2c061d88ull, 0xe932da1b0bab51d8ull, 0xfa96112d5fee971bull, 0xc834e12c0aaa4ec7ull, 0x3d86093d5f0e810bull, 0xe932da1b0bab51d8ull
static __device__ const u64 ROT[16] = {ROTT};

struct __attribute__((aligned(16))) Smem {
    unsigned cs[NSW][4][768];
    double acc[NOUT][DH];
    float ent[NOUT][BROWS];
    unsigned short wv[WALL];
    unsigned short wo[WALL];
    unsigned q[NIN][HALF];
    float2 kz[NIN][BROWS];
    float2 vz[NIN][BROWS];
    unsigned utab[64];
    unsigned line, gap;
    unsigned late, grab;
    unsigned rdy[NIN], cnt[NOUT];
    unsigned tabs, vrdy, swp;
    unsigned aline;
    unsigned tag_rdy, tag_lo, tag_hi, merged, cdone, cidx;
    unsigned slow;
    unsigned chead;
};

#define LDS_ADD(P, V) { u64 ex_; asm volatile("s_mov_b64 %0, exec\n\ts_mov_b64 exec, 1\n\tds_add_u32 %1, %2\n\ts_mov_b64 exec, %0" : "=&s"(ex_) : "v"(lds_addr(P)), "v"(V) : "memory"); }
#define LDS_OR(P, V) { u64 ex_; asm volatile("s_mov_b64 %0, exec\n\ts_mov_b64 exec, 1\n\tds_or_b32 %1, %2\n\ts_mov_b64 exec, %0" : "=&s"(ex_) : "v"(lds_addr(P)), "v"(V) : "memory"); }

__global__ __launch_bounds__(NTHREADS, 3) void attn_kernel(
    const unsigned short* __restrict__ q, const int* __restrict__ qk,
    const unsigned short* __restrict__ k_scales, const unsigned short* __restrict__ k_zeros,
    const int* __restrict__ qv,
    const unsigned short* __restrict__ v_scales, const unsigned short* __restrict__ v_zeros,
    const unsigned short* __restrict__ q_cos, const unsigned short* __restrict__ q_sin,
    const unsigned short* __restrict__ k_cos, const unsigned short* __restrict__ k_sin,
    __hip_bfloat16* __restrict__ out, unsigned char* __restrict__ ws)
{
    __shared__ Smem sm;
    const int tid = threadIdx.x;
    unsigned spins = SPIN0;
    const int lane = tid & 63;
    const int hw = __builtin_amdgcn_readfirstlane(tid >> 6);
    u64 t00; unsigned ka;
    asm volatile("s_memrealtime %0\n\ts_load_dword %1, %2, 0x40" : "=&s"(t00), "=&s"(ka) : "s"(__builtin_amdgcn_kernarg_segment_ptr()));
    if (hw == 0) {
        const char* pc0; asm volatile("s_getpc_b64 %0" : "=s"(pc0));
        asm volatile("global_load_dword a6, %0, %1" :: "v"(((blockIdx.x & ~7u) + (lane & 7)) * 128), "s"(pc0) : "a6", "memory");
    }
    const int wave = hw >= 4 ? hw - 4 : hw + NSW;
    const unsigned split = (blockIdx.x & 1) + (blockIdx.x & 2) * 4 + ((blockIdx.x >> 3) & 3) * 2;
    const unsigned side = (split ^ (blockIdx.x >> 2)) & 1, rank = blockIdx.x >> 5;
    const unsigned head0 = 2 * rank + side;
    const unsigned rf0 = (8 * split + 1) / 3, r3 = 8 * split + 1 - 3 * rf0;
    const unsigned koff = ((lane >> 5) * DH + 3 * (lane & 31)) * 4, koffb = koff + 384;
#define AR(S) (16 + (S) * 8)
#define GLD(A, OFF, BASE, IMM) asm volatile("global_load_dwordx3 a[%c0:%c1], %2, %3 offset:%4 nt" :: "i"(A), "i"((A) + 2), "v"(OFF), "s"(BASE), "i"(IMM))
#define ISSUE(S, J, BASE, OA, OB) { GLD(AR(S), OA, BASE, (J) * 1536); GLD(AR(S) + 4, OB, BASE, (J) * 1536); }
#define HEAD2 ((GSZ + 1 - 2 * side) * (SKV * DH * 4))
    const unsigned wo = (hw - 2) & 7;
    const bool pre = hw >= 4 && wo < PRE0;
    if (pre) {
        const char* a = (const char*)qk + (head0 * 512 + side * 256 + rf0 + wave + (wave >= 6 ? 80 : 0)) * 3072; const char* b = a + HEAD2;
        ISSUE(0, 0, a, koff, koffb) ISSUE(1, 1, a, koff, koffb) ISSUE(2, 0, b, koff, koffb) ISSUE(3, 1, b, koff, koffb)
    }
    asm volatile("s_waitcnt lgkmcnt(0)" : "+s"(t00), "+s"(ka));
    if (hw == 0) {
        sm.utab[lane] = ~0u;
        unsigned l0 = (unsigned)t00 + TOFF;
        if (KREL) { const unsigned t1 = NOW() + KREL; if ((int)(t1 - l0) > 0) l0 = t1; }
        if (lane < 32) (&sm.line)[lane] = lane == 3 ? 64u : (lane & 29) == 0 ? l0 : 0u;
        asm volatile("s_waitcnt lgkmcnt(0)" ::: "memory");
    }
    __builtin_amdgcn_s_barrier();
    CBAR();
    const u64 rtab = r3 == 1 ? 0x5756552c2b020100ull : r3 == 0 ? 0x5756552c2b2a0100ull : 0x57562d2c2b020100ull;
#define R8(I) (rf0 + ((unsigned)(rtab >> (8 * (I))) & 255u))

    if (__builtin_expect(wave < NSW, 1)) {
        const int cst = 2 * side;
        const unsigned r8w = R8((unsigned)wave);
        const char* cbase[3];
#pragma unroll
        for (int j = 0; j < 3; j++) { const int x = j * 64 + lane; cbase[j] = (x < 96 ? (const char*)k_cos + x * 16 : (const char*)k_sin + (x - 96) * 16) + r8w * (4 * DH * 2); }
        unsigned m0keep;
#define CS_DMA(W, C) { _Pragma("unroll") for (int j = 0; j < 3; j++) DMA16(cbase[j] + (C) * (512 * DH * 2), lds_addr(&sm.cs[0][0][0]) + ((W) * 4 + (C)) * 3072 + j * 1024); }
#define REPACK(W, C) { \
        unsigned* cw = &sm.cs[W][C][0]; \
        const unsigned* l = cw + rsel * (DH / 2) + qoff; \
        unsigned r[2][6]; \
        _Pragma("unroll") \
        for (int j = 0; j < 2; j++) { \
            unsigned e[4][2]; \
            _Pragma("unroll") \
            for (int k = 0; k < 4; k++) { \
                const unsigned* a = l + j * DH + (k & 1) * 48 + (k >> 1) * 384;       \
                unsigned x0 = a[0], x1 = a[1]; \
                e[k][0] = __builtin_amdgcn_alignbit(x1, x0, sh); \
                e[k][1] = x1 >> sh; \
            } \
            r[j][0] = (e[0][0] & 0xffffu) | (e[3][0] << 16); \
            r[j][1] = (e[0][0] >> 16) | (e[3][0] & 0xffff0000u); \
            r[j][2] = (e[0][1] & 0xffffu) | (e[3][1] << 16); \
            r[j][3] = (e[1][0] & 0xffffu) | (e[2][0] << 16); \
            r[j][4] = (e[1][0] >> 16) | (e[2][0] & 0xffff0000u); \
            r[j][5] = (e[1][1] & 0xffffu) | (e[2][1] << 16); \
        } \
        wave_sync(); \
        _Pragma("unroll") \
        for (int j = 0; j < 2; j++) { *(v4u*)&cw[j * 256 + lane * 4] = (v4u){r[j][0], r[j][1], r[j][2], r[j][3]}; *(v2u*)&cw[512 + j * 128 + lane * 2] = (v2u){r[j][4], r[j][5]}; } \
        wave_sync(); }
        const int sub = lane & 31;
        const int rsel = lane >> 5;
        const unsigned sh = (sub & 1) * 16;
        const int qoff = 3 * (sub >> 1) + (sub & 1);
        const unsigned voff = 3 * lane * 4;
        asm volatile("" ::: "a16", "a17", "a18", "a19", "a20", "a21", "a22", "a23", "a24", "a25", "a26", "a27", "a28", "a29", "a30", "a31",
                            "a32", "a33", "a34", "a35", "a36", "a37", "a38", "a39", "a40", "a41", "a42", "a43", "a44", "a45", "a46", "a47");
        asm volatile("" ::: "a48", "a49", "a50", "a51", "a52", "a53", "a54", "a55", "a56", "a57", "a58", "a59", "a60", "a61", "a62", "a63",
                            "a64", "a65", "a66", "a67", "a68", "a69", "a70", "a71", "a72", "a73", "a74", "a75", "a76", "a77", "a78", "a79");
        int ta[4][3], tb[4][3];
#define WAITT(P, S, N) asm volatile("s_waitcnt vmcnt(%6)\n\tv_accvgpr_read_b32 %0, a[%c7]\n\tv_accvgpr_read_b32 %1, a[%c8]\n\tv_accvgpr_read_b32 %2, a[%c9]\n\t" \
                              "v_accvgpr_read_b32 %3, a[%c10]\n\tv_accvgpr_read_b32 %4, a[%c11]\n\tv_accvgpr_read_b32 %5, a[%c12]" \
                              : "=v"(ta[P][0]), "=v"(ta[P][1]), "=v"(ta[P][2]), "=v"(tb[P][0]), "=v"(tb[P][1]), "=v"(tb[P][2]) \
                              : "i"(N), "i"(AR(S)), "i"(AR(S) + 1), "i"(AR(S) + 2), "i"(AR(S) + 4), "i"(AR(S) + 5), "i"(AR(S) + 6))
        const unsigned voffb = voff + 768;
        u64 ln = 0;
#define TDUE(G) (tdue(G) + ((G) >= PSW ? __builtin_amdgcn_readfirstlane((unsigned)(ln >> 32)) >> ((G) >= PSG ? 16 : 0) & 0xffffu : 0u))
#define GATE(DUE) { int d_ = (int)((DUE) - NOW()); if (d_ < 3000) while (d_ > 0) { __builtin_amdgcn_s_sleep(1); d_ = (int)((DUE) - NOW()); } }
#define REPORT() { const unsigned t_ = now - tdc; if (lane == 0) lds_st(&sm.late, t_); }
#define LINE() ln = LDV((u64*)&sm.line);
#define LDV(P) __hip_atomic_load(P, __ATOMIC_RELAXED, __HIP_MEMORY_SCOPE_WORKGROUP)
        unsigned u = 0, rg = cst * 8 + wave, np = 0;
        CS_DMA(wave, cst)

        const char* kb0 = (const char*)qk + (size_t)(head0 * 512 + r8w) * 3072;
#define KB(I) (kb0 + ((cst + (I)) & 3) * (128 * 3072))
        const char* kb; const char* kb2;
#define SISSUE(I) { kb = KB(I); kb2 = kb + HEAD2; const unsigned du_ = lds_ld(&sm.line) + tdue(16 * (I) + 2 * wo); GATE(du_) \
            ISSUE(((I) & 1) * 4, 0, kb, koff, koffb) ISSUE(((I) & 1) * 4 + 1, 1, kb, koff, koffb) ISSUE(((I) & 1) * 4 + 2, 0, kb2, koff, koffb) ISSUE(((I) & 1) * 4 + 3, 1, kb2, koff, koffb) }
        if (!pre) SISSUE(0)
        CS_DMA(wave, (cst + 1) & 3)
        if (pre) asm volatile("s_waitcnt vmcnt(3)" ::: "memory");
        asm volatile("s_waitcnt vmcnt(11)" ::: "memory");
        if (!pre) LDS_ADD(&sm.slow, NOW() - lds_ld(&sm.line) - tdue(2 * wo))
        REPACK(wave, cst)
        SISSUE(1)
#define TABDONE(I) LDS_OR(&sm.tabs, 1u << ((((cst + (I)) & 3) * 8) + wave))
        TABDONE(0)
        const float scale = 0.07216878364870322f;
        double acc[3] = {0.0, 0.0, 0.0};
        bool first = true, chg, none = false;
        unsigned Qp[3], Qn[3], Rp[3], Rn[3];
        float kz4[4], ksc, kres[4];
        v4u ca[4]; v2u cb[4];
        v2f qe[4][3], A[4];
        unsigned vut, nrg;
        const unsigned vr = ((lane >> 3) & 3) * 128 + R8((unsigned)lane & 7);
        unsigned g, mg;
        const unsigned grab_adr = lds_addr(&sm.grab);
        asm volatile("v_accvgpr_write_b32 a13, 1\n\tv_accvgpr_write_b32 a14, 2" ::: "a13", "a14", "a15");
#define GRAB(N) { u64 ex_; asm volatile("s_mov_b64 %0, exec\n\ts_mov_b64 exec, 1\n\tds_add_rtn_u32 a15, %1, a%c2\n\ts_mov_b64 exec, %0" : "=&s"(ex_) : "v"(grab_adr), "i"(12 + (N)) : "memory"); }
#define GRABBED() { unsigned t_; asm volatile("s_waitcnt lgkmcnt(0)\n\tv_accvgpr_read_b32 %1, a15\n\ts_nop 0\n\tv_readfirstlane_b32 %0, %1\n\ts_nop 1" : "=s"(g), "=v"(t_)); }
        unsigned oa, ob, un, tdn, tiss, tdc = 0;
#define PLAN() { \
            GRABBED() un = unit_of(g); chg = un != u; none = g >= NPC; \
            const bool nv = g >= NPC / 2; \
            unsigned hd = __builtin_amdgcn_readlane(vut, un); \
            while (hd == ~0u) { SLEEP(1) hd = lds_ld(&sm.utab[un]); } \
            nrg = (g + (hd >> 8)) & 31; hd &= 255; \
            unsigned boff, boff2; asm("s_mul_i32 %0, %1, 0xc00" : "=s"(boff) : "s"(hd * 512 + __builtin_amdgcn_readlane(vr, nrg))); \
            asm("s_mul_i32 %0, %1, 0xc00" : "=s"(boff2) : "s"(hd * 512 + __builtin_amdgcn_readlane(vr, nrg + 1))); \
            const char* b_ = nv ? (const char*)qv : (const char*)qk; \
            kb = b_ + boff; kb2 = b_ + boff2; \
            oa = nv ? voff : koff; ob = nv ? voffb : koffb; \
            tdn = TDUE(g); }
#define FIRE() { ISSUE(0, 0, kb, oa, ob) ISSUE(1, 1, kb, oa, ob) ISSUE(2, 0, kb2, oa, ob) ISSUE(3, 1, kb2, oa, ob) }
#define LAUNCH() { \
            LINE() \
            const unsigned du_ = __builtin_amdgcn_readfirstlane((unsigned)ln) + tdn; \
            GATE(du_) \
            ISSUE(0, 0, kb, oa, ob) ISSUE(1, 1, kb, oa, ob) \
            GATE(du_ + 10) \
            ISSUE(2, 0, kb2, oa, ob) ISSUE(3, 1, kb2, oa, ob) tiss = NOW(); }
#define ARRIVED() \
            const unsigned now = NOW(); \
            const bool stall = now - tiss > LJ && g - PST0 < NPC - 48 - PST0; \
            const bool late = !none && !stall && (int)(now + EARLY - __builtin_amdgcn_readfirstlane((unsigned)ln) - tdn) >= 0; \
            if (late) { FIRE() tiss = now; } \
            if (stall && lane == 0) __hip_atomic_fetch_max(&sm.line, (unsigned)ln + (now - tiss) - LOK2, __ATOMIC_RELAXED, __HIP_MEMORY_SCOPE_WORKGROUP); \
            REPORT()
#define ADVANCE(ISV) { \
            if (chg) FLUSH(ISV) \
            if (!none && !late) LAUNCH() \
            first = chg; u = un; rg = nrg; tdc = tdn + 10; }
#define K_TAB(MODE) { _Pragma("unroll") for (int p = 0; p < ((MODE) ? 2 : 4); p++) { \
            const unsigned* cw = &sm.cs[(rg & 7) + (p >> 1)][rg >> 3][0]; \
            ca[p] = *(const v4u*)&cw[(p & 1) * 256 + lane * 4]; cb[p] = *(const v2u*)&cw[512 + (p & 1) * 128 + lane * 2]; } }
#define K_PRE(MODE, P) { \
        float e0, e1, e2, f0, f1, f2; \
        const unsigned* qp_ = ((MODE) && (P) >= 2) ? Rp : Qp; const unsigned* qn_ = ((MODE) && (P) >= 2) ? Rn : Qn; \
        const int tp_ = (MODE) ? (P) & 1 : (P); \
        asm("v_dot2_f32_bf16 %0, %6, %12, 0\n\tv_dot2_f32_bf16 %3, %9, %15, 0\n\tv_dot2_f32_bf16 %1, %7, %13, 0\n\tv_dot2_f32_bf16 %4, %10, %16, 0\n\t" \
            "v_dot2_f32_bf16 %2, %8, %14, 0\n\tv_dot2_f32_bf16 %5, %11, %17, 0\n\ts_nop 2" \
            : "=&v"(e0), "=&v"(e1), "=&v"(e2), "=&v"(f0), "=&v"(f1), "=&v"(f2) \
            : "v"(qp_[0]), "v"(qp_[1]), "v"(qp_[2]), "v"(qn_[0]), "v"(qn_[1]), "v"(qn_[2]), \
              "v"(ca[tp_].x), "v"(ca[tp_].y), "v"(ca[tp_].z), "v"(ca[tp_].w), "v"(cb[tp_].x), "v"(cb[tp_].y)); \
        qe[P][0] = (v2f){e0, f0}; qe[P][1] = (v2f){e1, f1}; qe[P][2] = (v2f){e2, f2}; \
        A[P] = (qe[P][0] + qe[P][1] + qe[P][2]) * (v2f){-kz4[P], -kz4[P]}; }
#define K_WAIT(MODE, P) { \
        if ((MODE) == 0) { WAITT(P, P, 6 - 2 * (P)); } \
        else { WAITT(P, (((MODE) - 1) & 1) * 4 + (P), ((MODE) < 3 ? 20 : (MODE) == 3 ? 17 : 6) - 2 * (P)); } }
#define K_FMA(P) { \
        _Pragma("unroll") \
        for (int i = 0; i < 3; i++) { v2f x = {(float)ta[P][i], (float)tb[P][i]}; A[P] = qe[P][i] * x + A[P]; } \
        kres[P] = A[P].x + A[P].y; }
#define K_RED(MODE) { \
        const bool o1 = lane & 1, o2 = lane & 2; \
        float s01 = (o1 ? kres[1] : kres[0]) + __int_as_float(__builtin_amdgcn_update_dpp(0, __float_as_int(o1 ? kres[0] : kres[1]), 0xb1, 0xf, 0xf, true)); \
        float s23 = (o1 ? kres[3] : kres[2]) + __int_as_float(__builtin_amdgcn_update_dpp(0, __float_as_int(o1 ? kres[2] : kres[3]), 0xb1, 0xf, 0xf, true)); \
        float sx = (o2 ? s23 : s01) + __int_as_float(__builtin_amdgcn_update_dpp(0, __float_as_int(o2 ? s01 : s23), 0x4e, 0xf, 0xf, true)); \
        sx = dpp_add<0x124>(sx); sx = dpp_add<0x128>(sx); \
        const auto sw = __builtin_amdgcn_permlane16_swap(__float_as_uint(sx), __float_as_uint(sx), false, false); \
        sx = __uint_as_float(sw[0]) + __uint_as_float(sw[1]); \
        if ((lane & 28) == 0) { if (MODE) sm.ent[(lane >> 1) & 1][rg * 4 + 2 * (lane & 1) + rsel] = sx * ksc; else sm.ent[u % NOUT][rg * 4 + 2 * (lane & 3) + rsel] = sx * ksc; } }
#define K_PRE4(MODE) { K_PRE(MODE, 0) K_PRE(MODE, 1) K_PRE(MODE, 2) K_PRE(MODE, 3) asm volatile("" :: "v"(A[0]), "v"(A[1]), "v"(A[2]), "v"(A[3])); }
#define K_BODY(MODE) { K_PRE4(MODE) K_WAIT(MODE, 0) K_FMA(0) K_WAIT(MODE, 1) K_FMA(1) K_WAIT(MODE, 2) K_FMA(2) K_WAIT(MODE, 3) K_FMA(3) K_RED(MODE) }
#define V_FMA(J, W0, W1) { \
        _Pragma("unroll") \
        for (int i = 0; i < 3; i++) { \
            acc[i] = fma(W0, (double)ta[J][i], acc[i]); \
            acc[i] = fma(W1, (double)tb[J][i], acc[i]); \
        } }
#define V_PIN() asm volatile("" :: "v"(acc[0]), "v"(acc[1]), "v"(acc[2]));
#define RDYWAIT(SL, SEQ) { while (lds_ld(&sm.rdy[SL]) != (SEQ)) SLEEP(1); CBAR(); }
#define LOADZ() { _Pragma("unroll") for (int p = 0; p < 4; p++) kz4[p] = sm.kz[u % NIN][rg * 4 + 2 * p + rsel].x; ksc = sm.kz[u % NIN][rg * 4 + 2 * (lane & 3) + rsel].y * scale; }
#define LOADQ() { _Pragma("unroll") for (int i = 0; i < 3; i++) { Qp[i] = sm.q[u % NIN][3 * sub + i]; Qn[i] = __builtin_amdgcn_alignbit(Qp[i], Qp[i], 16) ^ 0x80000000u; } }
#define FLUSH(ISV) { \
            mg = __builtin_amdgcn_readfirstlane(mg); \
            while (u >= mg + NOUT) { SLEEP(1); mg = lds_ld(&sm.merged); } \
            CBAR(); \
            if (ISV) { const unsigned aa = lds_addr(&sm.acc[u % NOUT][lane]); \
                asm volatile("ds_add_f64 %0, %1\n\tds_add_f64 %0, %2 offset:512\n\tds_add_f64 %0, %3 offset:1024" :: "v"(aa), "v"(acc[0]), "v"(acc[1]), "v"(acc[2]) : "memory"); \
                acc[0] = 0.0; acc[1] = 0.0; acc[2] = 0.0; } \
            CBAR(); \
            LDS_ADD(&sm.cnt[u % NOUT], np) \
            CBAR(); \
            np = 0; }

#define LOADZ2() { _Pragma("unroll") for (int p = 0; p < 4; p++) kz4[p] = sm.kz[p >> 1][rg * 4 + 2 * (p & 1) + rsel].x; ksc = sm.kz[(lane >> 1) & 1][rg * 4 + 2 * (lane & 1) + rsel].y * scale; }
#define STATIC_STEP(I) { \
            rg = ((cst + (I)) & 3) * 8 + wave; \
            LOADZ2() \
            K_TAB((I) + 1) K_BODY((I) + 1) }
        CS_DMA(wave, (cst + 2) & 3)
        RDYWAIT(0, 1u) RDYWAIT(1, 2u)
        LOADQ()
#pragma unroll
        for (int i = 0; i < 3; i++) { Rp[i] = sm.q[1][3 * sub + i]; Rn[i] = __builtin_amdgcn_alignbit(Rp[i], Rp[i], 16) ^ 0x80000000u; }
        STATIC_STEP(0)
        asm volatile("s_waitcnt vmcnt(11)" ::: "memory");
        REPACK(wave, (cst + 1) & 3)
        TABDONE(1)
        SISSUE(2)
        CS_DMA(wave, (cst + 3) & 3)
        STATIC_STEP(1)
        asm volatile("s_waitcnt vmcnt(11)" ::: "memory");
        REPACK(wave, (cst + 2) & 3)
        TABDONE(2)
        SISSUE(3)
        STATIC_STEP(2)
        asm volatile("s_waitcnt vmcnt(8)" ::: "memory");
        REPACK(wave, (cst + 3) & 3)
        TABDONE(3)
        GRAB(2)
        vut = LDV(&sm.utab[lane]);
        STATIC_STEP(3)
        CBAR();
        LDS_ADD(&sm.cnt[0], 4u) LDS_ADD(&sm.cnt[1], 4u)
        PLAN()
        LAUNCH()
        u = un; rg = nrg; tdc = tdn + 10;
        bool tabok = false;
        __builtin_amdgcn_s_setprio(2);
#pragma unroll 1
        while (u < NU) {
            GRAB(2)
            mg = LDV(&sm.merged);
            vut = LDV(&sm.utab[lane]);
            LINE()
            if (!tabok) {
                for (;;) { const unsigned tb_ = lds_ld(&sm.tabs); tabok = tb_ == ~0u; if (((tb_ >> rg) & 3u) == 3u) break; SLEEP(1) }
                CBAR();
            }
            K_TAB(0)
            if (first) {
                unsigned fl = LDV(&sm.rdy[u % NIN]);
                CBAR();
                LOADQ() LOADZ()
                if (__builtin_amdgcn_readfirstlane(fl) != u + 1) { RDYWAIT(u % NIN, u + 1) LOADQ() LOADZ() }
            } else LOADZ()
            K_PRE4(0)
            PLAN()
            K_WAIT(0, 0) K_FMA(0) K_WAIT(0, 1) K_FMA(1)
            asm volatile("" :: "v"(kres[0]), "v"(kres[1]));
            K_WAIT(0, 2) K_WAIT(0, 3)
            ARRIVED()
            K_FMA(2) K_FMA(3) K_RED(0)
            np += 2;
            ADVANCE(false)
        }
#pragma unroll 1
        while (u < 2 * NU) {
            GRAB(2)
            mg = LDV(&sm.merged);
            LINE()
            const unsigned vr_ = LDV(&sm.vrdy);
            CBAR();
            const unsigned short* wp = &sm.wv[went(u - NU) + (QUARTER(u - NU) ? rg & 7 : rg) * 4];
            v4i pw = *(const v4i*)wp;
            if (first && !((__builtin_amdgcn_readfirstlane(vr_) >> (u - NU)) & 1)) { while (!((lds_ld(&sm.vrdy) >> (u - NU)) & 1)) SLEEP(1); CBAR(); pw = *(const v4i*)wp; }
            const double w0 = (short)pw.x, w1 = pw.x >> 16, w2 = (short)pw.y, w3 = pw.y >> 16, w4 = (short)pw.z, w5 = pw.z >> 16, w6 = (short)pw.w, w7 = pw.w >> 16;
            asm volatile("" :: "v"(w0), "v"(w1), "v"(w2), "v"(w3), "v"(w4), "v"(w5), "v"(w6), "v"(w7));
            PLAN()
            WAITT(0, 0, 6); V_FMA(0, w0, w1) V_PIN()
            WAITT(1, 1, 4); V_FMA(1, w2, w3) V_PIN()
            WAITT(2, 2, 2); V_FMA(2, w4, w5) V_PIN()
            WAITT(3, 3, 0);
            np += 2;
            if (none) { V_FMA(3, w6, w7) FLUSH(true) break; }
            ARRIVED()
            V_FMA(3, w6, w7)
            ADVANCE(true)
        }
    } else {
    u64 E1;
    if (wave == NSW) {
        unsigned ref;
        const unsigned sidx = (blockIdx.x & 7) | ((blockIdx.x >> 7) << 3);
#define SAMPLE(K) ((((blockIdx.x >> 3) - (blockIdx.x & 7) - (K) * 5) & 15) == 0)
#define BTAG(K) ((btag + (K)) & 0xfffffu)
#define POST(K, C) { if (lane == 0) __hip_atomic_store((u64*)(ws + (bd + (K) * RST + (sidx >> 3) * 256 + (sidx & 7) * 8)), (u64)(C) | ((u64)(((C) - ref) | BTAG(K) << 12) << 32), __ATOMIC_RELAXED, __HIP_MEMORY_SCOPE_AGENT); }
        const unsigned own = lds_ld(&sm.line);
        if (SAMPLE(0) && lane < NTRY) __hip_atomic_store((unsigned*)(ws + WS_HELLO + lane * 768) + sidx, own, __ATOMIC_RELAXED, __HIP_MEMORY_SCOPE_AGENT);
        const u64 E = __hip_atomic_load((const u64*)(ws + WS_EPOCH), __ATOMIC_RELAXED, __HIP_MEMORY_SCOPE_AGENT);
        E1 = E + 1;
        if (lane == 0) { sm.tag_lo = (unsigned)E1; sm.tag_hi = (unsigned)(E1 >> 32); CBAR(); lds_st(&sm.tag_rdy, 1u); }
        const unsigned btag = __builtin_amdgcn_readfirstlane((unsigned)E1) * 61u, bd = WS_BOARD + (btag & 7) * (NRND * RST);
        ref = own;
        int off = 0;
        {
            unsigned tries = 0, cur = own, tl = own;
            bool news = false;
#pragma unroll 1
            while (!news || cur != ref) {
                const unsigned now = NOW();
                if (tries < NTRY && (int)(now - own) >= (int)(TA + tries * TA2)) {
                    v16u x;
                    asm volatile("s_load_dwordx16 %0, %1, %2\n\ts_waitcnt lgkmcnt(0)" : "=&s"(x) : "s"(ws), "s"(__builtin_amdgcn_readfirstlane(WS_HELLO + tries * 768)) : "memory");
                    unsigned bad = 0; int sum = 0, dif = 0;
#pragma unroll
                    for (int i = 0; i < 16; i++) { bad |= x[i] - own + 512; sum += x[i] - x[0]; dif += i & 4 ? x[i] : -x[i]; }
                    tries++;
                    if (bad < 1024) { const unsigned com = x[0] + (sum >> 4) + abs(dif) * (8 - KC) / 128; ref = x[0] + ((sum + (blockIdx.x & 4 ? dif : -dif)) >> 4); off = ref - com;
                        tries = NTRY; tl = NOW(); if (lane == 0) lds_st(&sm.aline, com); }
                    continue;
                }
                if (!news && lds_ld(&sm.utab[31]) != ~0u) { news = true; CBAR(); if (cur == ref) tl = now; ref = lds_ld(&sm.aline) + off; tries = NTRY; }
                const int d = (now - tl) >> CSH;
                if (cur != ref && d) { tl += d << CSH; cur += min(max((int)(ref - cur), -d), d); if (lane == 0) lds_st(&sm.line, cur); }
                SLEEP(2)
            }
        }
        ref -= off;
        const unsigned ref0 = ref;
        const unsigned lead = (blockIdx.x >> 3) == 9 ? 0 : LEAD;
        unsigned posted = ref, lagf = ref, tpost = ref + TB1, tpoll = tpost + RDD + lead, kw = 0, kr = 0, cur = ref + off, khold = 0;
        int o = off; bool held = false;
#pragma unroll 1
        for (;;) {
            const unsigned p = lds_ld(&sm.grab);
            if (p >= NPC) break;
            lagf += (int)(lds_ld(&sm.late) - lagf) >> 1;
            { const unsigned l = lds_ld(&sm.line); if (l != cur) { cur = l; khold = kw; held = true; } }
            const unsigned now = NOW();
            if (o) { o = off * min(max((int)(ref0 + TC0 + (1 << TCS) - now), 0), 1 << TCS) >> TCS; if (!held && cur != ref + o) { cur = ref + o; if (lane == 0) lds_st(&sm.line, cur); } }
            if (kw < NRND && (int)(now - tpost) >= 0) {
                const unsigned c = ref + min((p >= PSL0 && (int)(lagf - cur) > LHI ? lagf - LOK : cur) - o - ref, 4000u);
                if (SAMPLE(kw) && (c != ref || (ref != posted && (sidx & 7) == (kw & 7)))) POST(kw, c)
                posted = ref;
                tpost += PER; kw++;
            }
            if (kr < NRND && (int)(now - tpoll) >= 0) {
                v16u xa, xb;
                asm volatile("s_load_dwordx16 %0, %2, %3\n\ts_load_dwordx16 %1, %2, %4\n\ts_waitcnt lgkmcnt(0)" : "=&s"(xa), "=&s"(xb)
                             : "s"(ws), "s"(__builtin_amdgcn_readfirstlane(bd + kr * RST)), "s"(__builtin_amdgcn_readfirstlane(bd + kr * RST + 256)) : "memory");
                int nb = 0, sg = 0;
#pragma unroll
                for (int i = 0; i < 16; i++) {
                    const unsigned lo = i < 8 ? xa[2 * i & 15] : xb[2 * i & 15], hi = i < 8 ? xa[(2 * i + 1) & 15] : xb[(2 * i + 1) & 15];
                    if (hi >> 12 == BTAG(kr) && lo - ref0 < 8000) { nb = max(nb, (int)(lo - (hi & 4095u) - ref)); sg += hi & 4095u; }
                }
                ref += nb + (sg >> 4);
                if (cur != ref + o && (kr >= khold || (int)(ref + o - cur) > 0)) { cur = ref + o; held = false; if (lane == 0) lds_st(&sm.line, cur); }
                tpoll += PER; kr++;
            }
            SLEEP(2)
        }
    } else if (wave == NSW + 1 || wave == NSW + 2) {
        const int ql = lane < 48 ? lane : 47;
        unsigned c1, c2, s1, s2;
#define QT_LOADS() { int qo_ = ql; asm volatile("" : "+v"(qo_)); \
                     c1 = ((const unsigned*)q_cos)[qo_]; c2 = ((const unsigned*)q_cos)[48 + qo_]; s1 = ((const unsigned*)q_sin)[qo_]; s2 = ((const unsigned*)q_sin)[48 + qo_]; }
        unsigned q1, q2, lkz, lks, lvz, lvs;
        const unsigned sofs = R8(((unsigned)lane >> 1) & 7) * 2 + (lane & 1);
#define UNIT_LOADS(HEAD, GRP) { \
        q1 = ((const unsigned*)(q + (size_t)(HEAD) * DH))[ql]; q2 = ((const unsigned*)(q + (size_t)(HEAD) * DH))[48 + ql]; \
        const size_t so = (size_t)(HEAD) * (SKV / 2) + (GRP) * 256 + sofs; \
        lkz = *((const unsigned*)k_zeros + so); lks = *((const unsigned*)k_scales + so); \
        lvz = *((const unsigned*)v_zeros + so); lvs = *((const unsigned*)v_scales + so); }
#define UNIT_FILL(SL, U) { \
            v4f qq; \
            qq.x = bfr(bfr(bflo(q1) * bflo(c1)) + bfr(-bflo(q2) * bflo(s1))); \
            qq.y = bfr(bfr(bflo(q2) * bflo(c2)) + bfr(bflo(q1) * bflo(s2))); \
            qq.z = bfr(bfr(bfhi(q1) * bfhi(c1)) + bfr(-bfhi(q2) * bfhi(s1))); \
            qq.w = bfr(bfr(bfhi(q2) * bfhi(c2)) + bfr(bfhi(q1) * bfhi(s2))); \
            *(v2u*)&sm.q[SL][2 * ql] = (v2u){(__float_as_uint(qq.x) >> 16) | __float_as_uint(qq.y), (__float_as_uint(qq.z) >> 16) | __float_as_uint(qq.w)}; \
            *(v4f*)&sm.kz[SL][2 * lane] = (v4f){bflo(lkz), bflo(lks), bfhi(lkz), bfhi(lks)}; \
            *(v4f*)&sm.vz[SL][2 * lane] = (v4f){bflo(lvz), bflo(lvs), bfhi(lvz), bfhi(lvs)}; \
            CBAR(); \
            if (lane == 0) lds_st(&sm.rdy[SL], (U) + 1); \
            CBAR(); }
        const unsigned dw = NSW + 2 - wave;
#define TOUCH() { const unsigned x_ = ((blockIdx.x >> 3) - 18) * 64 + lane; \
            const char* p_ = x_ < 384 ? (const char*)(x_ < 192 ? qk : qv) + (size_t)(x_ < 192 ? x_ : x_ - 192) * (2u << 20) + (x_ * 8 + (blockIdx.x & 7)) * 640 : x_ < 408 ? (const char*)out + (x_ - 384) * 4096 : (const char*)ws + WS_PART + (x_ - 408) * (1u << 20) + (blockIdx.x & 7) * 1280; \
            if (x_ < 408 + TWS) asm volatile("global_load_dword a0, %0, off nt" :: "v"(p_) : "a0"); }
        QT_LOADS()
        UNIT_LOADS(head0 + dw * (GSZ + 1 - 2 * side), lane >> 4)
        if (TCH == 1 && dw) TOUCH()
#pragma unroll 1
        for (unsigned u = dw;;) {
            while (u >= lds_ld(&sm.merged) + NIN) SLEEP(4);
            CBAR();
            UNIT_FILL(u % NIN, u)
            u += 2;
            if (u >= NU) break;
            if (u == 4) {
                const unsigned tpf = tdue(PSF - 1) + TFO;
                { int d; while ((d = (int)(lds_ld(&sm.line) + tpf - NOW())) > 0 && d < 3000) SLEEP(1) }
#define PF3(P) asm volatile("global_load_dwordx4 a[0:3], %0, %1\n\tglobal_load_dwordx4 a[0:3], %0, %1 offset:1024\n\tglobal_load_dwordx4 a[0:3], %0, %1 offset:2048" \
                            :: "v"(64 * SKV * 2 + blockIdx.x * 3072 + lane * 16), "s"(P) : "a0", "a1", "a2", "a3")
                PF3(k_zeros); PF3(k_scales); PF3(v_zeros); PF3(v_scales);
                asm volatile("global_load_dword a0, %0, %1" :: "v"(blockIdx.x * (DH * 2) + ql * 8), "s"(q) : "a0");
            }
            const unsigned tin = tdue(first_of(u) - (QUARTER(u) ? 24 : 0) - LEADP + (split & 7) * DPH);
            { int d; while ((d = (int)(lds_ld(&sm.line) + tin - NOW())) > 0 && d < 3000) SLEEP(4) }
            unsigned hd;
            while ((hd = lds_ld(&sm.utab[u])) == ~0u) SLEEP(2)
            UNIT_LOADS(hd & 255, QUARTER(u) ? rank & 3 : lane >> 4)
        }
        if (TV2 && dw) {
#pragma unroll 1
            for (unsigned i = 0; i < 6 + (TV2 > 1); i++) {
                const unsigned y = (blockIdx.x >> 3) + 32 * i; unsigned dm;
                const char* p_ = i < 6 ? (const char*)qv + (size_t)y * (2u << 20) + (y * 8 + (blockIdx.x & 7)) * 640 + 128 : (const char*)ws + WS_PART + (y & 3) * (1u << 20) + (y >> 2 & 7) * 4096 + (blockIdx.x & 7) * 1280;
                asm volatile("s_load_dword %0, %1, 0x0\n\ts_waitcnt lgkmcnt(0)" : "=&s"(dm) : "s"(p_) : "memory");
            }
        }
        while (lds_ld(&sm.tag_rdy) == 0) SLEEP(1);
        CBAR();
        E1 = (u64)lds_ld(&sm.tag_lo) | ((u64)lds_ld(&sm.tag_hi) << 32);
        if (wave == NSW + 1 && blockIdx.x == 0) {
            for (;;) {
                bool ok = true;
#pragma unroll
                for (int i = 0; i < NH / 64; i++) ok &= __hip_atomic_load((const u64*)(ws + WS_POST) + i * 64 + lane, __ATOMIC_RELAXED, __HIP_MEMORY_SCOPE_AGENT) >> 24 == (E1 << 24) >> 24;
                if (__builtin_amdgcn_ballot_w64(ok) == ~0ull) break;
                SLEEP(64);
            }
            if (lane == 0) __hip_atomic_store((u64*)(ws + WS_EPOCH), E1, __ATOMIC_RELAXED, __HIP_MEMORY_SCOPE_AGENT);
        }
        if (wave == NSW + 2) {
            const unsigned t16 = (unsigned)E1 & 0xffffu, t16b = ((unsigned)E1 + ((unsigned)E1 >> 16)) & 0xffffu;
            const __amdgpu_buffer_rsrc_t rsrc = __builtin_amdgcn_make_buffer_rsrc(ws + WS_SWAP + (blockIdx.x ^ 4) * 5120, 0, 5120, 0x00020000);
            const unsigned swp = lds_ld(&sm.swp);
            if (swp) {
                unsigned use = 0;
#pragma unroll
                for (int i = 0; i < 5; i++) use |= ((swp >> wunit(i * 64 + lane)) & 1) << i;
                const unsigned tg = tdue(PSG - 1) + (lds_ld(&sm.gap) & 0xffffu);
                while ((int)(NOW() - lds_ld(&sm.line) - tg) < 0) SLEEP(2)
                for (;;) {
                    v4u d[5]; bool ok = true;
#pragma unroll
                    for (int i = 0; i < 5; i++) d[i] = __builtin_amdgcn_raw_buffer_load_b128(rsrc, (use >> i) & 1 ? (i * 64 + lane) * 16 : 0x40000000, 0, SC1 | 2);
#pragma unroll
                    for (int i = 0; i < 5; i++) if ((use >> i) & 1) {
                        ok &= d[i].y >> 16 == t16 && d[i].w >> 16 == t16b;
                        unsigned* w = (unsigned*)&sm.wv[144 * NMIX + (i * 64 + lane) * 6];
                        w[0] = d[i].x; w[1] = (d[i].y & 0xffffu) | (d[i].z << 16); w[2] = (d[i].z >> 16) | (d[i].w << 16);
                    }
                    if (__builtin_amdgcn_ballot_w64(ok) == ~0ull) break;
                    SLEEP(32)
                }
                CBAR();
                LDS_OR(&sm.vrdy, swp)
            }
        }
    } else {
        const unsigned eu = lane >= NU ? lane - NU : lane, win = min(first_of(eu) >> 5, 15u), pair = QUARTER(eu) ? 2 * (eu - WQ) + (rank >> 2) : rank;
        const unsigned rot2 = ((const unsigned char*)ROT)[win * 8 + pair];
#define UENT(BIT) (win * 16 + 2 * pair + ((BIT) ^ side) | (QUARTER(eu) ? (rank - (eu - WQ)) * 8 & 31 : (rot2 >> ((BIT) ^ side) * 4 & 15) * 2) << 8)
        unsigned far = 0;
        if (TCH == 2 && lane < NMIX) lds_st(&sm.utab[lane], UENT(lane & 1));
        const unsigned hp = (blockIdx.x & 1) * NH + (blockIdx.x >> 1) * 4 + 2;
        if (blockIdx.x < 144) {
            const char* pb = (blockIdx.x < 128 ? (const char*)(hp >= NH ? qv : qk) + (size_t)(hp & (NH - 1)) * (SKV * DH * 4) + SKV * DH * 2
                                               : (const char*)ws + WS_PART + (8u << 20) + (blockIdx.x & 15) * 65536) + ((NOW() * 0x9e3779b1u) >> 26) * 128;
            unsigned mn[2] = {~0u, ~0u};
#pragma unroll 1
            for (unsigned i = 1; i < 2 * NPRB + 2; i++) {
                u64 t0, t1; unsigned dm;
                asm volatile("s_memtime %0\n\ts_waitcnt lgkmcnt(0)\n\ts_load_dword %2, %3, 0x0\n\ts_waitcnt lgkmcnt(0)\n\ts_memtime %1\n\ts_waitcnt lgkmcnt(0)"
                             : "=&s"(t0), "=&s"(t1), "=&s"(dm) : "s"(pb + i * 8192) : "memory");
                const unsigned t = (unsigned)(t1 - t0) < 330 || i == 1 ? ~0u : (unsigned)(t1 - t0);
                if (i & 1) mn[1] = min(mn[1], t); else mn[0] = min(mn[0], t);
            }
            far = mn[0] > mn[1];
        } else if (TCH == 2 && blockIdx.x >> 3 != MASTER >> 3) {
            const unsigned j = (blockIdx.x >> 3) - 18 - (blockIdx.x >> 3 > MASTER >> 3);
#pragma unroll 1
            for (unsigned i = 0; i < 32; i += TCW) {
                unsigned dm[TCW];
#pragma unroll
                for (int k = 0; k < TCW; k++) {
                    const unsigned x = min(j + 13 * (i + k), 407u);
                    const char* p_ = x < 384 ? (const char*)(x < 192 ? qk : qv) + (size_t)(x < 192 ? x : x - 192) * (2u << 20) + (x * 8 + (blockIdx.x & 7)) * 640 : (const char*)out + (x - 384) * 4096;
                    asm volatile("s_load_dword %0, %1, 0x0" : "=&s"(dm[k]) : "s"(p_) : "memory");
                }
#pragma unroll
                for (int k = 0; k < TCW; k++) asm volatile("s_waitcnt lgkmcnt(0)" :: "s"(dm[k]) : "memory");
            }
        }
        if (TCH != 2 && lane < NMIX) lds_st(&sm.utab[lane], UENT(lane & 1));
        while (lds_ld(&sm.tag_rdy) == 0) SLEEP(1);
        CBAR();
        E1 = (u64)lds_ld(&sm.tag_lo) | ((u64)lds_ld(&sm.tag_hi) << 32);
        {
            const unsigned own = lds_ld(&sm.line), tg = (unsigned)E1;
            if (lane == 0) __hip_atomic_store((u64*)(ws + WS_POST) + blockIdx.x, E1 << 24 | min(lds_ld(&sm.slow), 4000u) << 1 | ((far ^ (blockIdx.x >> 2) ^ (blockIdx.x < 128 ? 0x96 >> (hp & 7) : 0)) & 1), __ATOMIC_RELAXED, __HIP_MEMORY_SCOPE_AGENT);
            const __amdgpu_buffer_rsrc_t nrs = __builtin_amdgcn_make_buffer_rsrc(ws + WS_NEWS, 0, 8 * 1280, 0x00020000);
            if (blockIdx.x == MASTER) {
                while ((int)(NOW() - own) < TMAP) SLEEP(2)
                const unsigned a = tg << 24, b = (unsigned)(E1 >> 8);
                v4u x; v2u z; u64 v0, v1;
#pragma unroll 1
                for (int i = 0; i < 8; i++) {
                    x = __builtin_amdgcn_raw_buffer_load_b128(__builtin_amdgcn_make_buffer_rsrc(ws + WS_POST, 0, 1024, 0x00020000), lane * 16, 0, SC1);
                    z = __builtin_amdgcn_raw_buffer_load_b64(__builtin_amdgcn_make_buffer_rsrc(ws + WS_POST + 1024, 0, 128, 0x00020000), lane * 8, 0, SC1);
                    v0 = __builtin_amdgcn_ballot_w64(((x.x ^ a) >> 24 | (x.y ^ b)) == 0); v1 = __builtin_amdgcn_ballot_w64(((x.z ^ a) >> 24 | (x.w ^ b)) == 0);
                    if (__builtin_popcountll(v0) + __builtin_popcountll(v1) >= MINV) break;
                    SLEEP(8)
                }
                const u64 m0 = __builtin_amdgcn_ballot_w64(x.x & 1) & v0, m1 = __builtin_amdgcn_ballot_w64(x.z & 1) & v1;
                const int s0 = lane < 43 ? min(max(lane - VW, 4 * NMIX), 42 - 2 * VW) : min(max(lane - VW, 43), 63 - 2 * VW);
#define PC(M) __builtin_popcount((unsigned)((M) >> s0) & ((2u << 2 * VW) - 1))
                const u64 g0 = __builtin_amdgcn_ballot_w64(2 * PC(m0) > PC(v0)), g1 = __builtin_amdgcn_ballot_w64(2 * PC(m1) > PC(v1));
                const float sl = wave_sum(((v0 >> lane & 1) ? (float)(x.x >> 1 & 4095u) : 0.f) + ((v1 >> lane & 1) ? (float)(x.z >> 1 & 4095u) : 0.f)) / ((8.f - PRE0) * (__builtin_popcountll(v0) + __builtin_popcountll(v1)) + 1.f);
                const unsigned al = lds_ld(&sm.aline), mean = (al ? al : own) + (int)fmaxf(sl - SL0, 0.f);
                const bool vz = lane < 16 && ((z.x ^ a) >> 24 | (z.y ^ b)) == 0;
                const unsigned gw = 2 * __builtin_popcountll(__builtin_amdgcn_ballot_w64(vz && (z.x & 1))) > __builtin_popcountll(__builtin_amdgcn_ballot_w64(vz));
                const unsigned k = lane & 3;
                if (lane < 32 && k < 3) __builtin_amdgcn_raw_buffer_store_b128((v4u){k == 0 ? (unsigned)g0 : k == 1 ? (unsigned)g1 : mean, tg, k == 0 ? (unsigned)(g0 >> 32) : k == 1 ? (unsigned)(g1 >> 32) : gw, tg},
                                                                              nrs, (lane >> 2) * 1280 + k * 16, 0, SC1);
            }
            while ((int)(NOW() - own) < TMAP + TRES) SLEEP(2)
            v4u y;
            for (;;) {
                y = __builtin_amdgcn_raw_buffer_load_b128(nrs, lane < 3 ? (blockIdx.x & 7) * 1280 + lane * 16 : 0x40000000, 0, SC1);
                if (((unsigned)__builtin_amdgcn_ballot_w64(y.y == tg && y.w == tg) & 7u) == 7u) break;
                SLEEP(8)
            }
            const u64 f0 = (unsigned)__builtin_amdgcn_readlane(y.x, 0) | (u64)(unsigned)__builtin_amdgcn_readlane(y.z, 0) << 32, f1 = (unsigned)__builtin_amdgcn_readlane(y.x, 1) | (u64)(unsigned)__builtin_amdgcn_readlane(y.z, 1) << 32;
            if (lane == 2) { sm.aline = y.x - own + 500 < 1000 ? y.x : own; sm.chead = (blockIdx.x & ~5u) | ((blockIdx.x >> 2 ^ y.z) & 1) | (blockIdx.x & 1) << 2; }
            unsigned bit = ((unsigned)((lane >= NU ? f1 : f0) >> (4 * win + (pair >> 1))) ^ (0x66 >> pair)) & 1;
            if (eu < NMIX) bit = eu & 1;
            const u64 bm = __builtin_amdgcn_ballot_w64(bit);
            const unsigned nsw = __builtin_popcountll((f0 ^ f1) >> (4 * NMIX));
            if (lane == 0) { sm.swp = (unsigned)(bm ^ (bm >> NU)) & ((1u << NU) - 1); lds_st(&sm.gap, nsw ? (TG0 + TG1 * nsw / 8) * 0x10001u + ((TH0 + TH1 * nsw / 8) << 16) : 0u); }
            CBAR();
            lds_st(&sm.utab[lane], UENT(bit));
        }
        const unsigned swp = lds_ld(&sm.swp);
        const unsigned T = lds_ld(&sm.tag_lo);
        const unsigned t15 = T & 0x7fffu;
        const __amdgpu_buffer_rsrc_t rsrc = __builtin_amdgcn_make_buffer_rsrc(ws + WS_PART + split * 512, 0, NRW * NSPLIT * 512 - split * 512, 0x00020000);
        v2u* recs = (v2u*)&sm.cs[0][0][0];
#pragma unroll
        for (int i = 0; i < NOUT * 3; i++) (&sm.acc[0][0])[i * 64 + lane] = 0.0;
        CBAR();
        unsigned vut = sm.utab[NU + (lane & 31)] & 255;
        if (QUARTER((unsigned)lane) && (rank & 3)) vut = NH - 16 + (rank & 3) * 16 + ((vut & 15) ^ 1);
        __builtin_amdgcn_s_setprio(3);
        bool direct = false, last = false, dry = PDRY;
        unsigned po[NU / 2];
#pragma unroll
        for (int k = 0; k < NU / 2; k++) po[k] = __builtin_amdgcn_ds_bpermute((2 * k + (lane >> 5)) * 4, vut) * (NSPLIT * 512) + (lane & 31) * 16;
        const u64 rb_ = (u64)(ws + WS_PART + split * 512);
        const v4u rs_ = {(unsigned)rb_, (unsigned)(rb_ >> 32) & 0xffffu, NRW * NSPLIT * 512 - split * 512, 0x00020000u};
#pragma unroll 1
        for (unsigned u = 0; u < 2 * NU; u++) {
            const unsigned e = u % NU, so = u % NOUT;
            if (u >= NU) while (!((lds_ld(&sm.vrdy) >> e) & 1)) SLEEP(4)
            CBAR();
            const v4f h = *(const v4f*)&sm.wv[went(e) + (QUARTER(e) ? 32 : 128)];
            const unsigned row = __builtin_amdgcn_readlane(vut, e) * (NSPLIT * 512);
            const unsigned rc = (row & 15 * NSPLIT * 512 ^ NSPLIT * 512) + NRR * NSPLIT * 512;
            double a0, a1, a2;
            for (;;) {
                if (!direct && u == 2 * NU - 1 && (dry || lds_ld(&sm.grab) >= NPC - PUBP || last)) {
                    const unsigned so_ = dry ? 0x40000000u : 0u;
                    __builtin_amdgcn_raw_buffer_store_b64((v2u){__float_as_uint(h.z), T}, rsrc, lane < 16 ? lane * 8 | so_ : 0x40000000u, rc + (NCP - 1) * (GSZ * NSPLIT * 512), SC1);
#define PUB(K) asm volatile("buffer_store_dwordx4 a[%c0:%c0+3], %1, %2, 0 offen sc1" :: "i"(16 + 4 * (K)), "v"(po[K] | so_), "s"(rs_) : "memory");
                    PUB(5) PUB(6)
                    if (!dry && PDEL) { const unsigned t_ = lds_ld(&sm.line) + tdue(NPC - 2) + (lds_ld(&sm.gap) >> 16) + PDEL; int d_; while ((d_ = (int)(t_ - NOW())) > 0 && d_ < 300) SLEEP(PSLP) }
                    PUB(0) PUB(1) PUB(2) PUB(3) PUB(4) PUB(7) PUB(8)
                    direct = !dry; dry = false;
                }
                const unsigned cn = __hip_atomic_load(&sm.cnt[so], __ATOMIC_RELAXED, __HIP_MEMORY_SCOPE_WORKGROUP);
                CBAR();
                if (direct) { a0 = sm.acc[so][lane]; a1 = sm.acc[so][64 + lane]; a2 = sm.acc[so][128 + lane]; }
                if (__builtin_amdgcn_readfirstlane(cn) == (QUARTER(e) ? 8 : 32)) { if (direct || u != 2 * NU - 1) break; last = true; }
                if (direct) SLEEP(MSLP) else SLEEP(1)
            }
            CBAR();
            if (!direct) { a0 = sm.acc[so][lane]; a1 = sm.acc[so][64 + lane]; a2 = sm.acc[so][128 + lane]; }
            if (u < NU) {
                float2 lg = *(const float2*)&sm.ent[so][2 * lane];
                if (QUARTER(e) && lane >> 4 != (rank & 3)) lg = make_float2(-INFINITY, -INFINITY);
                const v4f zs = *(const v4f*)&sm.vz[u % NIN][2 * lane];
                const float m = wave_max(fmaxf(lg.x, lg.y));
                const float p0 = __expf(lg.x - m), p1 = __expf(lg.y - m);
                const float x0 = p0 * zs.y, x1 = p1 * zs.w;
                const unsigned eb = max(__float_as_uint(wave_max(fmaxf(fabsf(x0), fabsf(x1)))) >> 23, 14u);
                const float f = __uint_as_float((268u - eb) << 23);
                const float w0 = fminf(fmaxf(rintf(x0 * f), -32767.f), 32767.f), w1 = fminf(fmaxf(rintf(x1 * f), -32767.f), 32767.f);
                const float den = wave_sum(p0 + p1);
                const float bz = wave_sum(fmaf(w0, zs.x, w1 * zs.z));
                const float L = fmaf(m, 1.4426950408889634f, __builtin_amdgcn_logf(den));
                unsigned* wd = (unsigned*)(((swp >> u) & 1 ? sm.wo : sm.wv) + went(u));
                if (!QUARTER(u) || lane >> 4 == (rank & 3)) wd[QUARTER(u) ? lane & 15 : lane] = ((unsigned)(int)w0 & 0xffffu) | ((unsigned)(int)w1 << 16);
                if (lane == 0) *(v4f*)&wd[QUARTER(u) ? 16 : 64] = (v4f){ldexpf(1.f / den, (int)eb - 141), bz, L, 0.f};
                CBAR();
                if (!((swp >> u) & 1)) LDS_OR(&sm.vrdy, 1u << u)
            } else {
                const float n0 = (float)(a0 - (double)h.y) * h.x, n1 = (float)(a1 - (double)h.y) * h.x, n2 = (float)(a2 - (double)h.y) * h.x;
                float mx = fmaxf(fmaxf(fabsf(n0), fabsf(n1)), fabsf(n2));
                if (!direct) mx = wave_maxv(mx);
                const unsigned e2 = max(__float_as_uint(mx) >> 23, 14u);
                const float f2 = __uint_as_float((268u - e2) << 23);
                const unsigned m01 = __builtin_bit_cast(unsigned, __builtin_amdgcn_cvt_pk_i16((int)rintf(n0 * f2), (int)rintf(n1 * f2)));
                const unsigned m2 = __builtin_bit_cast(unsigned, __builtin_amdgcn_cvt_pk_i16((int)rintf(n2 * f2), 0));
                const unsigned bit = ((lane & 1 ? e2 : __float_as_uint(h.z)) >> (lane >> 1)) & 1u;
                const v2u r = {m01, m2 | (direct ? e2 << 16 | T << 24 : bit << 16 | t15 << 17)};
                if (direct) {
                    __builtin_amdgcn_raw_buffer_store_b64(r, rsrc, lane * 8, row, SC1);
#pragma unroll
                    for (int j = 0; j < NCP - 1; j++) __builtin_amdgcn_raw_buffer_store_b64(r, rsrc, lane * 8, rc + j * GSZ * NSPLIT * 512, SC1);
 }
                else recs[e * 64 + lane] = r;
                CBAR();
                sm.acc[so][lane] = 0.0; sm.acc[so][64 + lane] = 0.0; sm.acc[so][128 + lane] = 0.0;
            }
            CBAR();
            if (lane == 0) { lds_st(&sm.cnt[so], 0u); CBAR(); lds_st(&sm.merged, u + 1); }
            if (u == 2 * NU - 2) {
                wave_sync();
#pragma unroll
                for (int k = 0; k < NU / 2; k++) asm volatile("ds_read_b128 a[%c0:%c0+3], %1 offset:%2" :: "i"(16 + 4 * k), "v"(lds_addr(recs) + lane * 16), "i"(k * 1024) : "memory");
                asm volatile("s_waitcnt lgkmcnt(0)" ::: "memory");
            }
            if (u == NU - 1 && swp) {
                wave_sync();
                const __amdgpu_buffer_rsrc_t srs = __builtin_amdgcn_make_buffer_rsrc(ws + WS_SWAP + blockIdx.x * 5120, 0, 5120, 0x00020000);
                v4u d[5]; unsigned o[5];
#pragma unroll
                for (int i = 0; i < 5; i++) {
                    const unsigned* w = (const unsigned*)&sm.wo[144 * NMIX + (i * 64 + lane) * 6];
                    const unsigned d0 = w[0], d1 = w[1], d2 = w[2];
                    d[i] = (v4u){d0, (d1 & 0xffffu) | (T << 16), (d1 >> 16) | (d2 << 16), (d2 >> 16) | ((T + (T >> 16)) << 16)};
                    o[i] = (swp >> wunit(i * 64 + lane)) & 1 ? (i * 64 + lane) * 16 : 0x40000000;
                }
                while ((int)(NOW() - lds_ld(&sm.line)) < (int)tdue(PSW) + TWO) SLEEP(1)
#pragma unroll
                for (int i = 0; i < 5; i++) __builtin_amdgcn_raw_buffer_store_b128(d[i], srs, o[i], 0, SC1 | 2);
            }
        }
        return;
    }
    }
    __builtin_amdgcn_s_setprio(0);
    const u64 rb_ = (u64)(ws + WS_PART);
    const v4u rs_ = {(unsigned)rb_, (unsigned)(rb_ >> 32) & 0xffffu, NRW * NSPLIT * 512, 0x00020000u};
#define CGATE(DUE) { int d_ = (int)((DUE) - NOW()); if (d_ < 3000) while (d_ > 0) { __builtin_amdgcn_s_sleep(1); d_ = (int)((DUE) - NOW()); } }
#define TLAST() (dry ? NOW() - 1000 : lds_ld(&sm.line) + tdue(NPC - 2) + (lds_ld(&sm.gap) >> 16))
#pragma unroll 1
    for (bool dry = CDRY && wave > NSW;; dry = false) {
        const unsigned oob = dry ? 0x40000000u : 0u;
        int lane = tid & 63; asm volatile("" : "+v"(lane));
        unsigned c;
#define ADDRTN(P) { u64 ex_; asm volatile("s_mov_b64 %0, exec\n\ts_mov_b64 exec, 1\n\tds_add_rtn_u32 a15, %1, a13\n\ts_mov_b64 exec, %0" : "=&s"(ex_) : "v"(lds_addr(P)) : "a15", "memory"); }
#define ADDED(V) { unsigned t_; asm volatile("s_waitcnt lgkmcnt(0)\n\tv_accvgpr_read_b32 %1, a15\n\ts_nop 0\n\tv_readfirstlane_b32 %0, %1\n\ts_nop 1" : "=s"(V), "=v"(t_)); }
        asm volatile("v_accvgpr_write_b32 a13, %0\n\ts_nop 1" :: "v"(dry ? 0u : 1u) : "a13");
        if (dry) while (lds_ld(&sm.merged) < NU) SLEEP(8)
        ADDRTN(&sm.cidx) ADDED(c)
        if (c >= NSW) break;
        if (!dry) while (lds_ld(&sm.grab) < NPC + 2) SLEEP(4)
        const unsigned head = lds_ld(&sm.chead), t15 = lds_ld(&sm.tag_lo) & 0x7fffu;
        if (head >= NH - GSZ) {
            __builtin_amdgcn_s_setprio(3);
            const unsigned T = lds_ld(&sm.tag_lo);
            v2u hd;
            const unsigned tl = TLAST();
            CGATE(tl + THD)
            for (;;) {
                hd = __builtin_amdgcn_raw_buffer_load_b64(__builtin_amdgcn_make_buffer_rsrc(ws + WS_PART + (NRW - GSZ + (head & 15 ^ 1)) * (NSPLIT * 512), 0, NSPLIT * 512, 0x00020000), (lane & 15) * 512 | oob, 0, SC1);
                if (__builtin_amdgcn_ballot_w64(hd.y == T) == ~0ull || dry) break;
                SLEEP(4)
            }
            const float w = __builtin_amdgcn_exp2f(__uint_as_float(hd.x) - row_max(__uint_as_float(hd.x))), cw = w * __builtin_amdgcn_rcpf(row_sum(w));
            const float cf = __int_as_float(__builtin_amdgcn_ds_bpermute((2 * c + (lane >> 5)) * 4, __float_as_int(cw)));
            const unsigned r0 = head * (NSPLIT * 512) + c * 1024, r1 = (NRR - GSZ + (head & 15 ^ 1)) * (NSPLIT * 512) + c * 1024;
            double* ap = (dry ? (double*)&sm.cs[6][0][0] : &sm.acc[0][0]) + lane;
            if (!dry) while (lds_ld(&sm.merged) <= (2 * NU - 1) / NOUT * NOUT) SLEEP(1)
            const unsigned vo = lane * 16 | oob;
            v4u x; bool got;
#define LOOK(J) { CGATE(tl + TCA + (J) * TCD) asm volatile("buffer_load_dwordx4 a[%c0:%c0+3], %1, %2, %3 offen sc1" :: "i"(16 + 4 * (J)), "v"(vo), "s"(rs_), "s"((J) ? r1 + (J) * (GSZ * NSPLIT * 512) : r0) : "memory"); }
#define SEEN(J, N) { asm volatile("s_waitcnt vmcnt(%c5)\n\tv_accvgpr_read_b32 %0, a[%c4]\n\tv_accvgpr_read_b32 %1, a[%c4+1]\n\tv_accvgpr_read_b32 %2, a[%c4+2]\n\tv_accvgpr_read_b32 %3, a[%c4+3]" \
                             : "=v"(x.x), "=v"(x.y), "=v"(x.z), "=v"(x.w) : "i"(16 + 4 * (J)), "i"(N)); \
                got = __builtin_amdgcn_ballot_w64((((x.y ^ T << 24) | (x.w ^ T << 24)) >> 24) != 0) == 0; }
            LOOK(0) LOOK(1) SEEN(0, 1)
            if (!got) SEEN(1, 0)
            while (!got && !dry) { LOOK(2) SEEN(2, 0) SLEEP(0) }
            const double c0 = ldexpf(cf, (int)(x.y >> 16 & 255u) - 141), c1 = ldexpf(cf, (int)(x.w >> 16 & 255u) - 141);
            const double t0 = c0 * (double)(short)x.x, t1 = c0 * (double)((int)x.x >> 16), t2 = c0 * (double)(short)x.y, t3 = c1 * (double)(short)x.z, t4 = c1 * (double)((int)x.z >> 16), t5 = c1 * (double)(short)x.w;
#define SWADD(A, B) ({ const v2u a_ = __builtin_bit_cast(v2u, A), b_ = __builtin_bit_cast(v2u, B); \
                const auto l_ = __builtin_amdgcn_permlane32_swap(a_.x, b_.x, false, false); const auto h_ = __builtin_amdgcn_permlane32_swap(a_.y, b_.y, false, false); \
                __builtin_bit_cast(double, (v2u){l_[0], h_[0]}) + __builtin_bit_cast(double, (v2u){l_[1], h_[1]}); })
            const double s0 = SWADD(t0, t3), s1 = SWADD(t1, t4), s2 = SWADD(t2, t5);
            asm volatile("ds_add_f64 %0, %1\n\tds_add_f64 %0, %2 offset:512\n\tds_add_f64 %0, %3 offset:1024" :: "v"(lds_addr(ap)), "v"(s0), "v"(s1), "v"(s2) : "memory");
            ADDRTN(&sm.cdone)
            const double o0 = ap[0], o1 = ap[64], o2 = ap[128];
            ADDED(c)
            if (c == NSW - 1 || dry) {
                float f0 = o0, f1 = o1, f2 = o2;
                asm("" : "+v"(f0), "+v"(f1), "+v"(f2));
                const __amdgpu_buffer_rsrc_t ors = __builtin_amdgcn_make_buffer_rsrc(out + (size_t)head * DH, 0, DH * 2, 0x00020000);
                const unsigned oo = (lane & 31) * 12 + (lane >> 5) * 6 | oob;
                __builtin_amdgcn_raw_buffer_store_b16(__builtin_bit_cast(unsigned short, (__bf16)f0), ors, oo, 0, 0);
                __builtin_amdgcn_raw_buffer_store_b16(__builtin_bit_cast(unsigned short, (__bf16)f1), ors, oo + 2, 0, 0);
                __builtin_amdgcn_raw_buffer_store_b16(__builtin_bit_cast(unsigned short, (__bf16)f2), ors, oo + 4, 0, 0);
            }
            if (!dry) break;
            __builtin_amdgcn_s_setprio(0);
            continue;
        }
        const bool quart = head >> 4 == WQ;
        const unsigned vo = lane * 16 | oob;
        const unsigned ro = head * (NSPLIT * 512) + c * 1024, rx = (NH + (head & 15 ^ 1)) * (NSPLIT * 512) + c * 1024;
#define POLL(S, O) asm volatile("buffer_load_dwordx4 a[%c0:%c0+3], %1, %2, %3 offen sc1" :: "i"(16 + 4 * (S)), "v"(vo), "s"(rs_), "s"(O) : "memory");
#define TAKE(K) { \
                asm volatile("v_accvgpr_read_b32 %0, a[%c4]\n\tv_accvgpr_read_b32 %1, a[%c4+1]\n\tv_accvgpr_read_b32 %2, a[%c4+2]\n\tv_accvgpr_read_b32 %3, a[%c4+3]" \
                             : "=v"(x[K].x), "=v"(x[K].y), "=v"(x[K].z), "=v"(x[K].w) : "i"(16 + 4 * (K))); \
                bad |= (x[K].y >> 17 ^ t15) | (x[K].w >> 17 ^ t15); \
                const u64 bl = __builtin_amdgcn_ballot_w64((x[K].y & 0x10000u) != 0), be = __builtin_amdgcn_ballot_w64((x[K].w & 0x10000u) != 0); \
                L[K] = __uint_as_float(lane < 32 ? (unsigned)bl : (unsigned)(bl >> 32)); ex[K] = lane < 32 ? (unsigned)be : (unsigned)(be >> 32); \
                M = fmaxf(M, fmaxf(__uint_as_float((unsigned)bl), __uint_as_float((unsigned)(bl >> 32)))); }
#define SUMUP(K) { const float w_ = __builtin_amdgcn_exp2f(L[K] - M), c_ = ldexpf(w_, (int)(ex[K] & 255u) - 141); den += w_; \
                t[0] = fmaf(c_, (float)(short)x[K].x, t[0]); t[1] = fmaf(c_, (float)((int)x[K].x >> 16), t[1]); t[2] = fmaf(c_, (float)(short)x[K].y, t[2]); \
                t[3] = fmaf(c_, (float)(short)x[K].z, t[3]); t[4] = fmaf(c_, (float)((int)x[K].z >> 16), t[4]); t[5] = fmaf(c_, (float)(short)x[K].w, t[5]); }
        const unsigned tg = TLAST() + (quart ? TRQ : TRD);
        CGATE(tg)

        v4u x[4]; float L[4], M; unsigned ex[4];
        for (;;) {
            unsigned bad = 0; M = -3e38f;
            POLL(0, ro)
            if (quart) { POLL(1, rx) POLL(2, rx + GSZ * NSPLIT * 512) POLL(3, rx + 2 * GSZ * NSPLIT * 512) }
            __builtin_amdgcn_s_setprio(CPRIO);
            asm volatile("s_waitcnt vmcnt(0)" ::: "memory");
            TAKE(0)
            if (quart) { TAKE(1) TAKE(2) TAKE(3) }
            if (__builtin_amdgcn_ballot_w64(bad != 0) == 0 || dry) break;
            SLEEP(2)
        }
        float t[6] = {0.f, 0.f, 0.f, 0.f, 0.f, 0.f}, den = 0.f;
        SUMUP(0)
        if (quart) { SUMUP(1) SUMUP(2) SUMUP(3) }
#define SWADF(A, B) ({ const auto l_ = __builtin_amdgcn_permlane32_swap(__float_as_uint(A), __float_as_uint(B), false, false); __uint_as_float(l_[0]) + __uint_as_float(l_[1]); })
        v4f sum = {SWADF(t[0], t[3]), SWADF(t[1], t[4]), SWADF(t[2], t[5]), SWADF(den, den)};
        v4f* pb = (v4f*)((char*)&sm.cs[0][0][0] + 65536);
        float* pm = (float*)(pb + (NSW + 1) * 64);
        if (dry) c = NSW;
        pb[c * 64 + lane] = sum;
        if (lane == 0) pm[c] = M;
        CBAR();
        ADDRTN(&sm.cdone)
        ADDED(c)
        CBAR();
        if (c == NSW - 1 || dry) {
            const v4f ma = *(const v4f*)pm, mb = *(const v4f*)(pm + 4);
            v4f ps[NSW];
#pragma unroll
            for (int w = 0; w < NSW; w++) ps[w] = pb[w * 64 + lane];
            const float mw[NSW] = {ma.x, ma.y, ma.z, ma.w, mb.x, mb.y, mb.z, mb.w};
            float Mx = mw[0];
#pragma unroll
            for (int w = 1; w < NSW; w++) Mx = fmaxf(Mx, mw[w]);
            sum = (v4f){0.f, 0.f, 0.f, 0.f};
#pragma unroll
            for (int w = 0; w < NSW; w++) sum += ps[w] * __builtin_amdgcn_exp2f(mw[w] - Mx);
            const float inv = __builtin_amdgcn_rcpf(sum.w);
            const __amdgpu_buffer_rsrc_t ors = __builtin_amdgcn_make_buffer_rsrc(out + (size_t)head * DH, 0, DH * 2, 0x00020000);
            const unsigned oo = (lane & 31) * 12 + (lane >> 5) * 6 | oob;
            __builtin_amdgcn_raw_buffer_store_b16(__builtin_bit_cast(unsigned short, (__bf16)(sum.x * inv)), ors, oo, 0, 0);
            __builtin_amdgcn_raw_buffer_store_b16(__builtin_bit_cast(unsigned short, (__bf16)(sum.y * inv)), ors, oo + 2, 0, 0);
            __builtin_amdgcn_raw_buffer_store_b16(__builtin_bit_cast(unsigned short, (__bf16)(sum.z * inv)), ors, oo + 4, 0, 0);
        }
        if (!dry) break;
    }
}

extern "C" void kernel_launch(void* const* d_in, const int* in_sizes, int n_in,
                              void* d_out, int out_size, void* d_ws, size_t ws_size,
                              hipStream_t stream) {
    const unsigned short* q = (const unsigned short*)d_in[0];
    const int* qk = (const int*)d_in[1];
    const unsigned short* k_scales = (const unsigned short*)d_in[2];
    const unsigned short* k_zeros = (const unsigned short*)d_in[3];
    const int* qv = (const int*)d_in[4];
    const unsigned short* v_scales = (const unsigned short*)d_in[5];
    const unsigned short* v_zeros = (const unsigned short*)d_in[6];
    const unsigned short* q_cos = (const unsigned short*)d_in[7];
    const unsigned short* q_sin = (const unsigned short*)d_in[8];
    const unsigned short* k_cos = (const unsigned short*)d_in[9];
    const unsigned short* k_sin = (const unsigned short*)d_in[10];
    unsigned char* ws = (unsigned char*)d_ws;
    attn_kernel<<<NSPLIT * GSZ, NTHREADS, 0, stream>>>(q, qk, k_scales, k_zeros, qv, v_scales, v_zeros,
                                                       q_cos, q_sin, k_cos, k_sin, (__hip_bfloat16*)d_out, ws);
}
```

```cpp
#include <hip/hip_runtime.h>
#include <hip/hip_bf16.h>
#include <stdint.h>

#define DH 192
#define HALF 96
#define SKV 2048
#define NH 256
#define NSW 8
#define NWAVES (NSW + 4)
#define NTHREADS (64 * NWAVES)
#define BROWS 128
#define NSPLIT 16
#define GSZ 16
#define WQ 10
#define NU (NH / GSZ + 3)
#define NPC 1024
#define NRR (NH + 3 * GSZ)
#define NIN 8
#define NOUT 8
#define WALL (15 * 144 + 4 * 48)
#define SC1 16
#define NRND 48
#define RST 4352
#define TK 2
#define NMIX 3
#define TUNIT 311
#define T01 50
#define TMIX 7
#define TOFF 50
#define PSF 100
#define TGS 68
#define PSW (NPC / 2 + 28)
#define TG0 8
#define TG1 3
#define TWO -75
#define PSG (NPC / 2 + 60)
#define TH0 3
#define TH1 3
#define EARLY 10
#define LEADP 24
#define DPH 2
#define TA 210
#define TA2 70
#define CSH 2
#define NPRB 3
#define TMAP 520
#define MINV 96
#define VW 4
#define MASTER 203
#define TRES 190
#define TB1 1200
#define PER 300
#define RDD 100
#define LEAD 70
#define LHI 85
#define LOK 74
#define PSL0 124
#define PUBP -14
#define LJ 210
#define LOK2 75
#define NTRY 3
#define TFO 6
#define SL0 42.f
#define PST0 200
#define KC 4
#define TC0 1000
#define TCS 9
#define PRE0 3

#define KREL 12
#define PDEL 70
#define NCP 3
#define NRW (NRR + NCP * GSZ)
#define TRD 112
#define THD 55
#define TRQ 55
#define TCA 132
#define TCD 22
#define TWW 0
#define TWS 3
#define WS_EPOCH 0
#define WS_POST0 8192
#define WS_POST (WS_POST0 + ((unsigned)E1 & 15) * 2048)
#define WS_NEWS (49152 + 256)
#define WS_HELLO 59904
#define WS_BOARD 65536
#define WS_PART (2u << 20)
#define WS_SWAP (WS_PART + NRW * NSPLIT * 512)

typedef unsigned int v2u __attribute__((ext_vector_type(2)));
typedef unsigned int v16u __attribute__((ext_vector_type(16)));
typedef unsigned int v4u __attribute__((ext_vector_type(4)));
typedef int v4i __attribute__((ext_vector_type(4)));
typedef float v2f __attribute__((ext_vector_type(2)));
typedef float v4f __attribute__((ext_vector_type(4)));
typedef unsigned long long u64;

__device__ __forceinline__ float bflo(unsigned u) { return __uint_as_float(u << 16); }
__device__ __forceinline__ float bfhi(unsigned u) { return __uint_as_float(u & 0xffff0000u); }
__device__ __forceinline__ float bfr(float x) {
    __bf16 b = (__bf16)x;
    return __uint_as_float(((unsigned)__builtin_bit_cast(unsigned short, b)) << 16);
}

#define SLEEP(n) SLEEP_(n)
#define SLEEP_(n) asm volatile("s_sleep " #n "\n\tv_add_co_u32 %0, vcc, 1, %0\n\ts_cbranch_vccz 1f\n\ts_endpgm\n1:" : "+v"(spins) :: "vcc", "memory");
#define SPIN0 0xf0000000u
#define CBAR() asm volatile("" ::: "memory")

__device__ __forceinline__ void wave_sync() {
    __builtin_amdgcn_fence(__ATOMIC_RELEASE, "wavefront");
    __builtin_amdgcn_wave_barrier();
    __builtin_amdgcn_fence(__ATOMIC_ACQUIRE, "wavefront");
}
template <int CTRL>
__device__ __forceinline__ float dpp_add(float x) {
    int y = __builtin_amdgcn_update_dpp(0, __float_as_int(x), CTRL, 0xf, 0xf, true);
    return x + __int_as_float(y);
}
template <int CTRL>
__device__ __forceinline__ float dpp_max(float x) {
    int y = __builtin_amdgcn_update_dpp(__float_as_int(x), __float_as_int(x), CTRL, 0xf, 0xf, false);
    return fmaxf(x, __int_as_float(y));
}
__device__ __forceinline__ float row_sum(float x) { x = dpp_add<0x128>(x); x = dpp_add<0x124>(x); x = dpp_add<0x122>(x); return dpp_add<0x121>(x); }
__device__ __forceinline__ float row_max(float m) { m = dpp_max<0x128>(m); m = dpp_max<0x124>(m); m = dpp_max<0x122>(m); return dpp_max<0x121>(m); }
__device__ __forceinline__ float rdlane(float x, int l) { return __int_as_float(__builtin_amdgcn_readlane(__float_as_int(x), l)); }
__device__ __forceinline__ float wave_max(float m) { m = row_max(m); return fmaxf(fmaxf(rdlane(m, 0), rdlane(m, 16)), fmaxf(rdlane(m, 32), rdlane(m, 48))); }
__device__ __forceinline__ float wave_maxv(float m) {
    m = row_max(m);
    const auto a = __builtin_amdgcn_permlane16_swap(__float_as_uint(m), __float_as_uint(m), false, false);
    m = fmaxf(__uint_as_float(a[0]), __uint_as_float(a[1]));
    const auto b = __builtin_amdgcn_permlane32_swap(__float_as_uint(m), __float_as_uint(m), false, false);
    return fmaxf(__uint_as_float(b[0]), __uint_as_float(b[1]));
}
__device__ __forceinline__ float wave_sum(float x) { x = row_sum(x); return (rdlane(x, 0) + rdlane(x, 16)) + (rdlane(x, 32) + rdlane(x, 48)); }
__device__ __forceinline__ unsigned lds_ld(const unsigned* p) { return __builtin_amdgcn_readfirstlane(__hip_atomic_load(p, __ATOMIC_RELAXED, __HIP_MEMORY_SCOPE_WORKGROUP)); }
__device__ __forceinline__ unsigned lds_addr(const void* p) { return (unsigned)(size_t)(const __attribute__((address_space(3))) void*)p; }
#define DMA16(SRC, LDS) asm volatile("s_mov_b32 %0, m0\n\ts_mov_b32 m0, %2\n\ts_nop 0\n\tglobal_load_lds_dwordx4 %1, off\n\ts_mov_b32 m0, %0" : "=&s"(m0keep) : "v"(SRC), "s"(LDS) : "memory")
__device__ __forceinline__ void lds_st(unsigned* p, unsigned v) { __hip_atomic_store(p, v, __ATOMIC_RELAXED, __HIP_MEMORY_SCOPE_WORKGROUP); }
#define NOW() ((unsigned)__builtin_amdgcn_s_memrealtime())
__device__ __forceinline__ unsigned tdue(unsigned g) {
    return (g * TUNIT + min(g, 480u) * TK + min(g, 48u) * T01 + (min(g, 32u * NMIX) + min(max((int)g - NPC / 2, 0), 32 * NMIX)) * TMIX + (g >= PSF ? TGS * 32 : 0)) >> 5;
}

__device__ __forceinline__ unsigned unit_of(unsigned g) { const unsigned p = g & 511; return (g >> 9) * NU + (p >> 5) + min(max((int)p - 32 * WQ, 0) >> 3, 3); }
__device__ __forceinline__ unsigned first_of(unsigned e) { return 32 * e - 24 * min(max((int)e - WQ, 0), 4); }
#define QUARTER(E) ((E) - WQ < 4)
__device__ __forceinline__ unsigned went(unsigned e) { return 144 * e - 96 * min(max((int)e - WQ, 0), 4); }
__device__ __forceinline__ unsigned wunit(unsigned x) { x += 24 * NMIX; return x < 24 * WQ ? x / 24 : x < 24 * WQ + 32 ? WQ + (x - 24 * WQ) / 8 : (x + 64) / 24; }

#define ROTT 0xed8d2c3d2c35ed84ull, 0xba85aa165b03ac82ull, 0xee7efa489a38af71ull, 0xe932da1b0bab51d8ull, 0xfa96112d5fee971bull, 0x2c7e9ea5a544f071ull, 0x3d86093d5f0e810bull, 0xe932da1b0bab51d8ull, 0xfa96112d5fee971bull, 0xd834e62c5fa723e9ull, 0x1d7d1a3d2c061d88ull, 0xe932da1b0bab51d8ull, 0xfa96112d5fee971bull, 0xc834e12c0aaa4ec7ull, 0x3d86093d5f0e810bull, 0xe932da1b0bab51d8ull
static __device__ const u64 ROT[16] = {ROTT};

struct __attribute__((aligned(16))) Smem {
    unsigned cs[NSW][4][768];
    double acc[NOUT][DH];
    float ent[NOUT][BROWS];
    unsigned short wv[WALL];
    unsigned short wo[WALL];
    unsigned q[NIN][HALF];
    float2 kz[NIN][BROWS];
    float2 vz[NIN][BROWS];
    unsigned utab[64];
    unsigned line, gap;
    unsigned late, grab;
    unsigned rdy[NIN], cnt[NOUT];
    unsigned tabs, vrdy, swp;
    unsigned aline;
    unsigned tag_rdy, tag_lo, tag_hi, merged, cdone, cidx;
    unsigned slow;
    unsigned chead;
};

#define LDS_ADD(P, V) { u64 ex_; asm volatile("s_mov_b64 %0, exec\n\ts_mov_b64 exec, 1\n\tds_add_u32 %1, %2\n\ts_mov_b64 exec, %0" : "=&s"(ex_) : "v"(lds_addr(P)), "v"(V) : "memory"); }
#define LDS_OR(P, V) { u64 ex_; asm volatile("s_mov_b64 %0, exec\n\ts_mov_b64 exec, 1\n\tds_or_b32 %1, %2\n\ts_mov_b64 exec, %0" : "=&s"(ex_) : "v"(lds_addr(P)), "v"(V) : "memory"); }

__global__ __launch_bounds__(NTHREADS, 3) void attn_kernel(
    const unsigned short* __restrict__ q, const int* __restrict__ qk,
    const unsigned short* __restrict__ k_scales, const unsigned short* __restrict__ k_zeros,
    const int* __restrict__ qv,
    const unsigned short* __restrict__ v_scales, const unsigned short* __restrict__ v_zeros,
    const unsigned short* __restrict__ q_cos, const unsigned short* __restrict__ q_sin,
    const unsigned short* __restrict__ k_cos, const unsigned short* __restrict__ k_sin,
    __hip_bfloat16* __restrict__ out, unsigned char* __restrict__ ws)
{
    __shared__ Smem sm;
    const int tid = threadIdx.x;
    unsigned spins = SPIN0;
    const int lane = tid & 63;
    const int hw = __builtin_amdgcn_readfirstlane(tid >> 6);
    u64 t00; unsigned ka;
    asm volatile("s_memrealtime %0\n\ts_load_dword %1, %2, 0x40" : "=&s"(t00), "=&s"(ka) : "s"(__builtin_amdgcn_kernarg_segment_ptr()));
    if (hw == 0) {
        const char* pc0; asm volatile("s_getpc_b64 %0" : "=s"(pc0));
        asm volatile("global_load_dword a6, %0, %1" :: "v"(((blockIdx.x & ~7u) + (lane & 7)) * 128), "s"(pc0) : "a6", "memory");
    }
    const int wave = hw >= 4 ? hw - 4 : hw + NSW;
    const unsigned split = (blockIdx.x & 1) + (blockIdx.x & 2) * 4 + ((blockIdx.x >> 3) & 3) * 2;
    const unsigned side = (split ^ (blockIdx.x >> 2)) & 1, rank = blockIdx.x >> 5;
    const unsigned head0 = 2 * rank + side;
    const unsigned rf0 = (8 * split + 1) / 3, r3 = 8 * split + 1 - 3 * rf0;
    const unsigned koff = ((lane >> 5) * DH + 3 * (lane & 31)) * 4, koffb = koff + 384;
#define AR(S) (16 + (S) * 8)
#define GLD(A, OFF, BASE, IMM) asm volatile("global_load_dwordx3 a[%c0:%c1], %2, %3 offset:%4 nt" :: "i"(A), "i"((A) + 2), "v"(OFF), "s"(BASE), "i"(IMM))
#define ISSUE(S, J, BASE, OA, OB) { GLD(AR(S), OA, BASE, (J) * 1536); GLD(AR(S) + 4, OB, BASE, (J) * 1536); }
#define HEAD2 ((GSZ + 1 - 2 * side) * (SKV * DH * 4))
    const unsigned wo = (hw - 2) & 7;
    const bool pre = hw >= 4 && wo < PRE0;
    if (pre) {
        const char* a = (const char*)qk + (head0 * 512 + side * 256 + rf0 + wave + (wave >= 6 ? 80 : 0)) * 3072; const char* b = a + HEAD2;
        ISSUE(0, 0, a, koff, koffb) ISSUE(1, 1, a, koff, koffb) ISSUE(2, 0, b, koff, koffb) ISSUE(3, 1, b, koff, koffb)
    }
    asm volatile("s_waitcnt lgkmcnt(0)" : "+s"(t00), "+s"(ka));
    if (hw == 0) {
        sm.utab[lane] = ~0u;
        unsigned l0 = (unsigned)t00 + TOFF;
        if (KREL) { const unsigned t1 = NOW() + KREL; if ((int)(t1 - l0) > 0) l0 = t1; }
        if (lane < 32) (&sm.line)[lane] = lane == 3 ? 64u : (lane & 29) == 0 ? l0 : 0u;
        asm volatile("s_waitcnt lgkmcnt(0)" ::: "memory");
    }
    __builtin_amdgcn_s_barrier();
    CBAR();
    const u64 rtab = r3 == 1 ? 0x5756552c2b020100ull : r3 == 0 ? 0x5756552c2b2a0100ull : 0x57562d2c2b020100ull;
#define R8(I) (rf0 + ((unsigned)(rtab >> (8 * (I))) & 255u))
#define GATE(DUE) { int d_ = (int)((DUE) - NOW()); if (d_ < 3000) while (d_ > 0) { __builtin_amdgcn_s_sleep(1); d_ = (int)((DUE) - NOW()); } }
#define TLAST() (lds_ld(&sm.line) + tdue(NPC - 2) + (lds_ld(&sm.gap) >> 16))
    if (__builtin_expect(wave < NSW, 1)) {
        const int cst = 2 * side;
        const unsigned r8w = R8((unsigned)wave);
        const char* cbase[3];
#pragma unroll
        for (int j = 0; j < 3; j++) { const int x = j * 64 + lane; cbase[j] = (x < 96 ? (const char*)k_cos + x * 16 : (const char*)k_sin + (x - 96) * 16) + r8w * (4 * DH * 2); }
        unsigned m0keep;
#define CS_DMA(W, C) { _Pragma("unroll") for (int j = 0; j < 3; j++) DMA16(cbase[j] + (C) * (512 * DH * 2), lds_addr(&sm.cs[0][0][0]) + ((W) * 4 + (C)) * 3072 + j * 1024); }
#define REPACK(W, C) { \
        unsigned* cw = &sm.cs[W][C][0]; \
        const unsigned* l = cw + rsel * (DH / 2) + qoff; \
        unsigned r[2][6]; \
        _Pragma("unroll") \
        for (int j = 0; j < 2; j++) { \
            unsigned e[4][2]; \
            _Pragma("unroll") \
            for (int k = 0; k < 4; k++) { \
                const unsigned* a = l + j * DH + (k & 1) * 48 + (k >> 1) * 384;       \
                unsigned x0 = a[0], x1 = a[1]; \
                e[k][0] = __builtin_amdgcn_alignbit(x1, x0, sh); \
                e[k][1] = x1 >> sh; \
            } \
            r[j][0] = (e[0][0] & 0xffffu) | (e[3][0] << 16); \
            r[j][1] = (e[0][0] >> 16) | (e[3][0] & 0xffff0000u); \
            r[j][2] = (e[0][1] & 0xffffu) | (e[3][1] << 16); \
            r[j][3] = (e[1][0] & 0xffffu) | (e[2][0] << 16); \
            r[j][4] = (e[1][0] >> 16) | (e[2][0] & 0xffff0000u); \
            r[j][5] = (e[1][1] & 0xffffu) | (e[2][1] << 16); \
        } \
        wave_sync(); \
        _Pragma("unroll") \
        for (int j = 0; j < 2; j++) { *(v4u*)&cw[j * 256 + lane * 4] = (v4u){r[j][0], r[j][1], r[j][2], r[j][3]}; *(v2u*)&cw[512 + j * 128 + lane * 2] = (v2u){r[j][4], r[j][5]}; } \
        wave_sync(); }
        const int sub = lane & 31;
        const int rsel = lane >> 5;
        const unsigned sh = (sub & 1) * 16;
        const int qoff = 3 * (sub >> 1) + (sub & 1);
        const unsigned voff = 3 * lane * 4;
        asm volatile("" ::: "a16", "a17", "a18", "a19", "a20", "a21", "a22", "a23", "a24", "a25", "a26", "a27", "a28", "a29", "a30", "a31",
                            "a32", "a33", "a34", "a35", "a36", "a37", "a38", "a39", "a40", "a41", "a42", "a43", "a44", "a45", "a46", "a47");
        asm volatile("" ::: "a48", "a49", "a50", "a51", "a52", "a53", "a54", "a55", "a56", "a57", "a58", "a59", "a60", "a61", "a62", "a63",
                            "a64", "a65", "a66", "a67", "a68", "a69", "a70", "a71", "a72", "a73", "a74", "a75", "a76", "a77", "a78", "a79");
        int ta[4][3], tb[4][3];
#define WAITT(P, S, N) asm volatile("s_waitcnt vmcnt(%6)\n\tv_accvgpr_read_b32 %0, a[%c7]\n\tv_accvgpr_read_b32 %1, a[%c8]\n\tv_accvgpr_read_b32 %2, a[%c9]\n\t" \
                              "v_accvgpr_read_b32 %3, a[%c10]\n\tv_accvgpr_read_b32 %4, a[%c11]\n\tv_accvgpr_read_b32 %5, a[%c12]" \
                              : "=v"(ta[P][0]), "=v"(ta[P][1]), "=v"(ta[P][2]), "=v"(tb[P][0]), "=v"(tb[P][1]), "=v"(tb[P][2]) \
                              : "i"(N), "i"(AR(S)), "i"(AR(S) + 1), "i"(AR(S) + 2), "i"(AR(S) + 4), "i"(AR(S) + 5), "i"(AR(S) + 6))
        const unsigned voffb = voff + 768;
        u64 ln = 0;
#define TDUE(G) (tdue(G) + ((G) >= PSW ? __builtin_amdgcn_readfirstlane((unsigned)(ln >> 32)) >> ((G) >= PSG ? 16 : 0) & 0xffffu : 0u))
#define REPORT() { const unsigned t_ = now - tdc; if (lane == 0) lds_st(&sm.late, t_); }
#define LINE() ln = LDV((u64*)&sm.line);
#define LDV(P) __hip_atomic_load(P, __ATOMIC_RELAXED, __HIP_MEMORY_SCOPE_WORKGROUP)
        unsigned u = 0, rg = cst * 8 + wave, np = 0;
        CS_DMA(wave, cst)

        const char* kb0 = (const char*)qk + (size_t)(head0 * 512 + r8w) * 3072;
#define KB(I) (kb0 + ((cst + (I)) & 3) * (128 * 3072))
        const char* kb; const char* kb2;
#define SISSUE(I) { kb = KB(I); kb2 = kb + HEAD2; const unsigned du_ = lds_ld(&sm.line) + tdue(16 * (I) + 2 * wo); GATE(du_) \
            ISSUE(((I) & 1) * 4, 0, kb, koff, koffb) ISSUE(((I) & 1) * 4 + 1, 1, kb, koff, koffb) ISSUE(((I) & 1) * 4 + 2, 0, kb2, koff, koffb) ISSUE(((I) & 1) * 4 + 3, 1, kb2, koff, koffb) }
        if (!pre) SISSUE(0)
        CS_DMA(wave, (cst + 1) & 3)
        if (pre) asm volatile("s_waitcnt vmcnt(3)" ::: "memory");
        asm volatile("s_waitcnt vmcnt(11)" ::: "memory");
        if (!pre) LDS_ADD(&sm.slow, NOW() - lds_ld(&sm.line) - tdue(2 * wo))
        REPACK(wave, cst)
        SISSUE(1)
#define TABDONE(I) LDS_OR(&sm.tabs, 1u << ((((cst + (I)) & 3) * 8) + wave))
        TABDONE(0)
        const float scale = 0.07216878364870322f;
        double acc[3] = {0.0, 0.0, 0.0};
        bool first = true, chg, none = false;
        unsigned Qp[3], Qn[3], Rp[3], Rn[3];
        float kz4[4], ksc, kres[4];
        v4u ca[4]; v2u cb[4];
        v2f qe[4][3], A[4];
        unsigned vut, nrg;
        const unsigned vr = ((lane >> 3) & 3) * 128 + R8((unsigned)lane & 7);
        unsigned g, mg;
        const unsigned grab_adr = lds_addr(&sm.grab);
        asm volatile("v_accvgpr_write_b32 a13, 1\n\tv_accvgpr_write_b32 a14, 2" ::: "a13", "a14", "a15");
#define GRAB(N) { u64 ex_; asm volatile("s_mov_b64 %0, exec\n\ts_mov_b64 exec, 1\n\tds_add_rtn_u32 a15, %1, a%c2\n\ts_mov_b64 exec, %0" : "=&s"(ex_) : "v"(grab_adr), "i"(12 + (N)) : "memory"); }
#define GRABBED() { unsigned t_; asm volatile("s_waitcnt lgkmcnt(0)\n\tv_accvgpr_read_b32 %1, a15\n\ts_nop 0\n\tv_readfirstlane_b32 %0, %1\n\ts_nop 1" : "=s"(g), "=v"(t_)); }
        unsigned oa, ob, un, tdn, tiss, tdc = 0;
#define PLAN() { \
            GRABBED() un = unit_of(g); chg = un != u; none = g >= NPC; \
            const bool nv = g >= NPC / 2; \
            unsigned hd = __builtin_amdgcn_readlane(vut, un); \
            while (hd == ~0u) { SLEEP(1) hd = lds_ld(&sm.utab[un]); } \
            nrg = (g + (hd >> 8)) & 31; hd &= 255; \
            unsigned boff, boff2; asm("s_mul_i32 %0, %1, 0xc00" : "=s"(boff) : "s"(hd * 512 + __builtin_amdgcn_readlane(vr, nrg))); \
            asm("s_mul_i32 %0, %1, 0xc00" : "=s"(boff2) : "s"(hd * 512 + __builtin_amdgcn_readlane(vr, nrg + 1))); \
            const char* b_ = nv ? (const char*)qv : (const char*)qk; \
            kb = b_ + boff; kb2 = b_ + boff2; \
            oa = nv ? voff : koff; ob = nv ? voffb : koffb; \
            tdn = TDUE(g); }
#define FIRE() { ISSUE(0, 0, kb, oa, ob) ISSUE(1, 1, kb, oa, ob) ISSUE(2, 0, kb2, oa, ob) ISSUE(3, 1, kb2, oa, ob) }
#define LAUNCH() { \
            LINE() \
            const unsigned du_ = __builtin_amdgcn_readfirstlane((unsigned)ln) + tdn; \
            GATE(du_) \
            ISSUE(0, 0, kb, oa, ob) ISSUE(1, 1, kb, oa, ob) \
            GATE(du_ + 10) \
            ISSUE(2, 0, kb2, oa, ob) ISSUE(3, 1, kb2, oa, ob) tiss = NOW(); }
#define ARRIVED() \
            const unsigned now = NOW(); \
            const bool stall = now - tiss > LJ && g - PST0 < NPC - 48 - PST0; \
            const bool late = !none && !stall && (int)(now + EARLY - __builtin_amdgcn_readfirstlane((unsigned)ln) - tdn) >= 0; \
            if (late) { FIRE() tiss = now; } \
            if (stall && lane == 0) __hip_atomic_fetch_max(&sm.line, (unsigned)ln + (now - tiss) - LOK2, __ATOMIC_RELAXED, __HIP_MEMORY_SCOPE_WORKGROUP); \
            REPORT()
#define ADVANCE(ISV) { \
            if (chg) FLUSH(ISV) \
            if (!none && !late) LAUNCH() \
            first = chg; u = un; rg = nrg; tdc = tdn + 10; }
#define K_TAB(MODE) { _Pragma("unroll") for (int p = 0; p < ((MODE) ? 2 : 4); p++) { \
            const unsigned* cw = &sm.cs[(rg & 7) + (p >> 1)][rg >> 3][0]; \
            ca[p] = *(const v4u*)&cw[(p & 1) * 256 + lane * 4]; cb[p] = *(const v2u*)&cw[512 + (p & 1) * 128 + lane * 2]; } }
#define K_PRE(MODE, P) { \
        float e0, e1, e2, f0, f1, f2; \
        const unsigned* qp_ = ((MODE) && (P) >= 2) ? Rp : Qp; const unsigned* qn_ = ((MODE) && (P) >= 2) ? Rn : Qn; \
        const int tp_ = (MODE) ? (P) & 1 : (P); \
        asm("v_dot2_f32_bf16 %0, %6, %12, 0\n\tv_dot2_f32_bf16 %3, %9, %15, 0\n\tv_dot2_f32_bf16 %1, %7, %13, 0\n\tv_dot2_f32_bf16 %4, %10, %16, 0\n\t" \
            "v_dot2_f32_bf16 %2, %8, %14, 0\n\tv_dot2_f32_bf16 %5, %11, %17, 0\n\ts_nop 2" \
            : "=&v"(e0), "=&v"(e1), "=&v"(e2), "=&v"(f0), "=&v"(f1), "=&v"(f2) \
            : "v"(qp_[0]), "v"(qp_[1]), "v"(qp_[2]), "v"(qn_[0]), "v"(qn_[1]), "v"(qn_[2]), \
              "v"(ca[tp_].x), "v"(ca[tp_].y), "v"(ca[tp_].z), "v"(ca[tp_].w), "v"(cb[tp_].x), "v"(cb[tp_].y)); \
        qe[P][0] = (v2f){e0, f0}; qe[P][1] = (v2f){e1, f1}; qe[P][2] = (v2f){e2, f2}; \
        A[P] = (qe[P][0] + qe[P][1] + qe[P][2]) * (v2f){-kz4[P], -kz4[P]}; }
#define K_WAIT(MODE, P) { \
        if ((MODE) == 0) { WAITT(P, P, 6 - 2 * (P)); } \
        else { WAITT(P, (((MODE) - 1) & 1) * 4 + (P), ((MODE) < 3 ? 20 : (MODE) == 3 ? 17 : 6) - 2 * (P)); } }
#define K_FMA(P) { \
        _Pragma("unroll") \
        for (int i = 0; i < 3; i++) { v2f x = {(float)ta[P][i], (float)tb[P][i]}; A[P] = qe[P][i] * x + A[P]; } \
        kres[P] = A[P].x + A[P].y; }
#define K_RED(MODE) { \
        const bool o1 = lane & 1, o2 = lane & 2; \
        float s01 = (o1 ? kres[1] : kres[0]) + __int_as_float(__builtin_amdgcn_update_dpp(0, __float_as_int(o1 ? kres[0] : kres[1]), 0xb1, 0xf, 0xf, true)); \
        float s23 = (o1 ? kres[3] : kres[2]) + __int_as_float(__builtin_amdgcn_update_dpp(0, __float_as_int(o1 ? kres[2] : kres[3]), 0xb1, 0xf, 0xf, true)); \
        float sx = (o2 ? s23 : s01) + __int_as_float(__builtin_amdgcn_update_dpp(0, __float_as_int(o2 ? s01 : s23), 0x4e, 0xf, 0xf, true)); \
        sx = dpp_add<0x124>(sx); sx = dpp_add<0x128>(sx); \
        const auto sw = __builtin_amdgcn_permlane16_swap(__float_as_uint(sx), __float_as_uint(sx), false, false); \
        sx = __uint_as_float(sw[0]) + __uint_as_float(sw[1]); \
        if ((lane & 28) == 0) { if (MODE) sm.ent[(lane >> 1) & 1][rg * 4 + 2 * (lane & 1) + rsel] = sx * ksc; else sm.ent[u % NOUT][rg * 4 + 2 * (lane & 3) + rsel] = sx * ksc; } }
#define K_PRE4(MODE) { K_PRE(MODE, 0) K_PRE(MODE, 1) K_PRE(MODE, 2) K_PRE(MODE, 3) asm volatile("" :: "v"(A[0]), "v"(A[1]), "v"(A[2]), "v"(A[3])); }
#define K_BODY(MODE) { K_PRE4(MODE) K_WAIT(MODE, 0) K_FMA(0) K_WAIT(MODE, 1) K_FMA(1) K_WAIT(MODE, 2) K_FMA(2) K_WAIT(MODE, 3) K_FMA(3) K_RED(MODE) }
#define V_FMA(J, W0, W1) { \
        _Pragma("unroll") \
        for (int i = 0; i < 3; i++) { \
            acc[i] = fma(W0, (double)ta[J][i], acc[i]); \
            acc[i] = fma(W1, (double)tb[J][i], acc[i]); \
        } }
#define V_PIN() asm volatile("" :: "v"(acc[0]), "v"(acc[1]), "v"(acc[2]));
#define RDYWAIT(SL, SEQ) { while (lds_ld(&sm.rdy[SL]) != (SEQ)) SLEEP(1); CBAR(); }
#define LOADZ() { _Pragma("unroll") for (int p = 0; p < 4; p++) kz4[p] = sm.kz[u % NIN][rg * 4 + 2 * p + rsel].x; ksc = sm.kz[u % NIN][rg * 4 + 2 * (lane & 3) + rsel].y * scale; }
#define LOADQ() { _Pragma("unroll") for (int i = 0; i < 3; i++) { Qp[i] = sm.q[u % NIN][3 * sub + i]; Qn[i] = __builtin_amdgcn_alignbit(Qp[i], Qp[i], 16) ^ 0x80000000u; } }
#define FLUSH(ISV) { \
            mg = __builtin_amdgcn_readfirstlane(mg); \
            while (u >= mg + NOUT) { SLEEP(1); mg = lds_ld(&sm.merged); } \
            CBAR(); \
            if (ISV) { const unsigned aa = lds_addr(&sm.acc[u % NOUT][lane]); \
                asm volatile("ds_add_f64 %0, %1\n\tds_add_f64 %0, %2 offset:512\n\tds_add_f64 %0, %3 offset:1024" :: "v"(aa), "v"(acc[0]), "v"(acc[1]), "v"(acc[2]) : "memory"); \
                acc[0] = 0.0; acc[1] = 0.0; acc[2] = 0.0; } \
            CBAR(); \
            LDS_ADD(&sm.cnt[u % NOUT], np) \
            CBAR(); \
            np = 0; }

#define LOADZ2() { _Pragma("unroll") for (int p = 0; p < 4; p++) kz4[p] = sm.kz[p >> 1][rg * 4 + 2 * (p & 1) + rsel].x; ksc = sm.kz[(lane >> 1) & 1][rg * 4 + 2 * (lane & 1) + rsel].y * scale; }
#define STATIC_STEP(I) { \
            rg = ((cst + (I)) & 3) * 8 + wave; \
            LOADZ2() \
            K_TAB((I) + 1) K_BODY((I) + 1) }
        CS_DMA(wave, (cst + 2) & 3)
        RDYWAIT(0, 1u) RDYWAIT(1, 2u)
        LOADQ()
#pragma unroll
        for (int i = 0; i < 3; i++) { Rp[i] = sm.q[1][3 * sub + i]; Rn[i] = __builtin_amdgcn_alignbit(Rp[i], Rp[i], 16) ^ 0x80000000u; }
        STATIC_STEP(0)
        asm volatile("s_waitcnt vmcnt(11)" ::: "memory");
        REPACK(wave, (cst + 1) & 3)
        TABDONE(1)
        SISSUE(2)
        CS_DMA(wave, (cst + 3) & 3)
        STATIC_STEP(1)
        asm volatile("s_waitcnt vmcnt(11)" ::: "memory");
        REPACK(wave, (cst + 2) & 3)
        TABDONE(2)
        SISSUE(3)
        STATIC_STEP(2)
        asm volatile("s_waitcnt vmcnt(8)" ::: "memory");
        REPACK(wave, (cst + 3) & 3)
        TABDONE(3)
        GRAB(2)
        vut = LDV(&sm.utab[lane]);
        STATIC_STEP(3)
        CBAR();
        LDS_ADD(&sm.cnt[0], 4u) LDS_ADD(&sm.cnt[1], 4u)
        PLAN()
        LAUNCH()
        u = un; rg = nrg; tdc = tdn + 10;
        bool tabok = false;
        __builtin_amdgcn_s_setprio(2);
#pragma unroll 1
        while (u < NU) {
            GRAB(2)
            mg = LDV(&sm.merged);
            vut = LDV(&sm.utab[lane]);
            LINE()
            if (!tabok) {
                for (;;) { const unsigned tb_ = lds_ld(&sm.tabs); tabok = tb_ == ~0u; if (((tb_ >> rg) & 3u) == 3u) break; SLEEP(1) }
                CBAR();
            }
            K_TAB(0)
            if (first) {
                unsigned fl = LDV(&sm.rdy[u % NIN]);
                CBAR();
                LOADQ() LOADZ()
                if (__builtin_amdgcn_readfirstlane(fl) != u + 1) { RDYWAIT(u % NIN, u + 1) LOADQ() LOADZ() }
            } else LOADZ()
            K_PRE4(0)
            PLAN()
            K_WAIT(0, 0) K_FMA(0) K_WAIT(0, 1) K_FMA(1)
            asm volatile("" :: "v"(kres[0]), "v"(kres[1]));
            K_WAIT(0, 2) K_WAIT(0, 3)
            ARRIVED()
            K_FMA(2) K_FMA(3) K_RED(0)
            np += 2;
            ADVANCE(false)
        }
#pragma unroll 1
        while (u < 2 * NU) {
            GRAB(2)
            mg = LDV(&sm.merged);
            LINE()
            const unsigned vr_ = LDV(&sm.vrdy);
            CBAR();
            const unsigned short* wp = &sm.wv[went(u - NU) + (QUARTER(u - NU) ? rg & 7 : rg) * 4];
            v4i pw = *(const v4i*)wp;
            if (first && !((__builtin_amdgcn_readfirstlane(vr_) >> (u - NU)) & 1)) { while (!((lds_ld(&sm.vrdy) >> (u - NU)) & 1)) SLEEP(1); CBAR(); pw = *(const v4i*)wp; }
            const double w0 = (short)pw.x, w1 = pw.x >> 16, w2 = (short)pw.y, w3 = pw.y >> 16, w4 = (short)pw.z, w5 = pw.z >> 16, w6 = (short)pw.w, w7 = pw.w >> 16;
            asm volatile("" :: "v"(w0), "v"(w1), "v"(w2), "v"(w3), "v"(w4), "v"(w5), "v"(w6), "v"(w7));
            PLAN()
            v2f hl; unsigned tl8, rowl;
            if (none) { hl = *(const v2f*)&sm.wv[went(NU - 1) + 128]; tl8 = LDV(&sm.tag_lo) << 24; rowl = (__builtin_amdgcn_readlane(vut, 2 * NU - 1) & 255) * (NSPLIT * 512); asm volatile("" : "+v"(hl), "+v"(tl8)); }
            WAITT(0, 0, 6); V_FMA(0, w0, w1) V_PIN()
            WAITT(1, 1, 4); V_FMA(1, w2, w3) V_PIN()
            WAITT(2, 2, 2); V_FMA(2, w4, w5) V_PIN()
            WAITT(3, 3, 0);
            np += 2;
            if (none) {
                V_FMA(3, w6, w7)
                if (u != 2 * NU - 1) { FLUSH(true) break; }
                mg = __builtin_amdgcn_readfirstlane(mg);
                while (u >= mg + NOUT) { SLEEP(1); mg = lds_ld(&sm.merged); }
                const unsigned aa = lds_addr(&sm.acc[(2 * NU - 1) % NOUT][lane]);
                double a0, a1, a2; unsigned cn; u64 ex_;
                asm volatile("ds_add_f64 %5, %6\n\tds_add_f64 %5, %7 offset:512\n\tds_add_f64 %5, %8 offset:1024\n\t"
                             "s_mov_b64 %4, exec\n\ts_mov_b64 exec, 1\n\tds_add_rtn_u32 %3, %9, %10\n\ts_mov_b64 exec, %4\n\t"
                             "ds_read_b64 %0, %5\n\tds_read_b64 %1, %5 offset:512\n\tds_read_b64 %2, %5 offset:1024\n\ts_waitcnt lgkmcnt(0)"
                             : "=&v"(a0), "=&v"(a1), "=&v"(a2), "=&v"(cn), "=&s"(ex_) : "v"(aa), "v"(acc[0]), "v"(acc[1]), "v"(acc[2]), "v"(lds_addr(&sm.cnt[(2 * NU - 1) % NOUT])), "v"(np) : "memory");
                const unsigned vo = __builtin_amdgcn_readfirstlane(cn) + np == 32 ? lane * 8 : 0x40000000u;
                const float n0 = (float)(a0 - (double)hl.y) * hl.x, n1 = (float)(a1 - (double)hl.y) * hl.x, n2 = (float)(a2 - (double)hl.y) * hl.x;
                const unsigned e2 = max(__float_as_uint(fmaxf(fmaxf(fabsf(n0), fabsf(n1)), fabsf(n2))) >> 23, 14u);
                const float f2 = __uint_as_float((268u - e2) << 23);
                const v2u r = {__builtin_bit_cast(unsigned, __builtin_amdgcn_cvt_pk_i16((int)rintf(n0 * f2), (int)rintf(n1 * f2))),
                               __builtin_bit_cast(unsigned, __builtin_amdgcn_cvt_pk_i16((int)rintf(n2 * f2), 0)) | e2 << 16 | tl8};
                const __amdgpu_buffer_rsrc_t rsrc = __builtin_amdgcn_make_buffer_rsrc(ws + WS_PART + split * 512, 0, NRW * NSPLIT * 512 - split * 512, 0x00020000);
                __builtin_amdgcn_raw_buffer_store_b64(r, rsrc, vo, rowl, SC1);
#pragma unroll
                for (int j = 0; j < NCP - 1; j++) __builtin_amdgcn_raw_buffer_store_b64(r, rsrc, vo, (rowl & 15 * NSPLIT * 512 ^ NSPLIT * 512) + (NRR + j * GSZ) * NSPLIT * 512, SC1);
                if (blockIdx.x == 0 && vo < 4096) { const unsigned X = min((NOW() - TLAST()) / 4u, 99u) * 400; while (NOW() - (unsigned)t00 < 15000 + X) SLEEP(1) }
                break;
            }
            ARRIVED()
            V_FMA(3, w6, w7)
            ADVANCE(true)
        }
    } else {
    u64 E1;
    if (wave == NSW) {
        unsigned ref;
        const unsigned sidx = (blockIdx.x & 7) | ((blockIdx.x >> 7) << 3);
#define SAMPLE(K) ((((blockIdx.x >> 3) - (blockIdx.x & 7) - (K) * 5) & 15) == 0)
#define BTAG(K) ((btag + (K)) & 0xfffffu)
#define POST(K, C) { if (lane == 0) __hip_atomic_store((u64*)(ws + (bd + (K) * RST + (sidx >> 3) * 256 + (sidx & 7) * 8)), (u64)(C) | ((u64)(((C) - ref) | BTAG(K) << 12) << 32), __ATOMIC_RELAXED, __HIP_MEMORY_SCOPE_AGENT); }
        const unsigned own = lds_ld(&sm.line);
        if (SAMPLE(0) && lane < NTRY) __hip_atomic_store((unsigned*)(ws + WS_HELLO + lane * 768) + sidx, own, __ATOMIC_RELAXED, __HIP_MEMORY_SCOPE_AGENT);
        const u64 E = __hip_atomic_load((const u64*)(ws + WS_EPOCH), __ATOMIC_RELAXED, __HIP_MEMORY_SCOPE_AGENT);
        E1 = E + 1;
        if (lane == 0) { sm.tag_lo = (unsigned)E1; sm.tag_hi = (unsigned)(E1 >> 32); CBAR(); lds_st(&sm.tag_rdy, 1u); }
        if (TWW && blockIdx.x >> 3 == 5 && lane < 3)
            __hip_atomic_store((u64*)(ws + WS_PART + (lane == 0 ? 0 : lane == 1 ? (NRW - 1) * NSPLIT * 512 + 128 : (4u << 20) + 65536) + (blockIdx.x & 7) * 512), ~(E1 << 49), __ATOMIC_RELAXED, __HIP_MEMORY_SCOPE_AGENT);
        const unsigned btag = __builtin_amdgcn_readfirstlane((unsigned)E1) * 61u, bd = WS_BOARD + (btag & 7) * (NRND * RST);
        ref = own;
        int off = 0;
        {
            unsigned tries = 0, cur = own, tl = own;
            bool news = false;
#pragma unroll 1
            while (!news || cur != ref) {
                const unsigned now = NOW();
                if (tries < NTRY && (int)(now - own) >= (int)(TA + tries * TA2)) {
                    v16u x;
                    asm volatile("s_load_dwordx16 %0, %1, %2\n\ts_waitcnt lgkmcnt(0)" : "=&s"(x) : "s"(ws), "s"(__builtin_amdgcn_readfirstlane(WS_HELLO + tries * 768)) : "memory");
                    unsigned bad = 0; int sum = 0, dif = 0;
#pragma unroll
                    for (int i = 0; i < 16; i++) { bad |= x[i] - own + 512; sum += x[i] - x[0]; dif += i & 4 ? x[i] : -x[i]; }
                    tries++;
                    if (bad < 1024) { const unsigned com = x[0] + (sum >> 4) + abs(dif) * (8 - KC) / 128; ref = x[0] + ((sum + (blockIdx.x & 4 ? dif : -dif)) >> 4); off = ref - com;
                        tries = NTRY; tl = NOW(); if (lane == 0) lds_st(&sm.aline, com); }
                    continue;
                }
                if (!news && lds_ld(&sm.utab[31]) != ~0u) { news = true; CBAR(); if (cur == ref) tl = now; ref = lds_ld(&sm.aline) + off; tries = NTRY; }
                const int d = (now - tl) >> CSH;
                if (cur != ref && d) { tl += d << CSH; cur += min(max((int)(ref - cur), -d), d); if (lane == 0) lds_st(&sm.line, cur); }
                SLEEP(2)
            }
        }
        ref -= off;
        const unsigned ref0 = ref;
        const unsigned lead = (blockIdx.x >> 3) == 9 ? 0 : LEAD;
        unsigned posted = ref, lagf = ref, tpost = ref + TB1, tpoll = tpost + RDD + lead, kw = 0, kr = 0, cur = ref + off, khold = 0;
        int o = off; bool held = false;
#pragma unroll 1
        for (;;) {
            const unsigned p = lds_ld(&sm.grab);
            if (p >= NPC) break;
            lagf += (int)(lds_ld(&sm.late) - lagf) >> 1;
            { const unsigned l = lds_ld(&sm.line); if (l != cur) { cur = l; khold = kw; held = true; } }
            const unsigned now = NOW();
            if (o) { o = off * min(max((int)(ref0 + TC0 + (1 << TCS) - now), 0), 1 << TCS) >> TCS; if (!held && cur != ref + o) { cur = ref + o; if (lane == 0) lds_st(&sm.line, cur); } }
            if (kw < NRND && (int)(now - tpost) >= 0) {
                const unsigned c = ref + min((p >= PSL0 && (int)(lagf - cur) > LHI ? lagf - LOK : cur) - o - ref, 4000u);
                if (SAMPLE(kw) && (c != ref || (ref != posted && (sidx & 7) == (kw & 7)))) POST(kw, c)
                posted = ref;
                tpost += PER; kw++;
            }
            if (kr < NRND && (int)(now - tpoll) >= 0) {
                v16u xa, xb;
                asm volatile("s_load_dwordx16 %0, %2, %3\n\ts_load_dwordx16 %1, %2, %4\n\ts_waitcnt lgkmcnt(0)" : "=&s"(xa), "=&s"(xb)
                             : "s"(ws), "s"(__builtin_amdgcn_readfirstlane(bd + kr * RST)), "s"(__builtin_amdgcn_readfirstlane(bd + kr * RST + 256)) : "memory");
                int nb = 0, sg = 0;
#pragma unroll
                for (int i = 0; i < 16; i++) {
                    const unsigned lo = i < 8 ? xa[2 * i & 15] : xb[2 * i & 15], hi = i < 8 ? xa[(2 * i + 1) & 15] : xb[(2 * i + 1) & 15];
                    if (hi >> 12 == BTAG(kr) && lo - ref0 < 8000) { nb = max(nb, (int)(lo - (hi & 4095u) - ref)); sg += hi & 4095u; }
                }
                ref += nb + (sg >> 4);
                if (cur != ref + o && (kr >= khold || (int)(ref + o - cur) > 0)) { cur = ref + o; held = false; if (lane == 0) lds_st(&sm.line, cur); }
                tpoll += PER; kr++;
            }
            SLEEP(2)
        }
    } else if (wave == NSW + 1 || wave == NSW + 2) {
        const int ql = lane < 48 ? lane : 47;
        unsigned c1, c2, s1, s2;
#define QT_LOADS() { int qo_ = ql; asm volatile("" : "+v"(qo_)); \
                     c1 = ((const unsigned*)q_cos)[qo_]; c2 = ((const unsigned*)q_cos)[48 + qo_]; s1 = ((const unsigned*)q_sin)[qo_]; s2 = ((const unsigned*)q_sin)[48 + qo_]; }
        unsigned q1, q2, lkz, lks, lvz, lvs;
        const unsigned sofs = R8(((unsigned)lane >> 1) & 7) * 2 + (lane & 1);
#define UNIT_LOADS(HEAD, GRP) { \
        q1 = ((const unsigned*)(q + (size_t)(HEAD) * DH))[ql]; q2 = ((const unsigned*)(q + (size_t)(HEAD) * DH))[48 + ql]; \
        const size_t so = (size_t)(HEAD) * (SKV / 2) + (GRP) * 256 + sofs; \
        lkz = *((const unsigned*)k_zeros + so); lks = *((const unsigned*)k_scales + so); \
        lvz = *((const unsigned*)v_zeros + so); lvs = *((const unsigned*)v_scales + so); }
#define UNIT_FILL(SL, U) { \
            v4f qq; \
            qq.x = bfr(bfr(bflo(q1) * bflo(c1)) + bfr(-bflo(q2) * bflo(s1))); \
            qq.y = bfr(bfr(bflo(q2) * bflo(c2)) + bfr(bflo(q1) * bflo(s2))); \
            qq.z = bfr(bfr(bfhi(q1) * bfhi(c1)) + bfr(-bfhi(q2) * bfhi(s1))); \
            qq.w = bfr(bfr(bfhi(q2) * bfhi(c2)) + bfr(bfhi(q1) * bfhi(s2))); \
            *(v2u*)&sm.q[SL][2 * ql] = (v2u){(__float_as_uint(qq.x) >> 16) | __float_as_uint(qq.y), (__float_as_uint(qq.z) >> 16) | __float_as_uint(qq.w)}; \
            *(v4f*)&sm.kz[SL][2 * lane] = (v4f){bflo(lkz), bflo(lks), bfhi(lkz), bfhi(lks)}; \
            *(v4f*)&sm.vz[SL][2 * lane] = (v4f){bflo(lvz), bflo(lvs), bfhi(lvz), bfhi(lvs)}; \
            CBAR(); \
            if (lane == 0) lds_st(&sm.rdy[SL], (U) + 1); \
            CBAR(); }
        const unsigned dw = NSW + 2 - wave;
#define TOUCH() { const unsigned x_ = ((blockIdx.x >> 3) - 18) * 64 + lane; \
            const char* p_ = x_ < 384 ? (const char*)(x_ < 192 ? qk : qv) + (size_t)(x_ < 192 ? x_ : x_ - 192) * (2u << 20) + (x_ * 8 + (blockIdx.x & 7)) * 640 : x_ < 408 ? (const char*)out + (x_ - 384) * 4096 : (const char*)ws + WS_PART + (x_ - 408) * (2u << 20) + (blockIdx.x & 7) * 1280; \
            if (x_ < 408 + TWS) asm volatile("global_load_dword a0, %0, off nt" :: "v"(p_) : "a0"); }
        QT_LOADS()
        UNIT_LOADS(head0 + dw * (GSZ + 1 - 2 * side), lane >> 4)
        if (dw) TOUCH()
#pragma unroll 1
        for (unsigned u = dw;;) {
            while (u >= lds_ld(&sm.merged) + NIN) SLEEP(4);
            CBAR();
            UNIT_FILL(u % NIN, u)
            u += 2;
            if (u >= NU) break;
            if (u == 4) {
                const unsigned tpf = tdue(PSF - 1) + TFO;
                { int d; while ((d = (int)(lds_ld(&sm.line) + tpf - NOW())) > 0 && d < 3000) SLEEP(1) }
#define PF3(P) asm volatile("global_load_dwordx4 a[0:3], %0, %1\n\tglobal_load_dwordx4 a[0:3], %0, %1 offset:1024\n\tglobal_load_dwordx4 a[0:3], %0, %1 offset:2048" \
                            :: "v"(64 * SKV * 2 + blockIdx.x * 3072 + lane * 16), "s"(P) : "a0", "a1", "a2", "a3")
                PF3(k_zeros); PF3(k_scales); PF3(v_zeros); PF3(v_scales);
                asm volatile("global_load_dword a0, %0, %1" :: "v"(blockIdx.x * (DH * 2) + ql * 8), "s"(q) : "a0");
            }
            const unsigned tin = tdue(first_of(u) - (QUARTER(u) ? 24 : 0) - LEADP + (split & 7) * DPH);
            { int d; while ((d = (int)(lds_ld(&sm.line) + tin - NOW())) > 0 && d < 3000) SLEEP(4) }
            unsigned hd;
            while ((hd = lds_ld(&sm.utab[u])) == ~0u) SLEEP(2)
            UNIT_LOADS(hd & 255, QUARTER(u) ? rank & 3 : lane >> 4)
        }
        while (lds_ld(&sm.tag_rdy) == 0) SLEEP(1);
        CBAR();
        E1 = (u64)lds_ld(&sm.tag_lo) | ((u64)lds_ld(&sm.tag_hi) << 32);
        if (wave == NSW + 1 && blockIdx.x == 0) {
            for (;;) {
                bool ok = true;
#pragma unroll
                for (int i = 0; i < NH / 64; i++) ok &= __hip_atomic_load((const u64*)(ws + WS_POST) + i * 64 + lane, __ATOMIC_RELAXED, __HIP_MEMORY_SCOPE_AGENT) >> 24 == (E1 << 24) >> 24;
                if (__builtin_amdgcn_ballot_w64(ok) == ~0ull) break;
                SLEEP(64);
            }
            if (lane == 0) __hip_atomic_store((u64*)(ws + WS_EPOCH), E1, __ATOMIC_RELAXED, __HIP_MEMORY_SCOPE_AGENT);
        }
        if (wave == NSW + 2) {
            const unsigned t16 = (unsigned)E1 & 0xffffu, t16b = ((unsigned)E1 + ((unsigned)E1 >> 16)) & 0xffffu;
            const __amdgpu_buffer_rsrc_t rsrc = __builtin_amdgcn_make_buffer_rsrc(ws + WS_SWAP + (blockIdx.x ^ 4) * 5120, 0, 5120, 0x00020000);
            const unsigned swp = lds_ld(&sm.swp);
            if (swp) {
                unsigned use = 0;
#pragma unroll
                for (int i = 0; i < 5; i++) use |= ((swp >> wunit(i * 64 + lane)) & 1) << i;
                const unsigned tg = tdue(PSG - 1) + (lds_ld(&sm.gap) & 0xffffu);
                while ((int)(NOW() - lds_ld(&sm.line) - tg) < 0) SLEEP(2)
                for (;;) {
                    v4u d[5]; bool ok = true;
#pragma unroll
                    for (int i = 0; i < 5; i++) d[i] = __builtin_amdgcn_raw_buffer_load_b128(rsrc, (use >> i) & 1 ? (i * 64 + lane) * 16 : 0x40000000, 0, SC1 | 2);
#pragma unroll
                    for (int i = 0; i < 5; i++) if ((use >> i) & 1) {
                        ok &= d[i].y >> 16 == t16 && d[i].w >> 16 == t16b;
                        unsigned* w = (unsigned*)&sm.wv[144 * NMIX + (i * 64 + lane) * 6];
                        w[0] = d[i].x; w[1] = (d[i].y & 0xffffu) | (d[i].z << 16); w[2] = (d[i].z >> 16) | (d[i].w << 16);
                    }
                    if (__builtin_amdgcn_ballot_w64(ok) == ~0ull) break;
                    SLEEP(32)
                }
                CBAR();
                LDS_OR(&sm.vrdy, swp)
            }
        }
    } else {
        const unsigned eu = lane >= NU ? lane - NU : lane, win = min(first_of(eu) >> 5, 15u), pair = QUARTER(eu) ? 2 * (eu - WQ) + (rank >> 2) : rank;
        const unsigned rot2 = ((const unsigned char*)ROT)[win * 8 + pair];
#define UENT(BIT) (win * 16 + 2 * pair + ((BIT) ^ side) | (QUARTER(eu) ? (rank - (eu - WQ)) * 8 & 31 : (rot2 >> ((BIT) ^ side) * 4 & 15) * 2) << 8)
        unsigned far = 0;
        const unsigned hp = (blockIdx.x & 1) * NH + (blockIdx.x >> 1) * 4 + 2;
        if (blockIdx.x < 144) {
            const char* pb = (blockIdx.x < 128 ? (const char*)(hp >= NH ? qv : qk) + (size_t)(hp & (NH - 1)) * (SKV * DH * 4) + SKV * DH * 2
                                               : (const char*)ws + WS_PART + (8u << 20) + (blockIdx.x & 15) * 65536) + ((NOW() * 0x9e3779b1u) >> 26) * 128;
            unsigned mn[2] = {~0u, ~0u};
#pragma unroll 1
            for (unsigned i = 1; i < 2 * NPRB + 2; i++) {
                u64 t0, t1; unsigned dm;
                asm volatile("s_memtime %0\n\ts_waitcnt lgkmcnt(0)\n\ts_load_dword %2, %3, 0x0\n\ts_waitcnt lgkmcnt(0)\n\ts_memtime %1\n\ts_waitcnt lgkmcnt(0)"
                             : "=&s"(t0), "=&s"(t1), "=&s"(dm) : "s"(pb + i * 8192) : "memory");
                const unsigned t = (unsigned)(t1 - t0) < 330 || i == 1 ? ~0u : (unsigned)(t1 - t0);
                if (i & 1) mn[1] = min(mn[1], t); else mn[0] = min(mn[0], t);
            }
            far = mn[0] > mn[1];
        }
        if (lane < NMIX) lds_st(&sm.utab[lane], UENT(lane & 1));
        while (lds_ld(&sm.tag_rdy) == 0) SLEEP(1);
        CBAR();
        E1 = (u64)lds_ld(&sm.tag_lo) | ((u64)lds_ld(&sm.tag_hi) << 32);
        {
            const unsigned own = lds_ld(&sm.line), tg = (unsigned)E1;
            if (lane == 0) __hip_atomic_store((u64*)(ws + WS_POST) + blockIdx.x, E1 << 24 | min(lds_ld(&sm.slow), 4000u) << 1 | ((far ^ (blockIdx.x >> 2) ^ (blockIdx.x < 128 ? 0x96 >> (hp & 7) : 0)) & 1), __ATOMIC_RELAXED, __HIP_MEMORY_SCOPE_AGENT);
            const __amdgpu_buffer_rsrc_t nrs = __builtin_amdgcn_make_buffer_rsrc(ws + WS_NEWS, 0, 8 * 1280, 0x00020000);
            if (blockIdx.x == MASTER) {
                while ((int)(NOW() - own) < TMAP) SLEEP(2)
                const unsigned a = tg << 24, b = (unsigned)(E1 >> 8);
                v4u x; v2u z; u64 v0, v1;
#pragma unroll 1
                for (int i = 0; i < 8; i++) {
                    x = __builtin_amdgcn_raw_buffer_load_b128(__builtin_amdgcn_make_buffer_rsrc(ws + WS_POST, 0, 1024, 0x00020000), lane * 16, 0, SC1);
                    z = __builtin_amdgcn_raw_buffer_load_b64(__builtin_amdgcn_make_buffer_rsrc(ws + WS_POST + 1024, 0, 128, 0x00020000), lane * 8, 0, SC1);
                    v0 = __builtin_amdgcn_ballot_w64(((x.x ^ a) >> 24 | (x.y ^ b)) == 0); v1 = __builtin_amdgcn_ballot_w64(((x.z ^ a) >> 24 | (x.w ^ b)) == 0);
                    if (__builtin_popcountll(v0) + __builtin_popcountll(v1) >= MINV) break;
                    SLEEP(8)
                }
                const u64 m0 = __builtin_amdgcn_ballot_w64(x.x & 1) & v0, m1 = __builtin_amdgcn_ballot_w64(x.z & 1) & v1;
                const int s0 = lane < 43 ? min(max(lane - VW, 4 * NMIX), 42 - 2 * VW) : min(max(lane - VW, 43), 63 - 2 * VW);
#define PC(M) __builtin_popcount((unsigned)((M) >> s0) & ((2u << 2 * VW) - 1))
                const u64 g0 = __builtin_amdgcn_ballot_w64(2 * PC(m0) > PC(v0)), g1 = __builtin_amdgcn_ballot_w64(2 * PC(m1) > PC(v1));
                const float sl = wave_sum(((v0 >> lane & 1) ? (float)(x.x >> 1 & 4095u) : 0.f) + ((v1 >> lane & 1) ? (float)(x.z >> 1 & 4095u) : 0.f)) / ((8.f - PRE0) * (__builtin_popcountll(v0) + __builtin_popcountll(v1)) + 1.f);
                const unsigned al = lds_ld(&sm.aline), mean = (al ? al : own) + (int)fmaxf(sl - SL0, 0.f);
                const bool vz = lane < 16 && ((z.x ^ a) >> 24 | (z.y ^ b)) == 0;
                const unsigned gw = 2 * __builtin_popcountll(__builtin_amdgcn_ballot_w64(vz && (z.x & 1))) > __builtin_popcountll(__builtin_amdgcn_ballot_w64(vz));
                const unsigned k = lane & 3;
                if (lane < 32 && k < 3) __builtin_amdgcn_raw_buffer_store_b128((v4u){k == 0 ? (unsigned)g0 : k == 1 ? (unsigned)g1 : mean, tg, k == 0 ? (unsigned)(g0 >> 32) : k == 1 ? (unsigned)(g1 >> 32) : gw, tg},
                                                                              nrs, (lane >> 2) * 1280 + k * 16, 0, SC1);
            }
            while ((int)(NOW() - own) < TMAP + TRES) SLEEP(2)
            v4u y;
            for (;;) {
                y = __builtin_amdgcn_raw_buffer_load_b128(nrs, lane < 3 ? (blockIdx.x & 7) * 1280 + lane * 16 : 0x40000000, 0, SC1);
                if (((unsigned)__builtin_amdgcn_ballot_w64(y.y == tg && y.w == tg) & 7u) == 7u) break;
                SLEEP(8)
            }
            const u64 f0 = (unsigned)__builtin_amdgcn_readlane(y.x, 0) | (u64)(unsigned)__builtin_amdgcn_readlane(y.z, 0) << 32, f1 = (unsigned)__builtin_amdgcn_readlane(y.x, 1) | (u64)(unsigned)__builtin_amdgcn_readlane(y.z, 1) << 32;
            if (lane == 2) { sm.aline = y.x - own + 500 < 1000 ? y.x : own; sm.chead = (blockIdx.x & ~5u) | ((blockIdx.x >> 2 ^ y.z) & 1) | (blockIdx.x & 1) << 2; }
            unsigned bit = ((unsigned)((lane >= NU ? f1 : f0) >> (4 * win + (pair >> 1))) ^ (0x66 >> pair)) & 1;
            if (eu < NMIX) bit = eu & 1;
            const u64 bm = __builtin_amdgcn_ballot_w64(bit);
            const unsigned nsw = __builtin_popcountll((f0 ^ f1) >> (4 * NMIX));
            if (lane == 0) { sm.swp = (unsigned)(bm ^ (bm >> NU)) & ((1u << NU) - 1); lds_st(&sm.gap, nsw ? (TG0 + TG1 * nsw / 8) * 0x10001u + ((TH0 + TH1 * nsw / 8) << 16) : 0u); }
            CBAR();
            lds_st(&sm.utab[lane], UENT(bit));
        }
        const unsigned swp = lds_ld(&sm.swp);
        const unsigned T = lds_ld(&sm.tag_lo);
        const unsigned t15 = T & 0x7fffu;
        const __amdgpu_buffer_rsrc_t rsrc = __builtin_amdgcn_make_buffer_rsrc(ws + WS_PART + split * 512, 0, NRW * NSPLIT * 512 - split * 512, 0x00020000);
        v2u* recs = (v2u*)&sm.cs[0][0][0];
#pragma unroll
        for (int i = 0; i < NOUT * 3; i++) (&sm.acc[0][0])[i * 64 + lane] = 0.0;
        CBAR();
        unsigned vut = sm.utab[NU + (lane & 31)] & 255;
        if (QUARTER((unsigned)lane) && (rank & 3)) vut = NH - 16 + (rank & 3) * 16 + ((vut & 15) ^ 1);
        __builtin_amdgcn_s_setprio(3);
        unsigned po[NU / 2];
#pragma unroll
        for (int k = 0; k < NU / 2; k++) po[k] = __builtin_amdgcn_ds_bpermute((2 * k + (lane >> 5)) * 4, vut) * (NSPLIT * 512) + (lane & 31) * 16;
        const u64 rb_ = (u64)(ws + WS_PART + split * 512);
        const v4u rs_ = {(unsigned)rb_, (unsigned)(rb_ >> 32) & 0xffffu, NRW * NSPLIT * 512 - split * 512, 0x00020000u};
#pragma unroll 1
        for (unsigned u = 0; u < 2 * NU - 1; u++) {
            const unsigned e = u % NU, so = u % NOUT;
            if (u >= NU) while (!((lds_ld(&sm.vrdy) >> e) & 1)) SLEEP(4)
            CBAR();
            const v4f h = *(const v4f*)&sm.wv[went(e) + (QUARTER(e) ? 32 : 128)];
            while (lds_ld(&sm.cnt[so]) != (QUARTER(e) ? 8 : 32)) SLEEP(1)
            CBAR();
            const double a0 = sm.acc[so][lane], a1 = sm.acc[so][64 + lane], a2 = sm.acc[so][128 + lane];
            if (u < NU) {
                float2 lg = *(const float2*)&sm.ent[so][2 * lane];
                if (QUARTER(e) && lane >> 4 != (rank & 3)) lg = make_float2(-INFINITY, -INFINITY);
                const v4f zs = *(const v4f*)&sm.vz[u % NIN][2 * lane];
                const float m = wave_max(fmaxf(lg.x, lg.y));
                const float p0 = __expf(lg.x - m), p1 = __expf(lg.y - m);
                const float x0 = p0 * zs.y, x1 = p1 * zs.w;
                const unsigned eb = max(__float_as_uint(wave_max(fmaxf(fabsf(x0), fabsf(x1)))) >> 23, 14u);
                const float f = __uint_as_float((268u - eb) << 23);
                const float w0 = fminf(fmaxf(rintf(x0 * f), -32767.f), 32767.f), w1 = fminf(fmaxf(rintf(x1 * f), -32767.f), 32767.f);
                const float den = wave_sum(p0 + p1);
                const float bz = wave_sum(fmaf(w0, zs.x, w1 * zs.z));
                const float L = fmaf(m, 1.4426950408889634f, __builtin_amdgcn_logf(den));
                unsigned* wd = (unsigned*)(((swp >> u) & 1 ? sm.wo : sm.wv) + went(u));
                if (!QUARTER(u) || lane >> 4 == (rank & 3)) wd[QUARTER(u) ? lane & 15 : lane] = ((unsigned)(int)w0 & 0xffffu) | ((unsigned)(int)w1 << 16);
                if (lane == 0) *(v4f*)&wd[QUARTER(u) ? 16 : 64] = (v4f){ldexpf(1.f / den, (int)eb - 141), bz, L, 0.f};
                CBAR();
                if (!((swp >> u) & 1)) LDS_OR(&sm.vrdy, 1u << u)
            } else {
                const float n0 = (float)(a0 - (double)h.y) * h.x, n1 = (float)(a1 - (double)h.y) * h.x, n2 = (float)(a2 - (double)h.y) * h.x;
                const unsigned e2 = max(__float_as_uint(wave_maxv(fmaxf(fmaxf(fabsf(n0), fabsf(n1)), fabsf(n2)))) >> 23, 14u);
                const float f2 = __uint_as_float((268u - e2) << 23);
                const unsigned m01 = __builtin_bit_cast(unsigned, __builtin_amdgcn_cvt_pk_i16((int)rintf(n0 * f2), (int)rintf(n1 * f2)));
                const unsigned m2 = __builtin_bit_cast(unsigned, __builtin_amdgcn_cvt_pk_i16((int)rintf(n2 * f2), 0));
                const unsigned bit = ((lane & 1 ? e2 : __float_as_uint(h.z)) >> (lane >> 1)) & 1u;
                recs[e * 64 + lane] = (v2u){m01, m2 | bit << 16 | t15 << 17};
                CBAR();
                sm.acc[so][lane] = 0.0; sm.acc[so][64 + lane] = 0.0; sm.acc[so][128 + lane] = 0.0;
            }
            CBAR();
            if (lane == 0) { lds_st(&sm.cnt[so], 0u); CBAR(); lds_st(&sm.merged, u + 1); }
            if (u == NU - 1 && swp) {
                wave_sync();
                const __amdgpu_buffer_rsrc_t srs = __builtin_amdgcn_make_buffer_rsrc(ws + WS_SWAP + blockIdx.x * 5120, 0, 5120, 0x00020000);
                v4u d[5]; unsigned o[5];
#pragma unroll
                for (int i = 0; i < 5; i++) {
                    const unsigned* w = (const unsigned*)&sm.wo[144 * NMIX + (i * 64 + lane) * 6];
                    const unsigned d0 = w[0], d1 = w[1], d2 = w[2];
                    d[i] = (v4u){d0, (d1 & 0xffffu) | (T << 16), (d1 >> 16) | (d2 << 16), (d2 >> 16) | ((T + (T >> 16)) << 16)};
                    o[i] = (swp >> wunit(i * 64 + lane)) & 1 ? (i * 64 + lane) * 16 : 0x40000000;
                }
                while ((int)(NOW() - lds_ld(&sm.line)) < (int)tdue(PSW) + TWO) SLEEP(1)
#pragma unroll
                for (int i = 0; i < 5; i++) __builtin_amdgcn_raw_buffer_store_b128(d[i], srs, o[i], 0, SC1 | 2);
            }
        }
        wave_sync();
#pragma unroll
        for (int k = 0; k < NU / 2; k++) asm volatile("ds_read_b128 a[%c0:%c0+3], %1 offset:%2" :: "i"(16 + 4 * k), "v"(lds_addr(recs) + lane * 16), "i"(k * 1024) : "memory");
        asm volatile("s_waitcnt lgkmcnt(0)" ::: "memory");
        while (!((lds_ld(&sm.vrdy) >> (NU - 1)) & 1)) SLEEP(4)
        CBAR();
        const v2u hw_ = {*(const unsigned*)&sm.wv[went(NU - 1) + 132], T};
        const unsigned hrow = (NRW - GSZ + (__builtin_amdgcn_readlane(vut, NU - 1) & 15 ^ 1)) * (NSPLIT * 512);
        while (lds_ld(&sm.grab) < NPC - PUBP) SLEEP(1)
        __builtin_amdgcn_raw_buffer_store_b64(hw_, rsrc, lane < 16 ? lane * 8 : 0x40000000u, hrow, SC1);
#define PUB(K) asm volatile("buffer_store_dwordx4 a[%c0:%c0+3], %1, %2, 0 offen sc1" :: "i"(16 + 4 * (K)), "v"(po[K]), "s"(rs_) : "memory");
        PUB(WQ / 2) PUB(WQ / 2 + 1)
        { const unsigned t_ = TLAST() + PDEL; GATE(t_) }
        PUB(0) PUB(1) PUB(2) PUB(3) PUB(4) PUB(7) PUB(8)
        return;
    }
    }
    __builtin_amdgcn_s_setprio(0);
    unsigned c;
#define ADDRTN(P) { u64 ex_; asm volatile("s_mov_b64 %0, exec\n\ts_mov_b64 exec, 1\n\tds_add_rtn_u32 a15, %1, a13\n\ts_mov_b64 exec, %0" : "=&s"(ex_) : "v"(lds_addr(P)) : "a15", "memory"); }
#define ADDED(V) { unsigned t_; asm volatile("s_waitcnt lgkmcnt(0)\n\tv_accvgpr_read_b32 %1, a15\n\ts_nop 0\n\tv_readfirstlane_b32 %0, %1\n\ts_nop 1" : "=s"(V), "=v"(t_)); }
    asm volatile("v_accvgpr_write_b32 a13, 1\n\ts_nop 1" ::: "a13");
    ADDRTN(&sm.cidx) ADDED(c)
    if (c < NSW) {
        while (lds_ld(&sm.grab) < NPC + 2) SLEEP(4)
        const unsigned head = lds_ld(&sm.chead), T = lds_ld(&sm.tag_lo), tl = TLAST();
        const u64 rb_ = (u64)(ws + WS_PART);
        const v4u rs_ = {(unsigned)rb_, (unsigned)(rb_ >> 32) & 0xffffu, NRW * NSPLIT * 512, 0x00020000u};
        const unsigned ro = head * (NSPLIT * 512) + c * 1024, rx = (NH + (head & 15 ^ 1)) * (NSPLIT * 512) + c * 1024;
#define POLL(S, O) asm volatile("buffer_load_dwordx4 a[%c0:%c0+3], %1, %2, %3 offen sc1" :: "i"(16 + 4 * (S)), "v"(lane * 16), "s"(rs_), "s"(O) : "memory");
#define FETCH(X, S, N) asm volatile("s_waitcnt vmcnt(%c5)\n\tv_accvgpr_read_b32 %0, a[%c4]\n\tv_accvgpr_read_b32 %1, a[%c4+1]\n\tv_accvgpr_read_b32 %2, a[%c4+2]\n\tv_accvgpr_read_b32 %3, a[%c4+3]" \
                                    : "=v"((X).x), "=v"((X).y), "=v"((X).z), "=v"((X).w) : "i"(16 + 4 * (S)), "i"(N));
        float o0, o1, o2;
        if (head >= NH - GSZ) {
            __builtin_amdgcn_s_setprio(3);
            if (c == 0) {
                v2u hd;
                GATE(tl + THD)
                for (;;) {
                    hd = __builtin_amdgcn_raw_buffer_load_b64(__builtin_amdgcn_make_buffer_rsrc(ws + WS_PART + (NRW - GSZ + (head & 15 ^ 1)) * (NSPLIT * 512), 0, NSPLIT * 512, 0x00020000), (lane & 15) * 512, 0, SC1);
                    if (__builtin_amdgcn_ballot_w64(hd.y == T) == ~0ull) break;
                    SLEEP(4)
                }
                const float w = __builtin_amdgcn_exp2f(__uint_as_float(hd.x) - row_max(__uint_as_float(hd.x)));
                if (lane < 16) sm.ent[0][lane] = w * __builtin_amdgcn_rcpf(row_sum(w));
                CBAR();
                if (lane == 0) lds_st(&sm.tabs, 0u);
            }
            while (lds_ld(&sm.tabs) || lds_ld(&sm.merged) <= (2 * NU - 1) / NOUT * NOUT) SLEEP(1)
            CBAR();
            const float cf = sm.ent[0][2 * c + (lane >> 5)];
            v4u x; bool got;
#define SEEN(J, N) { FETCH(x, J, N) got = __builtin_amdgcn_ballot_w64((((x.y ^ T << 24) | (x.w ^ T << 24)) >> 24) != 0) == 0; }
            GATE(tl + TCA) POLL(0, ro) GATE(tl + TCA + TCD) POLL(1, rx + 3 * GSZ * NSPLIT * 512) SEEN(0, 1)
            if (!got) SEEN(1, 0)
            while (!got) { POLL(2, rx + 4 * GSZ * NSPLIT * 512) SEEN(2, 0) SLEEP(0) }
            const double c0 = ldexpf(cf, (int)(x.y >> 16 & 255u) - 141), c1 = ldexpf(cf, (int)(x.w >> 16 & 255u) - 141);
#define SWADD(A, B) ({ const v2u a_ = __builtin_bit_cast(v2u, A), b_ = __builtin_bit_cast(v2u, B); \
                const auto l_ = __builtin_amdgcn_permlane32_swap(a_.x, b_.x, false, false); const auto h_ = __builtin_amdgcn_permlane32_swap(a_.y, b_.y, false, false); \
                __builtin_bit_cast(double, (v2u){l_[0], h_[0]}) + __builtin_bit_cast(double, (v2u){l_[1], h_[1]}); })
            const double s0 = SWADD(c0 * (double)(short)x.x, c1 * (double)(short)x.z), s1 = SWADD(c0 * (double)((int)x.x >> 16), c1 * (double)((int)x.z >> 16)),
                         s2 = SWADD(c0 * (double)(short)x.y, c1 * (double)(short)x.w);
            asm volatile("ds_add_f64 %0, %1\n\tds_add_f64 %0, %2 offset:512\n\tds_add_f64 %0, %3 offset:1024" :: "v"(lds_addr(&sm.acc[0][lane])), "v"(s0), "v"(s1), "v"(s2) : "memory");
            ADDRTN(&sm.cdone)
            const double a0 = sm.acc[0][lane], a1 = sm.acc[0][64 + lane], a2 = sm.acc[0][128 + lane];
            ADDED(c)
            o0 = a0; o1 = a1; o2 = a2;
        } else {
            const bool quart = head >> 4 == WQ;
            const unsigned t15 = T & 0x7fffu;
#define TAKE(K) { FETCH(x[K], K, 0) \
                bad |= (x[K].y >> 17 ^ t15) | (x[K].w >> 17 ^ t15); \
                const u64 bl = __builtin_amdgcn_ballot_w64((x[K].y & 0x10000u) != 0), be = __builtin_amdgcn_ballot_w64((x[K].w & 0x10000u) != 0); \
                L[K] = __uint_as_float(lane < 32 ? (unsigned)bl : (unsigned)(bl >> 32)); ex[K] = lane < 32 ? (unsigned)be : (unsigned)(be >> 32); \
                M = fmaxf(M, fmaxf(__uint_as_float((unsigned)bl), __uint_as_float((unsigned)(bl >> 32)))); }
#define SUMUP(K) { const float w_ = __builtin_amdgcn_exp2f(L[K] - M), c_ = ldexpf(w_, (int)(ex[K] & 255u) - 141); den += w_; \
                t[0] = fmaf(c_, (float)(short)x[K].x, t[0]); t[1] = fmaf(c_, (float)((int)x[K].x >> 16), t[1]); t[2] = fmaf(c_, (float)(short)x[K].y, t[2]); \
                t[3] = fmaf(c_, (float)(short)x[K].z, t[3]); t[4] = fmaf(c_, (float)((int)x[K].z >> 16), t[4]); t[5] = fmaf(c_, (float)(short)x[K].w, t[5]); }
            const unsigned tg = tl + (quart ? TRQ : TRD);
            GATE(tg)
            v4u x[4]; float L[4], M; unsigned ex[4];
            for (;;) {
                unsigned bad = 0; M = -3e38f;
                POLL(0, ro)
                if (quart) { POLL(1, rx) POLL(2, rx + GSZ * NSPLIT * 512) POLL(3, rx + 2 * GSZ * NSPLIT * 512) }
                __builtin_amdgcn_s_setprio(3);
                TAKE(0)
                if (quart) { TAKE(1) TAKE(2) TAKE(3) }
                if (__builtin_amdgcn_ballot_w64(bad != 0) == 0) break;
                SLEEP(2)
            }
            float t[6] = {0.f, 0.f, 0.f, 0.f, 0.f, 0.f}, den = 0.f;
            SUMUP(0)
            if (quart) { SUMUP(1) SUMUP(2) SUMUP(3) }
#define SWADF(A, B) ({ const auto l_ = __builtin_amdgcn_permlane32_swap(__float_as_uint(A), __float_as_uint(B), false, false); __uint_as_float(l_[0]) + __uint_as_float(l_[1]); })
            v4f* pb = (v4f*)((char*)&sm.cs[0][0][0] + 65536);
            float* pm = (float*)(pb + NSW * 64);
            pb[c * 64 + lane] = (v4f){SWADF(t[0], t[3]), SWADF(t[1], t[4]), SWADF(t[2], t[5]), SWADF(den, den)};
            if (lane == 0) pm[c] = M;
            CBAR();
            ADDRTN(&sm.cdone)
            ADDED(c)
            CBAR();
            if (c == NSW - 1) {
                const v4f ma = *(const v4f*)pm, mb = *(const v4f*)(pm + 4);
                const float mw[NSW] = {ma.x, ma.y, ma.z, ma.w, mb.x, mb.y, mb.z, mb.w};
                float Mx = mw[0];
#pragma unroll
                for (int w = 1; w < NSW; w++) Mx = fmaxf(Mx, mw[w]);
                v4f sum = {0.f, 0.f, 0.f, 0.f};
#pragma unroll
                for (int w = 0; w < NSW; w++) sum += pb[w * 64 + lane] * __builtin_amdgcn_exp2f(mw[w] - Mx);
                const float inv = __builtin_amdgcn_rcpf(sum.w);
                o0 = sum.x * inv; o1 = sum.y * inv; o2 = sum.z * inv;
            }
        }
        if (c == NSW - 1) {
            asm("" : "+v"(o0), "+v"(o1), "+v"(o2));
            __hip_bfloat16* o = out + (size_t)head * DH + (lane & 31) * 6 + (lane >> 5) * 3;
            o[0] = __float2bfloat16(o0); o[1] = __float2bfloat16(o1); o[2] = __float2bfloat16(o2);
        }
    }
}

extern "C" void kernel_launch(void* const* d_in, const int* in_sizes, int n_in,
                              void* d_out, int out_size, void* d_ws, size_t ws_size,
                              hipStream_t stream) {
    const unsigned short* q = (const unsigned short*)d_in[0];
    const int* qk = (const int*)d_in[1];
    const unsigned short* k_scales = (const unsigned short*)d_in[2];
    const unsigned short* k_zeros = (const unsigned short*)d_in[3];
    const int* qv = (const int*)d_in[4];
    const unsigned short* v_scales = (const unsigned short*)d_in[5];
    const unsigned short* v_zeros = (const unsigned short*)d_in[6];
    const unsigned short* q_cos = (const unsigned short*)d_in[7];
    const unsigned short* q_sin = (const unsigned short*)d_in[8];
    const unsigned short* k_cos = (const unsigned short*)d_in[9];
    const unsigned short* k_sin = (const unsigned short*)d_in[10];
    unsigned char* ws = (unsigned char*)d_ws;
    attn_kernel<<<NSPLIT * GSZ, NTHREADS, 0, stream>>>(q, qk, k_scales, k_zeros, qv, v_scales, v_zeros,
                                                       q_cos, q_sin, k_cos, k_sin, (__hip_bfloat16*)d_out, ws);
}
```

```cpp
#include <hip/hip_runtime.h>
#include <hip/hip_bf16.h>
#include <stdint.h>

#define DH 192
#define HALF 96
#define SKV 2048
#define NH 256
#define NSW 8
#define NWAVES (NSW + 4)
#define NTHREADS (64 * NWAVES)
#define BROWS 128
#define NSPLIT 16
#define GSZ 16
#define WQ 10
#define NU (NH / GSZ + 3)
#define NPC 1024
#define NRR (NH + 3 * GSZ)
#define NIN 8
#define NOUT 8
#define WALL (15 * 144 + 4 * 48)
#define SC1 16
#define NRND 48
#define RST 4352
#define TK 2
#define NMIX 3
#define TUNIT 311
#define T01 50
#define TMIX 7
#define TOFF 50
#define PSF 100
#define TGS 68
#define PSW (NPC / 2 + 28)
#define TG0 8
#define TG1 3
#define TWO -75
#define PSG (NPC / 2 + 60)
#define TH0 3
#define TH1 3
#define EARLY 10
#define LEADP 24
#define DPH 2
#define TA 210
#define TA2 70
#define CSH 2
#define NPRB 3
#define TMAP 520
#define MINV 96
#define VW 4
#define MASTER 203
#define TRES 190
#define TB1 1200
#define PER 300
#define RDD 100
#define LEAD 70
#define LHI 85
#define LOK 74
#define PSL0 124
#define PUBP -14
#define LJ 210
#define LOK2 75
#define NTRY 3
#define TFO 6
#define SL0 42.f
#define PST0 200
#define KC 4
#define TC0 1000
#define TCS 9
#define PRE0 3

#define KREL 12
#define PDEL 70
#define NCP 3
#define NRW (NRR + NCP * GSZ)
#define TRD 112
#define THD 55
#define TRQ 55
#define TCA 132
#define TCD 22
#define WS_EPOCH 0
#define WS_POST0 8192
#define WS_POST (WS_POST0 + ((unsigned)E1 & 15) * 2048)
#define WS_NEWS (49152 + 256)
#define WS_HELLO 59904
#define WS_BOARD 65536
#define WS_PART (2u << 20)
#define WS_SWAP (WS_PART + NRW * NSPLIT * 512)

typedef unsigned int v2u __attribute__((ext_vector_type(2)));
typedef unsigned int v16u __attribute__((ext_vector_type(16)));
typedef unsigned int v4u __attribute__((ext_vector_type(4)));
typedef int v4i __attribute__((ext_vector_type(4)));
typedef float v2f __attribute__((ext_vector_type(2)));
typedef float v4f __attribute__((ext_vector_type(4)));
typedef unsigned long long u64;

__device__ __forceinline__ float bflo(unsigned u) { return __uint_as_float(u << 16); }
__device__ __forceinline__ float bfhi(unsigned u) { return __uint_as_float(u & 0xffff0000u); }
__device__ __forceinline__ float bfr(float x) {
    __bf16 b = (__bf16)x;
    return __uint_as_float(((unsigned)__builtin_bit_cast(unsigned short, b)) << 16);
}

#define SLEEP(n) SLEEP_(n)
#define SLEEP_(n) asm volatile("s_sleep " #n "\n\tv_add_co_u32 %0, vcc, 1, %0\n\ts_cbranch_vccz 1f\n\ts_endpgm\n1:" : "+v"(spins) :: "vcc", "memory");
#define SPIN0 0xf0000000u
#define CBAR() asm volatile("" ::: "memory")

__device__ __forceinline__ void wave_sync() {
    __builtin_amdgcn_fence(__ATOMIC_RELEASE, "wavefront");
    __builtin_amdgcn_wave_barrier();
    __builtin_amdgcn_fence(__ATOMIC_ACQUIRE, "wavefront");
}
template <int CTRL>
__device__ __forceinline__ float dpp_add(float x) {
    int y = __builtin_amdgcn_update_dpp(0, __float_as_int(x), CTRL, 0xf, 0xf, true);
    return x + __int_as_float(y);
}
template <int CTRL>
__device__ __forceinline__ float dpp_max(float x) {
    int y = __builtin_amdgcn_update_dpp(__float_as_int(x), __float_as_int(x), CTRL, 0xf, 0xf, false);
    return fmaxf(x, __int_as_float(y));
}
__device__ __forceinline__ float row_sum(float x) { x = dpp_add<0x128>(x); x = dpp_add<0x124>(x); x = dpp_add<0x122>(x); return dpp_add<0x121>(x); }
__device__ __forceinline__ float row_max(float m) { m = dpp_max<0x128>(m); m = dpp_max<0x124>(m); m = dpp_max<0x122>(m); return dpp_max<0x121>(m); }
__device__ __forceinline__ float rdlane(float x, int l) { return __int_as_float(__builtin_amdgcn_readlane(__float_as_int(x), l)); }
__device__ __forceinline__ float wave_max(float m) { m = row_max(m); return fmaxf(fmaxf(rdlane(m, 0), rdlane(m, 16)), fmaxf(rdlane(m, 32), rdlane(m, 48))); }
__device__ __forceinline__ float wave_maxv(float m) {
    m = row_max(m);
    const auto a = __builtin_amdgcn_permlane16_swap(__float_as_uint(m), __float_as_uint(m), false, false);
    m = fmaxf(__uint_as_float(a[0]), __uint_as_float(a[1]));
    const auto b = __builtin_amdgcn_permlane32_swap(__float_as_uint(m), __float_as_uint(m), false, false);
    return fmaxf(__uint_as_float(b[0]), __uint_as_float(b[1]));
}
__device__ __forceinline__ float wave_sum(float x) { x = row_sum(x); return (rdlane(x, 0) + rdlane(x, 16)) + (rdlane(x, 32) + rdlane(x, 48)); }
__device__ __forceinline__ unsigned lds_ld(const unsigned* p) { return __builtin_amdgcn_readfirstlane(__hip_atomic_load(p, __ATOMIC_RELAXED, __HIP_MEMORY_SCOPE_WORKGROUP)); }
__device__ __forceinline__ unsigned lds_addr(const void* p) { return (unsigned)(size_t)(const __attribute__((address_space(3))) void*)p; }
#define DMA16(SRC, LDS) asm volatile("s_mov_b32 %0, m0\n\ts_mov_b32 m0, %2\n\ts_nop 0\n\tglobal_load_lds_dwordx4 %1, off\n\ts_mov_b32 m0, %0" : "=&s"(m0keep) : "v"(SRC), "s"(LDS) : "memory")
__device__ __forceinline__ void lds_st(unsigned* p, unsigned v) { __hip_atomic_store(p, v, __ATOMIC_RELAXED, __HIP_MEMORY_SCOPE_WORKGROUP); }
#define NOW() ((unsigned)__builtin_amdgcn_s_memrealtime())
__device__ __forceinline__ unsigned tdue(unsigned g) {
    return (g * TUNIT + min(g, 480u) * TK + min(g, 48u) * T01 + (min(g, 32u * NMIX) + min(max((int)g - NPC / 2, 0), 32 * NMIX)) * TMIX + (g >= PSF ? TGS * 32 : 0)) >> 5;
}

__device__ __forceinline__ unsigned unit_of(unsigned g) { const unsigned p = g & 511; return (g >> 9) * NU + (p >> 5) + min(max((int)p - 32 * WQ, 0) >> 3, 3); }
__device__ __forceinline__ unsigned first_of(unsigned e) { return 32 * e - 24 * min(max((int)e - WQ, 0), 4); }
#define QUARTER(E) ((E) - WQ < 4)
__device__ __forceinline__ unsigned went(unsigned e) { return 144 * e - 96 * min(max((int)e - WQ, 0), 4); }
__device__ __forceinline__ unsigned wunit(unsigned x) { x += 24 * NMIX; return x < 24 * WQ ? x / 24 : x < 24 * WQ + 32 ? WQ + (x - 24 * WQ) / 8 : (x + 64) / 24; }

#define ROTT 0xed8d2c3d2c35ed84ull, 0xba85aa165b03ac82ull, 0xee7efa489a38af71ull, 0xe932da1b0bab51d8ull, 0xfa96112d5fee971bull, 0x2c7e9ea5a544f071ull, 0x3d86093d5f0e810bull, 0xe932da1b0bab51d8ull, 0xfa96112d5fee971bull, 0xd834e62c5fa723e9ull, 0x1d7d1a3d2c061d88ull, 0xe932da1b0bab51d8ull, 0xfa96112d5fee971bull, 0xc834e12c0aaa4ec7ull, 0x3d86093d5f0e810bull, 0xe932da1b0bab51d8ull
static __device__ const u64 ROT[16] = {ROTT};

struct __attribute__((aligned(16))) Smem {
    unsigned cs[NSW][4][768];
    double acc[NOUT][DH];
    float ent[NOUT][BROWS];
    unsigned short wv[WALL];
    unsigned short wo[WALL];
    unsigned q[NIN][HALF];
    float2 kz[NIN][BROWS];
    float2 vz[NIN][BROWS];
    unsigned utab[64];
    unsigned line, gap;
    unsigned late, grab;
    unsigned rdy[NIN], cnt[NOUT];
    unsigned tabs, vrdy, swp;
    unsigned aline;
    unsigned tag_rdy, tag_lo, tag_hi, merged, cdone, cidx;
    unsigned slow;
    unsigned chead;
};

#define LDS_ADD(P, V) { u64 ex_; asm volatile("s_mov_b64 %0, exec\n\ts_mov_b64 exec, 1\n\tds_add_u32 %1, %2\n\ts_mov_b64 exec, %0" : "=&s"(ex_) : "v"(lds_addr(P)), "v"(V) : "memory"); }
#define LDS_OR(P, V) { u64 ex_; asm volatile("s_mov_b64 %0, exec\n\ts_mov_b64 exec, 1\n\tds_or_b32 %1, %2\n\ts_mov_b64 exec, %0" : "=&s"(ex_) : "v"(lds_addr(P)), "v"(V) : "memory"); }

__global__ __launch_bounds__(NTHREADS, 3) void attn_kernel(
    const unsigned short* __restrict__ q, const int* __restrict__ qk,
    const unsigned short* __restrict__ k_scales, const unsigned short* __restrict__ k_zeros,
    const int* __restrict__ qv,
    const unsigned short* __restrict__ v_scales, const unsigned short* __restrict__ v_zeros,
    const unsigned short* __restrict__ q_cos, const unsigned short* __restrict__ q_sin,
    const unsigned short* __restrict__ k_cos, const unsigned short* __restrict__ k_sin,
    __hip_bfloat16* __restrict__ out, unsigned char* __restrict__ ws)
{
    __shared__ Smem sm;
    const int tid = threadIdx.x;
    unsigned spins = SPIN0;
    const int lane = tid & 63;
    const int hw = __builtin_amdgcn_readfirstlane(tid >> 6);
    u64 t00; unsigned ka;
    asm volatile("s_memrealtime %0\n\ts_load_dword %1, %2, 0x40" : "=&s"(t00), "=&s"(ka) : "s"(__builtin_amdgcn_kernarg_segment_ptr()));
    if (hw == 0) {
        const char* pc0; asm volatile("s_getpc_b64 %0" : "=s"(pc0));
        asm volatile("global_load_dword a6, %0, %1" :: "v"(((blockIdx.x & ~7u) + (lane & 7)) * 128), "s"(pc0) : "a6", "memory");
    }
    const int wave = hw >= 4 ? hw - 4 : hw + NSW;
    const unsigned split = (blockIdx.x & 1) + (blockIdx.x & 2) * 4 + ((blockIdx.x >> 3) & 3) * 2;
    const unsigned side = (split ^ (blockIdx.x >> 2)) & 1, rank = blockIdx.x >> 5;
    const unsigned head0 = 2 * rank + side;
    const unsigned rf0 = (8 * split + 1) / 3, r3 = 8 * split + 1 - 3 * rf0;
    const unsigned koff = ((lane >> 5) * DH + 3 * (lane & 31)) * 4, koffb = koff + 384;
#define AR(S) (16 + (S) * 8)
#define GLD(A, OFF, BASE, IMM) asm volatile("global_load_dwordx3 a[%c0:%c1], %2, %3 offset:%4 nt" :: "i"(A), "i"((A) + 2), "v"(OFF), "s"(BASE), "i"(IMM))
#define ISSUE(S, J, BASE, OA, OB) { GLD(AR(S), OA, BASE, (J) * 1536); GLD(AR(S) + 4, OB, BASE, (J) * 1536); }
#define HEAD2 ((GSZ + 1 - 2 * side) * (SKV * DH * 4))
    const unsigned wo = (hw - 2) & 7;
    const bool pre = hw >= 4 && wo < PRE0;
    if (pre) {
        const char* a = (const char*)qk + (head0 * 512 + side * 256 + rf0 + wave + (wave >= 6 ? 80 : 0)) * 3072; const char* b = a + HEAD2;
        ISSUE(0, 0, a, koff, koffb) ISSUE(1, 1, a, koff, koffb) ISSUE(2, 0, b, koff, koffb) ISSUE(3, 1, b, koff, koffb)
    }
    asm volatile("s_waitcnt lgkmcnt(0)" : "+s"(t00), "+s"(ka));
    if (hw == 0) {
        sm.utab[lane] = ~0u;
        unsigned l0 = (unsigned)t00 + TOFF;
        if (KREL) { const unsigned t1 = NOW() + KREL; if ((int)(t1 - l0) > 0) l0 = t1; }
        if (lane < 32) (&sm.line)[lane] = lane == 3 ? 64u : (lane & 29) == 0 ? l0 : 0u;
        asm volatile("s_waitcnt lgkmcnt(0)" ::: "memory");
    }
    __builtin_amdgcn_s_barrier();
    CBAR();
    const u64 rtab = r3 == 1 ? 0x5756552c2b020100ull : r3 == 0 ? 0x5756552c2b2a0100ull : 0x57562d2c2b020100ull;
#define R8(I) (rf0 + ((unsigned)(rtab >> (8 * (I))) & 255u))
#define GATE(DUE) { int d_ = (int)((DUE) - NOW()); if (d_ < 3000) while (d_ > 0) { __builtin_amdgcn_s_sleep(1); d_ = (int)((DUE) - NOW()); } }
#define TLAST() (lds_ld(&sm.line) + tdue(NPC - 2) + (lds_ld(&sm.gap) >> 16))
    if (__builtin_expect(wave < NSW, 1)) {
        const int cst = 2 * side;
        const unsigned r8w = R8((unsigned)wave);
        const char* cbase[3];
#pragma unroll
        for (int j = 0; j < 3; j++) { const int x = j * 64 + lane; cbase[j] = (x < 96 ? (const char*)k_cos + x * 16 : (const char*)k_sin + (x - 96) * 16) + r8w * (4 * DH * 2); }
        unsigned m0keep;
#define CS_DMA(W, C) { _Pragma("unroll") for (int j = 0; j < 3; j++) DMA16(cbase[j] + (C) * (512 * DH * 2), lds_addr(&sm.cs[0][0][0]) + ((W) * 4 + (C)) * 3072 + j * 1024); }
#define REPACK(W, C) { \
        unsigned* cw = &sm.cs[W][C][0]; \
        const unsigned* l = cw + rsel * (DH / 2) + qoff; \
        unsigned r[2][6]; \
        _Pragma("unroll") \
        for (int j = 0; j < 2; j++) { \
            unsigned e[4][2]; \
            _Pragma("unroll") \
            for (int k = 0; k < 4; k++) { \
                const unsigned* a = l + j * DH + (k & 1) * 48 + (k >> 1) * 384;       \
                unsigned x0 = a[0], x1 = a[1]; \
                e[k][0] = __builtin_amdgcn_alignbit(x1, x0, sh); \
                e[k][1] = x1 >> sh; \
            } \
            r[j][0] = (e[0][0] & 0xffffu) | (e[3][0] << 16); \
            r[j][1] = (e[0][0] >> 16) | (e[3][0] & 0xffff0000u); \
            r[j][2] = (e[0][1] & 0xffffu) | (e[3][1] << 16); \
            r[j][3] = (e[1][0] & 0xffffu) | (e[2][0] << 16); \
            r[j][4] = (e[1][0] >> 16) | (e[2][0] & 0xffff0000u); \
            r[j][5] = (e[1][1] & 0xffffu) | (e[2][1] << 16); \
        } \
        wave_sync(); \
        _Pragma("unroll") \
        for (int j = 0; j < 2; j++) { *(v4u*)&cw[j * 256 + lane * 4] = (v4u){r[j][0], r[j][1], r[j][2], r[j][3]}; *(v2u*)&cw[512 + j * 128 + lane * 2] = (v2u){r[j][4], r[j][5]}; } \
        wave_sync(); }
        const int sub = lane & 31;
        const int rsel = lane >> 5;
        const unsigned sh = (sub & 1) * 16;
        const int qoff = 3 * (sub >> 1) + (sub & 1);
        const unsigned voff = 3 * lane * 4;
        asm volatile("" ::: "a16", "a17", "a18", "a19", "a20", "a21", "a22", "a23", "a24", "a25", "a26", "a27", "a28", "a29", "a30", "a31",
                            "a32", "a33", "a34", "a35", "a36", "a37", "a38", "a39", "a40", "a41", "a42", "a43", "a44", "a45", "a46", "a47");
        asm volatile("" ::: "a48", "a49", "a50", "a51", "a52", "a53", "a54", "a55", "a56", "a57", "a58", "a59", "a60", "a61", "a62", "a63",
                            "a64", "a65", "a66", "a67", "a68", "a69", "a70", "a71", "a72", "a73", "a74", "a75", "a76", "a77", "a78", "a79");
        int ta[4][3], tb[4][3];
#define WAITT(P, S, N) asm volatile("s_waitcnt vmcnt(%6)\n\tv_accvgpr_read_b32 %0, a[%c7]\n\tv_accvgpr_read_b32 %1, a[%c8]\n\tv_accvgpr_read_b32 %2, a[%c9]\n\t" \
                              "v_accvgpr_read_b32 %3, a[%c10]\n\tv_accvgpr_read_b32 %4, a[%c11]\n\tv_accvgpr_read_b32 %5, a[%c12]" \
                              : "=v"(ta[P][0]), "=v"(ta[P][1]), "=v"(ta[P][2]), "=v"(tb[P][0]), "=v"(tb[P][1]), "=v"(tb[P][2]) \
                              : "i"(N), "i"(AR(S)), "i"(AR(S) + 1), "i"(AR(S) + 2), "i"(AR(S) + 4), "i"(AR(S) + 5), "i"(AR(S) + 6))
        const unsigned voffb = voff + 768;
        u64 ln = 0;
#define TDUE(G) (tdue(G) + ((G) >= PSW ? __builtin_amdgcn_readfirstlane((unsigned)(ln >> 32)) >> ((G) >= PSG ? 16 : 0) & 0xffffu : 0u))
#define REPORT() { const unsigned t_ = now - tdc; if (lane == 0) lds_st(&sm.late, t_); }
#define LINE() ln = LDV((u64*)&sm.line);
#define LDV(P) __hip_atomic_load(P, __ATOMIC_RELAXED, __HIP_MEMORY_SCOPE_WORKGROUP)
        unsigned u = 0, rg = cst * 8 + wave, np = 0;
        CS_DMA(wave, cst)

        const char* kb0 = (const char*)qk + (size_t)(head0 * 512 + r8w) * 3072;
#define KB(I) (kb0 + ((cst + (I)) & 3) * (128 * 3072))
        const char* kb; const char* kb2;
#define SISSUE(I) { kb = KB(I); kb2 = kb + HEAD2; const unsigned du_ = lds_ld(&sm.line) + tdue(16 * (I) + 2 * wo); GATE(du_) \
            ISSUE(((I) & 1) * 4, 0, kb, koff, koffb) ISSUE(((I) & 1) * 4 + 1, 1, kb, koff, koffb) ISSUE(((I) & 1) * 4 + 2, 0, kb2, koff, koffb) ISSUE(((I) & 1) * 4 + 3, 1, kb2, koff, koffb) }
        if (!pre) SISSUE(0)
        CS_DMA(wave, (cst + 1) & 3)
        if (pre) asm volatile("s_waitcnt vmcnt(3)" ::: "memory");
        asm volatile("s_waitcnt vmcnt(11)" ::: "memory");
        if (!pre) LDS_ADD(&sm.slow, NOW() - lds_ld(&sm.line) - tdue(2 * wo))
        REPACK(wave, cst)
        SISSUE(1)
#define TABDONE(I) LDS_OR(&sm.tabs, 1u << ((((cst + (I)) & 3) * 8) + wave))
        TABDONE(0)
        const float scale = 0.07216878364870322f;
        double acc[3] = {0.0, 0.0, 0.0};
        bool first = true, chg, none = false;
        unsigned Qp[3], Qn[3], Rp[3], Rn[3];
        float kz4[4], ksc, kres[4];
        v4u ca[4]; v2u cb[4];
        v2f qe[4][3], A[4];
        unsigned vut, nrg;
        const unsigned vr = ((lane >> 3) & 3) * 128 + R8((unsigned)lane & 7);
        unsigned g, mg;
        const unsigned grab_adr = lds_addr(&sm.grab);
        asm volatile("v_accvgpr_write_b32 a13, 1\n\tv_accvgpr_write_b32 a14, 2" ::: "a13", "a14", "a15");
#define GRAB(N) { u64 ex_; asm volatile("s_mov_b64 %0, exec\n\ts_mov_b64 exec, 1\n\tds_add_rtn_u32 a15, %1, a%c2\n\ts_mov_b64 exec, %0" : "=&s"(ex_) : "v"(grab_adr), "i"(12 + (N)) : "memory"); }
#define GRABBED() { unsigned t_; asm volatile("s_waitcnt lgkmcnt(0)\n\tv_accvgpr_read_b32 %1, a15\n\ts_nop 0\n\tv_readfirstlane_b32 %0, %1\n\ts_nop 1" : "=s"(g), "=v"(t_)); }
        unsigned oa, ob, un, tdn, tiss, tdc = 0;
#define PLAN() { \
            GRABBED() un = unit_of(g); chg = un != u; none = g >= NPC; \
            const bool nv = g >= NPC / 2; \
            unsigned hd = __builtin_amdgcn_readlane(vut, un); \
            while (hd == ~0u) { SLEEP(1) hd = lds_ld(&sm.utab[un]); } \
            nrg = (g + (hd >> 8)) & 31; hd &= 255; \
            unsigned boff, boff2; asm("s_mul_i32 %0, %1, 0xc00" : "=s"(boff) : "s"(hd * 512 + __builtin_amdgcn_readlane(vr, nrg))); \
            asm("s_mul_i32 %0, %1, 0xc00" : "=s"(boff2) : "s"(hd * 512 + __builtin_amdgcn_readlane(vr, nrg + 1))); \
            const char* b_ = nv ? (const char*)qv : (const char*)qk; \
            kb = b_ + boff; kb2 = b_ + boff2; \
            oa = nv ? voff : koff; ob = nv ? voffb : koffb; \
            tdn = TDUE(g); }
#define FIRE() { ISSUE(0, 0, kb, oa, ob) ISSUE(1, 1, kb, oa, ob) ISSUE(2, 0, kb2, oa, ob) ISSUE(3, 1, kb2, oa, ob) }
#define LAUNCH() { \
            LINE() \
            const unsigned du_ = __builtin_amdgcn_readfirstlane((unsigned)ln) + tdn; \
            GATE(du_) \
            ISSUE(0, 0, kb, oa, ob) ISSUE(1, 1, kb, oa, ob) \
            GATE(du_ + 10) \
            ISSUE(2, 0, kb2, oa, ob) ISSUE(3, 1, kb2, oa, ob) tiss = NOW(); }
#define ARRIVED() \
            const unsigned now = NOW(); \
            const bool stall = now - tiss > LJ && g - PST0 < NPC - 48 - PST0; \
            const bool late = !none && !stall && (int)(now + EARLY - __builtin_amdgcn_readfirstlane((unsigned)ln) - tdn) >= 0; \
            if (late) { FIRE() tiss = now; } \
            if (stall && lane == 0) __hip_atomic_fetch_max(&sm.line, (unsigned)ln + (now - tiss) - LOK2, __ATOMIC_RELAXED, __HIP_MEMORY_SCOPE_WORKGROUP); \
            REPORT()
#define ADVANCE(ISV) { \
            if (chg) FLUSH(ISV) \
            if (!none && !late) LAUNCH() \
            first = chg; u = un; rg = nrg; tdc = tdn + 10; }
#define K_TAB(MODE) { _Pragma("unroll") for (int p = 0; p < ((MODE) ? 2 : 4); p++) { \
            const unsigned* cw = &sm.cs[(rg & 7) + (p >> 1)][rg >> 3][0]; \
            ca[p] = *(const v4u*)&cw[(p & 1) * 256 + lane * 4]; cb[p] = *(const v2u*)&cw[512 + (p & 1) * 128 + lane * 2]; } }
#define K_PRE(MODE, P) { \
        float e0, e1, e2, f0, f1, f2; \
        const unsigned* qp_ = ((MODE) && (P) >= 2) ? Rp : Qp; const unsigned* qn_ = ((MODE) && (P) >= 2) ? Rn : Qn; \
        const int tp_ = (MODE) ? (P) & 1 : (P); \
        asm("v_dot2_f32_bf16 %0, %6, %12, 0\n\tv_dot2_f32_bf16 %3, %9, %15, 0\n\tv_dot2_f32_bf16 %1, %7, %13, 0\n\tv_dot2_f32_bf16 %4, %10, %16, 0\n\t" \
            "v_dot2_f32_bf16 %2, %8, %14, 0\n\tv_dot2_f32_bf16 %5, %11, %17, 0\n\ts_nop 2" \
            : "=&v"(e0), "=&v"(e1), "=&v"(e2), "=&v"(f0), "=&v"(f1), "=&v"(f2) \
            : "v"(qp_[0]), "v"(qp_[1]), "v"(qp_[2]), "v"(qn_[0]), "v"(qn_[1]), "v"(qn_[2]), \
              "v"(ca[tp_].x), "v"(ca[tp_].y), "v"(ca[tp_].z), "v"(ca[tp_].w), "v"(cb[tp_].x), "v"(cb[tp_].y)); \
        qe[P][0] = (v2f){e0, f0}; qe[P][1] = (v2f){e1, f1}; qe[P][2] = (v2f){e2, f2}; \
        A[P] = (qe[P][0] + qe[P][1] + qe[P][2]) * (v2f){-kz4[P], -kz4[P]}; }
#define K_WAIT(MODE, P) { \
        if ((MODE) == 0) { WAITT(P, P, 6 - 2 * (P)); } \
        else { WAITT(P, (((MODE) - 1) & 1) * 4 + (P), ((MODE) < 3 ? 20 : (MODE) == 3 ? 17 : 6) - 2 * (P)); } }
#define K_FMA(P) { \
        _Pragma("unroll") \
        for (int i = 0; i < 3; i++) { v2f x = {(float)ta[P][i], (float)tb[P][i]}; A[P] = qe[P][i] * x + A[P]; } \
        kres[P] = A[P].x + A[P].y; }
#define K_RED(MODE) { \
        const bool o1 = lane & 1, o2 = lane & 2; \
        float s01 = (o1 ? kres[1] : kres[0]) + __int_as_float(__builtin_amdgcn_update_dpp(0, __float_as_int(o1 ? kres[0] : kres[1]), 0xb1, 0xf, 0xf, true)); \
        float s23 = (o1 ? kres[3] : kres[2]) + __int_as_float(__builtin_amdgcn_update_dpp(0, __float_as_int(o1 ? kres[2] : kres[3]), 0xb1, 0xf, 0xf, true)); \
        float sx = (o2 ? s23 : s01) + __int_as_float(__builtin_amdgcn_update_dpp(0, __float_as_int(o2 ? s01 : s23), 0x4e, 0xf, 0xf, true)); \
        sx = dpp_add<0x124>(sx); sx = dpp_add<0x128>(sx); \
        const auto sw = __builtin_amdgcn_permlane16_swap(__float_as_uint(sx), __float_as_uint(sx), false, false); \
        sx = __uint_as_float(sw[0]) + __uint_as_float(sw[1]); \
        if ((lane & 28) == 0) { if (MODE) sm.ent[(lane >> 1) & 1][rg * 4 + 2 * (lane & 1) + rsel] = sx * ksc; else sm.ent[u % NOUT][rg * 4 + 2 * (lane & 3) + rsel] = sx * ksc; } }
#define K_PRE4(MODE) { K_PRE(MODE, 0) K_PRE(MODE, 1) K_PRE(MODE, 2) K_PRE(MODE, 3) asm volatile("" :: "v"(A[0]), "v"(A[1]), "v"(A[2]), "v"(A[3])); }
#define K_BODY(MODE) { K_PRE4(MODE) K_WAIT(MODE, 0) K_FMA(0) K_WAIT(MODE, 1) K_FMA(1) K_WAIT(MODE, 2) K_FMA(2) K_WAIT(MODE, 3) K_FMA(3) K_RED(MODE) }
#define V_FMA(J, W0, W1) { \
        _Pragma("unroll") \
        for (int i = 0; i < 3; i++) { \
            acc[i] = fma(W0, (double)ta[J][i], acc[i]); \
            acc[i] = fma(W1, (double)tb[J][i], acc[i]); \
        } }
#define V_PIN() asm volatile("" :: "v"(acc[0]), "v"(acc[1]), "v"(acc[2]));
#define RDYWAIT(SL, SEQ) { while (lds_ld(&sm.rdy[SL]) != (SEQ)) SLEEP(1); CBAR(); }
#define LOADZ() { _Pragma("unroll") for (int p = 0; p < 4; p++) kz4[p] = sm.kz[u % NIN][rg * 4 + 2 * p + rsel].x; ksc = sm.kz[u % NIN][rg * 4 + 2 * (lane & 3) + rsel].y * scale; }
#define LOADQ() { _Pragma("unroll") for (int i = 0; i < 3; i++) { Qp[i] = sm.q[u % NIN][3 * sub + i]; Qn[i] = __builtin_amdgcn_alignbit(Qp[i], Qp[i], 16) ^ 0x80000000u; } }
#define FLUSH(ISV) { \
            mg = __builtin_amdgcn_readfirstlane(mg); \
            while (u >= mg + NOUT) { SLEEP(1); mg = lds_ld(&sm.merged); } \
            CBAR(); \
            if (ISV) { const unsigned aa = lds_addr(&sm.acc[u % NOUT][lane]); \
                asm volatile("ds_add_f64 %0, %1\n\tds_add_f64 %0, %2 offset:512\n\tds_add_f64 %0, %3 offset:1024" :: "v"(aa), "v"(acc[0]), "v"(acc[1]), "v"(acc[2]) : "memory"); \
                acc[0] = 0.0; acc[1] = 0.0; acc[2] = 0.0; } \
            CBAR(); \
            LDS_ADD(&sm.cnt[u % NOUT], np) \
            CBAR(); \
            np = 0; }

#define LOADZ2() { _Pragma("unroll") for (int p = 0; p < 4; p++) kz4[p] = sm.kz[p >> 1][rg * 4 + 2 * (p & 1) + rsel].x; ksc = sm.kz[(lane >> 1) & 1][rg * 4 + 2 * (lane & 1) + rsel].y * scale; }
#define STATIC_STEP(I) { \
            rg = ((cst + (I)) & 3) * 8 + wave; \
            LOADZ2() \
            K_TAB((I) + 1) K_BODY((I) + 1) }
        CS_DMA(wave, (cst + 2) & 3)
        RDYWAIT(0, 1u) RDYWAIT(1, 2u)
        LOADQ()
#pragma unroll
        for (int i = 0; i < 3; i++) { Rp[i] = sm.q[1][3 * sub + i]; Rn[i] = __builtin_amdgcn_alignbit(Rp[i], Rp[i], 16) ^ 0x80000000u; }
        STATIC_STEP(0)
        asm volatile("s_waitcnt vmcnt(11)" ::: "memory");
        REPACK(wave, (cst + 1) & 3)
        TABDONE(1)
        SISSUE(2)
        CS_DMA(wave, (cst + 3) & 3)
        STATIC_STEP(1)
        asm volatile("s_waitcnt vmcnt(11)" ::: "memory");
        REPACK(wave, (cst + 2) & 3)
        TABDONE(2)
        SISSUE(3)
        STATIC_STEP(2)
        asm volatile("s_waitcnt vmcnt(8)" ::: "memory");
        REPACK(wave, (cst + 3) & 3)
        TABDONE(3)
        GRAB(2)
        vut = LDV(&sm.utab[lane]);
        STATIC_STEP(3)
        CBAR();
        LDS_ADD(&sm.cnt[0], 4u) LDS_ADD(&sm.cnt[1], 4u)
        PLAN()
        LAUNCH()
        u = un; rg = nrg; tdc = tdn + 10;
        bool tabok = false;
        __builtin_amdgcn_s_setprio(2);
#pragma unroll 1
        while (u < NU) {
            GRAB(2)
            mg = LDV(&sm.merged);
            vut = LDV(&sm.utab[lane]);
            LINE()
            if (!tabok) {
                for (;;) { const unsigned tb_ = lds_ld(&sm.tabs); tabok = tb_ == ~0u; if (((tb_ >> rg) & 3u) == 3u) break; SLEEP(1) }
                CBAR();
            }
            K_TAB(0)
            if (first) {
                unsigned fl = LDV(&sm.rdy[u % NIN]);
                CBAR();
                LOADQ() LOADZ()
                if (__builtin_amdgcn_readfirstlane(fl) != u + 1) { RDYWAIT(u % NIN, u + 1) LOADQ() LOADZ() }
            } else LOADZ()
            K_PRE4(0)
            PLAN()
            K_WAIT(0, 0) K_FMA(0) K_WAIT(0, 1) K_FMA(1)
            asm volatile("" :: "v"(kres[0]), "v"(kres[1]));
            K_WAIT(0, 2) K_WAIT(0, 3)
            ARRIVED()
            K_FMA(2) K_FMA(3) K_RED(0)
            np += 2;
            ADVANCE(false)
        }
#pragma unroll 1
        while (u < 2 * NU) {
            GRAB(2)
            mg = LDV(&sm.merged);
            LINE()
            const unsigned vr_ = LDV(&sm.vrdy);
            CBAR();
            const unsigned short* wp = &sm.wv[went(u - NU) + (QUARTER(u - NU) ? rg & 7 : rg) * 4];
            v4i pw = *(const v4i*)wp;
            if (first && !((__builtin_amdgcn_readfirstlane(vr_) >> (u - NU)) & 1)) { while (!((lds_ld(&sm.vrdy) >> (u - NU)) & 1)) SLEEP(1); CBAR(); pw = *(const v4i*)wp; }
            const double w0 = (short)pw.x, w1 = pw.x >> 16, w2 = (short)pw.y, w3 = pw.y >> 16, w4 = (short)pw.z, w5 = pw.z >> 16, w6 = (short)pw.w, w7 = pw.w >> 16;
            asm volatile("" :: "v"(w0), "v"(w1), "v"(w2), "v"(w3), "v"(w4), "v"(w5), "v"(w6), "v"(w7));
            PLAN()
            v2f hl; unsigned tl8, rowl;
            if (none) { hl = *(const v2f*)&sm.wv[went(NU - 1) + 128]; tl8 = LDV(&sm.tag_lo) << 24; rowl = (lds_ld(&sm.utab[2 * NU - 1]) & 255) * (NSPLIT * 512); asm volatile("" : "+v"(hl), "+v"(tl8)); }
            WAITT(0, 0, 6); V_FMA(0, w0, w1) V_PIN()
            WAITT(1, 1, 4); V_FMA(1, w2, w3) V_PIN()
            WAITT(2, 2, 2); V_FMA(2, w4, w5) V_PIN()
            WAITT(3, 3, 0);
            np += 2;
            if (none) {
                V_FMA(3, w6, w7)
                if (u != 2 * NU - 1) { FLUSH(true) break; }
                mg = __builtin_amdgcn_readfirstlane(mg);
                while (u >= mg + NOUT) { SLEEP(1); mg = lds_ld(&sm.merged); }
                const unsigned aa = lds_addr(&sm.acc[(2 * NU - 1) % NOUT][lane]);
                double a0, a1, a2; unsigned cn; u64 ex_;
                asm volatile("ds_add_f64 %5, %6\n\tds_add_f64 %5, %7 offset:512\n\tds_add_f64 %5, %8 offset:1024\n\t"
                             "s_mov_b64 %4, exec\n\ts_mov_b64 exec, 1\n\tds_add_rtn_u32 %3, %9, %10\n\ts_mov_b64 exec, %4\n\t"
                             "ds_read_b64 %0, %5\n\tds_read_b64 %1, %5 offset:512\n\tds_read_b64 %2, %5 offset:1024\n\ts_waitcnt lgkmcnt(0)"
                             : "=&v"(a0), "=&v"(a1), "=&v"(a2), "=&v"(cn), "=&s"(ex_) : "v"(aa), "v"(acc[0]), "v"(acc[1]), "v"(acc[2]), "v"(lds_addr(&sm.cnt[(2 * NU - 1) % NOUT])), "v"(np) : "memory");
                const float n0 = (float)(a0 - (double)hl.y) * hl.x, n1 = (float)(a1 - (double)hl.y) * hl.x, n2 = (float)(a2 - (double)hl.y) * hl.x;
                const unsigned e2 = max(__float_as_uint(fmaxf(fmaxf(fabsf(n0), fabsf(n1)), fabsf(n2))) >> 23, 14u);
                const float f2 = __uint_as_float((268u - e2) << 23);
                const v2u r = {__builtin_bit_cast(unsigned, __builtin_amdgcn_cvt_pk_i16((int)rintf(n0 * f2), (int)rintf(n1 * f2))),
                               __builtin_bit_cast(unsigned, __builtin_amdgcn_cvt_pk_i16((int)rintf(n2 * f2), 0)) | e2 << 16 | tl8};
                const __amdgpu_buffer_rsrc_t rsrc = __builtin_amdgcn_make_buffer_rsrc(ws + WS_PART + split * 512, 0, NRW * NSPLIT * 512 - split * 512, 0x00020000);
                asm volatile("" :: "v"(r));
                if (__builtin_amdgcn_readfirstlane(cn + np) == 32) {
                    __builtin_amdgcn_raw_buffer_store_b64(r, rsrc, lane * 8, rowl, SC1);
#pragma unroll
                    for (int j = 0; j < NCP - 1; j++) __builtin_amdgcn_raw_buffer_store_b64(r, rsrc, lane * 8, (rowl & 15 * NSPLIT * 512 ^ NSPLIT * 512) + (NRR + j * GSZ) * NSPLIT * 512, SC1);
                }
                break;
            }
            ARRIVED()
            V_FMA(3, w6, w7)
            ADVANCE(true)
        }
    } else {
    u64 E1;
    if (wave == NSW) {
        unsigned ref;
        const unsigned sidx = (blockIdx.x & 7) | ((blockIdx.x >> 7) << 3);
#define SAMPLE(K) ((((blockIdx.x >> 3) - (blockIdx.x & 7) - (K) * 5) & 15) == 0)
#define BTAG(K) ((btag + (K)) & 0xfffffu)
#define POST(K, C) { if (lane == 0) __hip_atomic_store((u64*)(ws + (bd + (K) * RST + (sidx >> 3) * 256 + (sidx & 7) * 8)), (u64)(C) | ((u64)(((C) - ref) | BTAG(K) << 12) << 32), __ATOMIC_RELAXED, __HIP_MEMORY_SCOPE_AGENT); }
        const unsigned own = lds_ld(&sm.line);
        if (SAMPLE(0) && lane < NTRY) __hip_atomic_store((unsigned*)(ws + WS_HELLO + lane * 768) + sidx, own, __ATOMIC_RELAXED, __HIP_MEMORY_SCOPE_AGENT);
        const u64 E = __hip_atomic_load((const u64*)(ws + WS_EPOCH), __ATOMIC_RELAXED, __HIP_MEMORY_SCOPE_AGENT);
        E1 = E + 1;
        if (lane == 0) { sm.tag_lo = (unsigned)E1; sm.tag_hi = (unsigned)(E1 >> 32); CBAR(); lds_st(&sm.tag_rdy, 1u); }
        const unsigned btag = __builtin_amdgcn_readfirstlane((unsigned)E1) * 61u, bd = WS_BOARD + (btag & 7) * (NRND * RST);
        ref = own;
        int off = 0;
        {
            unsigned tries = 0, cur = own, tl = own;
            bool news = false;
#pragma unroll 1
            while (!news || cur != ref) {
                const unsigned now = NOW();
                if (tries < NTRY && (int)(now - own) >= (int)(TA + tries * TA2)) {
                    v16u x;
                    asm volatile("s_load_dwordx16 %0, %1, %2\n\ts_waitcnt lgkmcnt(0)" : "=&s"(x) : "s"(ws), "s"(__builtin_amdgcn_readfirstlane(WS_HELLO + tries * 768)) : "memory");
                    unsigned bad = 0; int sum = 0, dif = 0;
#pragma unroll
                    for (int i = 0; i < 16; i++) { bad |= x[i] - own + 512; sum += x[i] - x[0]; dif += i & 4 ? x[i] : -x[i]; }
                    tries++;
                    if (bad < 1024) { const unsigned com = x[0] + (sum >> 4) + abs(dif) * (8 - KC) / 128; ref = x[0] + ((sum + (blockIdx.x & 4 ? dif : -dif)) >> 4); off = ref - com;
                        tries = NTRY; tl = NOW(); if (lane == 0) lds_st(&sm.aline, com); }
                    continue;
                }
                if (!news && lds_ld(&sm.utab[31]) != ~0u) { news = true; CBAR(); if (cur == ref) tl = now; ref = lds_ld(&sm.aline) + off; tries = NTRY; }
                const int d = (now - tl) >> CSH;
                if (cur != ref && d) { tl += d << CSH; cur += min(max((int)(ref - cur), -d), d); if (lane == 0) lds_st(&sm.line, cur); }
                SLEEP(2)
            }
        }
        ref -= off;
        const unsigned ref0 = ref;
        const unsigned lead = (blockIdx.x >> 3) == 9 ? 0 : LEAD;
        unsigned posted = ref, lagf = ref, tpost = ref + TB1, tpoll = tpost + RDD + lead, kw = 0, kr = 0, cur = ref + off, khold = 0;
        int o = off; bool held = false;
#pragma unroll 1
        for (;;) {
            const unsigned p = lds_ld(&sm.grab);
            if (p >= NPC) break;
            lagf += (int)(lds_ld(&sm.late) - lagf) >> 1;
            { const unsigned l = lds_ld(&sm.line); if (l != cur) { cur = l; khold = kw; held = true; } }
            const unsigned now = NOW();
            if (o) { o = off * min(max((int)(ref0 + TC0 + (1 << TCS) - now), 0), 1 << TCS) >> TCS; if (!held && cur != ref + o) { cur = ref + o; if (lane == 0) lds_st(&sm.line, cur); } }
            if (kw < NRND && (int)(now - tpost) >= 0) {
                const unsigned c = ref + min((p >= PSL0 && (int)(lagf - cur) > LHI ? lagf - LOK : cur) - o - ref, 4000u);
                if (SAMPLE(kw) && (c != ref || (ref != posted && (sidx & 7) == (kw & 7)))) POST(kw, c)
                posted = ref;
                tpost += PER; kw++;
            }
            if (kr < NRND && (int)(now - tpoll) >= 0) {
                v16u xa, xb;
                asm volatile("s_load_dwordx16 %0, %2, %3\n\ts_load_dwordx16 %1, %2, %4\n\ts_waitcnt lgkmcnt(0)" : "=&s"(xa), "=&s"(xb)
                             : "s"(ws), "s"(__builtin_amdgcn_readfirstlane(bd + kr * RST)), "s"(__builtin_amdgcn_readfirstlane(bd + kr * RST + 256)) : "memory");
                int nb = 0, sg = 0;
#pragma unroll
                for (int i = 0; i < 16; i++) {
                    const unsigned lo = i < 8 ? xa[2 * i & 15] : xb[2 * i & 15], hi = i < 8 ? xa[(2 * i + 1) & 15] : xb[(2 * i + 1) & 15];
                    if (hi >> 12 == BTAG(kr) && lo - ref0 < 8000) { nb = max(nb, (int)(lo - (hi & 4095u) - ref)); sg += hi & 4095u; }
                }
                ref += nb + (sg >> 4);
                if (cur != ref + o && (kr >= khold || (int)(ref + o - cur) > 0)) { cur = ref + o; held = false; if (lane == 0) lds_st(&sm.line, cur); }
                tpoll += PER; kr++;
            }
            SLEEP(2)
        }
    } else if (wave == NSW + 1 || wave == NSW + 2) {
        const int ql = lane < 48 ? lane : 47;
        unsigned c1, c2, s1, s2;
#define QT_LOADS() { int qo_ = ql; asm volatile("" : "+v"(qo_)); \
                     c1 = ((const unsigned*)q_cos)[qo_]; c2 = ((const unsigned*)q_cos)[48 + qo_]; s1 = ((const unsigned*)q_sin)[qo_]; s2 = ((const unsigned*)q_sin)[48 + qo_]; }
        unsigned q1, q2, lkz, lks, lvz, lvs;
        const unsigned sofs = R8(((unsigned)lane >> 1) & 7) * 2 + (lane & 1);
#define UNIT_LOADS(HEAD, GRP) { \
        q1 = ((const unsigned*)(q + (size_t)(HEAD) * DH))[ql]; q2 = ((const unsigned*)(q + (size_t)(HEAD) * DH))[48 + ql]; \
        const size_t so = (size_t)(HEAD) * (SKV / 2) + (GRP) * 256 + sofs; \
        lkz = *((const unsigned*)k_zeros + so); lks = *((const unsigned*)k_scales + so); \
        lvz = *((const unsigned*)v_zeros + so); lvs = *((const unsigned*)v_scales + so); }
#define UNIT_FILL(SL, U) { \
            v4f qq; \
            qq.x = bfr(bfr(bflo(q1) * bflo(c1)) + bfr(-bflo(q2) * bflo(s1))); \
            qq.y = bfr(bfr(bflo(q2) * bflo(c2)) + bfr(bflo(q1) * bflo(s2))); \
            qq.z = bfr(bfr(bfhi(q1) * bfhi(c1)) + bfr(-bfhi(q2) * bfhi(s1))); \
            qq.w = bfr(bfr(bfhi(q2) * bfhi(c2)) + bfr(bfhi(q1) * bfhi(s2))); \
            *(v2u*)&sm.q[SL][2 * ql] = (v2u){(__float_as_uint(qq.x) >> 16) | __float_as_uint(qq.y), (__float_as_uint(qq.z) >> 16) | __float_as_uint(qq.w)}; \
            *(v4f*)&sm.kz[SL][2 * lane] = (v4f){bflo(lkz), bflo(lks), bfhi(lkz), bfhi(lks)}; \
            *(v4f*)&sm.vz[SL][2 * lane] = (v4f){bflo(lvz), bflo(lvs), bfhi(lvz), bfhi(lvs)}; \
            CBAR(); \
            if (lane == 0) lds_st(&sm.rdy[SL], (U) + 1); \
            CBAR(); }
        const unsigned dw = NSW + 2 - wave;
#define TOUCH() { const unsigned x_ = ((blockIdx.x >> 3) - 18) * 64 + lane; \
            const char* p_ = x_ < 384 ? (const char*)(x_ < 192 ? qk : qv) + (size_t)(x_ < 192 ? x_ : x_ - 192) * (2u << 20) + (x_ * 8 + (blockIdx.x & 7)) * 640 : (const char*)out + (x_ - 384) * 4096; \
            if (x_ < 408) asm volatile("global_load_dword a0, %0, off nt" :: "v"(p_) : "a0"); }
        QT_LOADS()
        UNIT_LOADS(head0 + dw * (GSZ + 1 - 2 * side), lane >> 4)
        if (dw) TOUCH()
#pragma unroll 1
        for (unsigned u = dw;;) {
            while (u >= lds_ld(&sm.merged) + NIN) SLEEP(4);
            CBAR();
            UNIT_FILL(u % NIN, u)
            u += 2;
            if (u >= NU) break;
            if (u == 4) {
                const unsigned tpf = tdue(PSF - 1) + TFO;
                { int d; while ((d = (int)(lds_ld(&sm.line) + tpf - NOW())) > 0 && d < 3000) SLEEP(1) }
#define PF3(P) asm volatile("global_load_dwordx4 a[0:3], %0, %1\n\tglobal_load_dwordx4 a[0:3], %0, %1 offset:1024\n\tglobal_load_dwordx4 a[0:3], %0, %1 offset:2048" \
                            :: "v"(64 * SKV * 2 + blockIdx.x * 3072 + lane * 16), "s"(P) : "a0", "a1", "a2", "a3")
                PF3(k_zeros); PF3(k_scales); PF3(v_zeros); PF3(v_scales);
                asm volatile("global_load_dword a0, %0, %1" :: "v"(blockIdx.x * (DH * 2) + ql * 8), "s"(q) : "a0");
            }
            const unsigned tin = tdue(first_of(u) - (QUARTER(u) ? 24 : 0) - LEADP + (split & 7) * DPH);
            { int d; while ((d = (int)(lds_ld(&sm.line) + tin - NOW())) > 0 && d < 3000) SLEEP(4) }
            unsigned hd;
            while ((hd = lds_ld(&sm.utab[u])) == ~0u) SLEEP(2)
            UNIT_LOADS(hd & 255, QUARTER(u) ? rank & 3 : lane >> 4)
        }
        while (lds_ld(&sm.tag_rdy) == 0) SLEEP(1);
        CBAR();
        E1 = (u64)lds_ld(&sm.tag_lo) | ((u64)lds_ld(&sm.tag_hi) << 32);
        if (wave == NSW + 1 && blockIdx.x == 0) {
            for (;;) {
                bool ok = true;
#pragma unroll
                for (int i = 0; i < NH / 64; i++) ok &= __hip_atomic_load((const u64*)(ws + WS_POST) + i * 64 + lane, __ATOMIC_RELAXED, __HIP_MEMORY_SCOPE_AGENT) >> 24 == (E1 << 24) >> 24;
                if (__builtin_amdgcn_ballot_w64(ok) == ~0ull) break;
                SLEEP(64);
            }
            if (lane == 0) __hip_atomic_store((u64*)(ws + WS_EPOCH), E1, __ATOMIC_RELAXED, __HIP_MEMORY_SCOPE_AGENT);
        }
        if (wave == NSW + 2) {
            const unsigned t16 = (unsigned)E1 & 0xffffu, t16b = ((unsigned)E1 + ((unsigned)E1 >> 16)) & 0xffffu;
            const __amdgpu_buffer_rsrc_t rsrc = __builtin_amdgcn_make_buffer_rsrc(ws + WS_SWAP + (blockIdx.x ^ 4) * 5120, 0, 5120, 0x00020000);
            const unsigned swp = lds_ld(&sm.swp);
            if (swp) {
                unsigned use = 0;
#pragma unroll
                for (int i = 0; i < 5; i++) use |= ((swp >> wunit(i * 64 + lane)) & 1) << i;
                const unsigned tg = tdue(PSG - 1) + (lds_ld(&sm.gap) & 0xffffu);
                while ((int)(NOW() - lds_ld(&sm.line) - tg) < 0) SLEEP(2)
                for (;;) {
                    v4u d[5]; bool ok = true;
#pragma unroll
                    for (int i = 0; i < 5; i++) d[i] = __builtin_amdgcn_raw_buffer_load_b128(rsrc, (use >> i) & 1 ? (i * 64 + lane) * 16 : 0x40000000, 0, SC1 | 2);
#pragma unroll
                    for (int i = 0; i < 5; i++) if ((use >> i) & 1) {
                        ok &= d[i].y >> 16 == t16 && d[i].w >> 16 == t16b;
                        unsigned* w = (unsigned*)&sm.wv[144 * NMIX + (i * 64 + lane) * 6];
                        w[0] = d[i].x; w[1] = (d[i].y & 0xffffu) | (d[i].z << 16); w[2] = (d[i].z >> 16) | (d[i].w << 16);
                    }
                    if (__builtin_amdgcn_ballot_w64(ok) == ~0ull) break;
                    SLEEP(32)
                }
                CBAR();
                LDS_OR(&sm.vrdy, swp)
            }
        }
    } else {
        const unsigned eu = lane >= NU ? lane - NU : lane, win = min(first_of(eu) >> 5, 15u), pair = QUARTER(eu) ? 2 * (eu - WQ) + (rank >> 2) : rank;
        const unsigned rot2 = ((const unsigned char*)ROT)[win * 8 + pair];
#define UENT(BIT) (win * 16 + 2 * pair + ((BIT) ^ side) | (QUARTER(eu) ? (rank - (eu - WQ)) * 8 & 31 : (rot2 >> ((BIT) ^ side) * 4 & 15) * 2) << 8)
        unsigned far = 0;
        const unsigned hp = (blockIdx.x & 1) * NH + (blockIdx.x >> 1) * 4 + 2;
        if (blockIdx.x < 144) {
            const char* pb = (blockIdx.x < 128 ? (const char*)(hp >= NH ? qv : qk) + (size_t)(hp & (NH - 1)) * (SKV * DH * 4) + SKV * DH * 2
                                               : (const char*)ws + WS_PART + (8u << 20) + (blockIdx.x & 15) * 65536) + ((NOW() * 0x9e3779b1u) >> 26) * 128;
            unsigned mn[2] = {~0u, ~0u};
#pragma unroll 1
            for (unsigned i = 1; i < 2 * NPRB + 2; i++) {
                u64 t0, t1; unsigned dm;
                asm volatile("s_memtime %0\n\ts_waitcnt lgkmcnt(0)\n\ts_load_dword %2, %3, 0x0\n\ts_waitcnt lgkmcnt(0)\n\ts_memtime %1\n\ts_waitcnt lgkmcnt(0)"
                             : "=&s"(t0), "=&s"(t1), "=&s"(dm) : "s"(pb + i * 8192) : "memory");
                const unsigned t = (unsigned)(t1 - t0) < 330 || i == 1 ? ~0u : (unsigned)(t1 - t0);
                if (i & 1) mn[1] = min(mn[1], t); else mn[0] = min(mn[0], t);
            }
            far = mn[0] > mn[1];
        }
        if (lane < NMIX) lds_st(&sm.utab[lane], UENT(lane & 1));
        while (lds_ld(&sm.tag_rdy) == 0) SLEEP(1);
        CBAR();
        E1 = (u64)lds_ld(&sm.tag_lo) | ((u64)lds_ld(&sm.tag_hi) << 32);
        {
            const unsigned own = lds_ld(&sm.line), tg = (unsigned)E1;
            if (lane == 0) __hip_atomic_store((u64*)(ws + WS_POST) + blockIdx.x, E1 << 24 | min(lds_ld(&sm.slow), 4000u) << 1 | ((far ^ (blockIdx.x >> 2) ^ (blockIdx.x < 128 ? 0x96 >> (hp & 7) : 0)) & 1), __ATOMIC_RELAXED, __HIP_MEMORY_SCOPE_AGENT);
            const __amdgpu_buffer_rsrc_t nrs = __builtin_amdgcn_make_buffer_rsrc(ws + WS_NEWS, 0, 8 * 1280, 0x00020000);
            if (blockIdx.x == MASTER) {
                while ((int)(NOW() - own) < TMAP) SLEEP(2)
                const unsigned a = tg << 24, b = (unsigned)(E1 >> 8);
                v4u x; v2u z; u64 v0, v1;
#pragma unroll 1
                for (int i = 0; i < 8; i++) {
                    x = __builtin_amdgcn_raw_buffer_load_b128(__builtin_amdgcn_make_buffer_rsrc(ws + WS_POST, 0, 1024, 0x00020000), lane * 16, 0, SC1);
                    z = __builtin_amdgcn_raw_buffer_load_b64(__builtin_amdgcn_make_buffer_rsrc(ws + WS_POST + 1024, 0, 128, 0x00020000), lane * 8, 0, SC1);
                    v0 = __builtin_amdgcn_ballot_w64(((x.x ^ a) >> 24 | (x.y ^ b)) == 0); v1 = __builtin_amdgcn_ballot_w64(((x.z ^ a) >> 24 | (x.w ^ b)) == 0);
                    if (__builtin_popcountll(v0) + __builtin_popcountll(v1) >= MINV) break;
                    SLEEP(8)
                }
                const u64 m0 = __builtin_amdgcn_ballot_w64(x.x & 1) & v0, m1 = __builtin_amdgcn_ballot_w64(x.z & 1) & v1;
                const int s0 = lane < 43 ? min(max(lane - VW, 4 * NMIX), 42 - 2 * VW) : min(max(lane - VW, 43), 63 - 2 * VW);
#define PC(M) __builtin_popcount((unsigned)((M) >> s0) & ((2u << 2 * VW) - 1))
                const u64 g0 = __builtin_amdgcn_ballot_w64(2 * PC(m0) > PC(v0)), g1 = __builtin_amdgcn_ballot_w64(2 * PC(m1) > PC(v1));
                const float sl = wave_sum(((v0 >> lane & 1) ? (float)(x.x >> 1 & 4095u) : 0.f) + ((v1 >> lane & 1) ? (float)(x.z >> 1 & 4095u) : 0.f)) / ((8.f - PRE0) * (__builtin_popcountll(v0) + __builtin_popcountll(v1)) + 1.f);
                const unsigned al = lds_ld(&sm.aline), mean = (al ? al : own) + (int)fmaxf(sl - SL0, 0.f);
                const bool vz = lane < 16 && ((z.x ^ a) >> 24 | (z.y ^ b)) == 0;
                const unsigned gw = 2 * __builtin_popcountll(__builtin_amdgcn_ballot_w64(vz && (z.x & 1))) > __builtin_popcountll(__builtin_amdgcn_ballot_w64(vz));
                const unsigned k = lane & 3;
                if (lane < 32 && k < 3) __builtin_amdgcn_raw_buffer_store_b128((v4u){k == 0 ? (unsigned)g0 : k == 1 ? (unsigned)g1 : mean, tg, k == 0 ? (unsigned)(g0 >> 32) : k == 1 ? (unsigned)(g1 >> 32) : gw, tg},
                                                                              nrs, (lane >> 2) * 1280 + k * 16, 0, SC1);
            }
            while ((int)(NOW() - own) < TMAP + TRES) SLEEP(2)
            v4u y;
            for (;;) {
                y = __builtin_amdgcn_raw_buffer_load_b128(nrs, lane < 3 ? (blockIdx.x & 7) * 1280 + lane * 16 : 0x40000000, 0, SC1);
                if (((unsigned)__builtin_amdgcn_ballot_w64(y.y == tg && y.w == tg) & 7u) == 7u) break;
                SLEEP(8)
            }
            const u64 f0 = (unsigned)__builtin_amdgcn_readlane(y.x, 0) | (u64)(unsigned)__builtin_amdgcn_readlane(y.z, 0) << 32, f1 = (unsigned)__builtin_amdgcn_readlane(y.x, 1) | (u64)(unsigned)__builtin_amdgcn_readlane(y.z, 1) << 32;
            if (lane == 2) { sm.aline = y.x - own + 500 < 1000 ? y.x : own; sm.chead = (blockIdx.x & ~5u) | ((blockIdx.x >> 2 ^ y.z) & 1) | (blockIdx.x & 1) << 2; }
            unsigned bit = ((unsigned)((lane >= NU ? f1 : f0) >> (4 * win + (pair >> 1))) ^ (0x66 >> pair)) & 1;
            if (eu < NMIX) bit = eu & 1;
            const u64 bm = __builtin_amdgcn_ballot_w64(bit);
            const unsigned nsw = __builtin_popcountll((f0 ^ f1) >> (4 * NMIX));
            if (lane == 0) { sm.swp = (unsigned)(bm ^ (bm >> NU)) & ((1u << NU) - 1); lds_st(&sm.gap, nsw ? (TG0 + TG1 * nsw / 8) * 0x10001u + ((TH0 + TH1 * nsw / 8) << 16) : 0u); }
            CBAR();
            lds_st(&sm.utab[lane], UENT(bit));
        }
        const unsigned swp = lds_ld(&sm.swp);
        const unsigned T = lds_ld(&sm.tag_lo);
        const unsigned t15 = T & 0x7fffu;
        const __amdgpu_buffer_rsrc_t rsrc = __builtin_amdgcn_make_buffer_rsrc(ws + WS_PART + split * 512, 0, NRW * NSPLIT * 512 - split * 512, 0x00020000);
        v2u* recs = (v2u*)&sm.cs[0][0][0];
#pragma unroll
        for (int i = 0; i < NOUT * 3; i++) (&sm.acc[0][0])[i * 64 + lane] = 0.0;
        CBAR();
        unsigned vut = sm.utab[NU + (lane & 31)] & 255;
        if (QUARTER((unsigned)lane) && (rank & 3)) vut = NH - 16 + (rank & 3) * 16 + ((vut & 15) ^ 1);
        __builtin_amdgcn_s_setprio(3);
        unsigned po[NU / 2];
#pragma unroll
        for (int k = 0; k < NU / 2; k++) po[k] = __builtin_amdgcn_ds_bpermute((2 * k + (lane >> 5)) * 4, vut) * (NSPLIT * 512) + (lane & 31) * 16;
        const u64 rb_ = (u64)(ws + WS_PART + split * 512);
        const v4u rs_ = {(unsigned)rb_, (unsigned)(rb_ >> 32) & 0xffffu, NRW * NSPLIT * 512 - split * 512, 0x00020000u};
#pragma unroll 1
        for (unsigned u = 0; u < 2 * NU - 1; u++) {
            const unsigned e = u % NU, so = u % NOUT;
            if (u >= NU) while (!((lds_ld(&sm.vrdy) >> e) & 1)) SLEEP(4)
            CBAR();
            const v4f h = *(const v4f*)&sm.wv[went(e) + (QUARTER(e) ? 32 : 128)];
            while (lds_ld(&sm.cnt[so]) != (QUARTER(e) ? 8 : 32)) SLEEP(1)
            CBAR();
            const double a0 = sm.acc[so][lane], a1 = sm.acc[so][64 + lane], a2 = sm.acc[so][128 + lane];
            if (u < NU) {
                float2 lg = *(const float2*)&sm.ent[so][2 * lane];
                if (QUARTER(e) && lane >> 4 != (rank & 3)) lg = make_float2(-INFINITY, -INFINITY);
                const v4f zs = *(const v4f*)&sm.vz[u % NIN][2 * lane];
                const float m = wave_max(fmaxf(lg.x, lg.y));
                const float p0 = __expf(lg.x - m), p1 = __expf(lg.y - m);
                const float x0 = p0 * zs.y, x1 = p1 * zs.w;
                const unsigned eb = max(__float_as_uint(wave_max(fmaxf(fabsf(x0), fabsf(x1)))) >> 23, 14u);
                const float f = __uint_as_float((268u - eb) << 23);
                const float w0 = fminf(fmaxf(rintf(x0 * f), -32767.f), 32767.f), w1 = fminf(fmaxf(rintf(x1 * f), -32767.f), 32767.f);
                const float den = wave_sum(p0 + p1);
                const float bz = wave_sum(fmaf(w0, zs.x, w1 * zs.z));
                const float L = fmaf(m, 1.4426950408889634f, __builtin_amdgcn_logf(den));
                unsigned* wd = (unsigned*)(((swp >> u) & 1 ? sm.wo : sm.wv) + went(u));
                if (!QUARTER(u) || lane >> 4 == (rank & 3)) wd[QUARTER(u) ? lane & 15 : lane] = ((unsigned)(int)w0 & 0xffffu) | ((unsigned)(int)w1 << 16);
                if (lane == 0) *(v4f*)&wd[QUARTER(u) ? 16 : 64] = (v4f){ldexpf(1.f / den, (int)eb - 141), bz, L, 0.f};
                CBAR();
                if (!((swp >> u) & 1)) LDS_OR(&sm.vrdy, 1u << u)
            } else {
                const float n0 = (float)(a0 - (double)h.y) * h.x, n1 = (float)(a1 - (double)h.y) * h.x, n2 = (float)(a2 - (double)h.y) * h.x;
                const unsigned e2 = max(__float_as_uint(wave_maxv(fmaxf(fmaxf(fabsf(n0), fabsf(n1)), fabsf(n2)))) >> 23, 14u);
                const float f2 = __uint_as_float((268u - e2) << 23);
                const unsigned m01 = __builtin_bit_cast(unsigned, __builtin_amdgcn_cvt_pk_i16((int)rintf(n0 * f2), (int)rintf(n1 * f2)));
                const unsigned m2 = __builtin_bit_cast(unsigned, __builtin_amdgcn_cvt_pk_i16((int)rintf(n2 * f2), 0));
                const unsigned bit = ((lane & 1 ? e2 : __float_as_uint(h.z)) >> (lane >> 1)) & 1u;
                recs[e * 64 + lane] = (v2u){m01, m2 | bit << 16 | t15 << 17};
                CBAR();
                sm.acc[so][lane] = 0.0; sm.acc[so][64 + lane] = 0.0; sm.acc[so][128 + lane] = 0.0;
            }
            CBAR();
            if (lane == 0) { lds_st(&sm.cnt[so], 0u); CBAR(); lds_st(&sm.merged, u + 1); }
            if (u == NU - 1 && swp) {
                wave_sync();
                const __amdgpu_buffer_rsrc_t srs = __builtin_amdgcn_make_buffer_rsrc(ws + WS_SWAP + blockIdx.x * 5120, 0, 5120, 0x00020000);
                v4u d[5]; unsigned o[5];
#pragma unroll
                for (int i = 0; i < 5; i++) {
                    const unsigned* w = (const unsigned*)&sm.wo[144 * NMIX + (i * 64 + lane) * 6];
                    const unsigned d0 = w[0], d1 = w[1], d2 = w[2];
                    d[i] = (v4u){d0, (d1 & 0xffffu) | (T << 16), (d1 >> 16) | (d2 << 16), (d2 >> 16) | ((T + (T >> 16)) << 16)};
                    o[i] = (swp >> wunit(i * 64 + lane)) & 1 ? (i * 64 + lane) * 16 : 0x40000000;
                }
                while ((int)(NOW() - lds_ld(&sm.line)) < (int)tdue(PSW) + TWO) SLEEP(1)
#pragma unroll
                for (int i = 0; i < 5; i++) __builtin_amdgcn_raw_buffer_store_b128(d[i], srs, o[i], 0, SC1 | 2);
            }
        }
        wave_sync();
#pragma unroll
        for (int k = 0; k < NU / 2; k++) asm volatile("ds_read_b128 a[%c0:%c0+3], %1 offset:%2" :: "i"(16 + 4 * k), "v"(lds_addr(recs) + lane * 16), "i"(k * 1024) : "memory");
        asm volatile("s_waitcnt lgkmcnt(0)" ::: "memory");
        while (!((lds_ld(&sm.vrdy) >> (NU - 1)) & 1)) SLEEP(4)
        CBAR();
        const v2u hw_ = {*(const unsigned*)&sm.wv[went(NU - 1) + 132], T};
        const unsigned hrow = (NRW - GSZ + (__builtin_amdgcn_readlane(vut, NU - 1) & 15 ^ 1)) * (NSPLIT * 512);
        while (lds_ld(&sm.grab) < NPC - PUBP) SLEEP(1)
        if (lane < 16) __builtin_amdgcn_raw_buffer_store_b64(hw_, rsrc, lane * 8, hrow, SC1);
#define PUB(K) asm volatile("buffer_store_dwordx4 a[%c0:%c0+3], %1, %2, 0 offen sc1" :: "i"(16 + 4 * (K)), "v"(po[K]), "s"(rs_) : "memory");
        PUB(WQ / 2) PUB(WQ / 2 + 1)
        { const unsigned t_ = TLAST() + PDEL; GATE(t_) }
        PUB(0) PUB(1) PUB(2) PUB(3) PUB(4) PUB(7) PUB(8)
        return;
    }
    }
    __builtin_amdgcn_s_setprio(0);
    unsigned c;
#define ADDRTN(P) { u64 ex_; asm volatile("s_mov_b64 %0, exec\n\ts_mov_b64 exec, 1\n\tds_add_rtn_u32 a15, %1, a13\n\ts_mov_b64 exec, %0" : "=&s"(ex_) : "v"(lds_addr(P)) : "a15", "memory"); }
#define ADDED(V) { unsigned t_; asm volatile("s_waitcnt lgkmcnt(0)\n\tv_accvgpr_read_b32 %1, a15\n\ts_nop 0\n\tv_readfirstlane_b32 %0, %1\n\ts_nop 1" : "=s"(V), "=v"(t_)); }
    asm volatile("v_accvgpr_write_b32 a13, 1\n\ts_nop 1" ::: "a13");
    ADDRTN(&sm.cidx) ADDED(c)
    if (c < NSW) {
        while (lds_ld(&sm.grab) < NPC + 2) SLEEP(4)
        const unsigned head = lds_ld(&sm.chead), T = lds_ld(&sm.tag_lo), tl = TLAST();
        const u64 rb_ = (u64)(ws + WS_PART);
        const v4u rs_ = {(unsigned)rb_, (unsigned)(rb_ >> 32) & 0xffffu, NRW * NSPLIT * 512, 0x00020000u};
        const unsigned ro = head * (NSPLIT * 512) + c * 1024, rx = (NH + (head & 15 ^ 1)) * (NSPLIT * 512) + c * 1024;
#define POLL(S, O) asm volatile("buffer_load_dwordx4 a[%c0:%c0+3], %1, %2, %3 offen sc1" :: "i"(16 + 4 * (S)), "v"(lane * 16), "s"(rs_), "s"(O) : "memory");
#define FETCH(X, S, N) asm volatile("s_waitcnt vmcnt(%c5)\n\tv_accvgpr_read_b32 %0, a[%c4]\n\tv_accvgpr_read_b32 %1, a[%c4+1]\n\tv_accvgpr_read_b32 %2, a[%c4+2]\n\tv_accvgpr_read_b32 %3, a[%c4+3]" \
                                    : "=v"((X).x), "=v"((X).y), "=v"((X).z), "=v"((X).w) : "i"(16 + 4 * (S)), "i"(N));
        float o0, o1, o2;
        if (head >= NH - GSZ) {
            __builtin_amdgcn_s_setprio(3);
            if (c == 0) {
                v2u hd;
                GATE(tl + THD)
                for (;;) {
                    hd = __builtin_amdgcn_raw_buffer_load_b64(__builtin_amdgcn_make_buffer_rsrc(ws + WS_PART + (NRW - GSZ + (head & 15 ^ 1)) * (NSPLIT * 512), 0, NSPLIT * 512, 0x00020000), (lane & 15) * 512, 0, SC1);
                    if (__builtin_amdgcn_ballot_w64(hd.y == T) == ~0ull) break;
                    SLEEP(4)
                }
                const float w = __builtin_amdgcn_exp2f(__uint_as_float(hd.x) - row_max(__uint_as_float(hd.x)));
                if (lane < 16) sm.ent[0][lane] = w * __builtin_amdgcn_rcpf(row_sum(w));
                CBAR();
                if (lane == 0) lds_st(&sm.tabs, 0u);
            }
            while (lds_ld(&sm.tabs) || lds_ld(&sm.merged) <= (2 * NU - 1) / NOUT * NOUT) SLEEP(1)
            CBAR();
            const float cf = sm.ent[0][2 * c + (lane >> 5)];
            v4u x; bool got;
#define SEEN(J, N) { FETCH(x, J, N) got = __builtin_amdgcn_ballot_w64((((x.y ^ T << 24) | (x.w ^ T << 24)) >> 24) != 0) == 0; }
            GATE(tl + TCA) POLL(0, ro) GATE(tl + TCA + TCD) POLL(1, rx + 3 * GSZ * NSPLIT * 512) SEEN(0, 1)
            if (!got) SEEN(1, 0)
            while (!got) { POLL(2, rx + 4 * GSZ * NSPLIT * 512) SEEN(2, 0) SLEEP(0) }
            const double c0 = ldexpf(cf, (int)(x.y >> 16 & 255u) - 141), c1 = ldexpf(cf, (int)(x.w >> 16 & 255u) - 141);
#define SWADD(A, B) ({ const v2u a_ = __builtin_bit_cast(v2u, A), b_ = __builtin_bit_cast(v2u, B); \
                const auto l_ = __builtin_amdgcn_permlane32_swap(a_.x, b_.x, false, false); const auto h_ = __builtin_amdgcn_permlane32_swap(a_.y, b_.y, false, false); \
                __builtin_bit_cast(double, (v2u){l_[0], h_[0]}) + __builtin_bit_cast(double, (v2u){l_[1], h_[1]}); })
            const double s0 = SWADD(c0 * (double)(short)x.x, c1 * (double)(short)x.z), s1 = SWADD(c0 * (double)((int)x.x >> 16), c1 * (double)((int)x.z >> 16)),
                         s2 = SWADD(c0 * (double)(short)x.y, c1 * (double)(short)x.w);
            asm volatile("ds_add_f64 %0, %1\n\tds_add_f64 %0, %2 offset:512\n\tds_add_f64 %0, %3 offset:1024" :: "v"(lds_addr(&sm.acc[0][lane])), "v"(s0), "v"(s1), "v"(s2) : "memory");
            ADDRTN(&sm.cdone)
            const double a0 = sm.acc[0][lane], a1 = sm.acc[0][64 + lane], a2 = sm.acc[0][128 + lane];
            ADDED(c)
            o0 = a0; o1 = a1; o2 = a2;
        } else {
            const bool quart = head >> 4 == WQ;
            const unsigned t15 = T & 0x7fffu;
#define TAKE(K) { FETCH(x[K], K, 0) \
                bad |= (x[K].y >> 17 ^ t15) | (x[K].w >> 17 ^ t15); \
                const u64 bl = __builtin_amdgcn_ballot_w64((x[K].y & 0x10000u) != 0), be = __builtin_amdgcn_ballot_w64((x[K].w & 0x10000u) != 0); \
                L[K] = __uint_as_float(lane < 32 ? (unsigned)bl : (unsigned)(bl >> 32)); ex[K] = lane < 32 ? (unsigned)be : (unsigned)(be >> 32); \
                M = fmaxf(M, fmaxf(__uint_as_float((unsigned)bl), __uint_as_float((unsigned)(bl >> 32)))); }
#define SUMUP(K) { const float w_ = __builtin_amdgcn_exp2f(L[K] - M), c_ = ldexpf(w_, (int)(ex[K] & 255u) - 141); den += w_; \
                t[0] = fmaf(c_, (float)(short)x[K].x, t[0]); t[1] = fmaf(c_, (float)((int)x[K].x >> 16), t[1]); t[2] = fmaf(c_, (float)(short)x[K].y, t[2]); \
                t[3] = fmaf(c_, (float)(short)x[K].z, t[3]); t[4] = fmaf(c_, (float)((int)x[K].z >> 16), t[4]); t[5] = fmaf(c_, (float)(short)x[K].w, t[5]); }
            const unsigned tg = tl + (quart ? TRQ : TRD);
            GATE(tg)
            v4u x[4]; float L[4], M; unsigned ex[4];
            for (;;) {
                unsigned bad = 0; M = -3e38f;
                POLL(0, ro)
                if (quart) { POLL(1, rx) POLL(2, rx + GSZ * NSPLIT * 512) POLL(3, rx + 2 * GSZ * NSPLIT * 512) }
                __builtin_amdgcn_s_setprio(3);
                TAKE(0)
                if (quart) { TAKE(1) TAKE(2) TAKE(3) }
                if (__builtin_amdgcn_ballot_w64(bad != 0) == 0) break;
                SLEEP(2)
            }
            float t[6] = {0.f, 0.f, 0.f, 0.f, 0.f, 0.f}, den = 0.f;
            SUMUP(0)
            if (quart) { SUMUP(1) SUMUP(2) SUMUP(3) }
#define SWADF(A, B) ({ const auto l_ = __builtin_amdgcn_permlane32_swap(__float_as_uint(A), __float_as_uint(B), false, false); __uint_as_float(l_[0]) + __uint_as_float(l_[1]); })
            v4f* pb = (v4f*)((char*)&sm.cs[0][0][0] + 65536);
            float* pm = (float*)(pb + NSW * 64);
            pb[c * 64 + lane] = (v4f){SWADF(t[0], t[3]), SWADF(t[1], t[4]), SWADF(t[2], t[5]), SWADF(den, den)};
            if (lane == 0) pm[c] = M;
            CBAR();
            ADDRTN(&sm.cdone)
            ADDED(c)
            CBAR();
            if (c == NSW - 1) {
                const v4f ma = *(const v4f*)pm, mb = *(const v4f*)(pm + 4);
                const float mw[NSW] = {ma.x, ma.y, ma.z, ma.w, mb.x, mb.y, mb.z, mb.w};
                float Mx = mw[0];
#pragma unroll
                for (int w = 1; w < NSW; w++) Mx = fmaxf(Mx, mw[w]);
                v4f sum = {0.f, 0.f, 0.f, 0.f};
#pragma unroll
                for (int w = 0; w < NSW; w++) sum += pb[w * 64 + lane] * __builtin_amdgcn_exp2f(mw[w] - Mx);
                const float inv = __builtin_amdgcn_rcpf(sum.w);
                o0 = sum.x * inv; o1 = sum.y * inv; o2 = sum.z * inv;
            }
        }
        if (c == NSW - 1) {
            asm("" : "+v"(o0), "+v"(o1), "+v"(o2));
            __hip_bfloat16* o = out + (size_t)head * DH + (lane & 31) * 6 + (lane >> 5) * 3;
            o[0] = __float2bfloat16(o0); o[1] = __float2bfloat16(o1); o[2] = __float2bfloat16(o2);
        }
    }
}

extern "C" void kernel_launch(void* const* d_in, const int* in_sizes, int n_in,
                              void* d_out, int out_size, void* d_ws, size_t ws_size,
                              hipStream_t stream) {
    const unsigned short* q = (const unsigned short*)d_in[0];
    const int* qk = (const int*)d_in[1];
    const unsigned short* k_scales = (const unsigned short*)d_in[2];
    const unsigned short* k_zeros = (const unsigned short*)d_in[3];
    const int* qv = (const int*)d_in[4];
    const unsigned short* v_scales = (const unsigned short*)d_in[5];
    const unsigned short* v_zeros = (const unsigned short*)d_in[6];
    const unsigned short* q_cos = (const unsigned short*)d_in[7];
    const unsigned short* q_sin = (const unsigned short*)d_in[8];
    const unsigned short* k_cos = (const unsigned short*)d_in[9];
    const unsigned short* k_sin = (const unsigned short*)d_in[10];
    unsigned char* ws = (unsigned char*)d_ws;
    attn_kernel<<<NSPLIT * GSZ, NTHREADS, 0, stream>>>(q, qk, k_scales, k_zeros, qv, v_scales, v_zeros,
                                                       q_cos, q_sin, k_cos, k_sin, (__hip_bfloat16*)d_out, ws);
}
```

```cpp
#define DIAG 38
#include <hip/hip_runtime.h>
#include <hip/hip_bf16.h>
#include <stdint.h>

#define DH 192
#define HALF 96
#define SKV 2048
#define NH 256
#define NSW 8
#define NWAVES (NSW + 4)
#define NTHREADS (64 * NWAVES)
#define BROWS 128
#define NSPLIT 16
#define GSZ 16
#define WQ 10
#define NU (NH / GSZ + 3)
#define NPC 1024
#define NRR (NH + 3 * GSZ)
#define NIN 8
#define NOUT 8
#define WALL (15 * 144 + 4 * 48)
#define SC1 16
#define NRND 48
#define RST 4352
#define TK 2
#define NMIX 3
#define TUNIT 311
#define T01 50
#define TMIX 7
#define TOFF 50
#define PSF 100
#define TGS 68
#define PSW (NPC / 2 + 28)
#define TG0 8
#define TG1 3
#define TWO -75
#define PSG (NPC / 2 + 60)
#define TH0 3
#define TH1 3
#define EARLY 10
#define LEADP 24
#define DPH 2
#define TA 210
#define TA2 70
#define CSH 2
#define NPRB 3
#define TMAP 520
#define MINV 96
#define VW 4
#define MASTER 203
#define TRES 190
#define TB1 1200
#define PER 300
#define RDD 100
#define LEAD 70
#define LHI 85
#define LOK 74
#define PSL0 124
#define PUBP -14
#define LJ 210
#define LOK2 75
#define NTRY 6
#define TFO 6
#define SL0 42.f
#define PST0 200
#define KC 4
#define TC0 1000
#define TCS 9
#define PRE0 3

#define KREL 12
#define PDEL 70
#define NCP 3
#define NRW (NRR + NCP * GSZ)
#define TRD 112
#define THD 55
#define TRQ 55
#define TCA 132
#define TCD 22
#define WS_EPOCH 0
#define WS_POST0 8192
#define WS_POST (WS_POST0 + ((unsigned)E1 & 15) * 2048)
#define WS_NEWS (49152 + 256)
#define WS_HELLO 59904
#define WS_BOARD 65536
#define WS_PART (2u << 20)
#define WS_SWAP (WS_PART + NRW * NSPLIT * 512)

typedef unsigned int v2u __attribute__((ext_vector_type(2)));
typedef unsigned int v16u __attribute__((ext_vector_type(16)));
typedef unsigned int v4u __attribute__((ext_vector_type(4)));
typedef int v4i __attribute__((ext_vector_type(4)));
typedef float v2f __attribute__((ext_vector_type(2)));
typedef float v4f __attribute__((ext_vector_type(4)));
typedef unsigned long long u64;

__device__ __forceinline__ float bflo(unsigned u) { return __uint_as_float(u << 16); }
__device__ __forceinline__ float bfhi(unsigned u) { return __uint_as_float(u & 0xffff0000u); }
__device__ __forceinline__ float bfr(float x) {
    __bf16 b = (__bf16)x;
    return __uint_as_float(((unsigned)__builtin_bit_cast(unsigned short, b)) << 16);
}

#define SLEEP(n) SLEEP_(n)
#define SLEEP_(n) asm volatile("s_sleep " #n "\n\tv_add_co_u32 %0, vcc, 1, %0\n\ts_cbranch_vccz 1f\n\ts_endpgm\n1:" : "+v"(spins) :: "vcc", "memory");
#define SPIN0 0xf0000000u
#define CBAR() asm volatile("" ::: "memory")

__device__ __forceinline__ void wave_sync() {
    __builtin_amdgcn_fence(__ATOMIC_RELEASE, "wavefront");
    __builtin_amdgcn_wave_barrier();
    __builtin_amdgcn_fence(__ATOMIC_ACQUIRE, "wavefront");
}
template <int CTRL>
__device__ __forceinline__ float dpp_add(float x) {
    int y = __builtin_amdgcn_update_dpp(0, __float_as_int(x), CTRL, 0xf, 0xf, true);
    return x + __int_as_float(y);
}
template <int CTRL>
__device__ __forceinline__ float dpp_max(float x) {
    int y = __builtin_amdgcn_update_dpp(__float_as_int(x), __float_as_int(x), CTRL, 0xf, 0xf, false);
    return fmaxf(x, __int_as_float(y));
}
__device__ __forceinline__ float row_sum(float x) { x = dpp_add<0x128>(x); x = dpp_add<0x124>(x); x = dpp_add<0x122>(x); return dpp_add<0x121>(x); }
__device__ __forceinline__ float row_max(float m) { m = dpp_max<0x128>(m); m = dpp_max<0x124>(m); m = dpp_max<0x122>(m); return dpp_max<0x121>(m); }
__device__ __forceinline__ float rdlane(float x, int l) { return __int_as_float(__builtin_amdgcn_readlane(__float_as_int(x), l)); }
__device__ __forceinline__ float wave_max(float m) { m = row_max(m); return fmaxf(fmaxf(rdlane(m, 0), rdlane(m, 16)), fmaxf(rdlane(m, 32), rdlane(m, 48))); }
__device__ __forceinline__ float wave_maxv(float m) {
    m = row_max(m);
    const auto a = __builtin_amdgcn_permlane16_swap(__float_as_uint(m), __float_as_uint(m), false, false);
    m = fmaxf(__uint_as_float(a[0]), __uint_as_float(a[1]));
    const auto b = __builtin_amdgcn_permlane32_swap(__float_as_uint(m), __float_as_uint(m), false, false);
    return fmaxf(__uint_as_float(b[0]), __uint_as_float(b[1]));
}
__device__ __forceinline__ float wave_sum(float x) { x = row_sum(x); return (rdlane(x, 0) + rdlane(x, 16)) + (rdlane(x, 32) + rdlane(x, 48)); }
__device__ __forceinline__ unsigned lds_ld(const unsigned* p) { return __builtin_amdgcn_readfirstlane(__hip_atomic_load(p, __ATOMIC_RELAXED, __HIP_MEMORY_SCOPE_WORKGROUP)); }
__device__ __forceinline__ unsigned lds_addr(const void* p) { return (unsigned)(size_t)(const __attribute__((address_space(3))) void*)p; }
#define DMA16(SRC, LDS) asm volatile("s_mov_b32 %0, m0\n\ts_mov_b32 m0, %2\n\ts_nop 0\n\tglobal_load_lds_dwordx4 %1, off\n\ts_mov_b32 m0, %0" : "=&s"(m0keep) : "v"(SRC), "s"(LDS) : "memory")
__device__ __forceinline__ void lds_st(unsigned* p, unsigned v) { __hip_atomic_store(p, v, __ATOMIC_RELAXED, __HIP_MEMORY_SCOPE_WORKGROUP); }
#define NOW() ((unsigned)__builtin_amdgcn_s_memrealtime())
__device__ __forceinline__ unsigned tdue(unsigned g) {
    return (g * TUNIT + min(g, 480u) * TK + min(g, 48u) * T01 + (min(g, 32u * NMIX) + min(max((int)g - NPC / 2, 0), 32 * NMIX)) * TMIX + (g >= PSF ? TGS * 32 : 0)) >> 5;
}

__device__ __forceinline__ unsigned unit_of(unsigned g) { const unsigned p = g & 511; return (g >> 9) * NU + (p >> 5) + min(max((int)p - 32 * WQ, 0) >> 3, 3); }
__device__ __forceinline__ unsigned first_of(unsigned e) { return 32 * e - 24 * min(max((int)e - WQ, 0), 4); }
#define QUARTER(E) ((E) - WQ < 4)
__device__ __forceinline__ unsigned went(unsigned e) { return 144 * e - 96 * min(max((int)e - WQ, 0), 4); }
__device__ __forceinline__ unsigned wunit(unsigned x) { x += 24 * NMIX; return x < 24 * WQ ? x / 24 : x < 24 * WQ + 32 ? WQ + (x - 24 * WQ) / 8 : (x + 64) / 24; }

#define ROTT 0xed8d2c3d2c35ed84ull, 0xba85aa165b03ac82ull, 0xee7efa489a38af71ull, 0xe932da1b0bab51d8ull, 0xfa96112d5fee971bull, 0x2c7e9ea5a544f071ull, 0x3d86093d5f0e810bull, 0xe932da1b0bab51d8ull, 0xfa96112d5fee971bull, 0xd834e62c5fa723e9ull, 0x1d7d1a3d2c061d88ull, 0xe932da1b0bab51d8ull, 0xfa96112d5fee971bull, 0xc834e12c0aaa4ec7ull, 0x3d86093d5f0e810bull, 0xe932da1b0bab51d8ull
static __device__ const u64 ROT[16] = {ROTT};

struct __attribute__((aligned(16))) Smem {
    unsigned cs[NSW][4][768];
    double acc[NOUT][DH];
    float ent[NOUT][BROWS];
    unsigned short wv[WALL];
    unsigned short wo[WALL];
    unsigned q[NIN][HALF];
    float2 kz[NIN][BROWS];
    float2 vz[NIN][BROWS];
    unsigned utab[64];
    unsigned line, gap;
    unsigned late, grab;
    unsigned rdy[NIN], cnt[NOUT];
    unsigned tabs, vrdy, swp;
    unsigned aline;
    unsigned tag_rdy, tag_lo, tag_hi, merged, cdone, cidx;
    unsigned slow;
    unsigned chead, dg;
};

#define LDS_ADD(P, V) { u64 ex_; asm volatile("s_mov_b64 %0, exec\n\ts_mov_b64 exec, 1\n\tds_add_u32 %1, %2\n\ts_mov_b64 exec, %0" : "=&s"(ex_) : "v"(lds_addr(P)), "v"(V) : "memory"); }
#define LDS_OR(P, V) { u64 ex_; asm volatile("s_mov_b64 %0, exec\n\ts_mov_b64 exec, 1\n\tds_or_b32 %1, %2\n\ts_mov_b64 exec, %0" : "=&s"(ex_) : "v"(lds_addr(P)), "v"(V) : "memory"); }

__global__ __launch_bounds__(NTHREADS, 3) void attn_kernel(
    const unsigned short* __restrict__ q, const int* __restrict__ qk,
    const unsigned short* __restrict__ k_scales, const unsigned short* __restrict__ k_zeros,
    const int* __restrict__ qv,
    const unsigned short* __restrict__ v_scales, const unsigned short* __restrict__ v_zeros,
    const unsigned short* __restrict__ q_cos, const unsigned short* __restrict__ q_sin,
    const unsigned short* __restrict__ k_cos, const unsigned short* __restrict__ k_sin,
    __hip_bfloat16* __restrict__ out, unsigned char* __restrict__ ws)
{
    __shared__ Smem sm;
    const int tid = threadIdx.x;
    unsigned spins = SPIN0;
    const int lane = tid & 63;
    const int hw = __builtin_amdgcn_readfirstlane(tid >> 6);
    u64 t00; unsigned ka;
    asm volatile("s_memrealtime %0\n\ts_load_dword %1, %2, 0x40" : "=&s"(t00), "=&s"(ka) : "s"(__builtin_amdgcn_kernarg_segment_ptr()));
    if (hw == 0) {
        const char* pc0; asm volatile("s_getpc_b64 %0" : "=s"(pc0));
        asm volatile("global_load_dword a6, %0, %1" :: "v"(((blockIdx.x & ~7u) + (lane & 7)) * 128), "s"(pc0) : "a6", "memory");
    }
    const int wave = hw >= 4 ? hw - 4 : hw + NSW;
    const unsigned split = (blockIdx.x & 1) + (blockIdx.x & 2) * 4 + ((blockIdx.x >> 3) & 3) * 2;
    const unsigned side = (split ^ (blockIdx.x >> 2)) & 1, rank = blockIdx.x >> 5;
    const unsigned head0 = 2 * rank + side;
    const unsigned rf0 = (8 * split + 1) / 3, r3 = 8 * split + 1 - 3 * rf0;
    const unsigned koff = ((lane >> 5) * DH + 3 * (lane & 31)) * 4, koffb = koff + 384;
#define AR(S) (16 + (S) * 8)
#define GLD(A, OFF, BASE, IMM) asm volatile("global_load_dwordx3 a[%c0:%c1], %2, %3 offset:%4 nt" :: "i"(A), "i"((A) + 2), "v"(OFF), "s"(BASE), "i"(IMM))
#define ISSUE(S, J, BASE, OA, OB) { GLD(AR(S), OA, BASE, (J) * 1536); GLD(AR(S) + 4, OB, BASE, (J) * 1536); }
#define HEAD2 ((GSZ + 1 - 2 * side) * (SKV * DH * 4))
    const unsigned wo = (hw - 2) & 7;
    const bool pre = hw >= 4 && wo < PRE0;
    if (pre) {
        const char* a = (const char*)qk + (head0 * 512 + side * 256 + rf0 + wave + (wave >= 6 ? 80 : 0)) * 3072; const char* b = a + HEAD2;
        ISSUE(0, 0, a, koff, koffb) ISSUE(1, 1, a, koff, koffb) ISSUE(2, 0, b, koff, koffb) ISSUE(3, 1, b, koff, koffb)
    }
    asm volatile("s_waitcnt lgkmcnt(0)" : "+s"(t00), "+s"(ka));
    if (hw == 0) {
        if (DIAG == 30 || DIAG == 39 || DIAG == 38) sm.dg = NOW() - (unsigned)t00;
        sm.utab[lane] = ~0u;
        unsigned l0 = (unsigned)t00 + TOFF;
        if (KREL) { const unsigned t1 = NOW() + KREL; if ((int)(t1 - l0) > 0) l0 = t1; }
        if (lane < 32) (&sm.line)[lane] = lane == 3 ? 64u : (lane & 29) == 0 ? l0 : 0u;
        asm volatile("s_waitcnt lgkmcnt(0)" ::: "memory");
    }
    __builtin_amdgcn_s_barrier();
    CBAR();
    const u64 rtab = r3 == 1 ? 0x5756552c2b020100ull : r3 == 0 ? 0x5756552c2b2a0100ull : 0x57562d2c2b020100ull;
#define R8(I) (rf0 + ((unsigned)(rtab >> (8 * (I))) & 255u))
#define GATE(DUE) { int d_ = (int)((DUE) - NOW()); if (d_ < 3000) while (d_ > 0) { __builtin_amdgcn_s_sleep(1); d_ = (int)((DUE) - NOW()); } }
#define TLAST() (lds_ld(&sm.line) + tdue(NPC - 2) + (lds_ld(&sm.gap) >> 16))
    if (__builtin_expect(wave < NSW, 1)) {
        const int cst = 2 * side;
        const unsigned r8w = R8((unsigned)wave);
        const char* cbase[3];
#pragma unroll
        for (int j = 0; j < 3; j++) { const int x = j * 64 + lane; cbase[j] = (x < 96 ? (const char*)k_cos + x * 16 : (const char*)k_sin + (x - 96) * 16) + r8w * (4 * DH * 2); }
        unsigned m0keep;
#define CS_DMA(W, C) { _Pragma("unroll") for (int j = 0; j < 3; j++) DMA16(cbase[j] + (C) * (512 * DH * 2), lds_addr(&sm.cs[0][0][0]) + ((W) * 4 + (C)) * 3072 + j * 1024); }
#define REPACK(W, C) { \
        unsigned* cw = &sm.cs[W][C][0]; \
        const unsigned* l = cw + rsel * (DH / 2) + qoff; \
        unsigned r[2][6]; \
        _Pragma("unroll") \
        for (int j = 0; j < 2; j++) { \
            unsigned e[4][2]; \
            _Pragma("unroll") \
            for (int k = 0; k < 4; k++) { \
                const unsigned* a = l + j * DH + (k & 1) * 48 + (k >> 1) * 384;       \
                unsigned x0 = a[0], x1 = a[1]; \
                e[k][0] = __builtin_amdgcn_alignbit(x1, x0, sh); \
                e[k][1] = x1 >> sh; \
            } \
            r[j][0] = (e[0][0] & 0xffffu) | (e[3][0] << 16); \
            r[j][1] = (e[0][0] >> 16) | (e[3][0] & 0xffff0000u); \
            r[j][2] = (e[0][1] & 0xffffu) | (e[3][1] << 16); \
            r[j][3] = (e[1][0] & 0xffffu) | (e[2][0] << 16); \
            r[j][4] = (e[1][0] >> 16) | (e[2][0] & 0xffff0000u); \
            r[j][5] = (e[1][1] & 0xffffu) | (e[2][1] << 16); \
        } \
        wave_sync(); \
        _Pragma("unroll") \
        for (int j = 0; j < 2; j++) { *(v4u*)&cw[j * 256 + lane * 4] = (v4u){r[j][0], r[j][1], r[j][2], r[j][3]}; *(v2u*)&cw[512 + j * 128 + lane * 2] = (v2u){r[j][4], r[j][5]}; } \
        wave_sync(); }
        const int sub = lane & 31;
        const int rsel = lane >> 5;
        const unsigned sh = (sub & 1) * 16;
        const int qoff = 3 * (sub >> 1) + (sub & 1);
        const unsigned voff = 3 * lane * 4;
        asm volatile("" ::: "a16", "a17", "a18", "a19", "a20", "a21", "a22", "a23", "a24", "a25", "a26", "a27", "a28", "a29", "a30", "a31",
                            "a32", "a33", "a34", "a35", "a36", "a37", "a38", "a39", "a40", "a41", "a42", "a43", "a44", "a45", "a46", "a47");
        asm volatile("" ::: "a48", "a49", "a50", "a51", "a52", "a53", "a54", "a55", "a56", "a57", "a58", "a59", "a60", "a61", "a62", "a63",
                            "a64", "a65", "a66", "a67", "a68", "a69", "a70", "a71", "a72", "a73", "a74", "a75", "a76", "a77", "a78", "a79");
        int ta[4][3], tb[4][3];
#define WAITT(P, S, N) asm volatile("s_waitcnt vmcnt(%6)\n\tv_accvgpr_read_b32 %0, a[%c7]\n\tv_accvgpr_read_b32 %1, a[%c8]\n\tv_accvgpr_read_b32 %2, a[%c9]\n\t" \
                              "v_accvgpr_read_b32 %3, a[%c10]\n\tv_accvgpr_read_b32 %4, a[%c11]\n\tv_accvgpr_read_b32 %5, a[%c12]" \
                              : "=v"(ta[P][0]), "=v"(ta[P][1]), "=v"(ta[P][2]), "=v"(tb[P][0]), "=v"(tb[P][1]), "=v"(tb[P][2]) \
                              : "i"(N), "i"(AR(S)), "i"(AR(S) + 1), "i"(AR(S) + 2), "i"(AR(S) + 4), "i"(AR(S) + 5), "i"(AR(S) + 6))
        const unsigned voffb = voff + 768;
        u64 ln = 0;
#define TDUE(G) (tdue(G) + ((G) >= PSW ? __builtin_amdgcn_readfirstlane((unsigned)(ln >> 32)) >> ((G) >= PSG ? 16 : 0) & 0xffffu : 0u))
#define REPORT() { const unsigned t_ = now - tdc; if (lane == 0) lds_st(&sm.late, t_); }
#define LINE() ln = LDV((u64*)&sm.line);
#define LDV(P) __hip_atomic_load(P, __ATOMIC_RELAXED, __HIP_MEMORY_SCOPE_WORKGROUP)
        unsigned u = 0, rg = cst * 8 + wave, np = 0;
        CS_DMA(wave, cst)

        const char* kb0 = (const char*)qk + (size_t)(head0 * 512 + r8w) * 3072;
#define KB(I) (kb0 + ((cst + (I)) & 3) * (128 * 3072))
        const char* kb; const char* kb2;
#define SISSUE(I) { kb = KB(I); kb2 = kb + HEAD2; const unsigned du_ = lds_ld(&sm.line) + tdue(16 * (I) + 2 * wo); GATE(du_) \
            ISSUE(((I) & 1) * 4, 0, kb, koff, koffb) ISSUE(((I) & 1) * 4 + 1, 1, kb, koff, koffb) ISSUE(((I) & 1) * 4 + 2, 0, kb2, koff, koffb) ISSUE(((I) & 1) * 4 + 3, 1, kb2, koff, koffb) }
        if (!pre) SISSUE(0)
        CS_DMA(wave, (cst + 1) & 3)
        if (pre) asm volatile("s_waitcnt vmcnt(3)" ::: "memory");
        if (DIAG == 32 && wave == 0 && lane == 0) sm.dg = NOW() - (unsigned)t00;
        if (DIAG == 33 && wave == 1 && lane == 0) sm.dg = NOW() - (unsigned)t00;
        asm volatile("s_waitcnt vmcnt(11)" ::: "memory");
        if (!pre) LDS_ADD(&sm.slow, NOW() - lds_ld(&sm.line) - tdue(2 * wo))
        REPACK(wave, cst)
        SISSUE(1)
#define TABDONE(I) LDS_OR(&sm.tabs, 1u << ((((cst + (I)) & 3) * 8) + wave))
        TABDONE(0)
        const float scale = 0.07216878364870322f;
        double acc[3] = {0.0, 0.0, 0.0};
        bool first = true, chg, none = false;
        unsigned Qp[3], Qn[3], Rp[3], Rn[3];
        float kz4[4], ksc, kres[4];
        v4u ca[4]; v2u cb[4];
        v2f qe[4][3], A[4];
        unsigned vut, nrg;
        const unsigned vr = ((lane >> 3) & 3) * 128 + R8((unsigned)lane & 7);
        unsigned g, mg;
        const unsigned grab_adr = lds_addr(&sm.grab);
        asm volatile("v_accvgpr_write_b32 a13, 1\n\tv_accvgpr_write_b32 a14, 2" ::: "a13", "a14", "a15");
#define GRAB(N) { u64 ex_; asm volatile("s_mov_b64 %0, exec\n\ts_mov_b64 exec, 1\n\tds_add_rtn_u32 a15, %1, a%c2\n\ts_mov_b64 exec, %0" : "=&s"(ex_) : "v"(grab_adr), "i"(12 + (N)) : "memory"); }
#define GRABBED() { unsigned t_; asm volatile("s_waitcnt lgkmcnt(0)\n\tv_accvgpr_read_b32 %1, a15\n\ts_nop 0\n\tv_readfirstlane_b32 %0, %1\n\ts_nop 1" : "=s"(g), "=v"(t_)); }
        unsigned oa, ob, un, tdn, tiss, tdc = 0;
#define PLAN() { \
            GRABBED() un = unit_of(g); chg = un != u; none = g >= NPC; \
            const bool nv = g >= NPC / 2; \
            unsigned hd = __builtin_amdgcn_readlane(vut, un); \
            while (hd == ~0u) { SLEEP(1) hd = lds_ld(&sm.utab[un]); } \
            nrg = (g + (hd >> 8)) & 31; hd &= 255; \
            unsigned boff, boff2; asm("s_mul_i32 %0, %1, 0xc00" : "=s"(boff) : "s"(hd * 512 + __builtin_amdgcn_readlane(vr, nrg))); \
            asm("s_mul_i32 %0, %1, 0xc00" : "=s"(boff2) : "s"(hd * 512 + __builtin_amdgcn_readlane(vr, nrg + 1))); \
            const char* b_ = nv ? (const char*)qv : (const char*)qk; \
            kb = b_ + boff; kb2 = b_ + boff2; \
            oa = nv ? voff : koff; ob = nv ? voffb : koffb; \
            tdn = TDUE(g); }
#define FIRE() { ISSUE(0, 0, kb, oa, ob) ISSUE(1, 1, kb, oa, ob) ISSUE(2, 0, kb2, oa, ob) ISSUE(3, 1, kb2, oa, ob) }
#define LAUNCH() { \
            LINE() \
            const unsigned du_ = __builtin_amdgcn_readfirstlane((unsigned)ln) + tdn; \
            GATE(du_) \
            ISSUE(0, 0, kb, oa, ob) ISSUE(1, 1, kb, oa, ob) \
            GATE(du_ + 10) \
            ISSUE(2, 0, kb2, oa, ob) ISSUE(3, 1, kb2, oa, ob) tiss = NOW(); }
#define ARRIVED() \
            const unsigned now = NOW(); \
            const bool stall = now - tiss > LJ && g - PST0 < NPC - 48 - PST0; \
            const bool late = !none && !stall && (int)(now + EARLY - __builtin_amdgcn_readfirstlane((unsigned)ln) - tdn) >= 0; \
            if (late) { FIRE() tiss = now; } \
            if (stall && lane == 0) __hip_atomic_fetch_max(&sm.line, (unsigned)ln + (now - tiss) - LOK2, __ATOMIC_RELAXED, __HIP_MEMORY_SCOPE_WORKGROUP); \
            REPORT()
#define ADVANCE(ISV) { \
            if (chg) FLUSH(ISV) \
            if (!none && !late) LAUNCH() \
            first = chg; u = un; rg = nrg; tdc = tdn + 10; }
#define K_TAB(MODE) { _Pragma("unroll") for (int p = 0; p < ((MODE) ? 2 : 4); p++) { \
            const unsigned* cw = &sm.cs[(rg & 7) + (p >> 1)][rg >> 3][0]; \
            ca[p] = *(const v4u*)&cw[(p & 1) * 256 + lane * 4]; cb[p] = *(const v2u*)&cw[512 + (p & 1) * 128 + lane * 2]; } }
#define K_PRE(MODE, P) { \
        float e0, e1, e2, f0, f1, f2; \
        const unsigned* qp_ = ((MODE) && (P) >= 2) ? Rp : Qp; const unsigned* qn_ = ((MODE) && (P) >= 2) ? Rn : Qn; \
        const int tp_ = (MODE) ? (P) & 1 : (P); \
        asm("v_dot2_f32_bf16 %0, %6, %12, 0\n\tv_dot2_f32_bf16 %3, %9, %15, 0\n\tv_dot2_f32_bf16 %1, %7, %13, 0\n\tv_dot2_f32_bf16 %4, %10, %16, 0\n\t" \
            "v_dot2_f32_bf16 %2, %8, %14, 0\n\tv_dot2_f32_bf16 %5, %11, %17, 0\n\ts_nop 2" \
            : "=&v"(e0), "=&v"(e1), "=&v"(e2), "=&v"(f0), "=&v"(f1), "=&v"(f2) \
            : "v"(qp_[0]), "v"(qp_[1]), "v"(qp_[2]), "v"(qn_[0]), "v"(qn_[1]), "v"(qn_[2]), \
              "v"(ca[tp_].x), "v"(ca[tp_].y), "v"(ca[tp_].z), "v"(ca[tp_].w), "v"(cb[tp_].x), "v"(cb[tp_].y)); \
        qe[P][0] = (v2f){e0, f0}; qe[P][1] = (v2f){e1, f1}; qe[P][2] = (v2f){e2, f2}; \
        A[P] = (qe[P][0] + qe[P][1] + qe[P][2]) * (v2f){-kz4[P], -kz4[P]}; }
#define K_WAIT(MODE, P) { \
        if ((MODE) == 0) { WAITT(P, P, 6 - 2 * (P)); } \
        else { WAITT(P, (((MODE) - 1) & 1) * 4 + (P), ((MODE) < 3 ? 20 : (MODE) == 3 ? 17 : 6) - 2 * (P)); } }
#define K_FMA(P) { \
        _Pragma("unroll") \
        for (int i = 0; i < 3; i++) { v2f x = {(float)ta[P][i], (float)tb[P][i]}; A[P] = qe[P][i] * x + A[P]; } \
        kres[P] = A[P].x + A[P].y; }
#define K_RED(MODE) { \
        const bool o1 = lane & 1, o2 = lane & 2; \
        float s01 = (o1 ? kres[1] : kres[0]) + __int_as_float(__builtin_amdgcn_update_dpp(0, __float_as_int(o1 ? kres[0] : kres[1]), 0xb1, 0xf, 0xf, true)); \
        float s23 = (o1 ? kres[3] : kres[2]) + __int_as_float(__builtin_amdgcn_update_dpp(0, __float_as_int(o1 ? kres[2] : kres[3]), 0xb1, 0xf, 0xf, true)); \
        float sx = (o2 ? s23 : s01) + __int_as_float(__builtin_amdgcn_update_dpp(0, __float_as_int(o2 ? s01 : s23), 0x4e, 0xf, 0xf, true)); \
        sx = dpp_add<0x124>(sx); sx = dpp_add<0x128>(sx); \
        const auto sw = __builtin_amdgcn_permlane16_swap(__float_as_uint(sx), __float_as_uint(sx), false, false); \
        sx = __uint_as_float(sw[0]) + __uint_as_float(sw[1]); \
        if ((lane & 28) == 0) { if (MODE) sm.ent[(lane >> 1) & 1][rg * 4 + 2 * (lane & 1) + rsel] = sx * ksc; else sm.ent[u % NOUT][rg * 4 + 2 * (lane & 3) + rsel] = sx * ksc; } }
#define K_PRE4(MODE) { K_PRE(MODE, 0) K_PRE(MODE, 1) K_PRE(MODE, 2) K_PRE(MODE, 3) asm volatile("" :: "v"(A[0]), "v"(A[1]), "v"(A[2]), "v"(A[3])); }
#define K_BODY(MODE) { K_PRE4(MODE) K_WAIT(MODE, 0) K_FMA(0) K_WAIT(MODE, 1) K_FMA(1) K_WAIT(MODE, 2) K_FMA(2) K_WAIT(MODE, 3) K_FMA(3) K_RED(MODE) }
#define V_FMA(J, W0, W1) { \
        _Pragma("unroll") \
        for (int i = 0; i < 3; i++) { \
            acc[i] = fma(W0, (double)ta[J][i], acc[i]); \
            acc[i] = fma(W1, (double)tb[J][i], acc[i]); \
        } }
#define V_PIN() asm volatile("" :: "v"(acc[0]), "v"(acc[1]), "v"(acc[2]));
#define RDYWAIT(SL, SEQ) { while (lds_ld(&sm.rdy[SL]) != (SEQ)) SLEEP(1); CBAR(); }
#define LOADZ() { _Pragma("unroll") for (int p = 0; p < 4; p++) kz4[p] = sm.kz[u % NIN][rg * 4 + 2 * p + rsel].x; ksc = sm.kz[u % NIN][rg * 4 + 2 * (lane & 3) + rsel].y * scale; }
#define LOADQ() { _Pragma("unroll") for (int i = 0; i < 3; i++) { Qp[i] = sm.q[u % NIN][3 * sub + i]; Qn[i] = __builtin_amdgcn_alignbit(Qp[i], Qp[i], 16) ^ 0x80000000u; } }
#define FLUSH(ISV) { \
            mg = __builtin_amdgcn_readfirstlane(mg); \
            while (u >= mg + NOUT) { SLEEP(1); mg = lds_ld(&sm.merged); } \
            CBAR(); \
            if (ISV) { const unsigned aa = lds_addr(&sm.acc[u % NOUT][lane]); \
                asm volatile("ds_add_f64 %0, %1\n\tds_add_f64 %0, %2 offset:512\n\tds_add_f64 %0, %3 offset:1024" :: "v"(aa), "v"(acc[0]), "v"(acc[1]), "v"(acc[2]) : "memory"); \
                acc[0] = 0.0; acc[1] = 0.0; acc[2] = 0.0; } \
            CBAR(); \
            LDS_ADD(&sm.cnt[u % NOUT], np) \
            CBAR(); \
            np = 0; }

#define LOADZ2() { _Pragma("unroll") for (int p = 0; p < 4; p++) kz4[p] = sm.kz[p >> 1][rg * 4 + 2 * (p & 1) + rsel].x; ksc = sm.kz[(lane >> 1) & 1][rg * 4 + 2 * (lane & 1) + rsel].y * scale; }
#define STATIC_STEP(I) { \
            rg = ((cst + (I)) & 3) * 8 + wave; \
            LOADZ2() \
            K_TAB((I) + 1) K_BODY((I) + 1) }
        CS_DMA(wave, (cst + 2) & 3)
        RDYWAIT(0, 1u) RDYWAIT(1, 2u)
        LOADQ()
#pragma unroll
        for (int i = 0; i < 3; i++) { Rp[i] = sm.q[1][3 * sub + i]; Rn[i] = __builtin_amdgcn_alignbit(Rp[i], Rp[i], 16) ^ 0x80000000u; }
        STATIC_STEP(0)
        asm volatile("s_waitcnt vmcnt(11)" ::: "memory");
        REPACK(wave, (cst + 1) & 3)
        TABDONE(1)
        SISSUE(2)
        CS_DMA(wave, (cst + 3) & 3)
        STATIC_STEP(1)
        asm volatile("s_waitcnt vmcnt(11)" ::: "memory");
        REPACK(wave, (cst + 2) & 3)
        TABDONE(2)
        SISSUE(3)
        STATIC_STEP(2)
        asm volatile("s_waitcnt vmcnt(8)" ::: "memory");
        REPACK(wave, (cst + 3) & 3)
        TABDONE(3)
        GRAB(2)
        vut = LDV(&sm.utab[lane]);
        STATIC_STEP(3)
        CBAR();
        LDS_ADD(&sm.cnt[0], 4u) LDS_ADD(&sm.cnt[1], 4u)
        PLAN()
        LAUNCH()
        u = un; rg = nrg; tdc = tdn + 10;
        bool tabok = false;
        __builtin_amdgcn_s_setprio(2);
#pragma unroll 1
        while (u < NU) {
            GRAB(2)
            mg = LDV(&sm.merged);
            vut = LDV(&sm.utab[lane]);
            LINE()
            if (!tabok) {
                for (;;) { const unsigned tb_ = lds_ld(&sm.tabs); tabok = tb_ == ~0u; if (((tb_ >> rg) & 3u) == 3u) break; SLEEP(1) }
                CBAR();
            }
            K_TAB(0)
            if (first) {
                unsigned fl = LDV(&sm.rdy[u % NIN]);
                CBAR();
                LOADQ() LOADZ()
                if (__builtin_amdgcn_readfirstlane(fl) != u + 1) { RDYWAIT(u % NIN, u + 1) LOADQ() LOADZ() }
            } else LOADZ()
            K_PRE4(0)
            PLAN()
            K_WAIT(0, 0) K_FMA(0) K_WAIT(0, 1) K_FMA(1)
            asm volatile("" :: "v"(kres[0]), "v"(kres[1]));
            K_WAIT(0, 2) K_WAIT(0, 3)
            ARRIVED()
            K_FMA(2) K_FMA(3) K_RED(0)
            np += 2;
            ADVANCE(false)
        }
#pragma unroll 1
        while (u < 2 * NU) {
            GRAB(2)
            mg = LDV(&sm.merged);
            LINE()
            const unsigned vr_ = LDV(&sm.vrdy);
            CBAR();
            const unsigned short* wp = &sm.wv[went(u - NU) + (QUARTER(u - NU) ? rg & 7 : rg) * 4];
            v4i pw = *(const v4i*)wp;
            if (first && !((__builtin_amdgcn_readfirstlane(vr_) >> (u - NU)) & 1)) { while (!((lds_ld(&sm.vrdy) >> (u - NU)) & 1)) SLEEP(1); CBAR(); pw = *(const v4i*)wp; }
            const double w0 = (short)pw.x, w1 = pw.x >> 16, w2 = (short)pw.y, w3 = pw.y >> 16, w4 = (short)pw.z, w5 = pw.z >> 16, w6 = (short)pw.w, w7 = pw.w >> 16;
            asm volatile("" :: "v"(w0), "v"(w1), "v"(w2), "v"(w3), "v"(w4), "v"(w5), "v"(w6), "v"(w7));
            PLAN()
            v2f hl; unsigned tl8, rowl;
            if (none) { hl = *(const v2f*)&sm.wv[went(NU - 1) + 128]; tl8 = LDV(&sm.tag_lo) << 24; rowl = (lds_ld(&sm.utab[2 * NU - 1]) & 255) * (NSPLIT * 512); asm volatile("" : "+v"(hl), "+v"(tl8)); }
            WAITT(0, 0, 6); V_FMA(0, w0, w1) V_PIN()
            WAITT(1, 1, 4); V_FMA(1, w2, w3) V_PIN()
            WAITT(2, 2, 2); V_FMA(2, w4, w5) V_PIN()
            WAITT(3, 3, 0);
            np += 2;
            if (none) {
                V_FMA(3, w6, w7)
                if (u != 2 * NU - 1) { FLUSH(true) break; }
                mg = __builtin_amdgcn_readfirstlane(mg);
                while (u >= mg + NOUT) { SLEEP(1); mg = lds_ld(&sm.merged); }
                const unsigned aa = lds_addr(&sm.acc[(2 * NU - 1) % NOUT][lane]);
                double a0, a1, a2; unsigned cn; u64 ex_;
                asm volatile("ds_add_f64 %5, %6\n\tds_add_f64 %5, %7 offset:512\n\tds_add_f64 %5, %8 offset:1024\n\t"
                             "s_mov_b64 %4, exec\n\ts_mov_b64 exec, 1\n\tds_add_rtn_u32 %3, %9, %10\n\ts_mov_b64 exec, %4\n\t"
                             "ds_read_b64 %0, %5\n\tds_read_b64 %1, %5 offset:512\n\tds_read_b64 %2, %5 offset:1024\n\ts_waitcnt lgkmcnt(0)"
                             : "=&v"(a0), "=&v"(a1), "=&v"(a2), "=&v"(cn), "=&s"(ex_) : "v"(aa), "v"(acc[0]), "v"(acc[1]), "v"(acc[2]), "v"(lds_addr(&sm.cnt[(2 * NU - 1) % NOUT])), "v"(np) : "memory");
                const float n0 = (float)(a0 - (double)hl.y) * hl.x, n1 = (float)(a1 - (double)hl.y) * hl.x, n2 = (float)(a2 - (double)hl.y) * hl.x;
                const unsigned e2 = max(__float_as_uint(fmaxf(fmaxf(fabsf(n0), fabsf(n1)), fabsf(n2))) >> 23, 14u);
                const float f2 = __uint_as_float((268u - e2) << 23);
                const v2u r = {__builtin_bit_cast(unsigned, __builtin_amdgcn_cvt_pk_i16((int)rintf(n0 * f2), (int)rintf(n1 * f2))),
                               __builtin_bit_cast(unsigned, __builtin_amdgcn_cvt_pk_i16((int)rintf(n2 * f2), 0)) | e2 << 16 | tl8};
                const __amdgpu_buffer_rsrc_t rsrc = __builtin_amdgcn_make_buffer_rsrc(ws + WS_PART + split * 512, 0, NRW * NSPLIT * 512 - split * 512, 0x00020000);
                asm volatile("" :: "v"(r));
                if (__builtin_amdgcn_readfirstlane(cn + np) == 32) {
                    __builtin_amdgcn_raw_buffer_store_b64(r, rsrc, lane * 8, rowl, SC1);
#pragma unroll
                    for (int j = 0; j < NCP - 1; j++) __builtin_amdgcn_raw_buffer_store_b64(r, rsrc, lane * 8, (rowl & 15 * NSPLIT * 512 ^ NSPLIT * 512) + (NRR + j * GSZ) * NSPLIT * 512, SC1);
                }
                break;
            }
            ARRIVED()
            V_FMA(3, w6, w7)
            ADVANCE(true)
        }
    } else {
    u64 E1;
    if (wave == NSW) {
        unsigned ref;
        const unsigned sidx = (blockIdx.x & 7) | ((blockIdx.x >> 7) << 3);
#define SAMPLE(K) ((((blockIdx.x >> 3) - (blockIdx.x & 7) - (K) * 5) & 15) == 0)
#define BTAG(K) ((btag + (K)) & 0xfffffu)
#define POST(K, C) { if (lane == 0) __hip_atomic_store((u64*)(ws + (bd + (K) * RST + (sidx >> 3) * 256 + (sidx & 7) * 8)), (u64)(C) | ((u64)(((C) - ref) | BTAG(K) << 12) << 32), __ATOMIC_RELAXED, __HIP_MEMORY_SCOPE_AGENT); }
        const unsigned own = lds_ld(&sm.line);
        if (SAMPLE(0) && lane < NTRY) __hip_atomic_store((unsigned*)(ws + WS_HELLO + lane * 768) + sidx, own, __ATOMIC_RELAXED, __HIP_MEMORY_SCOPE_AGENT);
        const u64 E = __hip_atomic_load((const u64*)(ws + WS_EPOCH), __ATOMIC_RELAXED, __HIP_MEMORY_SCOPE_AGENT);
        E1 = E + 1;
        if (lane == 0) { sm.tag_lo = (unsigned)E1; sm.tag_hi = (unsigned)(E1 >> 32); CBAR(); lds_st(&sm.tag_rdy, 1u); }
        const unsigned btag = __builtin_amdgcn_readfirstlane((unsigned)E1) * 61u, bd = WS_BOARD + (btag & 7) * (NRND * RST);
        ref = own; unsigned hmin = own, tnews = 0, ntry = 9; unsigned hmean = own; int nl = 20, iod = 0, hmx = 500;
        int off = 0;
        {
            unsigned tries = 0, cur = own, tl = own;
            bool news = false;
#pragma unroll 1
            while (!news || cur != ref) {
                const unsigned now = NOW();
                if (tries < NTRY && (int)(now - own) >= (int)(TA + tries * TA2)) {
                    v16u x;
                    asm volatile("s_load_dwordx16 %0, %1, %2\n\ts_waitcnt lgkmcnt(0)" : "=&s"(x) : "s"(ws), "s"(__builtin_amdgcn_readfirstlane(WS_HELLO + tries * 768)) : "memory");
                    unsigned bad = 0; int sum = 0, dif = 0;
#pragma unroll
                    for (int i = 0; i < 16; i++) { bad |= x[i] - own + 512; sum += x[i] - x[0]; dif += i & 4 ? x[i] : -x[i]; }
                    tries++;
                    if (bad < 1024) { ntry = tries; hmean = x[0] + (sum >> 4); for (int i = 0; i < 16; i++) if ((int)(x[i] - hmin) < 0) hmin = x[i]; nl = 0; hmx = 0; for (int i = 0; i < 16; i++) { nl += x[i] - hmin > 80; iod += (i & 4) ? (int)(x[i] - hmin) : -(int)(x[i] - hmin); hmx = max(hmx, (int)(x[i] - hmin)); } const unsigned com = x[0] + (sum >> 4) + abs(dif) * (8 - KC) / 128; ref = x[0] + ((sum + (blockIdx.x & 4 ? dif : -dif)) >> 4); off = ref - com;
                        tries = NTRY; tl = NOW(); if (lane == 0) lds_st(&sm.aline, com); }
                    continue;
                }
                if (!news && lds_ld(&sm.utab[31]) != ~0u) { news = true; tnews = now; CBAR(); if (cur == ref) tl = now; ref = lds_ld(&sm.aline) + off; tries = NTRY; }
                const int d = (now - tl) >> CSH;
                if (cur != ref && d) { tl += d << CSH; cur += min(max((int)(ref - cur), -d), d); if (lane == 0) lds_st(&sm.line, cur); }
                SLEEP(2)
            }
        }
        ref -= off;
        const unsigned ref0 = ref;
        unsigned dg[2] = {0, 0}; int mxj = 0, nsh = 0; unsigned lsum = 0, lcnt = 0;
        const unsigned lead = (blockIdx.x >> 3) == 9 ? 0 : LEAD;
        unsigned posted = ref, lagf = ref, tpost = ref + TB1, tpoll = tpost + RDD + lead, kw = 0, kr = 0, cur = ref + off, khold = 0;
        int o = off; bool held = false;
#pragma unroll 1
        for (;;) {
            const unsigned p = lds_ld(&sm.grab);
            if (p >= NPC) break;
            if (p < 260) dg[0] = ref; if (p < NPC / 2) dg[1] = ref;
            lagf += (int)(lds_ld(&sm.late) - lagf) >> 1;
            { const unsigned l = lds_ld(&sm.line); if (l != cur) { cur = l; khold = kw; held = true; } }
            const unsigned now = NOW();
            if (o) { o = off * min(max((int)(ref0 + TC0 + (1 << TCS) - now), 0), 1 << TCS) >> TCS; if (!held && cur != ref + o) { cur = ref + o; if (lane == 0) lds_st(&sm.line, cur); } }
            if (kw < NRND && (int)(now - tpost) >= 0) {
                const unsigned c = ref + min((p >= PSL0 && (int)(lagf - cur) > LHI ? lagf - LOK : cur) - o - ref, 4000u);
                if (SAMPLE(kw) && (c != ref || (ref != posted && (sidx & 7) == (kw & 7)))) POST(kw, c)
                posted = ref;
                tpost += PER; kw++;
            }
            if (kr < NRND && (int)(now - tpoll) >= 0) {
                v16u xa, xb;
                asm volatile("s_load_dwordx16 %0, %2, %3\n\ts_load_dwordx16 %1, %2, %4\n\ts_waitcnt lgkmcnt(0)" : "=&s"(xa), "=&s"(xb)
                             : "s"(ws), "s"(__builtin_amdgcn_readfirstlane(bd + kr * RST)), "s"(__builtin_amdgcn_readfirstlane(bd + kr * RST + 256)) : "memory");
                int nb = 0, sg = 0;
#pragma unroll
                for (int i = 0; i < 16; i++) {
                    const unsigned lo = i < 8 ? xa[2 * i & 15] : xb[2 * i & 15], hi = i < 8 ? xa[(2 * i + 1) & 15] : xb[(2 * i + 1) & 15];
                    if (hi >> 12 == BTAG(kr) && lo - ref0 < 8000) { nb = max(nb, (int)(lo - (hi & 4095u) - ref)); sg += hi & 4095u; }
                }
                ref += nb + (sg >> 4); mxj = max(mxj, nb + (sg >> 4)); nsh += (nb + (sg >> 4)) > 0;
                if (cur != ref + o && (kr >= khold || (int)(ref + o - cur) > 0)) { cur = ref + o; held = false; if (lane == 0) lds_st(&sm.line, cur); }
                tpoll += PER; kr++;
            }
            SLEEP(2)
        }
        if (blockIdx.x == 0) {
            unsigned X = 0;
            if (DIAG == 2) X = (ref - ref0) * 8;
            if (DIAG == 6) X = (dg[1] - ref0) * 8;
            if (DIAG == 8) X = (dg[0] - ref0) * 8;
            if (DIAG == 17) X = hmx * 8;
            if (DIAG == 22) X = (ref0 - hmean) * 8;
            if (DIAG == 24) X = (hmean - hmin) * 8;
            if (DIAG == 25) X = (ref - hmin) * 4;
            if (DIAG == 27) X = __builtin_popcount(lds_ld(&sm.swp)) * 100;
            if (DIAG == 20) X = lds_ld(&sm.slow) * 2;
            if (DIAG >= 30 && DIAG < 40) X = lds_ld(&sm.dg) * 8;
            if (DIAG == 11) X = mxj * 8;
            X = min(X, 6000u);
            if (DIAG == 39) { const unsigned f3 = min((ref - hmin) / 40u, 11u), f2 = mxj < 40 ? 0 : mxj < 120 ? 1 : mxj < 220 ? 2 : 3, ka_ = lds_ld(&sm.dg), f1 = ka_ < 50 ? 0 : ka_ < 85 ? 1 : 2, f0 = hmx < 50 ? 0 : hmx < 80 ? 1 : hmx < 110 ? 2 : 3;
                X = (((f3 * 4 + f2) * 3 + f1) * 4 + f0) * 400; }
            if (DIAG == 38) { hmin = (unsigned)t00; X = (min((ref - hmin) / 10u, 49u) * 2 + (lds_ld(&sm.dg) >= 60)) * 400; }
            while (NOW() - hmin < 15000 + X) SLEEP(1)
        }
    } else if (wave == NSW + 1 || wave == NSW + 2) {
        const int ql = lane < 48 ? lane : 47;
        unsigned c1, c2, s1, s2;
#define QT_LOADS() { int qo_ = ql; asm volatile("" : "+v"(qo_)); \
                     c1 = ((const unsigned*)q_cos)[qo_]; c2 = ((const unsigned*)q_cos)[48 + qo_]; s1 = ((const unsigned*)q_sin)[qo_]; s2 = ((const unsigned*)q_sin)[48 + qo_]; }
        unsigned q1, q2, lkz, lks, lvz, lvs;
        const unsigned sofs = R8(((unsigned)lane >> 1) & 7) * 2 + (lane & 1);
#define UNIT_LOADS(HEAD, GRP) { \
        q1 = ((const unsigned*)(q + (size_t)(HEAD) * DH))[ql]; q2 = ((const unsigned*)(q + (size_t)(HEAD) * DH))[48 + ql]; \
        const size_t so = (size_t)(HEAD) * (SKV / 2) + (GRP) * 256 + sofs; \
        lkz = *((const unsigned*)k_zeros + so); lks = *((const unsigned*)k_scales + so); \
        lvz = *((const unsigned*)v_zeros + so); lvs = *((const unsigned*)v_scales + so); }
#define UNIT_FILL(SL, U) { \
            v4f qq; \
            qq.x = bfr(bfr(bflo(q1) * bflo(c1)) + bfr(-bflo(q2) * bflo(s1))); \
            qq.y = bfr(bfr(bflo(q2) * bflo(c2)) + bfr(bflo(q1) * bflo(s2))); \
            qq.z = bfr(bfr(bfhi(q1) * bfhi(c1)) + bfr(-bfhi(q2) * bfhi(s1))); \
            qq.w = bfr(bfr(bfhi(q2) * bfhi(c2)) + bfr(bfhi(q1) * bfhi(s2))); \
            *(v2u*)&sm.q[SL][2 * ql] = (v2u){(__float_as_uint(qq.x) >> 16) | __float_as_uint(qq.y), (__float_as_uint(qq.z) >> 16) | __float_as_uint(qq.w)}; \
            *(v4f*)&sm.kz[SL][2 * lane] = (v4f){bflo(lkz), bflo(lks), bfhi(lkz), bfhi(lks)}; \
            *(v4f*)&sm.vz[SL][2 * lane] = (v4f){bflo(lvz), bflo(lvs), bfhi(lvz), bfhi(lvs)}; \
            CBAR(); \
            if (lane == 0) lds_st(&sm.rdy[SL], (U) + 1); \
            CBAR(); }
        const unsigned dw = NSW + 2 - wave;
#define TOUCH() { const unsigned x_ = ((blockIdx.x >> 3) - 18) * 64 + lane; \
            const char* p_ = x_ < 384 ? (const char*)(x_ < 192 ? qk : qv) + (size_t)(x_ < 192 ? x_ : x_ - 192) * (2u << 20) + (x_ * 8 + (blockIdx.x & 7)) * 640 : (const char*)out + (x_ - 384) * 4096; \
            if (x_ < 408) asm volatile("global_load_dword a0, %0, off nt" :: "v"(p_) : "a0"); }
        QT_LOADS()
        UNIT_LOADS(head0 + dw * (GSZ + 1 - 2 * side), lane >> 4)
        if (dw) TOUCH()
#pragma unroll 1
        for (unsigned u = dw;;) {
            while (u >= lds_ld(&sm.merged) + NIN) SLEEP(4);
            CBAR();
            UNIT_FILL(u % NIN, u)
            u += 2;
            if (u >= NU) break;
            if (u == 4) {
                const unsigned tpf = tdue(PSF - 1) + TFO;
                { int d; while ((d = (int)(lds_ld(&sm.line) + tpf - NOW())) > 0 && d < 3000) SLEEP(1) }
#define PF3(P) asm volatile("global_load_dwordx4 a[0:3], %0, %1\n\tglobal_load_dwordx4 a[0:3], %0, %1 offset:1024\n\tglobal_load_dwordx4 a[0:3], %0, %1 offset:2048" \
                            :: "v"(64 * SKV * 2 + blockIdx.x * 3072 + lane * 16), "s"(P) : "a0", "a1", "a2", "a3")
                PF3(k_zeros); PF3(k_scales); PF3(v_zeros); PF3(v_scales);
                asm volatile("global_load_dword a0, %0, %1" :: "v"(blockIdx.x * (DH * 2) + ql * 8), "s"(q) : "a0");
            }
            const unsigned tin = tdue(first_of(u) - (QUARTER(u) ? 24 : 0) - LEADP + (split & 7) * DPH);
            { int d; while ((d = (int)(lds_ld(&sm.line) + tin - NOW())) > 0 && d < 3000) SLEEP(4) }
            unsigned hd;
            while ((hd = lds_ld(&sm.utab[u])) == ~0u) SLEEP(2)
            UNIT_LOADS(hd & 255, QUARTER(u) ? rank & 3 : lane >> 4)
        }
        while (lds_ld(&sm.tag_rdy) == 0) SLEEP(1);
        CBAR();
        E1 = (u64)lds_ld(&sm.tag_lo) | ((u64)lds_ld(&sm.tag_hi) << 32);
        if (wave == NSW + 1 && blockIdx.x == 0) {
            for (;;) {
                bool ok = true;
#pragma unroll
                for (int i = 0; i < NH / 64; i++) ok &= __hip_atomic_load((const u64*)(ws + WS_POST) + i * 64 + lane, __ATOMIC_RELAXED, __HIP_MEMORY_SCOPE_AGENT) >> 24 == (E1 << 24) >> 24;
                if (__builtin_amdgcn_ballot_w64(ok) == ~0ull) break;
                SLEEP(64);
            }
            if (lane == 0) __hip_atomic_store((u64*)(ws + WS_EPOCH), E1, __ATOMIC_RELAXED, __HIP_MEMORY_SCOPE_AGENT);
        }
        if (wave == NSW + 2) {
            const unsigned t16 = (unsigned)E1 & 0xffffu, t16b = ((unsigned)E1 + ((unsigned)E1 >> 16)) & 0xffffu;
            const __amdgpu_buffer_rsrc_t rsrc = __builtin_amdgcn_make_buffer_rsrc(ws + WS_SWAP + (blockIdx.x ^ 4) * 5120, 0, 5120, 0x00020000);
            const unsigned swp = lds_ld(&sm.swp);
            if (swp) {
                unsigned use = 0;
#pragma unroll
                for (int i = 0; i < 5; i++) use |= ((swp >> wunit(i * 64 + lane)) & 1) << i;
                const unsigned tg = tdue(PSG - 1) + (lds_ld(&sm.gap) & 0xffffu);
                while ((int)(NOW() - lds_ld(&sm.line) - tg) < 0) SLEEP(2)
                for (;;) {
                    v4u d[5]; bool ok = true;
#pragma unroll
                    for (int i = 0; i < 5; i++) d[i] = __builtin_amdgcn_raw_buffer_load_b128(rsrc, (use >> i) & 1 ? (i * 64 + lane) * 16 : 0x40000000, 0, SC1 | 2);
#pragma unroll
                    for (int i = 0; i < 5; i++) if ((use >> i) & 1) {
                        ok &= d[i].y >> 16 == t16 && d[i].w >> 16 == t16b;
                        unsigned* w = (unsigned*)&sm.wv[144 * NMIX + (i * 64 + lane) * 6];
                        w[0] = d[i].x; w[1] = (d[i].y & 0xffffu) | (d[i].z << 16); w[2] = (d[i].z >> 16) | (d[i].w << 16);
                    }
                    if (__builtin_amdgcn_ballot_w64(ok) == ~0ull) break;
                    SLEEP(32)
                }
                CBAR();
                LDS_OR(&sm.vrdy, swp)
            }
        }
    } else {
        const unsigned eu = lane >= NU ? lane - NU : lane, win = min(first_of(eu) >> 5, 15u), pair = QUARTER(eu) ? 2 * (eu - WQ) + (rank >> 2) : rank;
        const unsigned rot2 = ((const unsigned char*)ROT)[win * 8 + pair];
#define UENT(BIT) (win * 16 + 2 * pair + ((BIT) ^ side) | (QUARTER(eu) ? (rank - (eu - WQ)) * 8 & 31 : (rot2 >> ((BIT) ^ side) * 4 & 15) * 2) << 8)
        unsigned far = 0;
        const unsigned hp = (blockIdx.x & 1) * NH + (blockIdx.x >> 1) * 4 + 2;
        if (blockIdx.x < 144) {
            const char* pb = (blockIdx.x < 128 ? (const char*)(hp >= NH ? qv : qk) + (size_t)(hp & (NH - 1)) * (SKV * DH * 4) + SKV * DH * 2
                                               : (const char*)ws + WS_PART + (8u << 20) + (blockIdx.x & 15) * 65536) + ((NOW() * 0x9e3779b1u) >> 26) * 128;
            unsigned mn[2] = {~0u, ~0u};
#pragma unroll 1
            for (unsigned i = 1; i < 2 * NPRB + 2; i++) {
                u64 t0, t1; unsigned dm;
                asm volatile("s_memtime %0\n\ts_waitcnt lgkmcnt(0)\n\ts_load_dword %2, %3, 0x0\n\ts_waitcnt lgkmcnt(0)\n\ts_memtime %1\n\ts_waitcnt lgkmcnt(0)"
                             : "=&s"(t0), "=&s"(t1), "=&s"(dm) : "s"(pb + i * 8192) : "memory");
                const unsigned t = (unsigned)(t1 - t0) < 330 || i == 1 ? ~0u : (unsigned)(t1 - t0);
                if (i & 1) mn[1] = min(mn[1], t); else mn[0] = min(mn[0], t);
            }
            far = mn[0] > mn[1];
        }
        if (lane < NMIX) lds_st(&sm.utab[lane], UENT(lane & 1));
        while (lds_ld(&sm.tag_rdy) == 0) SLEEP(1);
        CBAR();
        E1 = (u64)lds_ld(&sm.tag_lo) | ((u64)lds_ld(&sm.tag_hi) << 32);
        {
            const unsigned own = lds_ld(&sm.line), tg = (unsigned)E1;
            if (lane == 0) __hip_atomic_store((u64*)(ws + WS_POST) + blockIdx.x, E1 << 24 | min(lds_ld(&sm.slow), 4000u) << 1 | ((far ^ (blockIdx.x >> 2) ^ (blockIdx.x < 128 ? 0x96 >> (hp & 7) : 0)) & 1), __ATOMIC_RELAXED, __HIP_MEMORY_SCOPE_AGENT);
            const __amdgpu_buffer_rsrc_t nrs = __builtin_amdgcn_make_buffer_rsrc(ws + WS_NEWS, 0, 8 * 1280, 0x00020000);
            if (blockIdx.x == MASTER) {
                while ((int)(NOW() - own) < TMAP) SLEEP(2)
                const unsigned a = tg << 24, b = (unsigned)(E1 >> 8);
                v4u x; v2u z; u64 v0, v1;
#pragma unroll 1
                for (int i = 0; i < 8; i++) {
                    x = __builtin_amdgcn_raw_buffer_load_b128(__builtin_amdgcn_make_buffer_rsrc(ws + WS_POST, 0, 1024, 0x00020000), lane * 16, 0, SC1);
                    z = __builtin_amdgcn_raw_buffer_load_b64(__builtin_amdgcn_make_buffer_rsrc(ws + WS_POST + 1024, 0, 128, 0x00020000), lane * 8, 0, SC1);
                    v0 = __builtin_amdgcn_ballot_w64(((x.x ^ a) >> 24 | (x.y ^ b)) == 0); v1 = __builtin_amdgcn_ballot_w64(((x.z ^ a) >> 24 | (x.w ^ b)) == 0);
                    if (__builtin_popcountll(v0) + __builtin_popcountll(v1) >= MINV) break;
                    SLEEP(8)
                }
                const u64 m0 = __builtin_amdgcn_ballot_w64(x.x & 1) & v0, m1 = __builtin_amdgcn_ballot_w64(x.z & 1) & v1;
                const int s0 = lane < 43 ? min(max(lane - VW, 4 * NMIX), 42 - 2 * VW) : min(max(lane - VW, 43), 63 - 2 * VW);
#define PC(M) __builtin_popcount((unsigned)((M) >> s0) & ((2u << 2 * VW) - 1))
                const u64 g0 = __builtin_amdgcn_ballot_w64(2 * PC(m0) > PC(v0)), g1 = __builtin_amdgcn_ballot_w64(2 * PC(m1) > PC(v1));
                const float sl = wave_sum(((v0 >> lane & 1) ? (float)(x.x >> 1 & 4095u) : 0.f) + ((v1 >> lane & 1) ? (float)(x.z >> 1 & 4095u) : 0.f)) / ((8.f - PRE0) * (__builtin_popcountll(v0) + __builtin_popcountll(v1)) + 1.f);
                const unsigned al = lds_ld(&sm.aline), mean = (al ? al : own) + (int)fmaxf(sl - SL0, 0.f);
                const bool vz = lane < 16 && ((z.x ^ a) >> 24 | (z.y ^ b)) == 0;
                const unsigned gw = 2 * __builtin_popcountll(__builtin_amdgcn_ballot_w64(vz && (z.x & 1))) > __builtin_popcountll(__builtin_amdgcn_ballot_w64(vz));
                const unsigned k = lane & 3;
                if (lane < 32 && k < 3) __builtin_amdgcn_raw_buffer_store_b128((v4u){k == 0 ? (unsigned)g0 : k == 1 ? (unsigned)g1 : mean, tg, k == 0 ? (unsigned)(g0 >> 32) : k == 1 ? (unsigned)(g1 >> 32) : gw, tg},
                                                                              nrs, (lane >> 2) * 1280 + k * 16, 0, SC1);
            }
            while ((int)(NOW() - own) < TMAP + TRES) SLEEP(2)
            v4u y;
            for (;;) {
                y = __builtin_amdgcn_raw_buffer_load_b128(nrs, lane < 3 ? (blockIdx.x & 7) * 1280 + lane * 16 : 0x40000000, 0, SC1);
                if (((unsigned)__builtin_amdgcn_ballot_w64(y.y == tg && y.w == tg) & 7u) == 7u) break;
                SLEEP(8)
            }
            const u64 f0 = (unsigned)__builtin_amdgcn_readlane(y.x, 0) | (u64)(unsigned)__builtin_amdgcn_readlane(y.z, 0) << 32, f1 = (unsigned)__builtin_amdgcn_readlane(y.x, 1) | (u64)(unsigned)__builtin_amdgcn_readlane(y.z, 1) << 32;
            if (lane == 2) { sm.aline = y.x - own + 500 < 1000 ? y.x : own; sm.chead = (blockIdx.x & ~5u) | ((blockIdx.x >> 2 ^ y.z) & 1) | (blockIdx.x & 1) << 2; }
            unsigned bit = ((unsigned)((lane >= NU ? f1 : f0) >> (4 * win + (pair >> 1))) ^ (0x66 >> pair)) & 1;
            if (eu < NMIX) bit = eu & 1;
            const u64 bm = __builtin_amdgcn_ballot_w64(bit);
            const unsigned nsw = __builtin_popcountll((f0 ^ f1) >> (4 * NMIX));
            if (lane == 0) { sm.swp = (unsigned)(bm ^ (bm >> NU)) & ((1u << NU) - 1); lds_st(&sm.gap, nsw ? (TG0 + TG1 * nsw / 8) * 0x10001u + ((TH0 + TH1 * nsw / 8) << 16) : 0u); }
            CBAR();
            lds_st(&sm.utab[lane], UENT(bit));
        }
        const unsigned swp = lds_ld(&sm.swp);
        const unsigned T = lds_ld(&sm.tag_lo);
        const unsigned t15 = T & 0x7fffu;
        const __amdgpu_buffer_rsrc_t rsrc = __builtin_amdgcn_make_buffer_rsrc(ws + WS_PART + split * 512, 0, NRW * NSPLIT * 512 - split * 512, 0x00020000);
        v2u* recs = (v2u*)&sm.cs[0][0][0];
#pragma unroll
        for (int i = 0; i < NOUT * 3; i++) (&sm.acc[0][0])[i * 64 + lane] = 0.0;
        CBAR();
        unsigned vut = sm.utab[NU + (lane & 31)] & 255;
        if (QUARTER((unsigned)lane) && (rank & 3)) vut = NH - 16 + (rank & 3) * 16 + ((vut & 15) ^ 1);
        __builtin_amdgcn_s_setprio(3);
        unsigned po[NU / 2];
#pragma unroll
        for (int k = 0; k < NU / 2; k++) po[k] = __builtin_amdgcn_ds_bpermute((2 * k + (lane >> 5)) * 4, vut) * (NSPLIT * 512) + (lane & 31) * 16;
        const u64 rb_ = (u64)(ws + WS_PART + split * 512);
        const v4u rs_ = {(unsigned)rb_, (unsigned)(rb_ >> 32) & 0xffffu, NRW * NSPLIT * 512 - split * 512, 0x00020000u};
#pragma unroll 1
        for (unsigned u = 0; u < 2 * NU - 1; u++) {
            const unsigned e = u % NU, so = u % NOUT;
            if (u >= NU) while (!((lds_ld(&sm.vrdy) >> e) & 1)) SLEEP(4)
            CBAR();
            const v4f h = *(const v4f*)&sm.wv[went(e) + (QUARTER(e) ? 32 : 128)];
            while (lds_ld(&sm.cnt[so]) != (QUARTER(e) ? 8 : 32)) SLEEP(1)
            CBAR();
            const double a0 = sm.acc[so][lane], a1 = sm.acc[so][64 + lane], a2 = sm.acc[so][128 + lane];
            if (u < NU) {
                float2 lg = *(const float2*)&sm.ent[so][2 * lane];
                if (QUARTER(e) && lane >> 4 != (rank & 3)) lg = make_float2(-INFINITY, -INFINITY);
                const v4f zs = *(const v4f*)&sm.vz[u % NIN][2 * lane];
                const float m = wave_max(fmaxf(lg.x, lg.y));
                const float p0 = __expf(lg.x - m), p1 = __expf(lg.y - m);
                const float x0 = p0 * zs.y, x1 = p1 * zs.w;
                const unsigned eb = max(__float_as_uint(wave_max(fmaxf(fabsf(x0), fabsf(x1)))) >> 23, 14u);
                const float f = __uint_as_float((268u - eb) << 23);
                const float w0 = fminf(fmaxf(rintf(x0 * f), -32767.f), 32767.f), w1 = fminf(fmaxf(rintf(x1 * f), -32767.f), 32767.f);
                const float den = wave_sum(p0 + p1);
                const float bz = wave_sum(fmaf(w0, zs.x, w1 * zs.z));
                const float L = fmaf(m, 1.4426950408889634f, __builtin_amdgcn_logf(den));
                unsigned* wd = (unsigned*)(((swp >> u) & 1 ? sm.wo : sm.wv) + went(u));
                if (!QUARTER(u) || lane >> 4 == (rank & 3)) wd[QUARTER(u) ? lane & 15 : lane] = ((unsigned)(int)w0 & 0xffffu) | ((unsigned)(int)w1 << 16);
                if (lane == 0) *(v4f*)&wd[QUARTER(u) ? 16 : 64] = (v4f){ldexpf(1.f / den, (int)eb - 141), bz, L, 0.f};
                CBAR();
                if (!((swp >> u) & 1)) LDS_OR(&sm.vrdy, 1u << u)
            } else {
                const float n0 = (float)(a0 - (double)h.y) * h.x, n1 = (float)(a1 - (double)h.y) * h.x, n2 = (float)(a2 - (double)h.y) * h.x;
                const unsigned e2 = max(__float_as_uint(wave_maxv(fmaxf(fmaxf(fabsf(n0), fabsf(n1)), fabsf(n2)))) >> 23, 14u);
                const float f2 = __uint_as_float((268u - e2) << 23);
                const unsigned m01 = __builtin_bit_cast(unsigned, __builtin_amdgcn_cvt_pk_i16((int)rintf(n0 * f2), (int)rintf(n1 * f2)));
                const unsigned m2 = __builtin_bit_cast(unsigned, __builtin_amdgcn_cvt_pk_i16((int)rintf(n2 * f2), 0));
                const unsigned bit = ((lane & 1 ? e2 : __float_as_uint(h.z)) >> (lane >> 1)) & 1u;
                recs[e * 64 + lane] = (v2u){m01, m2 | bit << 16 | t15 << 17};
                CBAR();
                sm.acc[so][lane] = 0.0; sm.acc[so][64 + lane] = 0.0; sm.acc[so][128 + lane] = 0.0;
            }
            CBAR();
            if (lane == 0) { lds_st(&sm.cnt[so], 0u); CBAR(); lds_st(&sm.merged, u + 1); }
            if (u == NU - 1 && swp) {
                wave_sync();
                const __amdgpu_buffer_rsrc_t srs = __builtin_amdgcn_make_buffer_rsrc(ws + WS_SWAP + blockIdx.x * 5120, 0, 5120, 0x00020000);
                v4u d[5]; unsigned o[5];
#pragma unroll
                for (int i = 0; i < 5; i++) {
                    const unsigned* w = (const unsigned*)&sm.wo[144 * NMIX + (i * 64 + lane) * 6];
                    const unsigned d0 = w[0], d1 = w[1], d2 = w[2];
                    d[i] = (v4u){d0, (d1 & 0xffffu) | (T << 16), (d1 >> 16) | (d2 << 16), (d2 >> 16) | ((T + (T >> 16)) << 16)};
                    o[i] = (swp >> wunit(i * 64 + lane)) & 1 ? (i * 64 + lane) * 16 : 0x40000000;
                }
                while ((int)(NOW() - lds_ld(&sm.line)) < (int)tdue(PSW) + TWO) SLEEP(1)
#pragma unroll
                for (int i = 0; i < 5; i++) __builtin_amdgcn_raw_buffer_store_b128(d[i], srs, o[i], 0, SC1 | 2);
            }
        }
        wave_sync();
#pragma unroll
        for (int k = 0; k < NU / 2; k++) asm volatile("ds_read_b128 a[%c0:%c0+3], %1 offset:%2" :: "i"(16 + 4 * k), "v"(lds_addr(recs) + lane * 16), "i"(k * 1024) : "memory");
        asm volatile("s_waitcnt lgkmcnt(0)" ::: "memory");
        while (!((lds_ld(&sm.vrdy) >> (NU - 1)) & 1)) SLEEP(4)
        CBAR();
        const v2u hw_ = {*(const unsigned*)&sm.wv[went(NU - 1) + 132], T};
        const unsigned hrow = (NRW - GSZ + (__builtin_amdgcn_readlane(vut, NU - 1) & 15 ^ 1)) * (NSPLIT * 512);
        while (lds_ld(&sm.grab) < NPC - PUBP) SLEEP(1)
        if (lane < 16) __builtin_amdgcn_raw_buffer_store_b64(hw_, rsrc, lane * 8, hrow, SC1);
#define PUB(K) asm volatile("buffer_store_dwordx4 a[%c0:%c0+3], %1, %2, 0 offen sc1" :: "i"(16 + 4 * (K)), "v"(po[K]), "s"(rs_) : "memory");
        PUB(WQ / 2) PUB(WQ / 2 + 1)
        { const unsigned t_ = TLAST() + PDEL; GATE(t_) }
        PUB(0) PUB(1) PUB(2) PUB(3) PUB(4) PUB(7) PUB(8)
        return;
    }
    }
    __builtin_amdgcn_s_setprio(0);
    unsigned c;
#define ADDRTN(P) { u64 ex_; asm volatile("s_mov_b64 %0, exec\n\ts_mov_b64 exec, 1\n\tds_add_rtn_u32 a15, %1, a13\n\ts_mov_b64 exec, %0" : "=&s"(ex_) : "v"(lds_addr(P)) : "a15", "memory"); }
#define ADDED(V) { unsigned t_; asm volatile("s_waitcnt lgkmcnt(0)\n\tv_accvgpr_read_b32 %1, a15\n\ts_nop 0\n\tv_readfirstlane_b32 %0, %1\n\ts_nop 1" : "=s"(V), "=v"(t_)); }
    asm volatile("v_accvgpr_write_b32 a13, 1\n\ts_nop 1" ::: "a13");
    ADDRTN(&sm.cidx) ADDED(c)
    if (c < NSW) {
        while (lds_ld(&sm.grab) < NPC + 2) SLEEP(4)
        const unsigned head = lds_ld(&sm.chead), T = lds_ld(&sm.tag_lo), tl = TLAST();
        const u64 rb_ = (u64)(ws + WS_PART);
        const v4u rs_ = {(unsigned)rb_, (unsigned)(rb_ >> 32) & 0xffffu, NRW * NSPLIT * 512, 0x00020000u};
        const unsigned ro = head * (NSPLIT * 512) + c * 1024, rx = (NH + (head & 15 ^ 1)) * (NSPLIT * 512) + c * 1024;
#define POLL(S, O) asm volatile("buffer_load_dwordx4 a[%c0:%c0+3], %1, %2, %3 offen sc1" :: "i"(16 + 4 * (S)), "v"(lane * 16), "s"(rs_), "s"(O) : "memory");
#define FETCH(X, S, N) asm volatile("s_waitcnt vmcnt(%c5)\n\tv_accvgpr_read_b32 %0, a[%c4]\n\tv_accvgpr_read_b32 %1, a[%c4+1]\n\tv_accvgpr_read_b32 %2, a[%c4+2]\n\tv_accvgpr_read_b32 %3, a[%c4+3]" \
                                    : "=v"((X).x), "=v"((X).y), "=v"((X).z), "=v"((X).w) : "i"(16 + 4 * (S)), "i"(N));
        float o0, o1, o2;
        if (head >= NH - GSZ) {
            __builtin_amdgcn_s_setprio(3);
            if (c == 0) {
                v2u hd;
                GATE(tl + THD)
                for (;;) {
                    hd = __builtin_amdgcn_raw_buffer_load_b64(__builtin_amdgcn_make_buffer_rsrc(ws + WS_PART + (NRW - GSZ + (head & 15 ^ 1)) * (NSPLIT * 512), 0, NSPLIT * 512, 0x00020000), (lane & 15) * 512, 0, SC1);
                    if (__builtin_amdgcn_ballot_w64(hd.y == T) == ~0ull) break;
                    SLEEP(4)
                }
                const float w = __builtin_amdgcn_exp2f(__uint_as_float(hd.x) - row_max(__uint_as_float(hd.x)));
                if (lane < 16) sm.ent[0][lane] = w * __builtin_amdgcn_rcpf(row_sum(w));
                CBAR();
                if (lane == 0) lds_st(&sm.tabs, 0u);
            }
            while (lds_ld(&sm.tabs) || lds_ld(&sm.merged) <= (2 * NU - 1) / NOUT * NOUT) SLEEP(1)
            CBAR();
            const float cf = sm.ent[0][2 * c + (lane >> 5)];
            v4u x; bool got;
#define SEEN(J, N) { FETCH(x, J, N) got = __builtin_amdgcn_ballot_w64((((x.y ^ T << 24) | (x.w ^ T << 24)) >> 24) != 0) == 0; }
            GATE(tl + TCA) POLL(0, ro) GATE(tl + TCA + TCD) POLL(1, rx + 3 * GSZ * NSPLIT * 512) SEEN(0, 1)
            if (!got) SEEN(1, 0)
            while (!got) { POLL(2, rx + 4 * GSZ * NSPLIT * 512) SEEN(2, 0) SLEEP(0) }
            const double c0 = ldexpf(cf, (int)(x.y >> 16 & 255u) - 141), c1 = ldexpf(cf, (int)(x.w >> 16 & 255u) - 141);
#define SWADD(A, B) ({ const v2u a_ = __builtin_bit_cast(v2u, A), b_ = __builtin_bit_cast(v2u, B); \
                const auto l_ = __builtin_amdgcn_permlane32_swap(a_.x, b_.x, false, false); const auto h_ = __builtin_amdgcn_permlane32_swap(a_.y, b_.y, false, false); \
                __builtin_bit_cast(double, (v2u){l_[0], h_[0]}) + __builtin_bit_cast(double, (v2u){l_[1], h_[1]}); })
            const double s0 = SWADD(c0 * (double)(short)x.x, c1 * (double)(short)x.z), s1 = SWADD(c0 * (double)((int)x.x >> 16), c1 * (double)((int)x.z >> 16)),
                         s2 = SWADD(c0 * (double)(short)x.y, c1 * (double)(short)x.w);
            asm volatile("ds_add_f64 %0, %1\n\tds_add_f64 %0, %2 offset:512\n\tds_add_f64 %0, %3 offset:1024" :: "v"(lds_addr(&sm.acc[0][lane])), "v"(s0), "v"(s1), "v"(s2) : "memory");
            ADDRTN(&sm.cdone)
            const double a0 = sm.acc[0][lane], a1 = sm.acc[0][64 + lane], a2 = sm.acc[0][128 + lane];
            ADDED(c)
            o0 = a0; o1 = a1; o2 = a2;
        } else {
            const bool quart = head >> 4 == WQ;
            const unsigned t15 = T & 0x7fffu;
#define TAKE(K) { FETCH(x[K], K, 0) \
                bad |= (x[K].y >> 17 ^ t15) | (x[K].w >> 17 ^ t15); \
                const u64 bl = __builtin_amdgcn_ballot_w64((x[K].y & 0x10000u) != 0), be = __builtin_amdgcn_ballot_w64((x[K].w & 0x10000u) != 0); \
                L[K] = __uint_as_float(lane < 32 ? (unsigned)bl : (unsigned)(bl >> 32)); ex[K] = lane < 32 ? (unsigned)be : (unsigned)(be >> 32); \
                M = fmaxf(M, fmaxf(__uint_as_float((unsigned)bl), __uint_as_float((unsigned)(bl >> 32)))); }
#define SUMUP(K) { const float w_ = __builtin_amdgcn_exp2f(L[K] - M), c_ = ldexpf(w_, (int)(ex[K] & 255u) - 141); den += w_; \
                t[0] = fmaf(c_, (float)(short)x[K].x, t[0]); t[1] = fmaf(c_, (float)((int)x[K].x >> 16), t[1]); t[2] = fmaf(c_, (float)(short)x[K].y, t[2]); \
                t[3] = fmaf(c_, (float)(short)x[K].z, t[3]); t[4] = fmaf(c_, (float)((int)x[K].z >> 16), t[4]); t[5] = fmaf(c_, (float)(short)x[K].w, t[5]); }
            const unsigned tg = tl + (quart ? TRQ : TRD);
            GATE(tg)
            v4u x[4]; float L[4], M; unsigned ex[4];
            for (;;) {
                unsigned bad = 0; M = -3e38f;
                POLL(0, ro)
                if (quart) { POLL(1, rx) POLL(2, rx + GSZ * NSPLIT * 512) POLL(3, rx + 2 * GSZ * NSPLIT * 512) }
                __builtin_amdgcn_s_setprio(3);
                TAKE(0)
                if (quart) { TAKE(1) TAKE(2) TAKE(3) }
                if (__builtin_amdgcn_ballot_w64(bad != 0) == 0) break;
                SLEEP(2)
            }
            float t[6] = {0.f, 0.f, 0.f, 0.f, 0.f, 0.f}, den = 0.f;
            SUMUP(0)
            if (quart) { SUMUP(1) SUMUP(2) SUMUP(3) }
#define SWADF(A, B) ({ const auto l_ = __builtin_amdgcn_permlane32_swap(__float_as_uint(A), __float_as_uint(B), false, false); __uint_as_float(l_[0]) + __uint_as_float(l_[1]); })
            v4f* pb = (v4f*)((char*)&sm.cs[0][0][0] + 65536);
            float* pm = (float*)(pb + NSW * 64);
            pb[c * 64 + lane] = (v4f){SWADF(t[0], t[3]), SWADF(t[1], t[4]), SWADF(t[2], t[5]), SWADF(den, den)};
            if (lane == 0) pm[c] = M;
            CBAR();
            ADDRTN(&sm.cdone)
            ADDED(c)
            CBAR();
            if (c == NSW - 1) {
                const v4f ma = *(const v4f*)pm, mb = *(const v4f*)(pm + 4);
                const float mw[NSW] = {ma.x, ma.y, ma.z, ma.w, mb.x, mb.y, mb.z, mb.w};
                float Mx = mw[0];
#pragma unroll
                for (int w = 1; w < NSW; w++) Mx = fmaxf(Mx, mw[w]);
                v4f sum = {0.f, 0.f, 0.f, 0.f};
#pragma unroll
                for (int w = 0; w < NSW; w++) sum += pb[w * 64 + lane] * __builtin_amdgcn_exp2f(mw[w] - Mx);
                const float inv = __builtin_amdgcn_rcpf(sum.w);
                o0 = sum.x * inv; o1 = sum.y * inv; o2 = sum.z * inv;
            }
        }
        if (c == NSW - 1) {
            asm("" : "+v"(o0), "+v"(o1), "+v"(o2));
            __hip_bfloat16* o = out + (size_t)head * DH + (lane & 31) * 6 + (lane >> 5) * 3;
            o[0] = __float2bfloat16(o0); o[1] = __float2bfloat16(o1); o[2] = __float2bfloat16(o2);
        }
    }
}

extern "C" void kernel_launch(void* const* d_in, const int* in_sizes, int n_in,
                              void* d_out, int out_size, void* d_ws, size_t ws_size,
                              hipStream_t stream) {
    const unsigned short* q = (const unsigned short*)d_in[0];
    const int* qk = (const int*)d_in[1];
    const unsigned short* k_scales = (const unsigned short*)d_in[2];
    const unsigned short* k_zeros = (const unsigned short*)d_in[3];
    const int* qv = (const int*)d_in[4];
    const unsigned short* v_scales = (const unsigned short*)d_in[5];
    const unsigned short* v_zeros = (const unsigned short*)d_in[6];
    const unsigned short* q_cos = (const unsigned short*)d_in[7];
    const unsigned short* q_sin = (const unsigned short*)d_in[8];
    const unsigned short* k_cos = (const unsigned short*)d_in[9];
    const unsigned short* k_sin = (const unsigned short*)d_in[10];
    unsigned char* ws = (unsigned char*)d_ws;
    attn_kernel<<<NSPLIT * GSZ, NTHREADS, 0, stream>>>(q, qk, k_scales, k_zeros, qv, v_scales, v_zeros,
                                                       q_cos, q_sin, k_cos, k_sin, (__hip_bfloat16*)d_out, ws);
}
```

```cpp
#include <hip/hip_runtime.h>
#include <hip/hip_bf16.h>
#include <stdint.h>

#define DH 192
#define HALF 96
#define SKV 2048
#define NH 256
#define NSW 8
#define NWAVES (NSW + 4)
#define NTHREADS (64 * NWAVES)
#define BROWS 128
#define NSPLIT 16
#define GSZ 16
#define WQ 10
#define NU (NH / GSZ + 3)
#define NPC 1024
#define NRR (NH + 3 * GSZ)
#define NIN 8
#define NOUT 8
#define WALL (15 * 144 + 4 * 48)
#define SC1 16
#define NRND 48
#define RST 4352
#define TK 2
#define NMIX 3
#define TUNIT 311
#define T01 50
#define TMIX 7
#define TOFF 50
#define PSF 100
#define TGS 68
#define PSW (NPC / 2 + 28)
#define TG0 8
#define TG1 3
#define TWO -75
#define PSG (NPC / 2 + 60)
#define TH0 3
#define TH1 3
#define EARLY 10
#define LEADP 24
#define DPH 2
#define TA 210
#define TA2 70
#define CSH 2
#define NPRB 3
#define TMAP 520
#define MINV 96
#define VW 4
#define MASTER 203
#define TRES 190
#define TB1 1200
#define PER 300
#define RDD 100
#define LEAD 70
#define LHI 85
#define LOK 74
#define PSL0 124
#define PUBP -14
#define LJ 210
#define LOK2 75
#define NTRY 3
#define TFO 6
#define SL0 42.f
#define PST0 200
#define KC 4
#define TC0 1000
#define TCS 9
#define PRE0 3

#define KREL 12
#define PDEL 70
#define NCP 3
#define NRW (NRR + NCP * GSZ)
#define TRD 112
#define THD 55
#define TRQ 55
#define TCA 132
#define TCD 22
#define TSD 560
#define WS_EPOCH 0
#define WS_POST0 8192
#define WS_POST (WS_POST0 + ((unsigned)E1 & 15) * 2048)
#define WS_NEWS (49152 + 256)
#define WS_HELLO 59904
#define WS_BOARD 65536
#define WS_PART (2u << 20)
#define WS_SWAP (WS_PART + NRW * NSPLIT * 512)

typedef unsigned int v2u __attribute__((ext_vector_type(2)));
typedef unsigned int v16u __attribute__((ext_vector_type(16)));
typedef unsigned int v4u __attribute__((ext_vector_type(4)));
typedef int v4i __attribute__((ext_vector_type(4)));
typedef float v2f __attribute__((ext_vector_type(2)));
typedef float v4f __attribute__((ext_vector_type(4)));
typedef unsigned long long u64;

__device__ __forceinline__ float bflo(unsigned u) { return __uint_as_float(u << 16); }
__device__ __forceinline__ float bfhi(unsigned u) { return __uint_as_float(u & 0xffff0000u); }
__device__ __forceinline__ float bfr(float x) {
    __bf16 b = (__bf16)x;
    return __uint_as_float(((unsigned)__builtin_bit_cast(unsigned short, b)) << 16);
}

#define SLEEP(n) SLEEP_(n)
#define SLEEP_(n) asm volatile("s_sleep " #n "\n\tv_add_co_u32 %0, vcc, 1, %0\n\ts_cbranch_vccz 1f\n\ts_endpgm\n1:" : "+v"(spins) :: "vcc", "memory");
#define SPIN0 0xf0000000u
#define CBAR() asm volatile("" ::: "memory")

__device__ __forceinline__ void wave_sync() {
    __builtin_amdgcn_fence(__ATOMIC_RELEASE, "wavefront");
    __builtin_amdgcn_wave_barrier();
    __builtin_amdgcn_fence(__ATOMIC_ACQUIRE, "wavefront");
}
template <int CTRL>
__device__ __forceinline__ float dpp_add(float x) {
    int y = __builtin_amdgcn_update_dpp(0, __float_as_int(x), CTRL, 0xf, 0xf, true);
    return x + __int_as_float(y);
}
template <int CTRL>
__device__ __forceinline__ float dpp_max(float x) {
    int y = __builtin_amdgcn_update_dpp(__float_as_int(x), __float_as_int(x), CTRL, 0xf, 0xf, false);
    return fmaxf(x, __int_as_float(y));
}
__device__ __forceinline__ float row_sum(float x) { x = dpp_add<0x128>(x); x = dpp_add<0x124>(x); x = dpp_add<0x122>(x); return dpp_add<0x121>(x); }
__device__ __forceinline__ float row_max(float m) { m = dpp_max<0x128>(m); m = dpp_max<0x124>(m); m = dpp_max<0x122>(m); return dpp_max<0x121>(m); }
__device__ __forceinline__ float rdlane(float x, int l) { return __int_as_float(__builtin_amdgcn_readlane(__float_as_int(x), l)); }
__device__ __forceinline__ float wave_max(float m) { m = row_max(m); return fmaxf(fmaxf(rdlane(m, 0), rdlane(m, 16)), fmaxf(rdlane(m, 32), rdlane(m, 48))); }
__device__ __forceinline__ float wave_maxv(float m) {
    m = row_max(m);
    const auto a = __builtin_amdgcn_permlane16_swap(__float_as_uint(m), __float_as_uint(m), false, false);
    m = fmaxf(__uint_as_float(a[0]), __uint_as_float(a[1]));
    const auto b = __builtin_amdgcn_permlane32_swap(__float_as_uint(m), __float_as_uint(m), false, false);
    return fmaxf(__uint_as_float(b[0]), __uint_as_float(b[1]));
}
__device__ __forceinline__ float wave_sum(float x) { x = row_sum(x); return (rdlane(x, 0) + rdlane(x, 16)) + (rdlane(x, 32) + rdlane(x, 48)); }
__device__ __forceinline__ unsigned lds_ld(const unsigned* p) { return __builtin_amdgcn_readfirstlane(__hip_atomic_load(p, __ATOMIC_RELAXED, __HIP_MEMORY_SCOPE_WORKGROUP)); }
__device__ __forceinline__ unsigned lds_addr(const void* p) { return (unsigned)(size_t)(const __attribute__((address_space(3))) void*)p; }
#define DMA16(SRC, LDS) asm volatile("s_mov_b32 %0, m0\n\ts_mov_b32 m0, %2\n\ts_nop 0\n\tglobal_load_lds_dwordx4 %1, off\n\ts_mov_b32 m0, %0" : "=&s"(m0keep) : "v"(SRC), "s"(LDS) : "memory")
__device__ __forceinline__ void lds_st(unsigned* p, unsigned v) { __hip_atomic_store(p, v, __ATOMIC_RELAXED, __HIP_MEMORY_SCOPE_WORKGROUP); }
#define NOW() ((unsigned)__builtin_amdgcn_s_memrealtime())
__device__ __forceinline__ unsigned tdue(unsigned g) {
    return (g * TUNIT + min(g, 480u) * TK + min(g, 48u) * T01 + (min(g, 32u * NMIX) + min(max((int)g - NPC / 2, 0), 32 * NMIX)) * TMIX + (g >= PSF ? TGS * 32 : 0)) >> 5;
}

__device__ __forceinline__ unsigned unit_of(unsigned g) { const unsigned p = g & 511; return (g >> 9) * NU + (p >> 5) + min(max((int)p - 32 * WQ, 0) >> 3, 3); }
__device__ __forceinline__ unsigned first_of(unsigned e) { return 32 * e - 24 * min(max((int)e - WQ, 0), 4); }
#define QUARTER(E) ((E) - WQ < 4)
__device__ __forceinline__ unsigned went(unsigned e) { return 144 * e - 96 * min(max((int)e - WQ, 0), 4); }
__device__ __forceinline__ unsigned wunit(unsigned x) { x += 24 * NMIX; return x < 24 * WQ ? x / 24 : x < 24 * WQ + 32 ? WQ + (x - 24 * WQ) / 8 : (x + 64) / 24; }

#define ROTT 0xed8d2c3d2c35ed84ull, 0xba85aa165b03ac82ull, 0xee7efa489a38af71ull, 0xe932da1b0bab51d8ull, 0xfa96112d5fee971bull, 0x2c7e9ea5a544f071ull, 0x3d86093d5f0e810bull, 0xe932da1b0bab51d8ull, 0xfa96112d5fee971bull, 0xd834e62c5fa723e9ull, 0x1d7d1a3d2c061d88ull, 0xe932da1b0bab51d8ull, 0xfa96112d5fee971bull, 0xc834e12c0aaa4ec7ull, 0x3d86093d5f0e810bull, 0xe932da1b0bab51d8ull
static __device__ const u64 ROT[16] = {ROTT};

struct __attribute__((aligned(16))) Smem {
    unsigned cs[NSW][4][768];
    double acc[NOUT][DH];
    float ent[NOUT][BROWS];
    unsigned short wv[WALL];
    unsigned short wo[WALL];
    unsigned q[NIN][HALF];
    float2 kz[NIN][BROWS];
    float2 vz[NIN][BROWS];
    unsigned utab[64];
    unsigned line, gap;
    unsigned late, grab;
    unsigned rdy[NIN], cnt[NOUT];
    unsigned tabs, vrdy, swp;
    unsigned aline;
    unsigned tag_rdy, tag_lo, tag_hi, merged, cdone, cidx;
    unsigned slow;
    unsigned chead;
};

#define LDS_ADD(P, V) { u64 ex_; asm volatile("s_mov_b64 %0, exec\n\ts_mov_b64 exec, 1\n\tds_add_u32 %1, %2\n\ts_mov_b64 exec, %0" : "=&s"(ex_) : "v"(lds_addr(P)), "v"(V) : "memory"); }
#define LDS_OR(P, V) { u64 ex_; asm volatile("s_mov_b64 %0, exec\n\ts_mov_b64 exec, 1\n\tds_or_b32 %1, %2\n\ts_mov_b64 exec, %0" : "=&s"(ex_) : "v"(lds_addr(P)), "v"(V) : "memory"); }

__global__ __launch_bounds__(NTHREADS, 3) void attn_kernel(
    const unsigned short* __restrict__ q, const int* __restrict__ qk,
    const unsigned short* __restrict__ k_scales, const unsigned short* __restrict__ k_zeros,
    const int* __restrict__ qv,
    const unsigned short* __restrict__ v_scales, const unsigned short* __restrict__ v_zeros,
    const unsigned short* __restrict__ q_cos, const unsigned short* __restrict__ q_sin,
    const unsigned short* __restrict__ k_cos, const unsigned short* __restrict__ k_sin,
    __hip_bfloat16* __restrict__ out, unsigned char* __restrict__ ws)
{
    __shared__ Smem sm;
    const int tid = threadIdx.x;
    unsigned spins = SPIN0;
    const int lane = tid & 63;
    const int hw = __builtin_amdgcn_readfirstlane(tid >> 6);
    u64 t00; unsigned ka;
    asm volatile("s_memrealtime %0\n\ts_load_dword %1, %2, 0x40" : "=&s"(t00), "=&s"(ka) : "s"(__builtin_amdgcn_kernarg_segment_ptr()));
    if (hw == 0) {
        const char* pc0; asm volatile("s_getpc_b64 %0" : "=s"(pc0));
        asm volatile("global_load_dword a6, %0, %1" :: "v"(((blockIdx.x & ~7u) + (lane & 7)) * 128), "s"(pc0) : "a6", "memory");
    }
    const int wave = hw >= 4 ? hw - 4 : hw + NSW;
    const unsigned split = (blockIdx.x & 1) + (blockIdx.x & 2) * 4 + ((blockIdx.x >> 3) & 3) * 2;
    const unsigned side = (split ^ (blockIdx.x >> 2)) & 1, rank = blockIdx.x >> 5;
    const unsigned head0 = 2 * rank + side;
    const unsigned rf0 = (8 * split + 1) / 3, r3 = 8 * split + 1 - 3 * rf0;
    const unsigned koff = ((lane >> 5) * DH + 3 * (lane & 31)) * 4, koffb = koff + 384;
#define AR(S) (16 + (S) * 8)
#define GLD(A, OFF, BASE, IMM) asm volatile("global_load_dwordx3 a[%c0:%c1], %2, %3 offset:%4 nt" :: "i"(A), "i"((A) + 2), "v"(OFF), "s"(BASE), "i"(IMM))
#define ISSUE(S, J, BASE, OA, OB) { GLD(AR(S), OA, BASE, (J) * 1536); GLD(AR(S) + 4, OB, BASE, (J) * 1536); }
#define HEAD2 ((GSZ + 1 - 2 * side) * (SKV * DH * 4))
    const unsigned wo = (hw - 2) & 7;
    const bool pre = hw >= 4 && wo < PRE0;
    if (pre) {
        const char* a = (const char*)qk + (head0 * 512 + side * 256 + rf0 + wave + (wave >= 6 ? 80 : 0)) * 3072; const char* b = a + HEAD2;
        ISSUE(0, 0, a, koff, koffb) ISSUE(1, 1, a, koff, koffb) ISSUE(2, 0, b, koff, koffb) ISSUE(3, 1, b, koff, koffb)
    }
    asm volatile("s_waitcnt lgkmcnt(0)" : "+s"(t00), "+s"(ka));
    if (hw == 0) {
        sm.utab[lane] = ~0u;
        unsigned l0 = (unsigned)t00 + TOFF;
        if (KREL) { const unsigned t1 = NOW() + KREL; if ((int)(t1 - l0) > 0) l0 = t1; }
        if (lane < 32) (&sm.line)[lane] = lane == 3 ? 64u : (lane & 29) == 0 ? l0 : 0u;
        asm volatile("s_waitcnt lgkmcnt(0)" ::: "memory");
    }
    __builtin_amdgcn_s_barrier();
    CBAR();
    const u64 rtab = r3 == 1 ? 0x5756552c2b020100ull : r3 == 0 ? 0x5756552c2b2a0100ull : 0x57562d2c2b020100ull;
#define R8(I) (rf0 + ((unsigned)(rtab >> (8 * (I))) & 255u))
#define GATE(DUE) { int d_ = (int)((DUE) - NOW()); if (d_ < 3000) while (d_ > 0) { __builtin_amdgcn_s_sleep(1); d_ = (int)((DUE) - NOW()); } }
#define TLAST() (lds_ld(&sm.line) + tdue(NPC - 2) + (lds_ld(&sm.gap) >> 16))
    if (__builtin_expect(wave < NSW, 1)) {
        const int cst = 2 * side;
        const unsigned r8w = R8((unsigned)wave);
        const char* cbase[3];
#pragma unroll
        for (int j = 0; j < 3; j++) { const int x = j * 64 + lane; cbase[j] = (x < 96 ? (const char*)k_cos + x * 16 : (const char*)k_sin + (x - 96) * 16) + r8w * (4 * DH * 2); }
        unsigned m0keep;
#define CS_DMA(W, C) { _Pragma("unroll") for (int j = 0; j < 3; j++) DMA16(cbase[j] + (C) * (512 * DH * 2), lds_addr(&sm.cs[0][0][0]) + ((W) * 4 + (C)) * 3072 + j * 1024); }
#define REPACK(W, C) { \
        unsigned* cw = &sm.cs[W][C][0]; \
        const unsigned* l = cw + rsel * (DH / 2) + qoff; \
        unsigned r[2][6]; \
        _Pragma("unroll") \
        for (int j = 0; j < 2; j++) { \
            unsigned e[4][2]; \
            _Pragma("unroll") \
            for (int k = 0; k < 4; k++) { \
                const unsigned* a = l + j * DH + (k & 1) * 48 + (k >> 1) * 384;       \
                unsigned x0 = a[0], x1 = a[1]; \
                e[k][0] = __builtin_amdgcn_alignbit(x1, x0, sh); \
                e[k][1] = x1 >> sh; \
            } \
            r[j][0] = (e[0][0] & 0xffffu) | (e[3][0] << 16); \
            r[j][1] = (e[0][0] >> 16) | (e[3][0] & 0xffff0000u); \
            r[j][2] = (e[0][1] & 0xffffu) | (e[3][1] << 16); \
            r[j][3] = (e[1][0] & 0xffffu) | (e[2][0] << 16); \
            r[j][4] = (e[1][0] >> 16) | (e[2][0] & 0xffff0000u); \
            r[j][5] = (e[1][1] & 0xffffu) | (e[2][1] << 16); \
        } \
        wave_sync(); \
        _Pragma("unroll") \
        for (int j = 0; j < 2; j++) { *(v4u*)&cw[j * 256 + lane * 4] = (v4u){r[j][0], r[j][1], r[j][2], r[j][3]}; *(v2u*)&cw[512 + j * 128 + lane * 2] = (v2u){r[j][4], r[j][5]}; } \
        wave_sync(); }
        const int sub = lane & 31;
        const int rsel = lane >> 5;
        const unsigned sh = (sub & 1) * 16;
        const int qoff = 3 * (sub >> 1) + (sub & 1);
        const unsigned voff = 3 * lane * 4;
        asm volatile("" ::: "a16", "a17", "a18", "a19", "a20", "a21", "a22", "a23", "a24", "a25", "a26", "a27", "a28", "a29", "a30", "a31",
                            "a32", "a33", "a34", "a35", "a36", "a37", "a38", "a39", "a40", "a41", "a42", "a43", "a44", "a45", "a46", "a47");
        asm volatile("" ::: "a48", "a49", "a50", "a51", "a52", "a53", "a54", "a55", "a56", "a57", "a58", "a59", "a60", "a61", "a62", "a63",
                            "a64", "a65", "a66", "a67", "a68", "a69", "a70", "a71", "a72", "a73", "a74", "a75", "a76", "a77", "a78", "a79");
        int ta[4][3], tb[4][3];
#define WAITT(P, S, N) asm volatile("s_waitcnt vmcnt(%6)\n\tv_accvgpr_read_b32 %0, a[%c7]\n\tv_accvgpr_read_b32 %1, a[%c8]\n\tv_accvgpr_read_b32 %2, a[%c9]\n\t" \
                              "v_accvgpr_read_b32 %3, a[%c10]\n\tv_accvgpr_read_b32 %4, a[%c11]\n\tv_accvgpr_read_b32 %5, a[%c12]" \
                              : "=v"(ta[P][0]), "=v"(ta[P][1]), "=v"(ta[P][2]), "=v"(tb[P][0]), "=v"(tb[P][1]), "=v"(tb[P][2]) \
                              : "i"(N), "i"(AR(S)), "i"(AR(S) + 1), "i"(AR(S) + 2), "i"(AR(S) + 4), "i"(AR(S) + 5), "i"(AR(S) + 6))
        const unsigned voffb = voff + 768;
        u64 ln = 0;
#define TDUE(G) (tdue(G) + ((G) >= PSW ? __builtin_amdgcn_readfirstlane((unsigned)(ln >> 32)) >> ((G) >= PSG ? 16 : 0) & 0xffffu : 0u))
#define REPORT() { const unsigned t_ = now - tdc; if (lane == 0) lds_st(&sm.late, t_); }
#define LINE() ln = LDV((u64*)&sm.line);
#define LDV(P) __hip_atomic_load(P, __ATOMIC_RELAXED, __HIP_MEMORY_SCOPE_WORKGROUP)
        unsigned u = 0, rg = cst * 8 + wave, np = 0;
        CS_DMA(wave, cst)

        const char* kb0 = (const char*)qk + (size_t)(head0 * 512 + r8w) * 3072;
#define KB(I) (kb0 + ((cst + (I)) & 3) * (128 * 3072))
        const char* kb; const char* kb2;
#define SISSUE(I) { kb = KB(I); kb2 = kb + HEAD2; const unsigned du_ = lds_ld(&sm.line) + tdue(16 * (I) + 2 * wo); GATE(du_) \
            ISSUE(((I) & 1) * 4, 0, kb, koff, koffb) ISSUE(((I) & 1) * 4 + 1, 1, kb, koff, koffb) ISSUE(((I) & 1) * 4 + 2, 0, kb2, koff, koffb) ISSUE(((I) & 1) * 4 + 3, 1, kb2, koff, koffb) }
        if (!pre) SISSUE(0)
        CS_DMA(wave, (cst + 1) & 3)
        if (pre) asm volatile("s_waitcnt vmcnt(3)" ::: "memory");
        asm volatile("s_waitcnt vmcnt(11)" ::: "memory");
        if (!pre) LDS_ADD(&sm.slow, NOW() - lds_ld(&sm.line) - tdue(2 * wo))
        REPACK(wave, cst)
        SISSUE(1)
#define TABDONE(I) LDS_OR(&sm.tabs, 1u << ((((cst + (I)) & 3) * 8) + wave))
        TABDONE(0)
        const float scale = 0.07216878364870322f;
        double acc[3] = {0.0, 0.0, 0.0};
        bool first = true, chg, none = false;
        unsigned Qp[3], Qn[3], Rp[3], Rn[3];
        float kz4[4], ksc, kres[4];
        v4u ca[4]; v2u cb[4];
        v2f qe[4][3], A[4];
        unsigned vut, nrg;
        const unsigned vr = ((lane >> 3) & 3) * 128 + R8((unsigned)lane & 7);
        unsigned g, mg;
        const unsigned grab_adr = lds_addr(&sm.grab);
        asm volatile("v_accvgpr_write_b32 a13, 1\n\tv_accvgpr_write_b32 a14, 2" ::: "a13", "a14", "a15");
#define GRAB(N) { u64 ex_; asm volatile("s_mov_b64 %0, exec\n\ts_mov_b64 exec, 1\n\tds_add_rtn_u32 a15, %1, a%c2\n\ts_mov_b64 exec, %0" : "=&s"(ex_) : "v"(grab_adr), "i"(12 + (N)) : "memory"); }
#define GRABBED() { unsigned t_; asm volatile("s_waitcnt lgkmcnt(0)\n\tv_accvgpr_read_b32 %1, a15\n\ts_nop 0\n\tv_readfirstlane_b32 %0, %1\n\ts_nop 1" : "=s"(g), "=v"(t_)); }
        unsigned oa, ob, un, tdn, tiss, tdc = 0;
#define PLAN() { \
            GRABBED() un = unit_of(g); chg = un != u; none = g >= NPC; \
            const bool nv = g >= NPC / 2; \
            unsigned hd = __builtin_amdgcn_readlane(vut, un); \
            while (hd == ~0u) { SLEEP(1) hd = lds_ld(&sm.utab[un]); } \
            nrg = (g + (hd >> 8)) & 31; hd &= 255; \
            unsigned boff, boff2; asm("s_mul_i32 %0, %1, 0xc00" : "=s"(boff) : "s"(hd * 512 + __builtin_amdgcn_readlane(vr, nrg))); \
            asm("s_mul_i32 %0, %1, 0xc00" : "=s"(boff2) : "s"(hd * 512 + __builtin_amdgcn_readlane(vr, nrg + 1))); \
            const char* b_ = nv ? (const char*)qv : (const char*)qk; \
            kb = b_ + boff; kb2 = b_ + boff2; \
            oa = nv ? voff : koff; ob = nv ? voffb : koffb; \
            tdn = TDUE(g); }
#define FIRE() { ISSUE(0, 0, kb, oa, ob) ISSUE(1, 1, kb, oa, ob) ISSUE(2, 0, kb2, oa, ob) ISSUE(3, 1, kb2, oa, ob) }
#define LAUNCH() { \
            LINE() \
            const unsigned du_ = __builtin_amdgcn_readfirstlane((unsigned)ln) + tdn; \
            GATE(du_) \
            ISSUE(0, 0, kb, oa, ob) ISSUE(1, 1, kb, oa, ob) \
            GATE(du_ + 10) \
            ISSUE(2, 0, kb2, oa, ob) ISSUE(3, 1, kb2, oa, ob) tiss = NOW(); }
#define ARRIVED() \
            const unsigned now = NOW(); \
            const bool stall = now - tiss > LJ && g - PST0 < NPC - 48 - PST0; \
            const bool late = !none && !stall && (int)(now + EARLY - __builtin_amdgcn_readfirstlane((unsigned)ln) - tdn) >= 0; \
            if (late) { FIRE() tiss = now; } \
            if (stall && lane == 0) __hip_atomic_fetch_max(&sm.line, (unsigned)ln + (now - tiss) - LOK2, __ATOMIC_RELAXED, __HIP_MEMORY_SCOPE_WORKGROUP); \
            REPORT()
#define ADVANCE(ISV) { \
            if (chg) FLUSH(ISV) \
            if (!none && !late) LAUNCH() \
            first = chg; u = un; rg = nrg; tdc = tdn + 10; }
#define K_TAB(MODE) { _Pragma("unroll") for (int p = 0; p < ((MODE) ? 2 : 4); p++) { \
            const unsigned* cw = &sm.cs[(rg & 7) + (p >> 1)][rg >> 3][0]; \
            ca[p] = *(const v4u*)&cw[(p & 1) * 256 + lane * 4]; cb[p] = *(const v2u*)&cw[512 + (p & 1) * 128 + lane * 2]; } }
#define K_PRE(MODE, P) { \
        float e0, e1, e2, f0, f1, f2; \
        const unsigned* qp_ = ((MODE) && (P) >= 2) ? Rp : Qp; const unsigned* qn_ = ((MODE) && (P) >= 2) ? Rn : Qn; \
        const int tp_ = (MODE) ? (P) & 1 : (P); \
        asm("v_dot2_f32_bf16 %0, %6, %12, 0\n\tv_dot2_f32_bf16 %3, %9, %15, 0\n\tv_dot2_f32_bf16 %1, %7, %13, 0\n\tv_dot2_f32_bf16 %4, %10, %16, 0\n\t" \
            "v_dot2_f32_bf16 %2, %8, %14, 0\n\tv_dot2_f32_bf16 %5, %11, %17, 0\n\ts_nop 2" \
            : "=&v"(e0), "=&v"(e1), "=&v"(e2), "=&v"(f0), "=&v"(f1), "=&v"(f2) \
            : "v"(qp_[0]), "v"(qp_[1]), "v"(qp_[2]), "v"(qn_[0]), "v"(qn_[1]), "v"(qn_[2]), \
              "v"(ca[tp_].x), "v"(ca[tp_].y), "v"(ca[tp_].z), "v"(ca[tp_].w), "v"(cb[tp_].x), "v"(cb[tp_].y)); \
        qe[P][0] = (v2f){e0, f0}; qe[P][1] = (v2f){e1, f1}; qe[P][2] = (v2f){e2, f2}; \
        A[P] = (qe[P][0] + qe[P][1] + qe[P][2]) * (v2f){-kz4[P], -kz4[P]}; }
#define K_WAIT(MODE, P) { \
        if ((MODE) == 0) { WAITT(P, P, 6 - 2 * (P)); } \
        else { WAITT(P, (((MODE) - 1) & 1) * 4 + (P), ((MODE) < 3 ? 20 : (MODE) == 3 ? 17 : 6) - 2 * (P)); } }
#define K_FMA(P) { \
        _Pragma("unroll") \
        for (int i = 0; i < 3; i++) { v2f x = {(float)ta[P][i], (float)tb[P][i]}; A[P] = qe[P][i] * x + A[P]; } \
        kres[P] = A[P].x + A[P].y; }
#define K_RED(MODE) { \
        const bool o1 = lane & 1, o2 = lane & 2; \
        float s01 = (o1 ? kres[1] : kres[0]) + __int_as_float(__builtin_amdgcn_update_dpp(0, __float_as_int(o1 ? kres[0] : kres[1]), 0xb1, 0xf, 0xf, true)); \
        float s23 = (o1 ? kres[3] : kres[2]) + __int_as_float(__builtin_amdgcn_update_dpp(0, __float_as_int(o1 ? kres[2] : kres[3]), 0xb1, 0xf, 0xf, true)); \
        float sx = (o2 ? s23 : s01) + __int_as_float(__builtin_amdgcn_update_dpp(0, __float_as_int(o2 ? s01 : s23), 0x4e, 0xf, 0xf, true)); \
        sx = dpp_add<0x124>(sx); sx = dpp_add<0x128>(sx); \
        const auto sw = __builtin_amdgcn_permlane16_swap(__float_as_uint(sx), __float_as_uint(sx), false, false); \
        sx = __uint_as_float(sw[0]) + __uint_as_float(sw[1]); \
        if ((lane & 28) == 0) { if (MODE) sm.ent[(lane >> 1) & 1][rg * 4 + 2 * (lane & 1) + rsel] = sx * ksc; else sm.ent[u % NOUT][rg * 4 + 2 * (lane & 3) + rsel] = sx * ksc; } }
#define K_PRE4(MODE) { K_PRE(MODE, 0) K_PRE(MODE, 1) K_PRE(MODE, 2) K_PRE(MODE, 3) asm volatile("" :: "v"(A[0]), "v"(A[1]), "v"(A[2]), "v"(A[3])); }
#define K_BODY(MODE) { K_PRE4(MODE) K_WAIT(MODE, 0) K_FMA(0) K_WAIT(MODE, 1) K_FMA(1) K_WAIT(MODE, 2) K_FMA(2) K_WAIT(MODE, 3) K_FMA(3) K_RED(MODE) }
#define V_FMA(J, W0, W1) { \
        _Pragma("unroll") \
        for (int i = 0; i < 3; i++) { \
            acc[i] = fma(W0, (double)ta[J][i], acc[i]); \
            acc[i] = fma(W1, (double)tb[J][i], acc[i]); \
        } }
#define V_PIN() asm volatile("" :: "v"(acc[0]), "v"(acc[1]), "v"(acc[2]));
#define RDYWAIT(SL, SEQ) { while (lds_ld(&sm.rdy[SL]) != (SEQ)) SLEEP(1); CBAR(); }
#define LOADZ() { _Pragma("unroll") for (int p = 0; p < 4; p++) kz4[p] = sm.kz[u % NIN][rg * 4 + 2 * p + rsel].x; ksc = sm.kz[u % NIN][rg * 4 + 2 * (lane & 3) + rsel].y * scale; }
#define LOADQ() { _Pragma("unroll") for (int i = 0; i < 3; i++) { Qp[i] = sm.q[u % NIN][3 * sub + i]; Qn[i] = __builtin_amdgcn_alignbit(Qp[i], Qp[i], 16) ^ 0x80000000u; } }
#define FLUSH(ISV) { \
            mg = __builtin_amdgcn_readfirstlane(mg); \
            while (u >= mg + NOUT) { SLEEP(1); mg = lds_ld(&sm.merged); } \
            CBAR(); \
            if (ISV) { const unsigned aa = lds_addr(&sm.acc[u % NOUT][lane]); \
                asm volatile("ds_add_f64 %0, %1\n\tds_add_f64 %0, %2 offset:512\n\tds_add_f64 %0, %3 offset:1024" :: "v"(aa), "v"(acc[0]), "v"(acc[1]), "v"(acc[2]) : "memory"); \
                acc[0] = 0.0; acc[1] = 0.0; acc[2] = 0.0; } \
            CBAR(); \
            LDS_ADD(&sm.cnt[u % NOUT], np) \
            CBAR(); \
            np = 0; }

#define LOADZ2() { _Pragma("unroll") for (int p = 0; p < 4; p++) kz4[p] = sm.kz[p >> 1][rg * 4 + 2 * (p & 1) + rsel].x; ksc = sm.kz[(lane >> 1) & 1][rg * 4 + 2 * (lane & 1) + rsel].y * scale; }
#define STATIC_STEP(I) { \
            rg = ((cst + (I)) & 3) * 8 + wave; \
            LOADZ2() \
            K_TAB((I) + 1) K_BODY((I) + 1) }
        CS_DMA(wave, (cst + 2) & 3)
        RDYWAIT(0, 1u) RDYWAIT(1, 2u)
        LOADQ()
#pragma unroll
        for (int i = 0; i < 3; i++) { Rp[i] = sm.q[1][3 * sub + i]; Rn[i] = __builtin_amdgcn_alignbit(Rp[i], Rp[i], 16) ^ 0x80000000u; }
        STATIC_STEP(0)
        asm volatile("s_waitcnt vmcnt(11)" ::: "memory");
        REPACK(wave, (cst + 1) & 3)
        TABDONE(1)
        SISSUE(2)
        CS_DMA(wave, (cst + 3) & 3)
        STATIC_STEP(1)
        asm volatile("s_waitcnt vmcnt(11)" ::: "memory");
        REPACK(wave, (cst + 2) & 3)
        TABDONE(2)
        SISSUE(3)
        STATIC_STEP(2)
        asm volatile("s_waitcnt vmcnt(8)" ::: "memory");
        REPACK(wave, (cst + 3) & 3)
        TABDONE(3)
        GRAB(2)
        vut = LDV(&sm.utab[lane]);
        STATIC_STEP(3)
        CBAR();
        LDS_ADD(&sm.cnt[0], 4u) LDS_ADD(&sm.cnt[1], 4u)
        PLAN()
        LAUNCH()
        u = un; rg = nrg; tdc = tdn + 10;
        bool tabok = false;
        __builtin_amdgcn_s_setprio(2);
#pragma unroll 1
        while (u < NU) {
            GRAB(2)
            mg = LDV(&sm.merged);
            vut = LDV(&sm.utab[lane]);
            LINE()
            if (!tabok) {
                for (;;) { const unsigned tb_ = lds_ld(&sm.tabs); tabok = tb_ == ~0u; if (((tb_ >> rg) & 3u) == 3u) break; SLEEP(1) }
                CBAR();
            }
            K_TAB(0)
            if (first) {
                unsigned fl = LDV(&sm.rdy[u % NIN]);
                CBAR();
                LOADQ() LOADZ()
                if (__builtin_amdgcn_readfirstlane(fl) != u + 1) { RDYWAIT(u % NIN, u + 1) LOADQ() LOADZ() }
            } else LOADZ()
            K_PRE4(0)
            PLAN()
            K_WAIT(0, 0) K_FMA(0) K_WAIT(0, 1) K_FMA(1)
            asm volatile("" :: "v"(kres[0]), "v"(kres[1]));
            K_WAIT(0, 2) K_WAIT(0, 3)
            ARRIVED()
            K_FMA(2) K_FMA(3) K_RED(0)
            np += 2;
            ADVANCE(false)
        }
#pragma unroll 1
        while (u < 2 * NU) {
            GRAB(2)
            mg = LDV(&sm.merged);
            LINE()
            const unsigned vr_ = LDV(&sm.vrdy);
            CBAR();
            const unsigned short* wp = &sm.wv[went(u - NU) + (QUARTER(u - NU) ? rg & 7 : rg) * 4];
            v4i pw = *(const v4i*)wp;
            if (first && !((__builtin_amdgcn_readfirstlane(vr_) >> (u - NU)) & 1)) { while (!((lds_ld(&sm.vrdy) >> (u - NU)) & 1)) SLEEP(1); CBAR(); pw = *(const v4i*)wp; }
            const double w0 = (short)pw.x, w1 = pw.x >> 16, w2 = (short)pw.y, w3 = pw.y >> 16, w4 = (short)pw.z, w5 = pw.z >> 16, w6 = (short)pw.w, w7 = pw.w >> 16;
            asm volatile("" :: "v"(w0), "v"(w1), "v"(w2), "v"(w3), "v"(w4), "v"(w5), "v"(w6), "v"(w7));
            PLAN()
            v2f hl; unsigned tl8, rowl;
            if (none) { hl = *(const v2f*)&sm.wv[went(NU - 1) + 128]; tl8 = LDV(&sm.tag_lo) << 24; rowl = (lds_ld(&sm.utab[2 * NU - 1]) & 255) * (NSPLIT * 512); asm volatile("" : "+v"(hl), "+v"(tl8)); }
            WAITT(0, 0, 6); V_FMA(0, w0, w1) V_PIN()
            WAITT(1, 1, 4); V_FMA(1, w2, w3) V_PIN()
            WAITT(2, 2, 2); V_FMA(2, w4, w5) V_PIN()
            WAITT(3, 3, 0);
            np += 2;
            if (none) {
                V_FMA(3, w6, w7)
                if (u != 2 * NU - 1) { FLUSH(true) break; }
                mg = __builtin_amdgcn_readfirstlane(mg);
                while (u >= mg + NOUT) { SLEEP(1); mg = lds_ld(&sm.merged); }
                const unsigned aa = lds_addr(&sm.acc[(2 * NU - 1) % NOUT][lane]);
                double a0, a1, a2; unsigned cn; u64 ex_;
                asm volatile("ds_add_f64 %5, %6\n\tds_add_f64 %5, %7 offset:512\n\tds_add_f64 %5, %8 offset:1024\n\t"
                             "s_mov_b64 %4, exec\n\ts_mov_b64 exec, 1\n\tds_add_rtn_u32 %3, %9, %10\n\ts_mov_b64 exec, %4\n\t"
                             "ds_read_b64 %0, %5\n\tds_read_b64 %1, %5 offset:512\n\tds_read_b64 %2, %5 offset:1024\n\ts_waitcnt lgkmcnt(0)"
                             : "=&v"(a0), "=&v"(a1), "=&v"(a2), "=&v"(cn), "=&s"(ex_) : "v"(aa), "v"(acc[0]), "v"(acc[1]), "v"(acc[2]), "v"(lds_addr(&sm.cnt[(2 * NU - 1) % NOUT])), "v"(np) : "memory");
                const float n0 = (float)(a0 - (double)hl.y) * hl.x, n1 = (float)(a1 - (double)hl.y) * hl.x, n2 = (float)(a2 - (double)hl.y) * hl.x;
                const unsigned e2 = max(__float_as_uint(fmaxf(fmaxf(fabsf(n0), fabsf(n1)), fabsf(n2))) >> 23, 14u);
                const float f2 = __uint_as_float((268u - e2) << 23);
                const v2u r = {__builtin_bit_cast(unsigned, __builtin_amdgcn_cvt_pk_i16((int)rintf(n0 * f2), (int)rintf(n1 * f2))),
                               __builtin_bit_cast(unsigned, __builtin_amdgcn_cvt_pk_i16((int)rintf(n2 * f2), 0)) | e2 << 16 | tl8};
                const __amdgpu_buffer_rsrc_t rsrc = __builtin_amdgcn_make_buffer_rsrc(ws + WS_PART + split * 512, 0, NRW * NSPLIT * 512 - split * 512, 0x00020000);
                asm volatile("" :: "v"(r));
                if (__builtin_amdgcn_readfirstlane(cn + np) == 32) {
                    __builtin_amdgcn_raw_buffer_store_b64(r, rsrc, lane * 8, rowl, SC1);
#pragma unroll
                    for (int j = 0; j < NCP - 1; j++) __builtin_amdgcn_raw_buffer_store_b64(r, rsrc, lane * 8, (rowl & 15 * NSPLIT * 512 ^ NSPLIT * 512) + (NRR + j * GSZ) * NSPLIT * 512, SC1);
                }
                break;
            }
            ARRIVED()
            V_FMA(3, w6, w7)
            ADVANCE(true)
        }
    } else {
    u64 E1;
    if (wave == NSW) {
        unsigned ref;
        const unsigned sidx = (blockIdx.x & 7) | ((blockIdx.x >> 7) << 3);
#define SAMPLE(K) ((((blockIdx.x >> 3) - (blockIdx.x & 7) - (K) * 5) & 15) == 0)
#define BTAG(K) ((btag + (K)) & 0xfffffu)
#define POST(K, C) { if (lane == 0) __hip_atomic_store((u64*)(ws + (bd + (K) * RST + (sidx >> 3) * 256 + (sidx & 7) * 8)), (u64)(C) | ((u64)(((C) - ref) | BTAG(K) << 12) << 32), __ATOMIC_RELAXED, __HIP_MEMORY_SCOPE_AGENT); }
        const unsigned own = lds_ld(&sm.line);
        if (SAMPLE(0) && lane < NTRY) __hip_atomic_store((unsigned*)(ws + WS_HELLO + lane * 768) + sidx, own, __ATOMIC_RELAXED, __HIP_MEMORY_SCOPE_AGENT);
        const u64 E = __hip_atomic_load((const u64*)(ws + WS_EPOCH), __ATOMIC_RELAXED, __HIP_MEMORY_SCOPE_AGENT);
        E1 = E + 1;
        if (lane == 0) { sm.tag_lo = (unsigned)E1; sm.tag_hi = (unsigned)(E1 >> 32); CBAR(); lds_st(&sm.tag_rdy, 1u); }
        const unsigned btag = __builtin_amdgcn_readfirstlane((unsigned)E1) * 61u, bd = WS_BOARD + (btag & 7) * (NRND * RST);
        ref = own;
        int off = 0;
        {
            unsigned tries = 0, cur = own, tl = own;
            bool news = false;
#pragma unroll 1
            while (!news || cur != ref) {
                const unsigned now = NOW();
                if (tries < NTRY && (int)(now - own) >= (int)(TA + tries * TA2)) {
                    v16u x;
                    asm volatile("s_load_dwordx16 %0, %1, %2\n\ts_waitcnt lgkmcnt(0)" : "=&s"(x) : "s"(ws), "s"(__builtin_amdgcn_readfirstlane(WS_HELLO + tries * 768)) : "memory");
                    unsigned bad = 0; int sum = 0, dif = 0;
#pragma unroll
                    for (int i = 0; i < 16; i++) { bad |= x[i] - own + 512; sum += x[i] - x[0]; dif += i & 4 ? x[i] : -x[i]; }
                    tries++;
                    if (bad < 1024) { const unsigned com = x[0] + (sum >> 4) + abs(dif) * (8 - KC) / 128; ref = x[0] + ((sum + (blockIdx.x & 4 ? dif : -dif)) >> 4); off = ref - com;
                        tries = NTRY; tl = NOW(); if (lane == 0) lds_st(&sm.aline, com); }
                    continue;
                }
                if (!news && lds_ld(&sm.utab[31]) != ~0u) { news = true; CBAR(); if (cur == ref) tl = now; ref = lds_ld(&sm.aline) + off; tries = NTRY; }
                const int d = (now - tl) >> CSH;
                if (cur != ref && d) { tl += d << CSH; cur += min(max((int)(ref - cur), -d), d); if (lane == 0) lds_st(&sm.line, cur); }
                SLEEP(2)
            }
        }
        ref -= off;
        const unsigned ref0 = ref;
        const unsigned lead = (blockIdx.x >> 3) == 9 ? 0 : LEAD;
        unsigned posted = ref, lagf = ref, tpost = ref + TB1, tpoll = tpost + RDD + lead, kw = 0, kr = 0, cur = ref + off, khold = 0;
        int o = off; bool held = false;
#pragma unroll 1
        for (;;) {
            const unsigned p = lds_ld(&sm.grab);
            if (p >= NPC) break;
            lagf += (int)(lds_ld(&sm.late) - lagf) >> 1;
            { const unsigned l = lds_ld(&sm.line); if (l != cur) { cur = l; khold = kw; held = true; } }
            const unsigned now = NOW();
            if (o) { o = off * min(max((int)(ref0 + TC0 + (1 << TCS) - now), 0), 1 << TCS) >> TCS; if (!held && cur != ref + o) { cur = ref + o; if (lane == 0) lds_st(&sm.line, cur); } }
            if (kw < NRND && (int)(now - tpost) >= 0) {
                const unsigned c = ref + min((p >= PSL0 && (int)(lagf - cur) > LHI ? lagf - LOK : cur) - o - ref, 4000u);
                if (SAMPLE(kw) && (c != ref || (ref != posted && (sidx & 7) == (kw & 7)))) POST(kw, c)
                posted = ref;
                tpost += PER; kw++;
            }
            if (kr < NRND && (int)(now - tpoll) >= 0) {
                v16u xa, xb;
                asm volatile("s_load_dwordx16 %0, %2, %3\n\ts_load_dwordx16 %1, %2, %4\n\ts_waitcnt lgkmcnt(0)" : "=&s"(xa), "=&s"(xb)
                             : "s"(ws), "s"(__builtin_amdgcn_readfirstlane(bd + kr * RST)), "s"(__builtin_amdgcn_readfirstlane(bd + kr * RST + 256)) : "memory");
                int nb = 0, sg = 0;
#pragma unroll
                for (int i = 0; i < 16; i++) {
                    const unsigned lo = i < 8 ? xa[2 * i & 15] : xb[2 * i & 15], hi = i < 8 ? xa[(2 * i + 1) & 15] : xb[(2 * i + 1) & 15];
                    if (hi >> 12 == BTAG(kr) && lo - ref0 < 8000) { nb = max(nb, (int)(lo - (hi & 4095u) - ref)); sg += hi & 4095u; }
                }
                ref += nb + (sg >> 4);
                if (cur != ref + o && (kr >= khold || (int)(ref + o - cur) > 0)) { cur = ref + o; held = false; if (lane == 0) lds_st(&sm.line, cur); }
                tpoll += PER; kr++;
            }
            SLEEP(2)
        }
    } else if (wave == NSW + 1 || wave == NSW + 2) {
        const int ql = lane < 48 ? lane : 47;
        unsigned c1, c2, s1, s2;
#define QT_LOADS() { int qo_ = ql; asm volatile("" : "+v"(qo_)); \
                     c1 = ((const unsigned*)q_cos)[qo_]; c2 = ((const unsigned*)q_cos)[48 + qo_]; s1 = ((const unsigned*)q_sin)[qo_]; s2 = ((const unsigned*)q_sin)[48 + qo_]; }
        unsigned q1, q2, lkz, lks, lvz, lvs;
        const unsigned sofs = R8(((unsigned)lane >> 1) & 7) * 2 + (lane & 1);
#define UNIT_LOADS(HEAD, GRP) { \
        q1 = ((const unsigned*)(q + (size_t)(HEAD) * DH))[ql]; q2 = ((const unsigned*)(q + (size_t)(HEAD) * DH))[48 + ql]; \
        const size_t so = (size_t)(HEAD) * (SKV / 2) + (GRP) * 256 + sofs; \
        lkz = *((const unsigned*)k_zeros + so); lks = *((const unsigned*)k_scales + so); \
        lvz = *((const unsigned*)v_zeros + so); lvs = *((const unsigned*)v_scales + so); }
#define UNIT_FILL(SL, U) { \
            v4f qq; \
            qq.x = bfr(bfr(bflo(q1) * bflo(c1)) + bfr(-bflo(q2) * bflo(s1))); \
            qq.y = bfr(bfr(bflo(q2) * bflo(c2)) + bfr(bflo(q1) * bflo(s2))); \
            qq.z = bfr(bfr(bfhi(q1) * bfhi(c1)) + bfr(-bfhi(q2) * bfhi(s1))); \
            qq.w = bfr(bfr(bfhi(q2) * bfhi(c2)) + bfr(bfhi(q1) * bfhi(s2))); \
            *(v2u*)&sm.q[SL][2 * ql] = (v2u){(__float_as_uint(qq.x) >> 16) | __float_as_uint(qq.y), (__float_as_uint(qq.z) >> 16) | __float_as_uint(qq.w)}; \
            *(v4f*)&sm.kz[SL][2 * lane] = (v4f){bflo(lkz), bflo(lks), bfhi(lkz), bfhi(lks)}; \
            *(v4f*)&sm.vz[SL][2 * lane] = (v4f){bflo(lvz), bflo(lvs), bfhi(lvz), bfhi(lvs)}; \
            CBAR(); \
            if (lane == 0) lds_st(&sm.rdy[SL], (U) + 1); \
            CBAR(); }
        const unsigned dw = NSW + 2 - wave;
        QT_LOADS()
        UNIT_LOADS(head0 + dw * (GSZ + 1 - 2 * side), lane >> 4)
#pragma unroll 1
        for (unsigned u = dw;;) {
            while (u >= lds_ld(&sm.merged) + NIN) SLEEP(4);
            CBAR();
            UNIT_FILL(u % NIN, u)
            if (u - 1 < 12) {
                const unsigned x_ = (blockIdx.x >> 3) + 32 * u - 8; unsigned dm;
                const char* p_ = x_ < 384 ? (const char*)(x_ < 192 ? qk : qv) + (size_t)(x_ < 192 ? x_ : x_ - 192) * (2u << 20) + (x_ * 8 + (blockIdx.x & 7)) * 640 : (const char*)out + (x_ - 384) * 4096;
                while ((int)(NOW() - lds_ld(&sm.line)) < TSD) SLEEP(2)
                asm volatile("s_load_dword %0, %1, 0x0\n\ts_waitcnt lgkmcnt(0)" : "=&s"(dm) : "s"(p_) : "memory");
            }
            u += 2;
            if (u >= NU) break;
            if (u == 4) {
                const unsigned tpf = tdue(PSF - 1) + TFO;
                { int d; while ((d = (int)(lds_ld(&sm.line) + tpf - NOW())) > 0 && d < 3000) SLEEP(1) }
#define PF3(P) asm volatile("global_load_dwordx4 a[0:3], %0, %1\n\tglobal_load_dwordx4 a[0:3], %0, %1 offset:1024\n\tglobal_load_dwordx4 a[0:3], %0, %1 offset:2048" \
                            :: "v"(64 * SKV * 2 + blockIdx.x * 3072 + lane * 16), "s"(P) : "a0", "a1", "a2", "a3")
                PF3(k_zeros); PF3(k_scales); PF3(v_zeros); PF3(v_scales);
                asm volatile("global_load_dword a0, %0, %1" :: "v"(blockIdx.x * (DH * 2) + ql * 8), "s"(q) : "a0");
            }
            const unsigned tin = tdue(first_of(u) - (QUARTER(u) ? 24 : 0) - LEADP + (split & 7) * DPH);
            { int d; while ((d = (int)(lds_ld(&sm.line) + tin - NOW())) > 0 && d < 3000) SLEEP(4) }
            unsigned hd;
            while ((hd = lds_ld(&sm.utab[u])) == ~0u) SLEEP(2)
            UNIT_LOADS(hd & 255, QUARTER(u) ? rank & 3 : lane >> 4)
        }
        while (lds_ld(&sm.tag_rdy) == 0) SLEEP(1);
        CBAR();
        E1 = (u64)lds_ld(&sm.tag_lo) | ((u64)lds_ld(&sm.tag_hi) << 32);
        if (wave == NSW + 1 && blockIdx.x == 0) {
            for (;;) {
                bool ok = true;
#pragma unroll
                for (int i = 0; i < NH / 64; i++) ok &= __hip_atomic_load((const u64*)(ws + WS_POST) + i * 64 + lane, __ATOMIC_RELAXED, __HIP_MEMORY_SCOPE_AGENT) >> 24 == (E1 << 24) >> 24;
                if (__builtin_amdgcn_ballot_w64(ok) == ~0ull) break;
                SLEEP(64);
            }
            if (lane == 0) __hip_atomic_store((u64*)(ws + WS_EPOCH), E1, __ATOMIC_RELAXED, __HIP_MEMORY_SCOPE_AGENT);
        }
        if (wave == NSW + 2) {
            const unsigned t16 = (unsigned)E1 & 0xffffu, t16b = ((unsigned)E1 + ((unsigned)E1 >> 16)) & 0xffffu;
            const __amdgpu_buffer_rsrc_t rsrc = __builtin_amdgcn_make_buffer_rsrc(ws + WS_SWAP + (blockIdx.x ^ 4) * 5120, 0, 5120, 0x00020000);
            const unsigned swp = lds_ld(&sm.swp);
            if (swp) {
                unsigned use = 0;
#pragma unroll
                for (int i = 0; i < 5; i++) use |= ((swp >> wunit(i * 64 + lane)) & 1) << i;
                const unsigned tg = tdue(PSG - 1) + (lds_ld(&sm.gap) & 0xffffu);
                while ((int)(NOW() - lds_ld(&sm.line) - tg) < 0) SLEEP(2)
                for (;;) {
                    v4u d[5]; bool ok = true;
#pragma unroll
                    for (int i = 0; i < 5; i++) d[i] = __builtin_amdgcn_raw_buffer_load_b128(rsrc, (use >> i) & 1 ? (i * 64 + lane) * 16 : 0x40000000, 0, SC1 | 2);
#pragma unroll
                    for (int i = 0; i < 5; i++) if ((use >> i) & 1) {
                        ok &= d[i].y >> 16 == t16 && d[i].w >> 16 == t16b;
                        unsigned* w = (unsigned*)&sm.wv[144 * NMIX + (i * 64 + lane) * 6];
                        w[0] = d[i].x; w[1] = (d[i].y & 0xffffu) | (d[i].z << 16); w[2] = (d[i].z >> 16) | (d[i].w << 16);
                    }
                    if (__builtin_amdgcn_ballot_w64(ok) == ~0ull) break;
                    SLEEP(32)
                }
                CBAR();
                LDS_OR(&sm.vrdy, swp)
            }
        }
    } else {
        const unsigned eu = lane >= NU ? lane - NU : lane, win = min(first_of(eu) >> 5, 15u), pair = QUARTER(eu) ? 2 * (eu - WQ) + (rank >> 2) : rank;
        const unsigned rot2 = ((const unsigned char*)ROT)[win * 8 + pair];
#define UENT(BIT) (win * 16 + 2 * pair + ((BIT) ^ side) | (QUARTER(eu) ? (rank - (eu - WQ)) * 8 & 31 : (rot2 >> ((BIT) ^ side) * 4 & 15) * 2) << 8)
        unsigned far = 0;
        const unsigned hp = (blockIdx.x & 1) * NH + (blockIdx.x >> 1) * 4 + 2;
        if (blockIdx.x < 144) {
            const char* pb = (blockIdx.x < 128 ? (const char*)(hp >= NH ? qv : qk) + (size_t)(hp & (NH - 1)) * (SKV * DH * 4) + SKV * DH * 2
                                               : (const char*)ws + WS_PART + (8u << 20) + (blockIdx.x & 15) * 65536) + ((NOW() * 0x9e3779b1u) >> 26) * 128;
            unsigned mn[2] = {~0u, ~0u};
#pragma unroll 1
            for (unsigned i = 1; i < 2 * NPRB + 2; i++) {
                u64 t0, t1; unsigned dm;
                asm volatile("s_memtime %0\n\ts_waitcnt lgkmcnt(0)\n\ts_load_dword %2, %3, 0x0\n\ts_waitcnt lgkmcnt(0)\n\ts_memtime %1\n\ts_waitcnt lgkmcnt(0)"
                             : "=&s"(t0), "=&s"(t1), "=&s"(dm) : "s"(pb + i * 8192) : "memory");
                if (i == 1) asm volatile("global_load_dword a0, %0, %1 nt" :: "v"(0), "s"(pb) : "a0");
                const unsigned t = (unsigned)(t1 - t0) < 330 || i == 1 ? ~0u : (unsigned)(t1 - t0);
                if (i & 1) mn[1] = min(mn[1], t); else mn[0] = min(mn[0], t);
            }
            far = mn[0] > mn[1];
        }
        if (lane < NMIX) lds_st(&sm.utab[lane], UENT(lane & 1));
        while (lds_ld(&sm.tag_rdy) == 0) SLEEP(1);
        CBAR();
        E1 = (u64)lds_ld(&sm.tag_lo) | ((u64)lds_ld(&sm.tag_hi) << 32);
        {
            const unsigned own = lds_ld(&sm.line), tg = (unsigned)E1;
            if (lane == 0) __hip_atomic_store((u64*)(ws + WS_POST) + blockIdx.x, E1 << 24 | min(lds_ld(&sm.slow), 4000u) << 1 | ((far ^ (blockIdx.x >> 2) ^ (blockIdx.x < 128 ? 0x96 >> (hp & 7) : 0)) & 1), __ATOMIC_RELAXED, __HIP_MEMORY_SCOPE_AGENT);
            const __amdgpu_buffer_rsrc_t nrs = __builtin_amdgcn_make_buffer_rsrc(ws + WS_NEWS, 0, 8 * 1280, 0x00020000);
            if (blockIdx.x == MASTER) {
                while ((int)(NOW() - own) < TMAP) SLEEP(2)
                const unsigned a = tg << 24, b = (unsigned)(E1 >> 8);
                v4u x; v2u z; u64 v0, v1;
#pragma unroll 1
                for (int i = 0; i < 8; i++) {
                    x = __builtin_amdgcn_raw_buffer_load_b128(__builtin_amdgcn_make_buffer_rsrc(ws + WS_POST, 0, 1024, 0x00020000), lane * 16, 0, SC1);
                    z = __builtin_amdgcn_raw_buffer_load_b64(__builtin_amdgcn_make_buffer_rsrc(ws + WS_POST + 1024, 0, 128, 0x00020000), lane * 8, 0, SC1);
                    v0 = __builtin_amdgcn_ballot_w64(((x.x ^ a) >> 24 | (x.y ^ b)) == 0); v1 = __builtin_amdgcn_ballot_w64(((x.z ^ a) >> 24 | (x.w ^ b)) == 0);
                    if (__builtin_popcountll(v0) + __builtin_popcountll(v1) >= MINV) break;
                    SLEEP(8)
                }
                const u64 m0 = __builtin_amdgcn_ballot_w64(x.x & 1) & v0, m1 = __builtin_amdgcn_ballot_w64(x.z & 1) & v1;
                const int s0 = lane < 43 ? min(max(lane - VW, 4 * NMIX), 42 - 2 * VW) : min(max(lane - VW, 43), 63 - 2 * VW);
#define PC(M) __builtin_popcount((unsigned)((M) >> s0) & ((2u << 2 * VW) - 1))
                const u64 g0 = __builtin_amdgcn_ballot_w64(2 * PC(m0) > PC(v0)), g1 = __builtin_amdgcn_ballot_w64(2 * PC(m1) > PC(v1));
                const float sl = wave_sum(((v0 >> lane & 1) ? (float)(x.x >> 1 & 4095u) : 0.f) + ((v1 >> lane & 1) ? (float)(x.z >> 1 & 4095u) : 0.f)) / ((8.f - PRE0) * (__builtin_popcountll(v0) + __builtin_popcountll(v1)) + 1.f);
                const unsigned al = lds_ld(&sm.aline), mean = (al ? al : own) + (int)fmaxf(sl - SL0, 0.f);
                const bool vz = lane < 16 && ((z.x ^ a) >> 24 | (z.y ^ b)) == 0;
                const unsigned gw = 2 * __builtin_popcountll(__builtin_amdgcn_ballot_w64(vz && (z.x & 1))) > __builtin_popcountll(__builtin_amdgcn_ballot_w64(vz));
                const unsigned k = lane & 3;
                if (lane < 32 && k < 3) __builtin_amdgcn_raw_buffer_store_b128((v4u){k == 0 ? (unsigned)g0 : k == 1 ? (unsigned)g1 : mean, tg, k == 0 ? (unsigned)(g0 >> 32) : k == 1 ? (unsigned)(g1 >> 32) : gw, tg},
                                                                              nrs, (lane >> 2) * 1280 + k * 16, 0, SC1);
            }
            while ((int)(NOW() - own) < TMAP + TRES) SLEEP(2)
            v4u y;
            for (;;) {
                y = __builtin_amdgcn_raw_buffer_load_b128(nrs, lane < 3 ? (blockIdx.x & 7) * 1280 + lane * 16 : 0x40000000, 0, SC1);
                if (((unsigned)__builtin_amdgcn_ballot_w64(y.y == tg && y.w == tg) & 7u) == 7u) break;
                SLEEP(8)
            }
            const u64 f0 = (unsigned)__builtin_amdgcn_readlane(y.x, 0) | (u64)(unsigned)__builtin_amdgcn_readlane(y.z, 0) << 32, f1 = (unsigned)__builtin_amdgcn_readlane(y.x, 1) | (u64)(unsigned)__builtin_amdgcn_readlane(y.z, 1) << 32;
            if (lane == 2) { sm.aline = y.x - own + 500 < 1000 ? y.x : own; sm.chead = (blockIdx.x & ~5u) | ((blockIdx.x >> 2 ^ y.z) & 1) | (blockIdx.x & 1) << 2; }
            unsigned bit = ((unsigned)((lane >= NU ? f1 : f0) >> (4 * win + (pair >> 1))) ^ (0x66 >> pair)) & 1;
            if (eu < NMIX) bit = eu & 1;
            const u64 bm = __builtin_amdgcn_ballot_w64(bit);
            const unsigned nsw = __builtin_popcountll((f0 ^ f1) >> (4 * NMIX));
            if (lane == 0) { sm.swp = (unsigned)(bm ^ (bm >> NU)) & ((1u << NU) - 1); lds_st(&sm.gap, nsw ? (TG0 + TG1 * nsw / 8) * 0x10001u + ((TH0 + TH1 * nsw / 8) << 16) : 0u); }
            CBAR();
            lds_st(&sm.utab[lane], UENT(bit));
        }
        const unsigned swp = lds_ld(&sm.swp);
        const unsigned T = lds_ld(&sm.tag_lo);
        const unsigned t15 = T & 0x7fffu;
        const __amdgpu_buffer_rsrc_t rsrc = __builtin_amdgcn_make_buffer_rsrc(ws + WS_PART + split * 512, 0, NRW * NSPLIT * 512 - split * 512, 0x00020000);
        v2u* recs = (v2u*)&sm.cs[0][0][0];
#pragma unroll
        for (int i = 0; i < NOUT * 3; i++) (&sm.acc[0][0])[i * 64 + lane] = 0.0;
        CBAR();
        unsigned vut = sm.utab[NU + (lane & 31)] & 255;
        if (QUARTER((unsigned)lane) && (rank & 3)) vut = NH - 16 + (rank & 3) * 16 + ((vut & 15) ^ 1);
        __builtin_amdgcn_s_setprio(3);
        unsigned po[NU / 2];
#pragma unroll
        for (int k = 0; k < NU / 2; k++) po[k] = __builtin_amdgcn_ds_bpermute((2 * k + (lane >> 5)) * 4, vut) * (NSPLIT * 512) + (lane & 31) * 16;
        const u64 rb_ = (u64)(ws + WS_PART + split * 512);
        const v4u rs_ = {(unsigned)rb_, (unsigned)(rb_ >> 32) & 0xffffu, NRW * NSPLIT * 512 - split * 512, 0x00020000u};
#pragma unroll 1
        for (unsigned u = 0; u < 2 * NU - 1; u++) {
            const unsigned e = u % NU, so = u % NOUT;
            if (u >= NU) while (!((lds_ld(&sm.vrdy) >> e) & 1)) SLEEP(4)
            CBAR();
            const v4f h = *(const v4f*)&sm.wv[went(e) + (QUARTER(e) ? 32 : 128)];
            while (lds_ld(&sm.cnt[so]) != (QUARTER(e) ? 8 : 32)) SLEEP(1)
            CBAR();
            const double a0 = sm.acc[so][lane], a1 = sm.acc[so][64 + lane], a2 = sm.acc[so][128 + lane];
            if (u < NU) {
                float2 lg = *(const float2*)&sm.ent[so][2 * lane];
                if (QUARTER(e) && lane >> 4 != (rank & 3)) lg = make_float2(-INFINITY, -INFINITY);
                const v4f zs = *(const v4f*)&sm.vz[u % NIN][2 * lane];
                const float m = wave_max(fmaxf(lg.x, lg.y));
                const float p0 = __expf(lg.x - m), p1 = __expf(lg.y - m);
                const float x0 = p0 * zs.y, x1 = p1 * zs.w;
                const unsigned eb = max(__float_as_uint(wave_max(fmaxf(fabsf(x0), fabsf(x1)))) >> 23, 14u);
                const float f = __uint_as_float((268u - eb) << 23);
                const float w0 = fminf(fmaxf(rintf(x0 * f), -32767.f), 32767.f), w1 = fminf(fmaxf(rintf(x1 * f), -32767.f), 32767.f);
                const float den = wave_sum(p0 + p1);
                const float bz = wave_sum(fmaf(w0, zs.x, w1 * zs.z));
                const float L = fmaf(m, 1.4426950408889634f, __builtin_amdgcn_logf(den));
                unsigned* wd = (unsigned*)(((swp >> u) & 1 ? sm.wo : sm.wv) + went(u));
                if (!QUARTER(u) || lane >> 4 == (rank & 3)) wd[QUARTER(u) ? lane & 15 : lane] = ((unsigned)(int)w0 & 0xffffu) | ((unsigned)(int)w1 << 16);
                if (lane == 0) *(v4f*)&wd[QUARTER(u) ? 16 : 64] = (v4f){ldexpf(1.f / den, (int)eb - 141), bz, L, 0.f};
                CBAR();
                if (!((swp >> u) & 1)) LDS_OR(&sm.vrdy, 1u << u)
            } else {
                const float n0 = (float)(a0 - (double)h.y) * h.x, n1 = (float)(a1 - (double)h.y) * h.x, n2 = (float)(a2 - (double)h.y) * h.x;
                const unsigned e2 = max(__float_as_uint(wave_maxv(fmaxf(fmaxf(fabsf(n0), fabsf(n1)), fabsf(n2)))) >> 23, 14u);
                const float f2 = __uint_as_float((268u - e2) << 23);
                const unsigned m01 = __builtin_bit_cast(unsigned, __builtin_amdgcn_cvt_pk_i16((int)rintf(n0 * f2), (int)rintf(n1 * f2)));
                const unsigned m2 = __builtin_bit_cast(unsigned, __builtin_amdgcn_cvt_pk_i16((int)rintf(n2 * f2), 0));
                const unsigned bit = ((lane & 1 ? e2 : __float_as_uint(h.z)) >> (lane >> 1)) & 1u;
                recs[e * 64 + lane] = (v2u){m01, m2 | bit << 16 | t15 << 17};
                CBAR();
                sm.acc[so][lane] = 0.0; sm.acc[so][64 + lane] = 0.0; sm.acc[so][128 + lane] = 0.0;
            }
            CBAR();
            if (lane == 0) { lds_st(&sm.cnt[so], 0u); CBAR(); lds_st(&sm.merged, u + 1); }
            if (u == NU - 1 && swp) {
                wave_sync();
                const __amdgpu_buffer_rsrc_t srs = __builtin_amdgcn_make_buffer_rsrc(ws + WS_SWAP + blockIdx.x * 5120, 0, 5120, 0x00020000);
                v4u d[5]; unsigned o[5];
#pragma unroll
                for (int i = 0; i < 5; i++) {
                    const unsigned* w = (const unsigned*)&sm.wo[144 * NMIX + (i * 64 + lane) * 6];
                    const unsigned d0 = w[0], d1 = w[1], d2 = w[2];
                    d[i] = (v4u){d0, (d1 & 0xffffu) | (T << 16), (d1 >> 16) | (d2 << 16), (d2 >> 16) | ((T + (T >> 16)) << 16)};
                    o[i] = (swp >> wunit(i * 64 + lane)) & 1 ? (i * 64 + lane) * 16 : 0x40000000;
                }
                while ((int)(NOW() - lds_ld(&sm.line)) < (int)tdue(PSW) + TWO) SLEEP(1)
#pragma unroll
                for (int i = 0; i < 5; i++) __builtin_amdgcn_raw_buffer_store_b128(d[i], srs, o[i], 0, SC1 | 2);
            }
        }
        wave_sync();
#pragma unroll
        for (int k = 0; k < NU / 2; k++) asm volatile("ds_read_b128 a[%c0:%c0+3], %1 offset:%2" :: "i"(16 + 4 * k), "v"(lds_addr(recs) + lane * 16), "i"(k * 1024) : "memory");
        asm volatile("s_waitcnt lgkmcnt(0)" ::: "memory");
        while (!((lds_ld(&sm.vrdy) >> (NU - 1)) & 1)) SLEEP(4)
        CBAR();
        const v2u hw_ = {*(const unsigned*)&sm.wv[went(NU - 1) + 132], T};
        const unsigned hrow = (NRW - GSZ + (__builtin_amdgcn_readlane(vut, NU - 1) & 15 ^ 1)) * (NSPLIT * 512);
        while (lds_ld(&sm.grab) < NPC - PUBP) SLEEP(1)
        if (lane < 16) __builtin_amdgcn_raw_buffer_store_b64(hw_, rsrc, lane * 8, hrow, SC1);
#define PUB(K) asm volatile("buffer_store_dwordx4 a[%c0:%c0+3], %1, %2, 0 offen sc1" :: "i"(16 + 4 * (K)), "v"(po[K]), "s"(rs_) : "memory");
        PUB(WQ / 2) PUB(WQ / 2 + 1)
        { const unsigned t_ = TLAST() + PDEL; GATE(t_) }
        PUB(0) PUB(1) PUB(2) PUB(3) PUB(4) PUB(7) PUB(8)
        return;
    }
    }
    __builtin_amdgcn_s_setprio(0);
    unsigned c;
#define ADDRTN(P) { u64 ex_; asm volatile("s_mov_b64 %0, exec\n\ts_mov_b64 exec, 1\n\tds_add_rtn_u32 a15, %1, a13\n\ts_mov_b64 exec, %0" : "=&s"(ex_) : "v"(lds_addr(P)) : "a15", "memory"); }
#define ADDED(V) { unsigned t_; asm volatile("s_waitcnt lgkmcnt(0)\n\tv_accvgpr_read_b32 %1, a15\n\ts_nop 0\n\tv_readfirstlane_b32 %0, %1\n\ts_nop 1" : "=s"(V), "=v"(t_)); }
    asm volatile("v_accvgpr_write_b32 a13, 1\n\ts_nop 1" ::: "a13");
    ADDRTN(&sm.cidx) ADDED(c)
    if (c < NSW) {
        while (lds_ld(&sm.grab) < NPC + 2) SLEEP(4)
        const unsigned head = lds_ld(&sm.chead), T = lds_ld(&sm.tag_lo), tl = TLAST();
        const u64 rb_ = (u64)(ws + WS_PART);
        const v4u rs_ = {(unsigned)rb_, (unsigned)(rb_ >> 32) & 0xffffu, NRW * NSPLIT * 512, 0x00020000u};
        const unsigned ro = head * (NSPLIT * 512) + c * 1024, rx = (NH + (head & 15 ^ 1)) * (NSPLIT * 512) + c * 1024;
#define POLL(S, O) asm volatile("buffer_load_dwordx4 a[%c0:%c0+3], %1, %2, %3 offen sc1" :: "i"(16 + 4 * (S)), "v"(lane * 16), "s"(rs_), "s"(O) : "memory");
#define FETCH(X, S, N) asm volatile("s_waitcnt vmcnt(%c5)\n\tv_accvgpr_read_b32 %0, a[%c4]\n\tv_accvgpr_read_b32 %1, a[%c4+1]\n\tv_accvgpr_read_b32 %2, a[%c4+2]\n\tv_accvgpr_read_b32 %3, a[%c4+3]" \
                                    : "=v"((X).x), "=v"((X).y), "=v"((X).z), "=v"((X).w) : "i"(16 + 4 * (S)), "i"(N));
        float o0, o1, o2;
        if (head >= NH - GSZ) {
            __builtin_amdgcn_s_setprio(3);
            if (c == 0) {
                v2u hd;
                GATE(tl + THD)
                for (;;) {
                    hd = __builtin_amdgcn_raw_buffer_load_b64(__builtin_amdgcn_make_buffer_rsrc(ws + WS_PART + (NRW - GSZ + (head & 15 ^ 1)) * (NSPLIT * 512), 0, NSPLIT * 512, 0x00020000), (lane & 15) * 512, 0, SC1);
                    if (__builtin_amdgcn_ballot_w64(hd.y == T) == ~0ull) break;
                    SLEEP(4)
                }
                const float w = __builtin_amdgcn_exp2f(__uint_as_float(hd.x) - row_max(__uint_as_float(hd.x)));
                if (lane < 16) sm.ent[0][lane] = w * __builtin_amdgcn_rcpf(row_sum(w));
                CBAR();
                if (lane == 0) lds_st(&sm.tabs, 0u);
            }
            while (lds_ld(&sm.tabs) || lds_ld(&sm.merged) <= (2 * NU - 1) / NOUT * NOUT) SLEEP(1)
            CBAR();
            const float cf = sm.ent[0][2 * c + (lane >> 5)];
            v4u x; bool got;
#define SEEN(J, N) { FETCH(x, J, N) got = __builtin_amdgcn_ballot_w64((((x.y ^ T << 24) | (x.w ^ T << 24)) >> 24) != 0) == 0; }
            GATE(tl + TCA) POLL(0, ro) GATE(tl + TCA + TCD) POLL(1, rx + 3 * GSZ * NSPLIT * 512) SEEN(0, 1)
            if (!got) SEEN(1, 0)
            while (!got) { POLL(2, rx + 4 * GSZ * NSPLIT * 512) SEEN(2, 0) SLEEP(0) }
            const double c0 = ldexpf(cf, (int)(x.y >> 16 & 255u) - 141), c1 = ldexpf(cf, (int)(x.w >> 16 & 255u) - 141);
#define SWADD(A, B) ({ const v2u a_ = __builtin_bit_cast(v2u, A), b_ = __builtin_bit_cast(v2u, B); \
                const auto l_ = __builtin_amdgcn_permlane32_swap(a_.x, b_.x, false, false); const auto h_ = __builtin_amdgcn_permlane32_swap(a_.y, b_.y, false, false); \
                __builtin_bit_cast(double, (v2u){l_[0], h_[0]}) + __builtin_bit_cast(double, (v2u){l_[1], h_[1]}); })
            const double s0 = SWADD(c0 * (double)(short)x.x, c1 * (double)(short)x.z), s1 = SWADD(c0 * (double)((int)x.x >> 16), c1 * (double)((int)x.z >> 16)),
                         s2 = SWADD(c0 * (double)(short)x.y, c1 * (double)(short)x.w);
            asm volatile("ds_add_f64 %0, %1\n\tds_add_f64 %0, %2 offset:512\n\tds_add_f64 %0, %3 offset:1024" :: "v"(lds_addr(&sm.acc[0][lane])), "v"(s0), "v"(s1), "v"(s2) : "memory");
            ADDRTN(&sm.cdone)
            const double a0 = sm.acc[0][lane], a1 = sm.acc[0][64 + lane], a2 = sm.acc[0][128 + lane];
            ADDED(c)
            o0 = a0; o1 = a1; o2 = a2;
        } else {
            const bool quart = head >> 4 == WQ;
            const unsigned t15 = T & 0x7fffu;
#define TAKE(K) { FETCH(x[K], K, 0) \
                bad |= (x[K].y >> 17 ^ t15) | (x[K].w >> 17 ^ t15); \
                const u64 bl = __builtin_amdgcn_ballot_w64((x[K].y & 0x10000u) != 0), be = __builtin_amdgcn_ballot_w64((x[K].w & 0x10000u) != 0); \
                L[K] = __uint_as_float(lane < 32 ? (unsigned)bl : (unsigned)(bl >> 32)); ex[K] = lane < 32 ? (unsigned)be : (unsigned)(be >> 32); \
                M = fmaxf(M, fmaxf(__uint_as_float((unsigned)bl), __uint_as_float((unsigned)(bl >> 32)))); }
#define SUMUP(K) { const float w_ = __builtin_amdgcn_exp2f(L[K] - M), c_ = ldexpf(w_, (int)(ex[K] & 255u) - 141); den += w_; \
                t[0] = fmaf(c_, (float)(short)x[K].x, t[0]); t[1] = fmaf(c_, (float)((int)x[K].x >> 16), t[1]); t[2] = fmaf(c_, (float)(short)x[K].y, t[2]); \
                t[3] = fmaf(c_, (float)(short)x[K].z, t[3]); t[4] = fmaf(c_, (float)((int)x[K].z >> 16), t[4]); t[5] = fmaf(c_, (float)(short)x[K].w, t[5]); }
            const unsigned tg = tl + (quart ? TRQ : TRD);
            GATE(tg)
            v4u x[4]; float L[4], M; unsigned ex[4];
            for (;;) {
                unsigned bad = 0; M = -3e38f;
                POLL(0, ro)
                if (quart) { POLL(1, rx) POLL(2, rx + GSZ * NSPLIT * 512) POLL(3, rx + 2 * GSZ * NSPLIT * 512) }
                __builtin_amdgcn_s_setprio(3);
                TAKE(0)
                if (quart) { TAKE(1) TAKE(2) TAKE(3) }
                if (__builtin_amdgcn_ballot_w64(bad != 0) == 0) break;
                SLEEP(2)
            }
            float t[6] = {0.f, 0.f, 0.f, 0.f, 0.f, 0.f}, den = 0.f;
            SUMUP(0)
            if (quart) { SUMUP(1) SUMUP(2) SUMUP(3) }
#define SWADF(A, B) ({ const auto l_ = __builtin_amdgcn_permlane32_swap(__float_as_uint(A), __float_as_uint(B), false, false); __uint_as_float(l_[0]) + __uint_as_float(l_[1]); })
            v4f* pb = (v4f*)((char*)&sm.cs[0][0][0] + 65536);
            float* pm = (float*)(pb + NSW * 64);
            pb[c * 64 + lane] = (v4f){SWADF(t[0], t[3]), SWADF(t[1], t[4]), SWADF(t[2], t[5]), SWADF(den, den)};
            if (lane == 0) pm[c] = M;
            CBAR();
            ADDRTN(&sm.cdone)
            ADDED(c)
            CBAR();
            if (c == NSW - 1) {
                const v4f ma = *(const v4f*)pm, mb = *(const v4f*)(pm + 4);
                const float mw[NSW] = {ma.x, ma.y, ma.z, ma.w, mb.x, mb.y, mb.z, mb.w};
                float Mx = mw[0];
#pragma unroll
                for (int w = 1; w < NSW; w++) Mx = fmaxf(Mx, mw[w]);
                v4f sum = {0.f, 0.f, 0.f, 0.f};
#pragma unroll
                for (int w = 0; w < NSW; w++) sum += pb[w * 64 + lane] * __builtin_amdgcn_exp2f(mw[w] - Mx);
                const float inv = __builtin_amdgcn_rcpf(sum.w);
                o0 = sum.x * inv; o1 = sum.y * inv; o2 = sum.z * inv;
            }
        }
        if (c == NSW - 1) {
            asm("" : "+v"(o0), "+v"(o1), "+v"(o2));
            __hip_bfloat16* o = out + (size_t)head * DH + (lane & 31) * 6 + (lane >> 5) * 3;
            o[0] = __float2bfloat16(o0); o[1] = __float2bfloat16(o1); o[2] = __float2bfloat16(o2);
        }
    }
}

extern "C" void kernel_launch(void* const* d_in, const int* in_sizes, int n_in,
                              void* d_out, int out_size, void* d_ws, size_t ws_size,
                              hipStream_t stream) {
    const unsigned short* q = (const unsigned short*)d_in[0];
    const int* qk = (const int*)d_in[1];
    const unsigned short* k_scales = (const unsigned short*)d_in[2];
    const unsigned short* k_zeros = (const unsigned short*)d_in[3];
    const int* qv = (const int*)d_in[4];
    const unsigned short* v_scales = (const unsigned short*)d_in[5];
    const unsigned short* v_zeros = (const unsigned short*)d_in[6];
    const unsigned short* q_cos = (const unsigned short*)d_in[7];
    const unsigned short* q_sin = (const unsigned short*)d_in[8];
    const unsigned short* k_cos = (const unsigned short*)d_in[9];
    const unsigned short* k_sin = (const unsigned short*)d_in[10];
    unsigned char* ws = (unsigned char*)d_ws;
    attn_kernel<<<NSPLIT * GSZ, NTHREADS, 0, stream>>>(q, qk, k_scales, k_zeros, qv, v_scales, v_zeros,
                                                       q_cos, q_sin, k_cos, k_sin, (__hip_bfloat16*)d_out, ws);
}
```

```cpp
#define DIAG 41
#include <hip/hip_runtime.h>
#include <hip/hip_bf16.h>
#include <stdint.h>

#define DH 192
#define HALF 96
#define SKV 2048
#define NH 256
#define NSW 8
#define NWAVES (NSW + 4)
#define NTHREADS (64 * NWAVES)
#define BROWS 128
#define NSPLIT 16
#define GSZ 16
#define WQ 10
#define NU (NH / GSZ + 3)
#define NPC 1024
#define NRR (NH + 3 * GSZ)
#define NIN 8
#define NOUT 8
#define WALL (15 * 144 + 4 * 48)
#define SC1 16
#define NRND 48
#define RST 4352
#define TK 2
#define NMIX 3
#define TUNIT 311
#define T01 50
#define TMIX 7
#define TOFF 50
#define PSF 100
#define TGS 85
#define PSW (NPC / 2 + 28)
#define TG0 8
#define TG1 3
#define TWO -75
#define PSG (NPC / 2 + 60)
#define TH0 3
#define TH1 3
#define EARLY 10
#define LEADP 24
#define DPH 2
#define TA 210
#define TA2 70
#define CSH 2
#define NPRB 3
#define TMAP 520
#define MINV 96
#define VW 4
#define MASTER 203
#define TRES 190
#define TB1 1200
#define PER 300
#define RDD 100
#define LEAD 70
#define LHI 85
#define LOK 74
#define PSL0 124
#define PUBP -14
#define LJ 210
#define LOK2 75
#define NTRY 6
#define TFO 6
#define SL0 42.f
#define PST0 200
#define KC 4
#define TC0 1000
#define TCS 9
#define PRE0 3

#define KREL 12
#define PDEL 70
#define NCP 3
#define NRW (NRR + NCP * GSZ)
#define TRD 112
#define THD 55
#define TRQ 55
#define TCA 132
#define TCD 22
#define TSD 560
#define WS_EPOCH 0
#define WS_POST0 8192
#define WS_POST (WS_POST0 + ((unsigned)E1 & 15) * 2048)
#define WS_NEWS (49152 + 256)
#define WS_HELLO 59904
#define WS_BOARD 65536
#define WS_PART (2u << 20)
#define WS_SWAP (WS_PART + NRW * NSPLIT * 512)

typedef unsigned int v2u __attribute__((ext_vector_type(2)));
typedef unsigned int v16u __attribute__((ext_vector_type(16)));
typedef unsigned int v4u __attribute__((ext_vector_type(4)));
typedef int v4i __attribute__((ext_vector_type(4)));
typedef float v2f __attribute__((ext_vector_type(2)));
typedef float v4f __attribute__((ext_vector_type(4)));
typedef unsigned long long u64;

__device__ __forceinline__ float bflo(unsigned u) { return __uint_as_float(u << 16); }
__device__ __forceinline__ float bfhi(unsigned u) { return __uint_as_float(u & 0xffff0000u); }
__device__ __forceinline__ float bfr(float x) {
    __bf16 b = (__bf16)x;
    return __uint_as_float(((unsigned)__builtin_bit_cast(unsigned short, b)) << 16);
}

#define SLEEP(n) SLEEP_(n)
#define SLEEP_(n) asm volatile("s_sleep " #n "\n\tv_add_co_u32 %0, vcc, 1, %0\n\ts_cbranch_vccz 1f\n\ts_endpgm\n1:" : "+v"(spins) :: "vcc", "memory");
#define SPIN0 0xf0000000u
#define CBAR() asm volatile("" ::: "memory")

__device__ __forceinline__ void wave_sync() {
    __builtin_amdgcn_fence(__ATOMIC_RELEASE, "wavefront");
    __builtin_amdgcn_wave_barrier();
    __builtin_amdgcn_fence(__ATOMIC_ACQUIRE, "wavefront");
}
template <int CTRL>
__device__ __forceinline__ float dpp_add(float x) {
    int y = __builtin_amdgcn_update_dpp(0, __float_as_int(x), CTRL, 0xf, 0xf, true);
    return x + __int_as_float(y);
}
template <int CTRL>
__device__ __forceinline__ float dpp_max(float x) {
    int y = __builtin_amdgcn_update_dpp(__float_as_int(x), __float_as_int(x), CTRL, 0xf, 0xf, false);
    return fmaxf(x, __int_as_float(y));
}
__device__ __forceinline__ float row_sum(float x) { x = dpp_add<0x128>(x); x = dpp_add<0x124>(x); x = dpp_add<0x122>(x); return dpp_add<0x121>(x); }
__device__ __forceinline__ float row_max(float m) { m = dpp_max<0x128>(m); m = dpp_max<0x124>(m); m = dpp_max<0x122>(m); return dpp_max<0x121>(m); }
__device__ __forceinline__ float rdlane(float x, int l) { return __int_as_float(__builtin_amdgcn_readlane(__float_as_int(x), l)); }
__device__ __forceinline__ float wave_max(float m) { m = row_max(m); return fmaxf(fmaxf(rdlane(m, 0), rdlane(m, 16)), fmaxf(rdlane(m, 32), rdlane(m, 48))); }
__device__ __forceinline__ float wave_maxv(float m) {
    m = row_max(m);
    const auto a = __builtin_amdgcn_permlane16_swap(__float_as_uint(m), __float_as_uint(m), false, false);
    m = fmaxf(__uint_as_float(a[0]), __uint_as_float(a[1]));
    const auto b = __builtin_amdgcn_permlane32_swap(__float_as_uint(m), __float_as_uint(m), false, false);
    return fmaxf(__uint_as_float(b[0]), __uint_as_float(b[1]));
}
__device__ __forceinline__ float wave_sum(float x) { x = row_sum(x); return (rdlane(x, 0) + rdlane(x, 16)) + (rdlane(x, 32) + rdlane(x, 48)); }
__device__ __forceinline__ unsigned lds_ld(const unsigned* p) { return __builtin_amdgcn_readfirstlane(__hip_atomic_load(p, __ATOMIC_RELAXED, __HIP_MEMORY_SCOPE_WORKGROUP)); }
__device__ __forceinline__ unsigned lds_addr(const void* p) { return (unsigned)(size_t)(const __attribute__((address_space(3))) void*)p; }
#define DMA16(SRC, LDS) asm volatile("s_mov_b32 %0, m0\n\ts_mov_b32 m0, %2\n\ts_nop 0\n\tglobal_load_lds_dwordx4 %1, off\n\ts_mov_b32 m0, %0" : "=&s"(m0keep) : "v"(SRC), "s"(LDS) : "memory")
__device__ __forceinline__ void lds_st(unsigned* p, unsigned v) { __hip_atomic_store(p, v, __ATOMIC_RELAXED, __HIP_MEMORY_SCOPE_WORKGROUP); }
#define NOW() ((unsigned)__builtin_amdgcn_s_memrealtime())
__device__ __forceinline__ unsigned tdue(unsigned g) {
    return (g * TUNIT + min(g, 480u) * TK + min(g, 48u) * T01 + (min(g, 32u * NMIX) + min(max((int)g - NPC / 2, 0), 32 * NMIX)) * TMIX + (g >= PSF ? TGS * 32 : 0)) >> 5;
}

__device__ __forceinline__ unsigned unit_of(unsigned g) { const unsigned p = g & 511; return (g >> 9) * NU + (p >> 5) + min(max((int)p - 32 * WQ, 0) >> 3, 3); }
__device__ __forceinline__ unsigned first_of(unsigned e) { return 32 * e - 24 * min(max((int)e - WQ, 0), 4); }
#define QUARTER(E) ((E) - WQ < 4)
__device__ __forceinline__ unsigned went(unsigned e) { return 144 * e - 96 * min(max((int)e - WQ, 0), 4); }
__device__ __forceinline__ unsigned wunit(unsigned x) { x += 24 * NMIX; return x < 24 * WQ ? x / 24 : x < 24 * WQ + 32 ? WQ + (x - 24 * WQ) / 8 : (x + 64) / 24; }

#define ROTT 0xed8d2c3d2c35ed84ull, 0xba85aa165b03ac82ull, 0xee7efa489a38af71ull, 0xe932da1b0bab51d8ull, 0xfa96112d5fee971bull, 0x2c7e9ea5a544f071ull, 0x3d86093d5f0e810bull, 0xe932da1b0bab51d8ull, 0xfa96112d5fee971bull, 0xd834e62c5fa723e9ull, 0x1d7d1a3d2c061d88ull, 0xe932da1b0bab51d8ull, 0xfa96112d5fee971bull, 0xc834e12c0aaa4ec7ull, 0x3d86093d5f0e810bull, 0xe932da1b0bab51d8ull
static __device__ const u64 ROT[16] = {ROTT};

struct __attribute__((aligned(16))) Smem {
    unsigned cs[NSW][4][768];
    double acc[NOUT][DH];
    float ent[NOUT][BROWS];
    unsigned short wv[WALL];
    unsigned short wo[WALL];
    unsigned q[NIN][HALF];
    float2 kz[NIN][BROWS];
    float2 vz[NIN][BROWS];
    unsigned utab[64];
    unsigned line, gap;
    unsigned late, grab;
    unsigned rdy[NIN], cnt[NOUT];
    unsigned tabs, vrdy, swp;
    unsigned aline;
    unsigned tag_rdy, tag_lo, tag_hi, merged, cdone, cidx;
    unsigned slow;
    unsigned chead, dg;
};

#define LDS_ADD(P, V) { u64 ex_; asm volatile("s_mov_b64 %0, exec\n\ts_mov_b64 exec, 1\n\tds_add_u32 %1, %2\n\ts_mov_b64 exec, %0" : "=&s"(ex_) : "v"(lds_addr(P)), "v"(V) : "memory"); }
#define LDS_OR(P, V) { u64 ex_; asm volatile("s_mov_b64 %0, exec\n\ts_mov_b64 exec, 1\n\tds_or_b32 %1, %2\n\ts_mov_b64 exec, %0" : "=&s"(ex_) : "v"(lds_addr(P)), "v"(V) : "memory"); }

__global__ __launch_bounds__(NTHREADS, 3) void attn_kernel(
    const unsigned short* __restrict__ q, const int* __restrict__ qk,
    const unsigned short* __restrict__ k_scales, const unsigned short* __restrict__ k_zeros,
    const int* __restrict__ qv,
    const unsigned short* __restrict__ v_scales, const unsigned short* __restrict__ v_zeros,
    const unsigned short* __restrict__ q_cos, const unsigned short* __restrict__ q_sin,
    const unsigned short* __restrict__ k_cos, const unsigned short* __restrict__ k_sin,
    __hip_bfloat16* __restrict__ out, unsigned char* __restrict__ ws)
{
    __shared__ Smem sm;
    const int tid = threadIdx.x;
    unsigned spins = SPIN0;
    const int lane = tid & 63;
    const int hw = __builtin_amdgcn_readfirstlane(tid >> 6);
    u64 t00; unsigned ka;
    asm volatile("s_memrealtime %0\n\ts_load_dword %1, %2, 0x40" : "=&s"(t00), "=&s"(ka) : "s"(__builtin_amdgcn_kernarg_segment_ptr()));
    if (hw == 0) {
        const char* pc0; asm volatile("s_getpc_b64 %0" : "=s"(pc0));
        asm volatile("global_load_dword a6, %0, %1" :: "v"(((blockIdx.x & ~7u) + (lane & 7)) * 128), "s"(pc0) : "a6", "memory");
    }
    const int wave = hw >= 4 ? hw - 4 : hw + NSW;
    const unsigned split = (blockIdx.x & 1) + (blockIdx.x & 2) * 4 + ((blockIdx.x >> 3) & 3) * 2;
    const unsigned side = (split ^ (blockIdx.x >> 2)) & 1, rank = blockIdx.x >> 5;
    const unsigned head0 = 2 * rank + side;
    const unsigned rf0 = (8 * split + 1) / 3, r3 = 8 * split + 1 - 3 * rf0;
    const unsigned koff = ((lane >> 5) * DH + 3 * (lane & 31)) * 4, koffb = koff + 384;
#define AR(S) (16 + (S) * 8)
#define GLD(A, OFF, BASE, IMM) asm volatile("global_load_dwordx3 a[%c0:%c1], %2, %3 offset:%4 nt" :: "i"(A), "i"((A) + 2), "v"(OFF), "s"(BASE), "i"(IMM))
#define ISSUE(S, J, BASE, OA, OB) { GLD(AR(S), OA, BASE, (J) * 1536); GLD(AR(S) + 4, OB, BASE, (J) * 1536); }
#define HEAD2 ((GSZ + 1 - 2 * side) * (SKV * DH * 4))
    const unsigned wo = (hw - 2) & 7;
    const bool pre = hw >= 4 && wo < PRE0;
    if (pre) {
        const char* a = (const char*)qk + (head0 * 512 + side * 256 + rf0 + wave + (wave >= 6 ? 80 : 0)) * 3072; const char* b = a + HEAD2;
        ISSUE(0, 0, a, koff, koffb) ISSUE(1, 1, a, koff, koffb) ISSUE(2, 0, b, koff, koffb) ISSUE(3, 1, b, koff, koffb)
    }
    asm volatile("s_waitcnt lgkmcnt(0)" : "+s"(t00), "+s"(ka));
    if (hw == 0) {
        if (DIAG == 30 || DIAG == 39 || DIAG == 38) sm.dg = NOW() - (unsigned)t00;
        sm.utab[lane] = ~0u;
        unsigned l0 = (unsigned)t00 + TOFF;
        if (KREL) { const unsigned t1 = NOW() + KREL; if ((int)(t1 - l0) > 0) l0 = t1; }
        if (lane < 32) (&sm.line)[lane] = lane == 3 ? 64u : (lane & 29) == 0 ? l0 : 0u;
        asm volatile("s_waitcnt lgkmcnt(0)" ::: "memory");
    }
    __builtin_amdgcn_s_barrier();
    CBAR();
    const u64 rtab = r3 == 1 ? 0x5756552c2b020100ull : r3 == 0 ? 0x5756552c2b2a0100ull : 0x57562d2c2b020100ull;
#define R8(I) (rf0 + ((unsigned)(rtab >> (8 * (I))) & 255u))
#define GATE(DUE) { int d_ = (int)((DUE) - NOW()); if (d_ < 3000) while (d_ > 0) { __builtin_amdgcn_s_sleep(1); d_ = (int)((DUE) - NOW()); } }
#define TLAST() (lds_ld(&sm.line) + tdue(NPC - 2) + (lds_ld(&sm.gap) >> 16))
    if (__builtin_expect(wave < NSW, 1)) {
        const int cst = 2 * side;
        const unsigned r8w = R8((unsigned)wave);
        const char* cbase[3];
#pragma unroll
        for (int j = 0; j < 3; j++) { const int x = j * 64 + lane; cbase[j] = (x < 96 ? (const char*)k_cos + x * 16 : (const char*)k_sin + (x - 96) * 16) + r8w * (4 * DH * 2); }
        unsigned m0keep;
#define CS_DMA(W, C) { _Pragma("unroll") for (int j = 0; j < 3; j++) DMA16(cbase[j] + (C) * (512 * DH * 2), lds_addr(&sm.cs[0][0][0]) + ((W) * 4 + (C)) * 3072 + j * 1024); }
#define REPACK(W, C) { \
        unsigned* cw = &sm.cs[W][C][0]; \
        const unsigned* l = cw + rsel * (DH / 2) + qoff; \
        unsigned r[2][6]; \
        _Pragma("unroll") \
        for (int j = 0; j < 2; j++) { \
            unsigned e[4][2]; \
            _Pragma("unroll") \
            for (int k = 0; k < 4; k++) { \
                const unsigned* a = l + j * DH + (k & 1) * 48 + (k >> 1) * 384;       \
                unsigned x0 = a[0], x1 = a[1]; \
                e[k][0] = __builtin_amdgcn_alignbit(x1, x0, sh); \
                e[k][1] = x1 >> sh; \
            } \
            r[j][0] = (e[0][0] & 0xffffu) | (e[3][0] << 16); \
            r[j][1] = (e[0][0] >> 16) | (e[3][0] & 0xffff0000u); \
            r[j][2] = (e[0][1] & 0xffffu) | (e[3][1] << 16); \
            r[j][3] = (e[1][0] & 0xffffu) | (e[2][0] << 16); \
            r[j][4] = (e[1][0] >> 16) | (e[2][0] & 0xffff0000u); \
            r[j][5] = (e[1][1] & 0xffffu) | (e[2][1] << 16); \
        } \
        wave_sync(); \
        _Pragma("unroll") \
        for (int j = 0; j < 2; j++) { *(v4u*)&cw[j * 256 + lane * 4] = (v4u){r[j][0], r[j][1], r[j][2], r[j][3]}; *(v2u*)&cw[512 + j * 128 + lane * 2] = (v2u){r[j][4], r[j][5]}; } \
        wave_sync(); }
        const int sub = lane & 31;
        const int rsel = lane >> 5;
        const unsigned sh = (sub & 1) * 16;
        const int qoff = 3 * (sub >> 1) + (sub & 1);
        const unsigned voff = 3 * lane * 4;
        asm volatile("" ::: "a16", "a17", "a18", "a19", "a20", "a21", "a22", "a23", "a24", "a25", "a26", "a27", "a28", "a29", "a30", "a31",
                            "a32", "a33", "a34", "a35", "a36", "a37", "a38", "a39", "a40", "a41", "a42", "a43", "a44", "a45", "a46", "a47");
        asm volatile("" ::: "a48", "a49", "a50", "a51", "a52", "a53", "a54", "a55", "a56", "a57", "a58", "a59", "a60", "a61", "a62", "a63",
                            "a64", "a65", "a66", "a67", "a68", "a69", "a70", "a71", "a72", "a73", "a74", "a75", "a76", "a77", "a78", "a79");
        int ta[4][3], tb[4][3];
#define WAITT(P, S, N) asm volatile("s_waitcnt vmcnt(%6)\n\tv_accvgpr_read_b32 %0, a[%c7]\n\tv_accvgpr_read_b32 %1, a[%c8]\n\tv_accvgpr_read_b32 %2, a[%c9]\n\t" \
                              "v_accvgpr_read_b32 %3, a[%c10]\n\tv_accvgpr_read_b32 %4, a[%c11]\n\tv_accvgpr_read_b32 %5, a[%c12]" \
                              : "=v"(ta[P][0]), "=v"(ta[P][1]), "=v"(ta[P][2]), "=v"(tb[P][0]), "=v"(tb[P][1]), "=v"(tb[P][2]) \
                              : "i"(N), "i"(AR(S)), "i"(AR(S) + 1), "i"(AR(S) + 2), "i"(AR(S) + 4), "i"(AR(S) + 5), "i"(AR(S) + 6))
        const unsigned voffb = voff + 768;
        u64 ln = 0;
#define TDUE(G) (tdue(G) + ((G) >= PSW ? __builtin_amdgcn_readfirstlane((unsigned)(ln >> 32)) >> ((G) >= PSG ? 16 : 0) & 0xffffu : 0u))
#define REPORT() { const unsigned t_ = now - tdc; if (lane == 0) lds_st(&sm.late, t_); }
#define LINE() ln = LDV((u64*)&sm.line);
#define LDV(P) __hip_atomic_load(P, __ATOMIC_RELAXED, __HIP_MEMORY_SCOPE_WORKGROUP)
        unsigned u = 0, rg = cst * 8 + wave, np = 0;
        CS_DMA(wave, cst)

        const char* kb0 = (const char*)qk + (size_t)(head0 * 512 + r8w) * 3072;
#define KB(I) (kb0 + ((cst + (I)) & 3) * (128 * 3072))
        const char* kb; const char* kb2;
#define SISSUE(I) { kb = KB(I); kb2 = kb + HEAD2; const unsigned du_ = lds_ld(&sm.line) + tdue(16 * (I) + 2 * wo); GATE(du_) \
            ISSUE(((I) & 1) * 4, 0, kb, koff, koffb) ISSUE(((I) & 1) * 4 + 1, 1, kb, koff, koffb) ISSUE(((I) & 1) * 4 + 2, 0, kb2, koff, koffb) ISSUE(((I) & 1) * 4 + 3, 1, kb2, koff, koffb) }
        if (!pre) SISSUE(0)
        CS_DMA(wave, (cst + 1) & 3)
        if (pre) asm volatile("s_waitcnt vmcnt(3)" ::: "memory");
        if (DIAG == 32 && wave == 0 && lane == 0) sm.dg = NOW() - (unsigned)t00;
        if (DIAG == 33 && wave == 1 && lane == 0) sm.dg = NOW() - (unsigned)t00;
        asm volatile("s_waitcnt vmcnt(11)" ::: "memory");
        if (!pre) LDS_ADD(&sm.slow, NOW() - lds_ld(&sm.line) - tdue(2 * wo))
        REPACK(wave, cst)
        SISSUE(1)
#define TABDONE(I) LDS_OR(&sm.tabs, 1u << ((((cst + (I)) & 3) * 8) + wave))
        TABDONE(0)
        const float scale = 0.07216878364870322f;
        double acc[3] = {0.0, 0.0, 0.0};
        bool first = true, chg, none = false;
        unsigned Qp[3], Qn[3], Rp[3], Rn[3];
        float kz4[4], ksc, kres[4];
        v4u ca[4]; v2u cb[4];
        v2f qe[4][3], A[4];
        unsigned vut, nrg;
        const unsigned vr = ((lane >> 3) & 3) * 128 + R8((unsigned)lane & 7);
        unsigned g, mg;
        const unsigned grab_adr = lds_addr(&sm.grab);
        asm volatile("v_accvgpr_write_b32 a13, 1\n\tv_accvgpr_write_b32 a14, 2" ::: "a13", "a14", "a15");
#define GRAB(N) { u64 ex_; asm volatile("s_mov_b64 %0, exec\n\ts_mov_b64 exec, 1\n\tds_add_rtn_u32 a15, %1, a%c2\n\ts_mov_b64 exec, %0" : "=&s"(ex_) : "v"(grab_adr), "i"(12 + (N)) : "memory"); }
#define GRABBED() { unsigned t_; asm volatile("s_waitcnt lgkmcnt(0)\n\tv_accvgpr_read_b32 %1, a15\n\ts_nop 0\n\tv_readfirstlane_b32 %0, %1\n\ts_nop 1" : "=s"(g), "=v"(t_)); }
        unsigned oa, ob, un, tdn, tiss, tdc = 0;
#define PLAN() { \
            GRABBED() un = unit_of(g); chg = un != u; none = g >= NPC; \
            const bool nv = g >= NPC / 2; \
            unsigned hd = __builtin_amdgcn_readlane(vut, un); \
            while (hd == ~0u) { SLEEP(1) hd = lds_ld(&sm.utab[un]); } \
            nrg = (g + (hd >> 8)) & 31; hd &= 255; \
            unsigned boff, boff2; asm("s_mul_i32 %0, %1, 0xc00" : "=s"(boff) : "s"(hd * 512 + __builtin_amdgcn_readlane(vr, nrg))); \
            asm("s_mul_i32 %0, %1, 0xc00" : "=s"(boff2) : "s"(hd * 512 + __builtin_amdgcn_readlane(vr, nrg + 1))); \
            const char* b_ = nv ? (const char*)qv : (const char*)qk; \
            kb = b_ + boff; kb2 = b_ + boff2; \
            oa = nv ? voff : koff; ob = nv ? voffb : koffb; \
            tdn = TDUE(g); }
#define FIRE() { ISSUE(0, 0, kb, oa, ob) ISSUE(1, 1, kb, oa, ob) ISSUE(2, 0, kb2, oa, ob) ISSUE(3, 1, kb2, oa, ob) }
#define LAUNCH() { \
            LINE() \
            const unsigned du_ = __builtin_amdgcn_readfirstlane((unsigned)ln) + tdn; \
            GATE(du_) \
            ISSUE(0, 0, kb, oa, ob) ISSUE(1, 1, kb, oa, ob) \
            GATE(du_ + 10) \
            ISSUE(2, 0, kb2, oa, ob) ISSUE(3, 1, kb2, oa, ob) tiss = NOW(); }
#define ARRIVED() \
            const unsigned now = NOW(); \
            const bool stall = now - tiss > LJ && g - PST0 < NPC - 48 - PST0; \
            const bool late = !none && !stall && (int)(now + EARLY - __builtin_amdgcn_readfirstlane((unsigned)ln) - tdn) >= 0; \
            if (late) { FIRE() tiss = now; } \
            if (stall && lane == 0) __hip_atomic_fetch_max(&sm.line, (unsigned)ln + (now - tiss) - LOK2, __ATOMIC_RELAXED, __HIP_MEMORY_SCOPE_WORKGROUP); \
            REPORT()
#define ADVANCE(ISV) { \
            if (chg) FLUSH(ISV) \
            if (!none && !late) LAUNCH() \
            first = chg; u = un; rg = nrg; tdc = tdn + 10; }
#define K_TAB(MODE) { _Pragma("unroll") for (int p = 0; p < ((MODE) ? 2 : 4); p++) { \
            const unsigned* cw = &sm.cs[(rg & 7) + (p >> 1)][rg >> 3][0]; \
            ca[p] = *(const v4u*)&cw[(p & 1) * 256 + lane * 4]; cb[p] = *(const v2u*)&cw[512 + (p & 1) * 128 + lane * 2]; } }
#define K_PRE(MODE, P) { \
        float e0, e1, e2, f0, f1, f2; \
        const unsigned* qp_ = ((MODE) && (P) >= 2) ? Rp : Qp; const unsigned* qn_ = ((MODE) && (P) >= 2) ? Rn : Qn; \
        const int tp_ = (MODE) ? (P) & 1 : (P); \
        asm("v_dot2_f32_bf16 %0, %6, %12, 0\n\tv_dot2_f32_bf16 %3, %9, %15, 0\n\tv_dot2_f32_bf16 %1, %7, %13, 0\n\tv_dot2_f32_bf16 %4, %10, %16, 0\n\t" \
            "v_dot2_f32_bf16 %2, %8, %14, 0\n\tv_dot2_f32_bf16 %5, %11, %17, 0\n\ts_nop 2" \
            : "=&v"(e0), "=&v"(e1), "=&v"(e2), "=&v"(f0), "=&v"(f1), "=&v"(f2) \
            : "v"(qp_[0]), "v"(qp_[1]), "v"(qp_[2]), "v"(qn_[0]), "v"(qn_[1]), "v"(qn_[2]), \
              "v"(ca[tp_].x), "v"(ca[tp_].y), "v"(ca[tp_].z), "v"(ca[tp_].w), "v"(cb[tp_].x), "v"(cb[tp_].y)); \
        qe[P][0] = (v2f){e0, f0}; qe[P][1] = (v2f){e1, f1}; qe[P][2] = (v2f){e2, f2}; \
        A[P] = (qe[P][0] + qe[P][1] + qe[P][2]) * (v2f){-kz4[P], -kz4[P]}; }
#define K_WAIT(MODE, P) { \
        if ((MODE) == 0) { WAITT(P, P, 6 - 2 * (P)); } \
        else { WAITT(P, (((MODE) - 1) & 1) * 4 + (P), ((MODE) < 3 ? 20 : (MODE) == 3 ? 17 : 6) - 2 * (P)); } }
#define K_FMA(P) { \
        _Pragma("unroll") \
        for (int i = 0; i < 3; i++) { v2f x = {(float)ta[P][i], (float)tb[P][i]}; A[P] = qe[P][i] * x + A[P]; } \
        kres[P] = A[P].x + A[P].y; }
#define K_RED(MODE) { \
        const bool o1 = lane & 1, o2 = lane & 2; \
        float s01 = (o1 ? kres[1] : kres[0]) + __int_as_float(__builtin_amdgcn_update_dpp(0, __float_as_int(o1 ? kres[0] : kres[1]), 0xb1, 0xf, 0xf, true)); \
        float s23 = (o1 ? kres[3] : kres[2]) + __int_as_float(__builtin_amdgcn_update_dpp(0, __float_as_int(o1 ? kres[2] : kres[3]), 0xb1, 0xf, 0xf, true)); \
        float sx = (o2 ? s23 : s01) + __int_as_float(__builtin_amdgcn_update_dpp(0, __float_as_int(o2 ? s01 : s23), 0x4e, 0xf, 0xf, true)); \
        sx = dpp_add<0x124>(sx); sx = dpp_add<0x128>(sx); \
        const auto sw = __builtin_amdgcn_permlane16_swap(__float_as_uint(sx), __float_as_uint(sx), false, false); \
        sx = __uint_as_float(sw[0]) + __uint_as_float(sw[1]); \
        if ((lane & 28) == 0) { if (MODE) sm.ent[(lane >> 1) & 1][rg * 4 + 2 * (lane & 1) + rsel] = sx * ksc; else sm.ent[u % NOUT][rg * 4 + 2 * (lane & 3) + rsel] = sx * ksc; } }
#define K_PRE4(MODE) { K_PRE(MODE, 0) K_PRE(MODE, 1) K_PRE(MODE, 2) K_PRE(MODE, 3) asm volatile("" :: "v"(A[0]), "v"(A[1]), "v"(A[2]), "v"(A[3])); }
#define K_BODY(MODE) { K_PRE4(MODE) K_WAIT(MODE, 0) K_FMA(0) K_WAIT(MODE, 1) K_FMA(1) K_WAIT(MODE, 2) K_FMA(2) K_WAIT(MODE, 3) K_FMA(3) K_RED(MODE) }
#define V_FMA(J, W0, W1) { \
        _Pragma("unroll") \
        for (int i = 0; i < 3; i++) { \
            acc[i] = fma(W0, (double)ta[J][i], acc[i]); \
            acc[i] = fma(W1, (double)tb[J][i], acc[i]); \
        } }
#define V_PIN() asm volatile("" :: "v"(acc[0]), "v"(acc[1]), "v"(acc[2]));
#define RDYWAIT(SL, SEQ) { while (lds_ld(&sm.rdy[SL]) != (SEQ)) SLEEP(1); CBAR(); }
#define LOADZ() { _Pragma("unroll") for (int p = 0; p < 4; p++) kz4[p] = sm.kz[u % NIN][rg * 4 + 2 * p + rsel].x; ksc = sm.kz[u % NIN][rg * 4 + 2 * (lane & 3) + rsel].y * scale; }
#define LOADQ() { _Pragma("unroll") for (int i = 0; i < 3; i++) { Qp[i] = sm.q[u % NIN][3 * sub + i]; Qn[i] = __builtin_amdgcn_alignbit(Qp[i], Qp[i], 16) ^ 0x80000000u; } }
#define FLUSH(ISV) { \
            mg = __builtin_amdgcn_readfirstlane(mg); \
            while (u >= mg + NOUT) { SLEEP(1); mg = lds_ld(&sm.merged); } \
            CBAR(); \
            if (ISV) { const unsigned aa = lds_addr(&sm.acc[u % NOUT][lane]); \
                asm volatile("ds_add_f64 %0, %1\n\tds_add_f64 %0, %2 offset:512\n\tds_add_f64 %0, %3 offset:1024" :: "v"(aa), "v"(acc[0]), "v"(acc[1]), "v"(acc[2]) : "memory"); \
                acc[0] = 0.0; acc[1] = 0.0; acc[2] = 0.0; } \
            CBAR(); \
            LDS_ADD(&sm.cnt[u % NOUT], np) \
            CBAR(); \
            np = 0; }

#define LOADZ2() { _Pragma("unroll") for (int p = 0; p < 4; p++) kz4[p] = sm.kz[p >> 1][rg * 4 + 2 * (p & 1) + rsel].x; ksc = sm.kz[(lane >> 1) & 1][rg * 4 + 2 * (lane & 1) + rsel].y * scale; }
#define STATIC_STEP(I) { \
            rg = ((cst + (I)) & 3) * 8 + wave; \
            LOADZ2() \
            K_TAB((I) + 1) K_BODY((I) + 1) }
        CS_DMA(wave, (cst + 2) & 3)
        RDYWAIT(0, 1u) RDYWAIT(1, 2u)
        LOADQ()
#pragma unroll
        for (int i = 0; i < 3; i++) { Rp[i] = sm.q[1][3 * sub + i]; Rn[i] = __builtin_amdgcn_alignbit(Rp[i], Rp[i], 16) ^ 0x80000000u; }
        STATIC_STEP(0)
        asm volatile("s_waitcnt vmcnt(11)" ::: "memory");
        REPACK(wave, (cst + 1) & 3)
        TABDONE(1)
        SISSUE(2)
        CS_DMA(wave, (cst + 3) & 3)
        STATIC_STEP(1)
        asm volatile("s_waitcnt vmcnt(11)" ::: "memory");
        REPACK(wave, (cst + 2) & 3)
        TABDONE(2)
        SISSUE(3)
        STATIC_STEP(2)
        asm volatile("s_waitcnt vmcnt(8)" ::: "memory");
        REPACK(wave, (cst + 3) & 3)
        TABDONE(3)
        GRAB(2)
        vut = LDV(&sm.utab[lane]);
        STATIC_STEP(3)
        CBAR();
        LDS_ADD(&sm.cnt[0], 4u) LDS_ADD(&sm.cnt[1], 4u)
        PLAN()
        LAUNCH()
        u = un; rg = nrg; tdc = tdn + 10;
        bool tabok = false;
        __builtin_amdgcn_s_setprio(2);
#pragma unroll 1
        while (u < NU) {
            GRAB(2)
            mg = LDV(&sm.merged);
            vut = LDV(&sm.utab[lane]);
            LINE()
            if (!tabok) {
                for (;;) { const unsigned tb_ = lds_ld(&sm.tabs); tabok = tb_ == ~0u; if (((tb_ >> rg) & 3u) == 3u) break; SLEEP(1) }
                CBAR();
            }
            K_TAB(0)
            if (first) {
                unsigned fl = LDV(&sm.rdy[u % NIN]);
                CBAR();
                LOADQ() LOADZ()
                if (__builtin_amdgcn_readfirstlane(fl) != u + 1) { RDYWAIT(u % NIN, u + 1) LOADQ() LOADZ() }
            } else LOADZ()
            K_PRE4(0)
            PLAN()
            K_WAIT(0, 0) K_FMA(0) K_WAIT(0, 1) K_FMA(1)
            asm volatile("" :: "v"(kres[0]), "v"(kres[1]));
            K_WAIT(0, 2) K_WAIT(0, 3)
            ARRIVED()
            K_FMA(2) K_FMA(3) K_RED(0)
            np += 2;
            ADVANCE(false)
        }
#pragma unroll 1
        while (u < 2 * NU) {
            GRAB(2)
            mg = LDV(&sm.merged);
            LINE()
            const unsigned vr_ = LDV(&sm.vrdy);
            CBAR();
            const unsigned short* wp = &sm.wv[went(u - NU) + (QUARTER(u - NU) ? rg & 7 : rg) * 4];
            v4i pw = *(const v4i*)wp;
            if (first && !((__builtin_amdgcn_readfirstlane(vr_) >> (u - NU)) & 1)) { while (!((lds_ld(&sm.vrdy) >> (u - NU)) & 1)) SLEEP(1); CBAR(); pw = *(const v4i*)wp; }
            const double w0 = (short)pw.x, w1 = pw.x >> 16, w2 = (short)pw.y, w3 = pw.y >> 16, w4 = (short)pw.z, w5 = pw.z >> 16, w6 = (short)pw.w, w7 = pw.w >> 16;
            asm volatile("" :: "v"(w0), "v"(w1), "v"(w2), "v"(w3), "v"(w4), "v"(w5), "v"(w6), "v"(w7));
            PLAN()
            v2f hl; unsigned tl8, rowl;
            if (none) { hl = *(const v2f*)&sm.wv[went(NU - 1) + 128]; tl8 = LDV(&sm.tag_lo) << 24; rowl = (lds_ld(&sm.utab[2 * NU - 1]) & 255) * (NSPLIT * 512); asm volatile("" : "+v"(hl), "+v"(tl8)); }
            WAITT(0, 0, 6); V_FMA(0, w0, w1) V_PIN()
            WAITT(1, 1, 4); V_FMA(1, w2, w3) V_PIN()
            WAITT(2, 2, 2); V_FMA(2, w4, w5) V_PIN()
            WAITT(3, 3, 0);
            np += 2;
            if (none) {
                V_FMA(3, w6, w7)
                if (u != 2 * NU - 1) { FLUSH(true) break; }
                mg = __builtin_amdgcn_readfirstlane(mg);
                while (u >= mg + NOUT) { SLEEP(1); mg = lds_ld(&sm.merged); }
                const unsigned aa = lds_addr(&sm.acc[(2 * NU - 1) % NOUT][lane]);
                double a0, a1, a2; unsigned cn; u64 ex_;
                asm volatile("ds_add_f64 %5, %6\n\tds_add_f64 %5, %7 offset:512\n\tds_add_f64 %5, %8 offset:1024\n\t"
                             "s_mov_b64 %4, exec\n\ts_mov_b64 exec, 1\n\tds_add_rtn_u32 %3, %9, %10\n\ts_mov_b64 exec, %4\n\t"
                             "ds_read_b64 %0, %5\n\tds_read_b64 %1, %5 offset:512\n\tds_read_b64 %2, %5 offset:1024\n\ts_waitcnt lgkmcnt(0)"
                             : "=&v"(a0), "=&v"(a1), "=&v"(a2), "=&v"(cn), "=&s"(ex_) : "v"(aa), "v"(acc[0]), "v"(acc[1]), "v"(acc[2]), "v"(lds_addr(&sm.cnt[(2 * NU - 1) % NOUT])), "v"(np) : "memory");
                const float n0 = (float)(a0 - (double)hl.y) * hl.x, n1 = (float)(a1 - (double)hl.y) * hl.x, n2 = (float)(a2 - (double)hl.y) * hl.x;
                const unsigned e2 = max(__float_as_uint(fmaxf(fmaxf(fabsf(n0), fabsf(n1)), fabsf(n2))) >> 23, 14u);
                const float f2 = __uint_as_float((268u - e2) << 23);
                const v2u r = {__builtin_bit_cast(unsigned, __builtin_amdgcn_cvt_pk_i16((int)rintf(n0 * f2), (int)rintf(n1 * f2))),
                               __builtin_bit_cast(unsigned, __builtin_amdgcn_cvt_pk_i16((int)rintf(n2 * f2), 0)) | e2 << 16 | tl8};
                const __amdgpu_buffer_rsrc_t rsrc = __builtin_amdgcn_make_buffer_rsrc(ws + WS_PART + split * 512, 0, NRW * NSPLIT * 512 - split * 512, 0x00020000);
                asm volatile("" :: "v"(r));
                if (__builtin_amdgcn_readfirstlane(cn + np) == 32) {
                    __builtin_amdgcn_raw_buffer_store_b64(r, rsrc, lane * 8, rowl, SC1);
#pragma unroll
                    for (int j = 0; j < NCP - 1; j++) __builtin_amdgcn_raw_buffer_store_b64(r, rsrc, lane * 8, (rowl & 15 * NSPLIT * 512 ^ NSPLIT * 512) + (NRR + j * GSZ) * NSPLIT * 512, SC1);
                }
                break;
            }
            ARRIVED()
            V_FMA(3, w6, w7)
            ADVANCE(true)
        }
    } else {
    u64 E1;
    if (wave == NSW) {
        unsigned ref;
        const unsigned sidx = (blockIdx.x & 7) | ((blockIdx.x >> 7) << 3);
#define SAMPLE(K) ((((blockIdx.x >> 3) - (blockIdx.x & 7) - (K) * 5) & 15) == 0)
#define BTAG(K) ((btag + (K)) & 0xfffffu)
#define POST(K, C) { if (lane == 0) __hip_atomic_store((u64*)(ws + (bd + (K) * RST + (sidx >> 3) * 256 + (sidx & 7) * 8)), (u64)(C) | ((u64)(((C) - ref) | BTAG(K) << 12) << 32), __ATOMIC_RELAXED, __HIP_MEMORY_SCOPE_AGENT); }
        const unsigned own = lds_ld(&sm.line);
        if (SAMPLE(0) && lane < NTRY) __hip_atomic_store((unsigned*)(ws + WS_HELLO + lane * 768) + sidx, own, __ATOMIC_RELAXED, __HIP_MEMORY_SCOPE_AGENT);
        const u64 E = __hip_atomic_load((const u64*)(ws + WS_EPOCH), __ATOMIC_RELAXED, __HIP_MEMORY_SCOPE_AGENT);
        E1 = E + 1;
        if (lane == 0) { sm.tag_lo = (unsigned)E1; sm.tag_hi = (unsigned)(E1 >> 32); CBAR(); lds_st(&sm.tag_rdy, 1u); }
        const unsigned btag = __builtin_amdgcn_readfirstlane((unsigned)E1) * 61u, bd = WS_BOARD + (btag & 7) * (NRND * RST);
        ref = own; unsigned hmin = own, tnews = 0, ntry = 9; unsigned hmean = own; int nl = 20, iod = 0, hmx = 500;
        int off = 0;
        {
            unsigned tries = 0, cur = own, tl = own;
            bool news = false;
#pragma unroll 1
            while (!news || cur != ref) {
                const unsigned now = NOW();
                if (tries < NTRY && (int)(now - own) >= (int)(TA + tries * TA2)) {
                    v16u x;
                    asm volatile("s_load_dwordx16 %0, %1, %2\n\ts_waitcnt lgkmcnt(0)" : "=&s"(x) : "s"(ws), "s"(__builtin_amdgcn_readfirstlane(WS_HELLO + tries * 768)) : "memory");
                    unsigned bad = 0; int sum = 0, dif = 0;
#pragma unroll
                    for (int i = 0; i < 16; i++) { bad |= x[i] - own + 512; sum += x[i] - x[0]; dif += i & 4 ? x[i] : -x[i]; }
                    tries++;
                    if (bad < 1024) { ntry = tries; hmean = x[0] + (sum >> 4); for (int i = 0; i < 16; i++) if ((int)(x[i] - hmin) < 0) hmin = x[i]; nl = 0; hmx = 0; for (int i = 0; i < 16; i++) { nl += x[i] - hmin > 80; iod += (i & 4) ? (int)(x[i] - hmin) : -(int)(x[i] - hmin); hmx = max(hmx, (int)(x[i] - hmin)); } const unsigned com = x[0] + (sum >> 4) + abs(dif) * (8 - KC) / 128; ref = x[0] + ((sum + (blockIdx.x & 4 ? dif : -dif)) >> 4); off = ref - com;
                        tries = NTRY; tl = NOW(); if (lane == 0) lds_st(&sm.aline, com); }
                    continue;
                }
                if (!news && lds_ld(&sm.utab[31]) != ~0u) { news = true; tnews = now; CBAR(); if (cur == ref) tl = now; ref = lds_ld(&sm.aline) + off; tries = NTRY; }
                const int d = (now - tl) >> CSH;
                if (cur != ref && d) { tl += d << CSH; cur += min(max((int)(ref - cur), -d), d); if (lane == 0) lds_st(&sm.line, cur); }
                SLEEP(2)
            }
        }
        ref -= off;
        const unsigned ref0 = ref;
        unsigned dg[2] = {0, 0}; int mxj = 0, nsh = 0; unsigned lsum = 0, lcnt = 0;
        const unsigned lead = (blockIdx.x >> 3) == 9 ? 0 : LEAD;
        unsigned posted = ref, lagf = ref, tpost = ref + TB1, tpoll = tpost + RDD + lead, kw = 0, kr = 0, cur = ref + off, khold = 0;
        int o = off; bool held = false;
#pragma unroll 1
        for (;;) {
            const unsigned p = lds_ld(&sm.grab);
            if (p >= NPC) break;
            if (p < 260) dg[0] = ref; if (p < NPC / 2) dg[1] = ref;
            lagf += (int)(lds_ld(&sm.late) - lagf) >> 1;
            { const unsigned l = lds_ld(&sm.line); if (l != cur) { cur = l; khold = kw; held = true; } }
            const unsigned now = NOW();
            if (o) { o = off * min(max((int)(ref0 + TC0 + (1 << TCS) - now), 0), 1 << TCS) >> TCS; if (!held && cur != ref + o) { cur = ref + o; if (lane == 0) lds_st(&sm.line, cur); } }
            if (kw < NRND && (int)(now - tpost) >= 0) {
                const unsigned c = ref + min((p >= PSL0 && (int)(lagf - cur) > LHI ? lagf - LOK : cur) - o - ref, 4000u);
                if (SAMPLE(kw) && (c != ref || (ref != posted && (sidx & 7) == (kw & 7)))) POST(kw, c)
                posted = ref;
                tpost += PER; kw++;
            }
            if (kr < NRND && (int)(now - tpoll) >= 0) {
                v16u xa, xb;
                asm volatile("s_load_dwordx16 %0, %2, %3\n\ts_load_dwordx16 %1, %2, %4\n\ts_waitcnt lgkmcnt(0)" : "=&s"(xa), "=&s"(xb)
                             : "s"(ws), "s"(__builtin_amdgcn_readfirstlane(bd + kr * RST)), "s"(__builtin_amdgcn_readfirstlane(bd + kr * RST + 256)) : "memory");
                int nb = 0, sg = 0;
#pragma unroll
                for (int i = 0; i < 16; i++) {
                    const unsigned lo = i < 8 ? xa[2 * i & 15] : xb[2 * i & 15], hi = i < 8 ? xa[(2 * i + 1) & 15] : xb[(2 * i + 1) & 15];
                    if (hi >> 12 == BTAG(kr) && lo - ref0 < 8000) { nb = max(nb, (int)(lo - (hi & 4095u) - ref)); sg += hi & 4095u; }
                }
                ref += nb + (sg >> 4); mxj = max(mxj, nb + (sg >> 4)); nsh += (nb + (sg >> 4)) > 0;
                if (cur != ref + o && (kr >= khold || (int)(ref + o - cur) > 0)) { cur = ref + o; held = false; if (lane == 0) lds_st(&sm.line, cur); }
                tpoll += PER; kr++;
            }
            SLEEP(2)
        }
        if (blockIdx.x == 0) {
            unsigned X = 0;
            if (DIAG == 2) X = (ref - ref0) * 8;
            if (DIAG == 6) X = (dg[1] - ref0) * 8;
            if (DIAG == 8) X = (dg[0] - ref0) * 8;
            if (DIAG == 17) X = hmx * 8;
            if (DIAG == 22) X = (ref0 - hmean) * 8;
            if (DIAG == 24) X = (hmean - hmin) * 8;
            if (DIAG == 25) X = (ref - hmin) * 4;
            if (DIAG == 27) X = __builtin_popcount(lds_ld(&sm.swp)) * 100;
            if (DIAG == 20) X = lds_ld(&sm.slow) * 2;
            if (DIAG >= 30 && DIAG < 40) X = lds_ld(&sm.dg) * 8;
            if (DIAG == 11) X = mxj * 8;
            X = min(X, 6000u);
            if (DIAG == 39) { const unsigned f3 = min((ref - hmin) / 40u, 11u), f2 = mxj < 40 ? 0 : mxj < 120 ? 1 : mxj < 220 ? 2 : 3, ka_ = lds_ld(&sm.dg), f1 = ka_ < 50 ? 0 : ka_ < 85 ? 1 : 2, f0 = hmx < 50 ? 0 : hmx < 80 ? 1 : hmx < 110 ? 2 : 3;
                X = (((f3 * 4 + f2) * 3 + f1) * 4 + f0) * 400; }
            if (DIAG == 41) { hmin = (unsigned)t00; X = min((dg[0] - ref0) / 4u, 99u) * 400; }
            if (DIAG == 38) { hmin = (unsigned)t00; X = (min((ref - hmin) / 10u, 49u) * 2 + (lds_ld(&sm.dg) >= 60)) * 400; }
            while (NOW() - hmin < 15000 + X) SLEEP(1)
        }
    } else if (wave == NSW + 1 || wave == NSW + 2) {
        const int ql = lane < 48 ? lane : 47;
        unsigned c1, c2, s1, s2;
#define QT_LOADS() { int qo_ = ql; asm volatile("" : "+v"(qo_)); \
                     c1 = ((const unsigned*)q_cos)[qo_]; c2 = ((const unsigned*)q_cos)[48 + qo_]; s1 = ((const unsigned*)q_sin)[qo_]; s2 = ((const unsigned*)q_sin)[48 + qo_]; }
        unsigned q1, q2, lkz, lks, lvz, lvs;
        const unsigned sofs = R8(((unsigned)lane >> 1) & 7) * 2 + (lane & 1);
#define UNIT_LOADS(HEAD, GRP) { \
        q1 = ((const unsigned*)(q + (size_t)(HEAD) * DH))[ql]; q2 = ((const unsigned*)(q + (size_t)(HEAD) * DH))[48 + ql]; \
        const size_t so = (size_t)(HEAD) * (SKV / 2) + (GRP) * 256 + sofs; \
        lkz = *((const unsigned*)k_zeros + so); lks = *((const unsigned*)k_scales + so); \
        lvz = *((const unsigned*)v_zeros + so); lvs = *((const unsigned*)v_scales + so); }
#define UNIT_FILL(SL, U) { \
            v4f qq; \
            qq.x = bfr(bfr(bflo(q1) * bflo(c1)) + bfr(-bflo(q2) * bflo(s1))); \
            qq.y = bfr(bfr(bflo(q2) * bflo(c2)) + bfr(bflo(q1) * bflo(s2))); \
            qq.z = bfr(bfr(bfhi(q1) * bfhi(c1)) + bfr(-bfhi(q2) * bfhi(s1))); \
            qq.w = bfr(bfr(bfhi(q2) * bfhi(c2)) + bfr(bfhi(q1) * bfhi(s2))); \
            *(v2u*)&sm.q[SL][2 * ql] = (v2u){(__float_as_uint(qq.x) >> 16) | __float_as_uint(qq.y), (__float_as_uint(qq.z) >> 16) | __float_as_uint(qq.w)}; \
            *(v4f*)&sm.kz[SL][2 * lane] = (v4f){bflo(lkz), bflo(lks), bfhi(lkz), bfhi(lks)}; \
            *(v4f*)&sm.vz[SL][2 * lane] = (v4f){bflo(lvz), bflo(lvs), bfhi(lvz), bfhi(lvs)}; \
            CBAR(); \
            if (lane == 0) lds_st(&sm.rdy[SL], (U) + 1); \
            CBAR(); }
        const unsigned dw = NSW + 2 - wave;
        QT_LOADS()
        UNIT_LOADS(head0 + dw * (GSZ + 1 - 2 * side), lane >> 4)
#pragma unroll 1
        for (unsigned u = dw;;) {
            while (u >= lds_ld(&sm.merged) + NIN) SLEEP(4);
            CBAR();
            UNIT_FILL(u % NIN, u)
            if (u - 1 < 12) {
                const unsigned x_ = (blockIdx.x >> 3) + 32 * u - 8; unsigned dm;
                const char* p_ = x_ < 384 ? (const char*)(x_ < 192 ? qk : qv) + (size_t)(x_ < 192 ? x_ : x_ - 192) * (2u << 20) + (x_ * 8 + (blockIdx.x & 7)) * 640 : (const char*)out + (x_ - 384) * 4096;
                while ((int)(NOW() - lds_ld(&sm.line)) < TSD) SLEEP(2)
                asm volatile("s_load_dword %0, %1, 0x0\n\ts_waitcnt lgkmcnt(0)" : "=&s"(dm) : "s"(p_) : "memory");
            }
            u += 2;
            if (u >= NU) break;
            if (u == 4) {
                const unsigned tpf = tdue(PSF - 1) + TFO;
                { int d; while ((d = (int)(lds_ld(&sm.line) + tpf - NOW())) > 0 && d < 3000) SLEEP(1) }
#define PF3(P) asm volatile("global_load_dwordx4 a[0:3], %0, %1\n\tglobal_load_dwordx4 a[0:3], %0, %1 offset:1024\n\tglobal_load_dwordx4 a[0:3], %0, %1 offset:2048" \
                            :: "v"(64 * SKV * 2 + blockIdx.x * 3072 + lane * 16), "s"(P) : "a0", "a1", "a2", "a3")
                PF3(k_zeros); PF3(k_scales); PF3(v_zeros); PF3(v_scales);
                asm volatile("global_load_dword a0, %0, %1" :: "v"(blockIdx.x * (DH * 2) + ql * 8), "s"(q) : "a0");
            }
            const unsigned tin = tdue(first_of(u) - (QUARTER(u) ? 24 : 0) - LEADP + (split & 7) * DPH);
            { int d; while ((d = (int)(lds_ld(&sm.line) + tin - NOW())) > 0 && d < 3000) SLEEP(4) }
            unsigned hd;
            while ((hd = lds_ld(&sm.utab[u])) == ~0u) SLEEP(2)
            UNIT_LOADS(hd & 255, QUARTER(u) ? rank & 3 : lane >> 4)
        }
        while (lds_ld(&sm.tag_rdy) == 0) SLEEP(1);
        CBAR();
        E1 = (u64)lds_ld(&sm.tag_lo) | ((u64)lds_ld(&sm.tag_hi) << 32);
        if (wave == NSW + 1 && blockIdx.x == 0) {
            for (;;) {
                bool ok = true;
#pragma unroll
                for (int i = 0; i < NH / 64; i++) ok &= __hip_atomic_load((const u64*)(ws + WS_POST) + i * 64 + lane, __ATOMIC_RELAXED, __HIP_MEMORY_SCOPE_AGENT) >> 24 == (E1 << 24) >> 24;
                if (__builtin_amdgcn_ballot_w64(ok) == ~0ull) break;
                SLEEP(64);
            }
            if (lane == 0) __hip_atomic_store((u64*)(ws + WS_EPOCH), E1, __ATOMIC_RELAXED, __HIP_MEMORY_SCOPE_AGENT);
        }
        if (wave == NSW + 2) {
            const unsigned t16 = (unsigned)E1 & 0xffffu, t16b = ((unsigned)E1 + ((unsigned)E1 >> 16)) & 0xffffu;
            const __amdgpu_buffer_rsrc_t rsrc = __builtin_amdgcn_make_buffer_rsrc(ws + WS_SWAP + (blockIdx.x ^ 4) * 5120, 0, 5120, 0x00020000);
            const unsigned swp = lds_ld(&sm.swp);
            if (swp) {
                unsigned use = 0;
#pragma unroll
                for (int i = 0; i < 5; i++) use |= ((swp >> wunit(i * 64 + lane)) & 1) << i;
                const unsigned tg = tdue(PSG - 1) + (lds_ld(&sm.gap) & 0xffffu);
                while ((int)(NOW() - lds_ld(&sm.line) - tg) < 0) SLEEP(2)
                for (;;) {
                    v4u d[5]; bool ok = true;
#pragma unroll
                    for (int i = 0; i < 5; i++) d[i] = __builtin_amdgcn_raw_buffer_load_b128(rsrc, (use >> i) & 1 ? (i * 64 + lane) * 16 : 0x40000000, 0, SC1 | 2);
#pragma unroll
                    for (int i = 0; i < 5; i++) if ((use >> i) & 1) {
                        ok &= d[i].y >> 16 == t16 && d[i].w >> 16 == t16b;
                        unsigned* w = (unsigned*)&sm.wv[144 * NMIX + (i * 64 + lane) * 6];
                        w[0] = d[i].x; w[1] = (d[i].y & 0xffffu) | (d[i].z << 16); w[2] = (d[i].z >> 16) | (d[i].w << 16);
                    }
                    if (__builtin_amdgcn_ballot_w64(ok) == ~0ull) break;
                    SLEEP(32)
                }
                CBAR();
                LDS_OR(&sm.vrdy, swp)
            }
        }
    } else {
        const unsigned eu = lane >= NU ? lane - NU : lane, win = min(first_of(eu) >> 5, 15u), pair = QUARTER(eu) ? 2 * (eu - WQ) + (rank >> 2) : rank;
        const unsigned rot2 = ((const unsigned char*)ROT)[win * 8 + pair];
#define UENT(BIT) (win * 16 + 2 * pair + ((BIT) ^ side) | (QUARTER(eu) ? (rank - (eu - WQ)) * 8 & 31 : (rot2 >> ((BIT) ^ side) * 4 & 15) * 2) << 8)
        unsigned far = 0;
        const unsigned hp = (blockIdx.x & 1) * NH + (blockIdx.x >> 1) * 4 + 2;
        if (blockIdx.x < 144) {
            const char* pb = (blockIdx.x < 128 ? (const char*)(hp >= NH ? qv : qk) + (size_t)(hp & (NH - 1)) * (SKV * DH * 4) + SKV * DH * 2
                                               : (const char*)ws + WS_PART + (8u << 20) + (blockIdx.x & 15) * 65536) + ((NOW() * 0x9e3779b1u) >> 26) * 128;
            unsigned mn[2] = {~0u, ~0u};
#pragma unroll 1
            for (unsigned i = 1; i < 2 * NPRB + 2; i++) {
                u64 t0, t1; unsigned dm;
                asm volatile("s_memtime %0\n\ts_waitcnt lgkmcnt(0)\n\ts_load_dword %2, %3, 0x0\n\ts_waitcnt lgkmcnt(0)\n\ts_memtime %1\n\ts_waitcnt lgkmcnt(0)"
                             : "=&s"(t0), "=&s"(t1), "=&s"(dm) : "s"(pb + i * 8192) : "memory");
                if (i == 1) asm volatile("global_load_dword a0, %0, %1 nt" :: "v"(0), "s"(pb) : "a0");
                const unsigned t = (unsigned)(t1 - t0) < 330 || i == 1 ? ~0u : (unsigned)(t1 - t0);
                if (i & 1) mn[1] = min(mn[1], t); else mn[0] = min(mn[0], t);
            }
            far = mn[0] > mn[1];
        }
        if (lane < NMIX) lds_st(&sm.utab[lane], UENT(lane & 1));
        while (lds_ld(&sm.tag_rdy) == 0) SLEEP(1);
        CBAR();
        E1 = (u64)lds_ld(&sm.tag_lo) | ((u64)lds_ld(&sm.tag_hi) << 32);
        {
            const unsigned own = lds_ld(&sm.line), tg = (unsigned)E1;
            if (lane == 0) __hip_atomic_store((u64*)(ws + WS_POST) + blockIdx.x, E1 << 24 | min(lds_ld(&sm.slow), 4000u) << 1 | ((far ^ (blockIdx.x >> 2) ^ (blockIdx.x < 128 ? 0x96 >> (hp & 7) : 0)) & 1), __ATOMIC_RELAXED, __HIP_MEMORY_SCOPE_AGENT);
            const __amdgpu_buffer_rsrc_t nrs = __builtin_amdgcn_make_buffer_rsrc(ws + WS_NEWS, 0, 8 * 1280, 0x00020000);
            if (blockIdx.x == MASTER) {
                while ((int)(NOW() - own) < TMAP) SLEEP(2)
                const unsigned a = tg << 24, b = (unsigned)(E1 >> 8);
                v4u x; v2u z; u64 v0, v1;
#pragma unroll 1
                for (int i = 0; i < 8; i++) {
                    x = __builtin_amdgcn_raw_buffer_load_b128(__builtin_amdgcn_make_buffer_rsrc(ws + WS_POST, 0, 1024, 0x00020000), lane * 16, 0, SC1);
                    z = __builtin_amdgcn_raw_buffer_load_b64(__builtin_amdgcn_make_buffer_rsrc(ws + WS_POST + 1024, 0, 128, 0x00020000), lane * 8, 0, SC1);
                    v0 = __builtin_amdgcn_ballot_w64(((x.x ^ a) >> 24 | (x.y ^ b)) == 0); v1 = __builtin_amdgcn_ballot_w64(((x.z ^ a) >> 24 | (x.w ^ b)) == 0);
                    if (__builtin_popcountll(v0) + __builtin_popcountll(v1) >= MINV) break;
                    SLEEP(8)
                }
                const u64 m0 = __builtin_amdgcn_ballot_w64(x.x & 1) & v0, m1 = __builtin_amdgcn_ballot_w64(x.z & 1) & v1;
                const int s0 = lane < 43 ? min(max(lane - VW, 4 * NMIX), 42 - 2 * VW) : min(max(lane - VW, 43), 63 - 2 * VW);
#define PC(M) __builtin_popcount((unsigned)((M) >> s0) & ((2u << 2 * VW) - 1))
                const u64 g0 = __builtin_amdgcn_ballot_w64(2 * PC(m0) > PC(v0)), g1 = __builtin_amdgcn_ballot_w64(2 * PC(m1) > PC(v1));
                const float sl = wave_sum(((v0 >> lane & 1) ? (float)(x.x >> 1 & 4095u) : 0.f) + ((v1 >> lane & 1) ? (float)(x.z >> 1 & 4095u) : 0.f)) / ((8.f - PRE0) * (__builtin_popcountll(v0) + __builtin_popcountll(v1)) + 1.f);
                const unsigned al = lds_ld(&sm.aline), mean = (al ? al : own) + (int)fmaxf(sl - SL0, 0.f);
                const bool vz = lane < 16 && ((z.x ^ a) >> 24 | (z.y ^ b)) == 0;
                const unsigned gw = 2 * __builtin_popcountll(__builtin_amdgcn_ballot_w64(vz && (z.x & 1))) > __builtin_popcountll(__builtin_amdgcn_ballot_w64(vz));
                const unsigned k = lane & 3;
                if (lane < 32 && k < 3) __builtin_amdgcn_raw_buffer_store_b128((v4u){k == 0 ? (unsigned)g0 : k == 1 ? (unsigned)g1 : mean, tg, k == 0 ? (unsigned)(g0 >> 32) : k == 1 ? (unsigned)(g1 >> 32) : gw, tg},
                                                                              nrs, (lane >> 2) * 1280 + k * 16, 0, SC1);
            }
            while ((int)(NOW() - own) < TMAP + TRES) SLEEP(2)
            v4u y;
            for (;;) {
                y = __builtin_amdgcn_raw_buffer_load_b128(nrs, lane < 3 ? (blockIdx.x & 7) * 1280 + lane * 16 : 0x40000000, 0, SC1);
                if (((unsigned)__builtin_amdgcn_ballot_w64(y.y == tg && y.w == tg) & 7u) == 7u) break;
                SLEEP(8)
            }
            const u64 f0 = (unsigned)__builtin_amdgcn_readlane(y.x, 0) | (u64)(unsigned)__builtin_amdgcn_readlane(y.z, 0) << 32, f1 = (unsigned)__builtin_amdgcn_readlane(y.x, 1) | (u64)(unsigned)__builtin_amdgcn_readlane(y.z, 1) << 32;
            if (lane == 2) { sm.aline = y.x - own + 500 < 1000 ? y.x : own; sm.chead = (blockIdx.x & ~5u) | ((blockIdx.x >> 2 ^ y.z) & 1) | (blockIdx.x & 1) << 2; }
            unsigned bit = ((unsigned)((lane >= NU ? f1 : f0) >> (4 * win + (pair >> 1))) ^ (0x66 >> pair)) & 1;
            if (eu < NMIX) bit = eu & 1;
            const u64 bm = __builtin_amdgcn_ballot_w64(bit);
            const unsigned nsw = __builtin_popcountll((f0 ^ f1) >> (4 * NMIX));
            if (lane == 0) { sm.swp = (unsigned)(bm ^ (bm >> NU)) & ((1u << NU) - 1); lds_st(&sm.gap, nsw ? (TG0 + TG1 * nsw / 8) * 0x10001u + ((TH0 + TH1 * nsw / 8) << 16) : 0u); }
            CBAR();
            lds_st(&sm.utab[lane], UENT(bit));
        }
        const unsigned swp = lds_ld(&sm.swp);
        const unsigned T = lds_ld(&sm.tag_lo);
        const unsigned t15 = T & 0x7fffu;
        const __amdgpu_buffer_rsrc_t rsrc = __builtin_amdgcn_make_buffer_rsrc(ws + WS_PART + split * 512, 0, NRW * NSPLIT * 512 - split * 512, 0x00020000);
        v2u* recs = (v2u*)&sm.cs[0][0][0];
#pragma unroll
        for (int i = 0; i < NOUT * 3; i++) (&sm.acc[0][0])[i * 64 + lane] = 0.0;
        CBAR();
        unsigned vut = sm.utab[NU + (lane & 31)] & 255;
        if (QUARTER((unsigned)lane) && (rank & 3)) vut = NH - 16 + (rank & 3) * 16 + ((vut & 15) ^ 1);
        __builtin_amdgcn_s_setprio(3);
        unsigned po[NU / 2];
#pragma unroll
        for (int k = 0; k < NU / 2; k++) po[k] = __builtin_amdgcn_ds_bpermute((2 * k + (lane >> 5)) * 4, vut) * (NSPLIT * 512) + (lane & 31) * 16;
        const u64 rb_ = (u64)(ws + WS_PART + split * 512);
        const v4u rs_ = {(unsigned)rb_, (unsigned)(rb_ >> 32) & 0xffffu, NRW * NSPLIT * 512 - split * 512, 0x00020000u};
#pragma unroll 1
        for (unsigned u = 0; u < 2 * NU - 1; u++) {
            const unsigned e = u % NU, so = u % NOUT;
            if (u >= NU) while (!((lds_ld(&sm.vrdy) >> e) & 1)) SLEEP(4)
            CBAR();
            const v4f h = *(const v4f*)&sm.wv[went(e) + (QUARTER(e) ? 32 : 128)];
            while (lds_ld(&sm.cnt[so]) != (QUARTER(e) ? 8 : 32)) SLEEP(1)
            CBAR();
            const double a0 = sm.acc[so][lane], a1 = sm.acc[so][64 + lane], a2 = sm.acc[so][128 + lane];
            if (u < NU) {
                float2 lg = *(const float2*)&sm.ent[so][2 * lane];
                if (QUARTER(e) && lane >> 4 != (rank & 3)) lg = make_float2(-INFINITY, -INFINITY);
                const v4f zs = *(const v4f*)&sm.vz[u % NIN][2 * lane];
                const float m = wave_max(fmaxf(lg.x, lg.y));
                const float p0 = __expf(lg.x - m), p1 = __expf(lg.y - m);
                const float x0 = p0 * zs.y, x1 = p1 * zs.w;
                const unsigned eb = max(__float_as_uint(wave_max(fmaxf(fabsf(x0), fabsf(x1)))) >> 23, 14u);
                const float f = __uint_as_float((268u - eb) << 23);
                const float w0 = fminf(fmaxf(rintf(x0 * f), -32767.f), 32767.f), w1 = fminf(fmaxf(rintf(x1 * f), -32767.f), 32767.f);
                const float den = wave_sum(p0 + p1);
                const float bz = wave_sum(fmaf(w0, zs.x, w1 * zs.z));
                const float L = fmaf(m, 1.4426950408889634f, __builtin_amdgcn_logf(den));
                unsigned* wd = (unsigned*)(((swp >> u) & 1 ? sm.wo : sm.wv) + went(u));
                if (!QUARTER(u) || lane >> 4 == (rank & 3)) wd[QUARTER(u) ? lane & 15 : lane] = ((unsigned)(int)w0 & 0xffffu) | ((unsigned)(int)w1 << 16);
                if (lane == 0) *(v4f*)&wd[QUARTER(u) ? 16 : 64] = (v4f){ldexpf(1.f / den, (int)eb - 141), bz, L, 0.f};
                CBAR();
                if (!((swp >> u) & 1)) LDS_OR(&sm.vrdy, 1u << u)
            } else {
                const float n0 = (float)(a0 - (double)h.y) * h.x, n1 = (float)(a1 - (double)h.y) * h.x, n2 = (float)(a2 - (double)h.y) * h.x;
                const unsigned e2 = max(__float_as_uint(wave_maxv(fmaxf(fmaxf(fabsf(n0), fabsf(n1)), fabsf(n2)))) >> 23, 14u);
                const float f2 = __uint_as_float((268u - e2) << 23);
                const unsigned m01 = __builtin_bit_cast(unsigned, __builtin_amdgcn_cvt_pk_i16((int)rintf(n0 * f2), (int)rintf(n1 * f2)));
                const unsigned m2 = __builtin_bit_cast(unsigned, __builtin_amdgcn_cvt_pk_i16((int)rintf(n2 * f2), 0));
                const unsigned bit = ((lane & 1 ? e2 : __float_as_uint(h.z)) >> (lane >> 1)) & 1u;
                recs[e * 64 + lane] = (v2u){m01, m2 | bit << 16 | t15 << 17};
                CBAR();
                sm.acc[so][lane] = 0.0; sm.acc[so][64 + lane] = 0.0; sm.acc[so][128 + lane] = 0.0;
            }
            CBAR();
            if (lane == 0) { lds_st(&sm.cnt[so], 0u); CBAR(); lds_st(&sm.merged, u + 1); }
            if (u == NU - 1 && swp) {
                wave_sync();
                const __amdgpu_buffer_rsrc_t srs = __builtin_amdgcn_make_buffer_rsrc(ws + WS_SWAP + blockIdx.x * 5120, 0, 5120, 0x00020000);
                v4u d[5]; unsigned o[5];
#pragma unroll
                for (int i = 0; i < 5; i++) {
                    const unsigned* w = (const unsigned*)&sm.wo[144 * NMIX + (i * 64 + lane) * 6];
                    const unsigned d0 = w[0], d1 = w[1], d2 = w[2];
                    d[i] = (v4u){d0, (d1 & 0xffffu) | (T << 16), (d1 >> 16) | (d2 << 16), (d2 >> 16) | ((T + (T >> 16)) << 16)};
                    o[i] = (swp >> wunit(i * 64 + lane)) & 1 ? (i * 64 + lane) * 16 : 0x40000000;
                }
                while ((int)(NOW() - lds_ld(&sm.line)) < (int)tdue(PSW) + TWO) SLEEP(1)
#pragma unroll
                for (int i = 0; i < 5; i++) __builtin_amdgcn_raw_buffer_store_b128(d[i], srs, o[i], 0, SC1 | 2);
            }
        }
        wave_sync();
#pragma unroll
        for (int k = 0; k < NU / 2; k++) asm volatile("ds_read_b128 a[%c0:%c0+3], %1 offset:%2" :: "i"(16 + 4 * k), "v"(lds_addr(recs) + lane * 16), "i"(k * 1024) : "memory");
        asm volatile("s_waitcnt lgkmcnt(0)" ::: "memory");
        while (!((lds_ld(&sm.vrdy) >> (NU - 1)) & 1)) SLEEP(4)
        CBAR();
        const v2u hw_ = {*(const unsigned*)&sm.wv[went(NU - 1) + 132], T};
        const unsigned hrow = (NRW - GSZ + (__builtin_amdgcn_readlane(vut, NU - 1) & 15 ^ 1)) * (NSPLIT * 512);
        while (lds_ld(&sm.grab) < NPC - PUBP) SLEEP(1)
        if (lane < 16) __builtin_amdgcn_raw_buffer_store_b64(hw_, rsrc, lane * 8, hrow, SC1);
#define PUB(K) asm volatile("buffer_store_dwordx4 a[%c0:%c0+3], %1, %2, 0 offen sc1" :: "i"(16 + 4 * (K)), "v"(po[K]), "s"(rs_) : "memory");
        PUB(WQ / 2) PUB(WQ / 2 + 1)
        { const unsigned t_ = TLAST() + PDEL; GATE(t_) }
        PUB(0) PUB(1) PUB(2) PUB(3) PUB(4) PUB(7) PUB(8)
        return;
    }
    }
    __builtin_amdgcn_s_setprio(0);
    unsigned c;
#define ADDRTN(P) { u64 ex_; asm volatile("s_mov_b64 %0, exec\n\ts_mov_b64 exec, 1\n\tds_add_rtn_u32 a15, %1, a13\n\ts_mov_b64 exec, %0" : "=&s"(ex_) : "v"(lds_addr(P)) : "a15", "memory"); }
#define ADDED(V) { unsigned t_; asm volatile("s_waitcnt lgkmcnt(0)\n\tv_accvgpr_read_b32 %1, a15\n\ts_nop 0\n\tv_readfirstlane_b32 %0, %1\n\ts_nop 1" : "=s"(V), "=v"(t_)); }
    asm volatile("v_accvgpr_write_b32 a13, 1\n\ts_nop 1" ::: "a13");
    ADDRTN(&sm.cidx) ADDED(c)
    if (c < NSW) {
        while (lds_ld(&sm.grab) < NPC + 2) SLEEP(4)
        const unsigned head = lds_ld(&sm.chead), T = lds_ld(&sm.tag_lo), tl = TLAST();
        const u64 rb_ = (u64)(ws + WS_PART);
        const v4u rs_ = {(unsigned)rb_, (unsigned)(rb_ >> 32) & 0xffffu, NRW * NSPLIT * 512, 0x00020000u};
        const unsigned ro = head * (NSPLIT * 512) + c * 1024, rx = (NH + (head & 15 ^ 1)) * (NSPLIT * 512) + c * 1024;
#define POLL(S, O) asm volatile("buffer_load_dwordx4 a[%c0:%c0+3], %1, %2, %3 offen sc1" :: "i"(16 + 4 * (S)), "v"(lane * 16), "s"(rs_), "s"(O) : "memory");
#define FETCH(X, S, N) asm volatile("s_waitcnt vmcnt(%c5)\n\tv_accvgpr_read_b32 %0, a[%c4]\n\tv_accvgpr_read_b32 %1, a[%c4+1]\n\tv_accvgpr_read_b32 %2, a[%c4+2]\n\tv_accvgpr_read_b32 %3, a[%c4+3]" \
                                    : "=v"((X).x), "=v"((X).y), "=v"((X).z), "=v"((X).w) : "i"(16 + 4 * (S)), "i"(N));
        float o0, o1, o2;
        if (head >= NH - GSZ) {
            __builtin_amdgcn_s_setprio(3);
            if (c == 0) {
                v2u hd;
                GATE(tl + THD)
                for (;;) {
                    hd = __builtin_amdgcn_raw_buffer_load_b64(__builtin_amdgcn_make_buffer_rsrc(ws + WS_PART + (NRW - GSZ + (head & 15 ^ 1)) * (NSPLIT * 512), 0, NSPLIT * 512, 0x00020000), (lane & 15) * 512, 0, SC1);
                    if (__builtin_amdgcn_ballot_w64(hd.y == T) == ~0ull) break;
                    SLEEP(4)
                }
                const float w = __builtin_amdgcn_exp2f(__uint_as_float(hd.x) - row_max(__uint_as_float(hd.x)));
                if (lane < 16) sm.ent[0][lane] = w * __builtin_amdgcn_rcpf(row_sum(w));
                CBAR();
                if (lane == 0) lds_st(&sm.tabs, 0u);
            }
            while (lds_ld(&sm.tabs) || lds_ld(&sm.merged) <= (2 * NU - 1) / NOUT * NOUT) SLEEP(1)
            CBAR();
            const float cf = sm.ent[0][2 * c + (lane >> 5)];
            v4u x; bool got;
#define SEEN(J, N) { FETCH(x, J, N) got = __builtin_amdgcn_ballot_w64((((x.y ^ T << 24) | (x.w ^ T << 24)) >> 24) != 0) == 0; }
            GATE(tl + TCA) POLL(0, ro) GATE(tl + TCA + TCD) POLL(1, rx + 3 * GSZ * NSPLIT * 512) SEEN(0, 1)
            if (!got) SEEN(1, 0)
            while (!got) { POLL(2, rx + 4 * GSZ * NSPLIT * 512) SEEN(2, 0) SLEEP(0) }
            const double c0 = ldexpf(cf, (int)(x.y >> 16 & 255u) - 141), c1 = ldexpf(cf, (int)(x.w >> 16 & 255u) - 141);
#define SWADD(A, B) ({ const v2u a_ = __builtin_bit_cast(v2u, A), b_ = __builtin_bit_cast(v2u, B); \
                const auto l_ = __builtin_amdgcn_permlane32_swap(a_.x, b_.x, false, false); const auto h_ = __builtin_amdgcn_permlane32_swap(a_.y, b_.y, false, false); \
                __builtin_bit_cast(double, (v2u){l_[0], h_[0]}) + __builtin_bit_cast(double, (v2u){l_[1], h_[1]}); })
            const double s0 = SWADD(c0 * (double)(short)x.x, c1 * (double)(short)x.z), s1 = SWADD(c0 * (double)((int)x.x >> 16), c1 * (double)((int)x.z >> 16)),
                         s2 = SWADD(c0 * (double)(short)x.y, c1 * (double)(short)x.w);
            asm volatile("ds_add_f64 %0, %1\n\tds_add_f64 %0, %2 offset:512\n\tds_add_f64 %0, %3 offset:1024" :: "v"(lds_addr(&sm.acc[0][lane])), "v"(s0), "v"(s1), "v"(s2) : "memory");
            ADDRTN(&sm.cdone)
            const double a0 = sm.acc[0][lane], a1 = sm.acc[0][64 + lane], a2 = sm.acc[0][128 + lane];
            ADDED(c)
            o0 = a0; o1 = a1; o2 = a2;
        } else {
            const bool quart = head >> 4 == WQ;
            const unsigned t15 = T & 0x7fffu;
#define TAKE(K) { FETCH(x[K], K, 0) \
                bad |= (x[K].y >> 17 ^ t15) | (x[K].w >> 17 ^ t15); \
                const u64 bl = __builtin_amdgcn_ballot_w64((x[K].y & 0x10000u) != 0), be = __builtin_amdgcn_ballot_w64((x[K].w & 0x10000u) != 0); \
                L[K] = __uint_as_float(lane < 32 ? (unsigned)bl : (unsigned)(bl >> 32)); ex[K] = lane < 32 ? (unsigned)be : (unsigned)(be >> 32); \
                M = fmaxf(M, fmaxf(__uint_as_float((unsigned)bl), __uint_as_float((unsigned)(bl >> 32)))); }
#define SUMUP(K) { const float w_ = __builtin_amdgcn_exp2f(L[K] - M), c_ = ldexpf(w_, (int)(ex[K] & 255u) - 141); den += w_; \
                t[0] = fmaf(c_, (float)(short)x[K].x, t[0]); t[1] = fmaf(c_, (float)((int)x[K].x >> 16), t[1]); t[2] = fmaf(c_, (float)(short)x[K].y, t[2]); \
                t[3] = fmaf(c_, (float)(short)x[K].z, t[3]); t[4] = fmaf(c_, (float)((int)x[K].z >> 16), t[4]); t[5] = fmaf(c_, (float)(short)x[K].w, t[5]); }
            const unsigned tg = tl + (quart ? TRQ : TRD);
            GATE(tg)
            v4u x[4]; float L[4], M; unsigned ex[4];
            for (;;) {
                unsigned bad = 0; M = -3e38f;
                POLL(0, ro)
                if (quart) { POLL(1, rx) POLL(2, rx + GSZ * NSPLIT * 512) POLL(3, rx + 2 * GSZ * NSPLIT * 512) }
                __builtin_amdgcn_s_setprio(3);
                TAKE(0)
                if (quart) { TAKE(1) TAKE(2) TAKE(3) }
                if (__builtin_amdgcn_ballot_w64(bad != 0) == 0) break;
                SLEEP(2)
            }
            float t[6] = {0.f, 0.f, 0.f, 0.f, 0.f, 0.f}, den = 0.f;
            SUMUP(0)
            if (quart) { SUMUP(1) SUMUP(2) SUMUP(3) }
#define SWADF(A, B) ({ const auto l_ = __builtin_amdgcn_permlane32_swap(__float_as_uint(A), __float_as_uint(B), false, false); __uint_as_float(l_[0]) + __uint_as_float(l_[1]); })
            v4f* pb = (v4f*)((char*)&sm.cs[0][0][0] + 65536);
            float* pm = (float*)(pb + NSW * 64);
            pb[c * 64 + lane] = (v4f){SWADF(t[0], t[3]), SWADF(t[1], t[4]), SWADF(t[2], t[5]), SWADF(den, den)};
            if (lane == 0) pm[c] = M;
            CBAR();
            ADDRTN(&sm.cdone)
            ADDED(c)
            CBAR();
            if (c == NSW - 1) {
                const v4f ma = *(const v4f*)pm, mb = *(const v4f*)(pm + 4);
                const float mw[NSW] = {ma.x, ma.y, ma.z, ma.w, mb.x, mb.y, mb.z, mb.w};
                float Mx = mw[0];
#pragma unroll
                for (int w = 1; w < NSW; w++) Mx = fmaxf(Mx, mw[w]);
                v4f sum = {0.f, 0.f, 0.f, 0.f};
#pragma unroll
                for (int w = 0; w < NSW; w++) sum += pb[w * 64 + lane] * __builtin_amdgcn_exp2f(mw[w] - Mx);
                const float inv = __builtin_amdgcn_rcpf(sum.w);
                o0 = sum.x * inv; o1 = sum.y * inv; o2 = sum.z * inv;
            }
        }
        if (c == NSW - 1) {
            asm("" : "+v"(o0), "+v"(o1), "+v"(o2));
            __hip_bfloat16* o = out + (size_t)head * DH + (lane & 31) * 6 + (lane >> 5) * 3;
            o[0] = __float2bfloat16(o0); o[1] = __float2bfloat16(o1); o[2] = __float2bfloat16(o2);
        }
    }
}

extern "C" void kernel_launch(void* const* d_in, const int* in_sizes, int n_in,
                              void* d_out, int out_size, void* d_ws, size_t ws_size,
                              hipStream_t stream) {
    const unsigned short* q = (const unsigned short*)d_in[0];
    const int* qk = (const int*)d_in[1];
    const unsigned short* k_scales = (const unsigned short*)d_in[2];
    const unsigned short* k_zeros = (const unsigned short*)d_in[3];
    const int* qv = (const int*)d_in[4];
    const unsigned short* v_scales = (const unsigned short*)d_in[5];
    const unsigned short* v_zeros = (const unsigned short*)d_in[6];
    const unsigned short* q_cos = (const unsigned short*)d_in[7];
    const unsigned short* q_sin = (const unsigned short*)d_in[8];
    const unsigned short* k_cos = (const unsigned short*)d_in[9];
    const unsigned short* k_sin = (const unsigned short*)d_in[10];
    unsigned char* ws = (unsigned char*)d_ws;
    attn_kernel<<<NSPLIT * GSZ, NTHREADS, 0, stream>>>(q, qk, k_scales, k_zeros, qv, v_scales, v_zeros,
                                                       q_cos, q_sin, k_cos, k_sin, (__hip_bfloat16*)d_out, ws);
}
```

```cpp
#include <hip/hip_runtime.h>
#include <hip/hip_bf16.h>
#include <stdint.h>

#define DH 192
#define HALF 96
#define SKV 2048
#define NH 256
#define NSW 8
#define NWAVES (NSW + 4)
#define NTHREADS (64 * NWAVES)
#define BROWS 128
#define NSPLIT 16
#define GSZ 16
#define WQ 10
#define NU (NH / GSZ + 3)
#define NPC 1024
#define NRR (NH + 3 * GSZ)
#define NIN 8
#define NOUT 8
#define WALL (15 * 144 + 4 * 48)
#define SC1 16
#define NRND 48
#define RST 4352
#define TK 2
#define NMIX 3
#define TUNIT 311
#define T01 50
#define TMIX 7
#define TOFF 50
#define PSF 100
#define TGS 84
#define PSW (NPC / 2 + 28)
#define TG0 8
#define TG1 5
#define TWO -80
#define PSG (NPC / 2 + 60)
#define TH0 3
#define TH1 3
#define EARLY 10
#define LEADP 24
#define DPH 2
#define TA 210
#define TA2 70
#define CSH 2
#define NPRB 3
#define TMAP 520
#define MINV 96
#define VW 4
#define MASTER 203
#define TRES 190
#define TB1 1200
#define PER 300
#define RDD 100
#define LEAD 70
#define LHI 85
#define LOK 74
#define PSL0 124
#define PUBP -14
#define LJ 210
#define LOK2 75
#define NTRY 3
#define TFO 6
#define SL0 42.f
#define PST0 200
#define KC 4
#define TC0 1000
#define TCS 9
#define PRE0 3

#define KREL 12
#define PDEL 70
#define NCP 3
#define NRW (NRR + NCP * GSZ)
#define TRD 112
#define THD 55
#define TRQ 55
#define TCA 132
#define TCD 22
#define TSD 560
#define WS_EPOCH 0
#define WS_POST0 8192
#define WS_POST (WS_POST0 + ((unsigned)E1 & 15) * 2048)
#define WS_NEWS (49152 + 256)
#define WS_HELLO 59904
#define WS_BOARD 65536
#define WS_PART (2u << 20)
#define WS_SWAP (WS_PART + NRW * NSPLIT * 512)

typedef unsigned int v2u __attribute__((ext_vector_type(2)));
typedef unsigned int v16u __attribute__((ext_vector_type(16)));
typedef unsigned int v4u __attribute__((ext_vector_type(4)));
typedef int v4i __attribute__((ext_vector_type(4)));
typedef float v2f __attribute__((ext_vector_type(2)));
typedef float v4f __attribute__((ext_vector_type(4)));
typedef unsigned long long u64;

__device__ __forceinline__ float bflo(unsigned u) { return __uint_as_float(u << 16); }
__device__ __forceinline__ float bfhi(unsigned u) { return __uint_as_float(u & 0xffff0000u); }
__device__ __forceinline__ float bfr(float x) {
    __bf16 b = (__bf16)x;
    return __uint_as_float(((unsigned)__builtin_bit_cast(unsigned short, b)) << 16);
}

#define SLEEP(n) SLEEP_(n)
#define SLEEP_(n) asm volatile("s_sleep " #n "\n\tv_add_co_u32 %0, vcc, 1, %0\n\ts_cbranch_vccz 1f\n\ts_endpgm\n1:" : "+v"(spins) :: "vcc", "memory");
#define SPIN0 0xf0000000u
#define CBAR() asm volatile("" ::: "memory")

__device__ __forceinline__ void wave_sync() {
    __builtin_amdgcn_fence(__ATOMIC_RELEASE, "wavefront");
    __builtin_amdgcn_wave_barrier();
    __builtin_amdgcn_fence(__ATOMIC_ACQUIRE, "wavefront");
}
template <int CTRL>
__device__ __forceinline__ float dpp_add(float x) {
    int y = __builtin_amdgcn_update_dpp(0, __float_as_int(x), CTRL, 0xf, 0xf, true);
    return x + __int_as_float(y);
}
template <int CTRL>
__device__ __forceinline__ float dpp_max(float x) {
    int y = __builtin_amdgcn_update_dpp(__float_as_int(x), __float_as_int(x), CTRL, 0xf, 0xf, false);
    return fmaxf(x, __int_as_float(y));
}
__device__ __forceinline__ float row_sum(float x) { x = dpp_add<0x128>(x); x = dpp_add<0x124>(x); x = dpp_add<0x122>(x); return dpp_add<0x121>(x); }
__device__ __forceinline__ float row_max(float m) { m = dpp_max<0x128>(m); m = dpp_max<0x124>(m); m = dpp_max<0x122>(m); return dpp_max<0x121>(m); }
__device__ __forceinline__ float rdlane(float x, int l) { return __int_as_float(__builtin_amdgcn_readlane(__float_as_int(x), l)); }
__device__ __forceinline__ float wave_max(float m) { m = row_max(m); return fmaxf(fmaxf(rdlane(m, 0), rdlane(m, 16)), fmaxf(rdlane(m, 32), rdlane(m, 48))); }
__device__ __forceinline__ float wave_maxv(float m) {
    m = row_max(m);
    const auto a = __builtin_amdgcn_permlane16_swap(__float_as_uint(m), __float_as_uint(m), false, false);
    m = fmaxf(__uint_as_float(a[0]), __uint_as_float(a[1]));
    const auto b = __builtin_amdgcn_permlane32_swap(__float_as_uint(m), __float_as_uint(m), false, false);
    return fmaxf(__uint_as_float(b[0]), __uint_as_float(b[1]));
}
__device__ __forceinline__ float wave_sum(float x) { x = row_sum(x); return (rdlane(x, 0) + rdlane(x, 16)) + (rdlane(x, 32) + rdlane(x, 48)); }
__device__ __forceinline__ unsigned lds_ld(const unsigned* p) { return __builtin_amdgcn_readfirstlane(__hip_atomic_load(p, __ATOMIC_RELAXED, __HIP_MEMORY_SCOPE_WORKGROUP)); }
__device__ __forceinline__ unsigned lds_addr(const void* p) { return (unsigned)(size_t)(const __attribute__((address_space(3))) void*)p; }
#define DMA16(SRC, LDS) asm volatile("s_mov_b32 %0, m0\n\ts_mov_b32 m0, %2\n\ts_nop 0\n\tglobal_load_lds_dwordx4 %1, off\n\ts_mov_b32 m0, %0" : "=&s"(m0keep) : "v"(SRC), "s"(LDS) : "memory")
__device__ __forceinline__ void lds_st(unsigned* p, unsigned v) { __hip_atomic_store(p, v, __ATOMIC_RELAXED, __HIP_MEMORY_SCOPE_WORKGROUP); }
#define NOW() ((unsigned)__builtin_amdgcn_s_memrealtime())
__device__ __forceinline__ unsigned tdue(unsigned g) {
    return (g * TUNIT + min(g, 480u) * TK + min(g, 48u) * T01 + (min(g, 32u * NMIX) + min(max((int)g - NPC / 2, 0), 32 * NMIX)) * TMIX + (g >= PSF ? TGS * 32 : 0)) >> 5;
}

__device__ __forceinline__ unsigned unit_of(unsigned g) { const unsigned p = g & 511; return (g >> 9) * NU + (p >> 5) + min(max((int)p - 32 * WQ, 0) >> 3, 3); }
__device__ __forceinline__ unsigned first_of(unsigned e) { return 32 * e - 24 * min(max((int)e - WQ, 0), 4); }
#define QUARTER(E) ((E) - WQ < 4)
__device__ __forceinline__ unsigned went(unsigned e) { return 144 * e - 96 * min(max((int)e - WQ, 0), 4); }
__device__ __forceinline__ unsigned wunit(unsigned x) { x += 24 * NMIX; return x < 24 * WQ ? x / 24 : x < 24 * WQ + 32 ? WQ + (x - 24 * WQ) / 8 : (x + 64) / 24; }

#define ROTT 0xed8d2c3d2c35ed84ull, 0xba85aa165b03ac82ull, 0xee7efa489a38af71ull, 0xe932da1b0bab51d8ull, 0xfa96112d5fee971bull, 0x2c7e9ea5a544f071ull, 0x3d86093d5f0e810bull, 0xe932da1b0bab51d8ull, 0xfa96112d5fee971bull, 0xd834e62c5fa723e9ull, 0x1d7d1a3d2c061d88ull, 0xe932da1b0bab51d8ull, 0xfa96112d5fee971bull, 0xc834e12c0aaa4ec7ull, 0x3d86093d5f0e810bull, 0xe932da1b0bab51d8ull
static __device__ const u64 ROT[16] = {ROTT};

struct __attribute__((aligned(16))) Smem {
    unsigned cs[NSW][4][768];
    double acc[NOUT][DH];
    float ent[NOUT][BROWS];
    unsigned short wv[WALL];
    unsigned short wo[WALL];
    unsigned q[NIN][HALF];
    float2 kz[NIN][BROWS];
    float2 vz[NIN][BROWS];
    unsigned utab[64];
    unsigned line, gap;
    unsigned late, grab;
    unsigned rdy[NIN], cnt[NOUT];
    unsigned tabs, vrdy, swp;
    unsigned aline;
    unsigned tag_rdy, tag_lo, tag_hi, merged, cdone, cidx;
    unsigned slow;
    unsigned chead;
};

#define LDS_ADD(P, V) { u64 ex_; asm volatile("s_mov_b64 %0, exec\n\ts_mov_b64 exec, 1\n\tds_add_u32 %1, %2\n\ts_mov_b64 exec, %0" : "=&s"(ex_) : "v"(lds_addr(P)), "v"(V) : "memory"); }
#define LDS_OR(P, V) { u64 ex_; asm volatile("s_mov_b64 %0, exec\n\ts_mov_b64 exec, 1\n\tds_or_b32 %1, %2\n\ts_mov_b64 exec, %0" : "=&s"(ex_) : "v"(lds_addr(P)), "v"(V) : "memory"); }

__global__ __launch_bounds__(NTHREADS, 3) void attn_kernel(
    const unsigned short* __restrict__ q, const int* __restrict__ qk,
    const unsigned short* __restrict__ k_scales, const unsigned short* __restrict__ k_zeros,
    const int* __restrict__ qv,
    const unsigned short* __restrict__ v_scales, const unsigned short* __restrict__ v_zeros,
    const unsigned short* __restrict__ q_cos, const unsigned short* __restrict__ q_sin,
    const unsigned short* __restrict__ k_cos, const unsigned short* __restrict__ k_sin,
    __hip_bfloat16* __restrict__ out, unsigned char* __restrict__ ws)
{
    __shared__ Smem sm;
    const int tid = threadIdx.x;
    unsigned spins = SPIN0;
    const int lane = tid & 63;
    const int hw = __builtin_amdgcn_readfirstlane(tid >> 6);
    u64 t00; unsigned ka;
    asm volatile("s_memrealtime %0\n\ts_load_dword %1, %2, 0x40" : "=&s"(t00), "=&s"(ka) : "s"(__builtin_amdgcn_kernarg_segment_ptr()));
    if (hw == 0) {
        const char* pc0; asm volatile("s_getpc_b64 %0" : "=s"(pc0));
        asm volatile("global_load_dword a6, %0, %1" :: "v"(((blockIdx.x & ~7u) + (lane & 7)) * 128), "s"(pc0) : "a6", "memory");
    }
    const int wave = hw >= 4 ? hw - 4 : hw + NSW;
    const unsigned split = (blockIdx.x & 1) + (blockIdx.x & 2) * 4 + ((blockIdx.x >> 3) & 3) * 2;
    const unsigned side = (split ^ (blockIdx.x >> 2)) & 1, rank = blockIdx.x >> 5;
    const unsigned head0 = 2 * rank + side;
    const unsigned rf0 = (8 * split + 1) / 3, r3 = 8 * split + 1 - 3 * rf0;
    const unsigned koff = ((lane >> 5) * DH + 3 * (lane & 31)) * 4, koffb = koff + 384;
#define AR(S) (16 + (S) * 8)
#define GLD(A, OFF, BASE, IMM) asm volatile("global_load_dwordx3 a[%c0:%c1], %2, %3 offset:%4 nt" :: "i"(A), "i"((A) + 2), "v"(OFF), "s"(BASE), "i"(IMM))
#define ISSUE(S, J, BASE, OA, OB) { GLD(AR(S), OA, BASE, (J) * 1536); GLD(AR(S) + 4, OB, BASE, (J) * 1536); }
#define HEAD2 ((GSZ + 1 - 2 * side) * (SKV * DH * 4))
    const unsigned wo = (hw - 2) & 7;
    const bool pre = hw >= 4 && wo < PRE0;
    if (pre) {
        const char* a = (const char*)qk + (head0 * 512 + side * 256 + rf0 + wave + (wave >= 6 ? 80 : 0)) * 3072; const char* b = a + HEAD2;
        ISSUE(0, 0, a, koff, koffb) ISSUE(1, 1, a, koff, koffb) ISSUE(2, 0, b, koff, koffb) ISSUE(3, 1, b, koff, koffb)
    }
    asm volatile("s_waitcnt lgkmcnt(0)" : "+s"(t00), "+s"(ka));
    if (hw == 0) {
        sm.utab[lane] = ~0u;
        unsigned l0 = (unsigned)t00 + TOFF;
        if (KREL) { const unsigned t1 = NOW() + KREL; if ((int)(t1 - l0) > 0) l0 = t1; }
        if (lane < 32) (&sm.line)[lane] = lane == 3 ? 64u : (lane & 29) == 0 ? l0 : 0u;
        asm volatile("s_waitcnt lgkmcnt(0)" ::: "memory");
    }
    __builtin_amdgcn_s_barrier();
    CBAR();
    const u64 rtab = r3 == 1 ? 0x5756552c2b020100ull : r3 == 0 ? 0x5756552c2b2a0100ull : 0x57562d2c2b020100ull;
#define R8(I) (rf0 + ((unsigned)(rtab >> (8 * (I))) & 255u))
#define GATE(DUE) { int d_ = (int)((DUE) - NOW()); if (d_ < 3000) while (d_ > 0) { __builtin_amdgcn_s_sleep(1); d_ = (int)((DUE) - NOW()); } }
#define TLAST() (lds_ld(&sm.line) + tdue(NPC - 2) + (lds_ld(&sm.gap) >> 16))
    if (__builtin_expect(wave < NSW, 1)) {
        const int cst = 2 * side;
        const unsigned r8w = R8((unsigned)wave);
        const char* cbase[3];
#pragma unroll
        for (int j = 0; j < 3; j++) { const int x = j * 64 + lane; cbase[j] = (x < 96 ? (const char*)k_cos + x * 16 : (const char*)k_sin + (x - 96) * 16) + r8w * (4 * DH * 2); }
        unsigned m0keep;
#define CS_DMA(W, C) { _Pragma("unroll") for (int j = 0; j < 3; j++) DMA16(cbase[j] + (C) * (512 * DH * 2), lds_addr(&sm.cs[0][0][0]) + ((W) * 4 + (C)) * 3072 + j * 1024); }
#define REPACK(W, C) { \
        unsigned* cw = &sm.cs[W][C][0]; \
        const unsigned* l = cw + rsel * (DH / 2) + qoff; \
        unsigned r[2][6]; \
        _Pragma("unroll") \
        for (int j = 0; j < 2; j++) { \
            unsigned e[4][2]; \
            _Pragma("unroll") \
            for (int k = 0; k < 4; k++) { \
                const unsigned* a = l + j * DH + (k & 1) * 48 + (k >> 1) * 384;       \
                unsigned x0 = a[0], x1 = a[1]; \
                e[k][0] = __builtin_amdgcn_alignbit(x1, x0, sh); \
                e[k][1] = x1 >> sh; \
            } \
            r[j][0] = (e[0][0] & 0xffffu) | (e[3][0] << 16); \
            r[j][1] = (e[0][0] >> 16) | (e[3][0] & 0xffff0000u); \
            r[j][2] = (e[0][1] & 0xffffu) | (e[3][1] << 16); \
            r[j][3] = (e[1][0] & 0xffffu) | (e[2][0] << 16); \
            r[j][4] = (e[1][0] >> 16) | (e[2][0] & 0xffff0000u); \
            r[j][5] = (e[1][1] & 0xffffu) | (e[2][1] << 16); \
        } \
        wave_sync(); \
        _Pragma("unroll") \
        for (int j = 0; j < 2; j++) { *(v4u*)&cw[j * 256 + lane * 4] = (v4u){r[j][0], r[j][1], r[j][2], r[j][3]}; *(v2u*)&cw[512 + j * 128 + lane * 2] = (v2u){r[j][4], r[j][5]}; } \
        wave_sync(); }
        const int sub = lane & 31;
        const int rsel = lane >> 5;
        const unsigned sh = (sub & 1) * 16;
        const int qoff = 3 * (sub >> 1) + (sub & 1);
        const unsigned voff = 3 * lane * 4;
        asm volatile("" ::: "a16", "a17", "a18", "a19", "a20", "a21", "a22", "a23", "a24", "a25", "a26", "a27", "a28", "a29", "a30", "a31",
                            "a32", "a33", "a34", "a35", "a36", "a37", "a38", "a39", "a40", "a41", "a42", "a43", "a44", "a45", "a46", "a47");
        asm volatile("" ::: "a48", "a49", "a50", "a51", "a52", "a53", "a54", "a55", "a56", "a57", "a58", "a59", "a60", "a61", "a62", "a63",
                            "a64", "a65", "a66", "a67", "a68", "a69", "a70", "a71", "a72", "a73", "a74", "a75", "a76", "a77", "a78", "a79");
        int ta[4][3], tb[4][3];
#define WAITT(P, S, N) asm volatile("s_waitcnt vmcnt(%6)\n\tv_accvgpr_read_b32 %0, a[%c7]\n\tv_accvgpr_read_b32 %1, a[%c8]\n\tv_accvgpr_read_b32 %2, a[%c9]\n\t" \
                              "v_accvgpr_read_b32 %3, a[%c10]\n\tv_accvgpr_read_b32 %4, a[%c11]\n\tv_accvgpr_read_b32 %5, a[%c12]" \
                              : "=v"(ta[P][0]), "=v"(ta[P][1]), "=v"(ta[P][2]), "=v"(tb[P][0]), "=v"(tb[P][1]), "=v"(tb[P][2]) \
                              : "i"(N), "i"(AR(S)), "i"(AR(S) + 1), "i"(AR(S) + 2), "i"(AR(S) + 4), "i"(AR(S) + 5), "i"(AR(S) + 6))
        const unsigned voffb = voff + 768;
        u64 ln = 0;
#define TDUE(G) (tdue(G) + ((G) >= PSW ? __builtin_amdgcn_readfirstlane((unsigned)(ln >> 32)) >> ((G) >= PSG ? 16 : 0) & 0xffffu : 0u))
#define REPORT() { const unsigned t_ = now - tdc; if (lane == 0) lds_st(&sm.late, t_); }
#define LINE() ln = LDV((u64*)&sm.line);
#define LDV(P) __hip_atomic_load(P, __ATOMIC_RELAXED, __HIP_MEMORY_SCOPE_WORKGROUP)
        unsigned u = 0, rg = cst * 8 + wave, np = 0;
        CS_DMA(wave, cst)

        const char* kb0 = (const char*)qk + (size_t)(head0 * 512 + r8w) * 3072;
#define KB(I) (kb0 + ((cst + (I)) & 3) * (128 * 3072))
        const char* kb; const char* kb2;
#define SISSUE(I) { kb = KB(I); kb2 = kb + HEAD2; const unsigned du_ = lds_ld(&sm.line) + tdue(16 * (I) + 2 * wo); GATE(du_) \
            ISSUE(((I) & 1) * 4, 0, kb, koff, koffb) ISSUE(((I) & 1) * 4 + 1, 1, kb, koff, koffb) ISSUE(((I) & 1) * 4 + 2, 0, kb2, koff, koffb) ISSUE(((I) & 1) * 4 + 3, 1, kb2, koff, koffb) }
        if (!pre) SISSUE(0)
        CS_DMA(wave, (cst + 1) & 3)
        if (pre) asm volatile("s_waitcnt vmcnt(3)" ::: "memory");
        asm volatile("s_waitcnt vmcnt(11)" ::: "memory");
        if (!pre) LDS_ADD(&sm.slow, NOW() - lds_ld(&sm.line) - tdue(2 * wo))
        REPACK(wave, cst)
        SISSUE(1)
#define TABDONE(I) LDS_OR(&sm.tabs, 1u << ((((cst + (I)) & 3) * 8) + wave))
        TABDONE(0)
        const float scale = 0.07216878364870322f;
        double acc[3] = {0.0, 0.0, 0.0};
        bool first = true, chg, none = false;
        unsigned Qp[3], Qn[3], Rp[3], Rn[3];
        float kz4[4], ksc, kres[4];
        v4u ca[4]; v2u cb[4];
        v2f qe[4][3], A[4];
        unsigned vut, nrg;
        const unsigned vr = ((lane >> 3) & 3) * 128 + R8((unsigned)lane & 7);
        unsigned g, mg;
        const unsigned grab_adr = lds_addr(&sm.grab);
        asm volatile("v_accvgpr_write_b32 a13, 1\n\tv_accvgpr_write_b32 a14, 2" ::: "a13", "a14", "a15");
#define GRAB(N) { u64 ex_; asm volatile("s_mov_b64 %0, exec\n\ts_mov_b64 exec, 1\n\tds_add_rtn_u32 a15, %1, a%c2\n\ts_mov_b64 exec, %0" : "=&s"(ex_) : "v"(grab_adr), "i"(12 + (N)) : "memory"); }
#define GRABBED() { unsigned t_; asm volatile("s_waitcnt lgkmcnt(0)\n\tv_accvgpr_read_b32 %1, a15\n\ts_nop 0\n\tv_readfirstlane_b32 %0, %1\n\ts_nop 1" : "=s"(g), "=v"(t_)); }
        unsigned oa, ob, un, tdn, tiss, tdc = 0;
#define PLAN() { \
            GRABBED() un = unit_of(g); chg = un != u; none = g >= NPC; \
            const bool nv = g >= NPC / 2; \
            unsigned hd = __builtin_amdgcn_readlane(vut, un); \
            while (hd == ~0u) { SLEEP(1) hd = lds_ld(&sm.utab[un]); } \
            nrg = (g + (hd >> 8)) & 31; hd &= 255; \
            unsigned boff, boff2; asm("s_mul_i32 %0, %1, 0xc00" : "=s"(boff) : "s"(hd * 512 + __builtin_amdgcn_readlane(vr, nrg))); \
            asm("s_mul_i32 %0, %1, 0xc00" : "=s"(boff2) : "s"(hd * 512 + __builtin_amdgcn_readlane(vr, nrg + 1))); \
            const char* b_ = nv ? (const char*)qv : (const char*)qk; \
            kb = b_ + boff; kb2 = b_ + boff2; \
            oa = nv ? voff : koff; ob = nv ? voffb : koffb; \
            tdn = TDUE(g); }
#define FIRE() { ISSUE(0, 0, kb, oa, ob) ISSUE(1, 1, kb, oa, ob) ISSUE(2, 0, kb2, oa, ob) ISSUE(3, 1, kb2, oa, ob) }
#define LAUNCH() { \
            LINE() \
            const unsigned du_ = __builtin_amdgcn_readfirstlane((unsigned)ln) + tdn; \
            GATE(du_) \
            ISSUE(0, 0, kb, oa, ob) ISSUE(1, 1, kb, oa, ob) \
            GATE(du_ + 10) \
            ISSUE(2, 0, kb2, oa, ob) ISSUE(3, 1, kb2, oa, ob) tiss = NOW(); }
#define ARRIVED() \
            const unsigned now = NOW(); \
            const bool stall = now - tiss > LJ && g - PST0 < NPC - 48 - PST0; \
            const bool late = !none && !stall && (int)(now + EARLY - __builtin_amdgcn_readfirstlane((unsigned)ln) - tdn) >= 0; \
            if (late) { FIRE() tiss = now; } \
            if (stall && lane == 0) __hip_atomic_fetch_max(&sm.line, (unsigned)ln + (now - tiss) - LOK2, __ATOMIC_RELAXED, __HIP_MEMORY_SCOPE_WORKGROUP); \
            REPORT()
#define ADVANCE(ISV) { \
            if (chg) FLUSH(ISV) \
            if (!none && !late) LAUNCH() \
            first = chg; u = un; rg = nrg; tdc = tdn + 10; }
#define K_TAB(MODE) { _Pragma("unroll") for (int p = 0; p < ((MODE) ? 2 : 4); p++) { \
            const unsigned* cw = &sm.cs[(rg & 7) + (p >> 1)][rg >> 3][0]; \
            ca[p] = *(const v4u*)&cw[(p & 1) * 256 + lane * 4]; cb[p] = *(const v2u*)&cw[512 + (p & 1) * 128 + lane * 2]; } }
#define K_PRE(MODE, P) { \
        float e0, e1, e2, f0, f1, f2; \
        const unsigned* qp_ = ((MODE) && (P) >= 2) ? Rp : Qp; const unsigned* qn_ = ((MODE) && (P) >= 2) ? Rn : Qn; \
        const int tp_ = (MODE) ? (P) & 1 : (P); \
        asm("v_dot2_f32_bf16 %0, %6, %12, 0\n\tv_dot2_f32_bf16 %3, %9, %15, 0\n\tv_dot2_f32_bf16 %1, %7, %13, 0\n\tv_dot2_f32_bf16 %4, %10, %16, 0\n\t" \
            "v_dot2_f32_bf16 %2, %8, %14, 0\n\tv_dot2_f32_bf16 %5, %11, %17, 0\n\ts_nop 2" \
            : "=&v"(e0), "=&v"(e1), "=&v"(e2), "=&v"(f0), "=&v"(f1), "=&v"(f2) \
            : "v"(qp_[0]), "v"(qp_[1]), "v"(qp_[2]), "v"(qn_[0]), "v"(qn_[1]), "v"(qn_[2]), \
              "v"(ca[tp_].x), "v"(ca[tp_].y), "v"(ca[tp_].z), "v"(ca[tp_].w), "v"(cb[tp_].x), "v"(cb[tp_].y)); \
        qe[P][0] = (v2f){e0, f0}; qe[P][1] = (v2f){e1, f1}; qe[P][2] = (v2f){e2, f2}; \
        A[P] = (qe[P][0] + qe[P][1] + qe[P][2]) * (v2f){-kz4[P], -kz4[P]}; }
#define K_WAIT(MODE, P) { \
        if ((MODE) == 0) { WAITT(P, P, 6 - 2 * (P)); } \
        else { WAITT(P, (((MODE) - 1) & 1) * 4 + (P), ((MODE) < 3 ? 20 : (MODE) == 3 ? 17 : 6) - 2 * (P)); } }
#define K_FMA(P) { \
        _Pragma("unroll") \
        for (int i = 0; i < 3; i++) { v2f x = {(float)ta[P][i], (float)tb[P][i]}; A[P] = qe[P][i] * x + A[P]; } \
        kres[P] = A[P].x + A[P].y; }
#define K_RED(MODE) { \
        const bool o1 = lane & 1, o2 = lane & 2; \
        float s01 = (o1 ? kres[1] : kres[0]) + __int_as_float(__builtin_amdgcn_update_dpp(0, __float_as_int(o1 ? kres[0] : kres[1]), 0xb1, 0xf, 0xf, true)); \
        float s23 = (o1 ? kres[3] : kres[2]) + __int_as_float(__builtin_amdgcn_update_dpp(0, __float_as_int(o1 ? kres[2] : kres[3]), 0xb1, 0xf, 0xf, true)); \
        float sx = (o2 ? s23 : s01) + __int_as_float(__builtin_amdgcn_update_dpp(0, __float_as_int(o2 ? s01 : s23), 0x4e, 0xf, 0xf, true)); \
        sx = dpp_add<0x124>(sx); sx = dpp_add<0x128>(sx); \
        const auto sw = __builtin_amdgcn_permlane16_swap(__float_as_uint(sx), __float_as_uint(sx), false, false); \
        sx = __uint_as_float(sw[0]) + __uint_as_float(sw[1]); \
        if ((lane & 28) == 0) { if (MODE) sm.ent[(lane >> 1) & 1][rg * 4 + 2 * (lane & 1) + rsel] = sx * ksc; else sm.ent[u % NOUT][rg * 4 + 2 * (lane & 3) + rsel] = sx * ksc; } }
#define K_PRE4(MODE) { K_PRE(MODE, 0) K_PRE(MODE, 1) K_PRE(MODE, 2) K_PRE(MODE, 3) asm volatile("" :: "v"(A[0]), "v"(A[1]), "v"(A[2]), "v"(A[3])); }
#define K_BODY(MODE) { K_PRE4(MODE) K_WAIT(MODE, 0) K_FMA(0) K_WAIT(MODE, 1) K_FMA(1) K_WAIT(MODE, 2) K_FMA(2) K_WAIT(MODE, 3) K_FMA(3) K_RED(MODE) }
#define V_FMA(J, W0, W1) { \
        _Pragma("unroll") \
        for (int i = 0; i < 3; i++) { \
            acc[i] = fma(W0, (double)ta[J][i], acc[i]); \
            acc[i] = fma(W1, (double)tb[J][i], acc[i]); \
        } }
#define V_PIN() asm volatile("" :: "v"(acc[0]), "v"(acc[1]), "v"(acc[2]));
#define RDYWAIT(SL, SEQ) { while (lds_ld(&sm.rdy[SL]) != (SEQ)) SLEEP(1); CBAR(); }
#define LOADZ() { _Pragma("unroll") for (int p = 0; p < 4; p++) kz4[p] = sm.kz[u % NIN][rg * 4 + 2 * p + rsel].x; ksc = sm.kz[u % NIN][rg * 4 + 2 * (lane & 3) + rsel].y * scale; }
#define LOADQ() { _Pragma("unroll") for (int i = 0; i < 3; i++) { Qp[i] = sm.q[u % NIN][3 * sub + i]; Qn[i] = __builtin_amdgcn_alignbit(Qp[i], Qp[i], 16) ^ 0x80000000u; } }
#define FLUSH(ISV) { \
            mg = __builtin_amdgcn_readfirstlane(mg); \
            while (u >= mg + NOUT) { SLEEP(1); mg = lds_ld(&sm.merged); } \
            CBAR(); \
            if (ISV) { const unsigned aa = lds_addr(&sm.acc[u % NOUT][lane]); \
                asm volatile("ds_add_f64 %0, %1\n\tds_add_f64 %0, %2 offset:512\n\tds_add_f64 %0, %3 offset:1024" :: "v"(aa), "v"(acc[0]), "v"(acc[1]), "v"(acc[2]) : "memory"); \
                acc[0] = 0.0; acc[1] = 0.0; acc[2] = 0.0; } \
            CBAR(); \
            LDS_ADD(&sm.cnt[u % NOUT], np) \
            CBAR(); \
            np = 0; }

#define LOADZ2() { _Pragma("unroll") for (int p = 0; p < 4; p++) kz4[p] = sm.kz[p >> 1][rg * 4 + 2 * (p & 1) + rsel].x; ksc = sm.kz[(lane >> 1) & 1][rg * 4 + 2 * (lane & 1) + rsel].y * scale; }
#define STATIC_STEP(I) { \
            rg = ((cst + (I)) & 3) * 8 + wave; \
            LOADZ2() \
            K_TAB((I) + 1) K_BODY((I) + 1) }
        CS_DMA(wave, (cst + 2) & 3)
        RDYWAIT(0, 1u) RDYWAIT(1, 2u)
        LOADQ()
#pragma unroll
        for (int i = 0; i < 3; i++) { Rp[i] = sm.q[1][3 * sub + i]; Rn[i] = __builtin_amdgcn_alignbit(Rp[i], Rp[i], 16) ^ 0x80000000u; }
        STATIC_STEP(0)
        asm volatile("s_waitcnt vmcnt(11)" ::: "memory");
        REPACK(wave, (cst + 1) & 3)
        TABDONE(1)
        SISSUE(2)
        CS_DMA(wave, (cst + 3) & 3)
        STATIC_STEP(1)
        asm volatile("s_waitcnt vmcnt(11)" ::: "memory");
        REPACK(wave, (cst + 2) & 3)
        TABDONE(2)
        SISSUE(3)
        STATIC_STEP(2)
        asm volatile("s_waitcnt vmcnt(8)" ::: "memory");
        REPACK(wave, (cst + 3) & 3)
        TABDONE(3)
        GRAB(2)
        vut = LDV(&sm.utab[lane]);
        STATIC_STEP(3)
        CBAR();
        LDS_ADD(&sm.cnt[0], 4u) LDS_ADD(&sm.cnt[1], 4u)
        PLAN()
        LAUNCH()
        u = un; rg = nrg; tdc = tdn + 10;
        bool tabok = false;
        __builtin_amdgcn_s_setprio(2);
#pragma unroll 1
        while (u < NU) {
            GRAB(2)
            mg = LDV(&sm.merged);
            vut = LDV(&sm.utab[lane]);
            LINE()
            if (!tabok) {
                for (;;) { const unsigned tb_ = lds_ld(&sm.tabs); tabok = tb_ == ~0u; if (((tb_ >> rg) & 3u) == 3u) break; SLEEP(1) }
                CBAR();
            }
            K_TAB(0)
            if (first) {
                unsigned fl = LDV(&sm.rdy[u % NIN]);
                CBAR();
                LOADQ() LOADZ()
                if (__builtin_amdgcn_readfirstlane(fl) != u + 1) { RDYWAIT(u % NIN, u + 1) LOADQ() LOADZ() }
            } else LOADZ()
            K_PRE4(0)
            PLAN()
            K_WAIT(0, 0) K_FMA(0) K_WAIT(0, 1) K_FMA(1)
            asm volatile("" :: "v"(kres[0]), "v"(kres[1]));
            K_WAIT(0, 2) K_WAIT(0, 3)
            ARRIVED()
            K_FMA(2) K_FMA(3) K_RED(0)
            np += 2;
            ADVANCE(false)
        }
#pragma unroll 1
        while (u < 2 * NU) {
            GRAB(2)
            mg = LDV(&sm.merged);
            LINE()
            const unsigned vr_ = LDV(&sm.vrdy);
            CBAR();
            const unsigned short* wp = &sm.wv[went(u - NU) + (QUARTER(u - NU) ? rg & 7 : rg) * 4];
            v4i pw = *(const v4i*)wp;
            if (first && !((__builtin_amdgcn_readfirstlane(vr_) >> (u - NU)) & 1)) { while (!((lds_ld(&sm.vrdy) >> (u - NU)) & 1)) SLEEP(1); CBAR(); pw = *(const v4i*)wp; }
            const double w0 = (short)pw.x, w1 = pw.x >> 16, w2 = (short)pw.y, w3 = pw.y >> 16, w4 = (short)pw.z, w5 = pw.z >> 16, w6 = (short)pw.w, w7 = pw.w >> 16;
            asm volatile("" :: "v"(w0), "v"(w1), "v"(w2), "v"(w3), "v"(w4), "v"(w5), "v"(w6), "v"(w7));
            PLAN()
            v2f hl; unsigned tl8, rowl;
            if (none) { hl = *(const v2f*)&sm.wv[went(NU - 1) + 128]; tl8 = LDV(&sm.tag_lo) << 24; rowl = (lds_ld(&sm.utab[2 * NU - 1]) & 255) * (NSPLIT * 512); asm volatile("" : "+v"(hl), "+v"(tl8)); }
            WAITT(0, 0, 6); V_FMA(0, w0, w1) V_PIN()
            WAITT(1, 1, 4); V_FMA(1, w2, w3) V_PIN()
            WAITT(2, 2, 2); V_FMA(2, w4, w5) V_PIN()
            WAITT(3, 3, 0);
            np += 2;
            if (none) {
                V_FMA(3, w6, w7)
                if (u != 2 * NU - 1) { FLUSH(true) break; }
                mg = __builtin_amdgcn_readfirstlane(mg);
                while (u >= mg + NOUT) { SLEEP(1); mg = lds_ld(&sm.merged); }
                const unsigned aa = lds_addr(&sm.acc[(2 * NU - 1) % NOUT][lane]);
                double a0, a1, a2; unsigned cn; u64 ex_;
                asm volatile("ds_add_f64 %5, %6\n\tds_add_f64 %5, %7 offset:512\n\tds_add_f64 %5, %8 offset:1024\n\t"
                             "s_mov_b64 %4, exec\n\ts_mov_b64 exec, 1\n\tds_add_rtn_u32 %3, %9, %10\n\ts_mov_b64 exec, %4\n\t"
                             "ds_read_b64 %0, %5\n\tds_read_b64 %1, %5 offset:512\n\tds_read_b64 %2, %5 offset:1024\n\ts_waitcnt lgkmcnt(0)"
                             : "=&v"(a0), "=&v"(a1), "=&v"(a2), "=&v"(cn), "=&s"(ex_) : "v"(aa), "v"(acc[0]), "v"(acc[1]), "v"(acc[2]), "v"(lds_addr(&sm.cnt[(2 * NU - 1) % NOUT])), "v"(np) : "memory");
                const float n0 = (float)(a0 - (double)hl.y) * hl.x, n1 = (float)(a1 - (double)hl.y) * hl.x, n2 = (float)(a2 - (double)hl.y) * hl.x;
                const unsigned e2 = max(__float_as_uint(fmaxf(fmaxf(fabsf(n0), fabsf(n1)), fabsf(n2))) >> 23, 14u);
                const float f2 = __uint_as_float((268u - e2) << 23);
                const v2u r = {__builtin_bit_cast(unsigned, __builtin_amdgcn_cvt_pk_i16((int)rintf(n0 * f2), (int)rintf(n1 * f2))),
                               __builtin_bit_cast(unsigned, __builtin_amdgcn_cvt_pk_i16((int)rintf(n2 * f2), 0)) | e2 << 16 | tl8};
                const __amdgpu_buffer_rsrc_t rsrc = __builtin_amdgcn_make_buffer_rsrc(ws + WS_PART + split * 512, 0, NRW * NSPLIT * 512 - split * 512, 0x00020000);
                asm volatile("" :: "v"(r));
                if (__builtin_amdgcn_readfirstlane(cn + np) == 32) {
                    __builtin_amdgcn_raw_buffer_store_b64(r, rsrc, lane * 8, rowl, SC1);
#pragma unroll
                    for (int j = 0; j < NCP - 1; j++) __builtin_amdgcn_raw_buffer_store_b64(r, rsrc, lane * 8, (rowl & 15 * NSPLIT * 512 ^ NSPLIT * 512) + (NRR + j * GSZ) * NSPLIT * 512, SC1);
                }
                break;
            }
            ARRIVED()
            V_FMA(3, w6, w7)
            ADVANCE(true)
        }
    } else {
    u64 E1;
    if (wave == NSW) {
        unsigned ref;
        const unsigned sidx = (blockIdx.x & 7) | ((blockIdx.x >> 7) << 3);
#define SAMPLE(K) ((((blockIdx.x >> 3) - (blockIdx.x & 7) - (K) * 5) & 15) == 0)
#define BTAG(K) ((btag + (K)) & 0xfffffu)
#define POST(K, C) { if (lane == 0) __hip_atomic_store((u64*)(ws + (bd + (K) * RST + (sidx >> 3) * 256 + (sidx & 7) * 8)), (u64)(C) | ((u64)(((C) - ref) | BTAG(K) << 12) << 32), __ATOMIC_RELAXED, __HIP_MEMORY_SCOPE_AGENT); }
        const unsigned own = lds_ld(&sm.line);
        if (SAMPLE(0) && lane < NTRY) __hip_atomic_store((unsigned*)(ws + WS_HELLO + lane * 768) + sidx, own, __ATOMIC_RELAXED, __HIP_MEMORY_SCOPE_AGENT);
        const u64 E = __hip_atomic_load((const u64*)(ws + WS_EPOCH), __ATOMIC_RELAXED, __HIP_MEMORY_SCOPE_AGENT);
        E1 = E + 1;
        if (lane == 0) { sm.tag_lo = (unsigned)E1; sm.tag_hi = (unsigned)(E1 >> 32); CBAR(); lds_st(&sm.tag_rdy, 1u); }
        const unsigned btag = __builtin_amdgcn_readfirstlane((unsigned)E1) * 61u, bd = WS_BOARD + (btag & 7) * (NRND * RST);
        ref = own;
        int off = 0;
        {
            unsigned tries = 0, cur = own, tl = own;
            bool news = false;
#pragma unroll 1
            while (!news || cur != ref) {
                const unsigned now = NOW();
                if (tries < NTRY && (int)(now - own) >= (int)(TA + tries * TA2)) {
                    v16u x;
                    asm volatile("s_load_dwordx16 %0, %1, %2\n\ts_waitcnt lgkmcnt(0)" : "=&s"(x) : "s"(ws), "s"(__builtin_amdgcn_readfirstlane(WS_HELLO + tries * 768)) : "memory");
                    unsigned bad = 0; int sum = 0, dif = 0;
#pragma unroll
                    for (int i = 0; i < 16; i++) { bad |= x[i] - own + 512; sum += x[i] - x[0]; dif += i & 4 ? x[i] : -x[i]; }
                    tries++;
                    if (bad < 1024) { const unsigned com = x[0] + (sum >> 4) + abs(dif) * (8 - KC) / 128; ref = x[0] + ((sum + (blockIdx.x & 4 ? dif : -dif)) >> 4); off = ref - com;
                        tries = NTRY; tl = NOW(); if (lane == 0) lds_st(&sm.aline, com); }
                    continue;
                }
                if (!news && lds_ld(&sm.utab[31]) != ~0u) { news = true; CBAR(); if (cur == ref) tl = now; ref = lds_ld(&sm.aline) + off; tries = NTRY; }
                const int d = (now - tl) >> CSH;
                if (cur != ref && d) { tl += d << CSH; cur += min(max((int)(ref - cur), -d), d); if (lane == 0) lds_st(&sm.line, cur); }
                SLEEP(2)
            }
        }
        ref -= off;
        const unsigned ref0 = ref;
        const unsigned lead = (blockIdx.x >> 3) == 9 ? 0 : LEAD;
        unsigned posted = ref, lagf = ref, tpost = ref + TB1, tpoll = tpost + RDD + lead, kw = 0, kr = 0, cur = ref + off, khold = 0;
        int o = off; bool held = false;
#pragma unroll 1
        for (;;) {
            const unsigned p = lds_ld(&sm.grab);
            if (p >= NPC) break;
            lagf += (int)(lds_ld(&sm.late) - lagf) >> 1;
            { const unsigned l = lds_ld(&sm.line); if (l != cur) { cur = l; khold = kw; held = true; } }
            const unsigned now = NOW();
            if (o) { o = off * min(max((int)(ref0 + TC0 + (1 << TCS) - now), 0), 1 << TCS) >> TCS; if (!held && cur != ref + o) { cur = ref + o; if (lane == 0) lds_st(&sm.line, cur); } }
            if (kw < NRND && (int)(now - tpost) >= 0) {
                const unsigned c = ref + min((p >= PSL0 && (int)(lagf - cur) > LHI ? lagf - LOK : cur) - o - ref, 4000u);
                if (SAMPLE(kw) && (c != ref || (ref != posted && (sidx & 7) == (kw & 7)))) POST(kw, c)
                posted = ref;
                tpost += PER; kw++;
            }
            if (kr < NRND && (int)(now - tpoll) >= 0) {
                v16u xa, xb;
                asm volatile("s_load_dwordx16 %0, %2, %3\n\ts_load_dwordx16 %1, %2, %4\n\ts_waitcnt lgkmcnt(0)" : "=&s"(xa), "=&s"(xb)
                             : "s"(ws), "s"(__builtin_amdgcn_readfirstlane(bd + kr * RST)), "s"(__builtin_amdgcn_readfirstlane(bd + kr * RST + 256)) : "memory");
                int nb = 0, sg = 0;
#pragma unroll
                for (int i = 0; i < 16; i++) {
                    const unsigned lo = i < 8 ? xa[2 * i & 15] : xb[2 * i & 15], hi = i < 8 ? xa[(2 * i + 1) & 15] : xb[(2 * i + 1) & 15];
                    if (hi >> 12 == BTAG(kr) && lo - ref0 < 8000) { nb = max(nb, (int)(lo - (hi & 4095u) - ref)); sg += hi & 4095u; }
                }
                ref += nb + (sg >> 4);
                if (cur != ref + o && (kr >= khold || (int)(ref + o - cur) > 0)) { cur = ref + o; held = false; if (lane == 0) lds_st(&sm.line, cur); }
                tpoll += PER; kr++;
            }
            SLEEP(2)
        }
    } else if (wave == NSW + 1 || wave == NSW + 2) {
        const int ql = lane < 48 ? lane : 47;
        unsigned c1, c2, s1, s2;
#define QT_LOADS() { int qo_ = ql; asm volatile("" : "+v"(qo_)); \
                     c1 = ((const unsigned*)q_cos)[qo_]; c2 = ((const unsigned*)q_cos)[48 + qo_]; s1 = ((const unsigned*)q_sin)[qo_]; s2 = ((const unsigned*)q_sin)[48 + qo_]; }
        unsigned q1, q2, lkz, lks, lvz, lvs;
        const unsigned sofs = R8(((unsigned)lane >> 1) & 7) * 2 + (lane & 1);
#define UNIT_LOADS(HEAD, GRP) { \
        q1 = ((const unsigned*)(q + (size_t)(HEAD) * DH))[ql]; q2 = ((const unsigned*)(q + (size_t)(HEAD) * DH))[48 + ql]; \
        const size_t so = (size_t)(HEAD) * (SKV / 2) + (GRP) * 256 + sofs; \
        lkz = *((const unsigned*)k_zeros + so); lks = *((const unsigned*)k_scales + so); \
        lvz = *((const unsigned*)v_zeros + so); lvs = *((const unsigned*)v_scales + so); }
#define UNIT_FILL(SL, U) { \
            v4f qq; \
            qq.x = bfr(bfr(bflo(q1) * bflo(c1)) + bfr(-bflo(q2) * bflo(s1))); \
            qq.y = bfr(bfr(bflo(q2) * bflo(c2)) + bfr(bflo(q1) * bflo(s2))); \
            qq.z = bfr(bfr(bfhi(q1) * bfhi(c1)) + bfr(-bfhi(q2) * bfhi(s1))); \
            qq.w = bfr(bfr(bfhi(q2) * bfhi(c2)) + bfr(bfhi(q1) * bfhi(s2))); \
            *(v2u*)&sm.q[SL][2 * ql] = (v2u){(__float_as_uint(qq.x) >> 16) | __float_as_uint(qq.y), (__float_as_uint(qq.z) >> 16) | __float_as_uint(qq.w)}; \
            *(v4f*)&sm.kz[SL][2 * lane] = (v4f){bflo(lkz), bflo(lks), bfhi(lkz), bfhi(lks)}; \
            *(v4f*)&sm.vz[SL][2 * lane] = (v4f){bflo(lvz), bflo(lvs), bfhi(lvz), bfhi(lvs)}; \
            CBAR(); \
            if (lane == 0) lds_st(&sm.rdy[SL], (U) + 1); \
            CBAR(); }
        const unsigned dw = NSW + 2 - wave;
        QT_LOADS()
        UNIT_LOADS(head0 + dw * (GSZ + 1 - 2 * side), lane >> 4)
#pragma unroll 1
        for (unsigned u = dw;;) {
            while (u >= lds_ld(&sm.merged) + NIN) SLEEP(4);
            CBAR();
            UNIT_FILL(u % NIN, u)
            if (u - 1 < 12) {
                const unsigned x_ = (blockIdx.x >> 3) + 32 * u - 8; unsigned dm;
                const char* p_ = x_ < 384 ? (const char*)(x_ < 192 ? qk : qv) + (size_t)(x_ < 192 ? x_ : x_ - 192) * (2u << 20) + (x_ * 8 + (blockIdx.x & 7)) * 640 : (const char*)out + (x_ - 384) * 4096;
                while ((int)(NOW() - lds_ld(&sm.line)) < TSD) SLEEP(2)
                asm volatile("s_load_dword %0, %1, 0x0\n\ts_waitcnt lgkmcnt(0)" : "=&s"(dm) : "s"(p_) : "memory");
            }
            u += 2;
            if (u >= NU) break;
            if (u == 4) {
                const unsigned tpf = tdue(PSF - 1) + TFO;
                { int d; while ((d = (int)(lds_ld(&sm.line) + tpf - NOW())) > 0 && d < 3000) SLEEP(1) }
#define PF3(P) asm volatile("global_load_dwordx4 a[0:3], %0, %1\n\tglobal_load_dwordx4 a[0:3], %0, %1 offset:1024\n\tglobal_load_dwordx4 a[0:3], %0, %1 offset:2048" \
                            :: "v"(64 * SKV * 2 + blockIdx.x * 3072 + lane * 16), "s"(P) : "a0", "a1", "a2", "a3")
                PF3(k_zeros); PF3(k_scales); PF3(v_zeros); PF3(v_scales);
                asm volatile("global_load_dword a0, %0, %1" :: "v"(blockIdx.x * (DH * 2) + ql * 8), "s"(q) : "a0");
            }
            const unsigned tin = tdue(first_of(u) - (QUARTER(u) ? 24 : 0) - LEADP + (split & 7) * DPH);
            { int d; while ((d = (int)(lds_ld(&sm.line) + tin - NOW())) > 0 && d < 3000) SLEEP(4) }
            unsigned hd;
            while ((hd = lds_ld(&sm.utab[u])) == ~0u) SLEEP(2)
            UNIT_LOADS(hd & 255, QUARTER(u) ? rank & 3 : lane >> 4)
        }
        while (lds_ld(&sm.tag_rdy) == 0) SLEEP(1);
        CBAR();
        E1 = (u64)lds_ld(&sm.tag_lo) | ((u64)lds_ld(&sm.tag_hi) << 32);
        if (wave == NSW + 1 && blockIdx.x == 0) {
            for (;;) {
                bool ok = true;
#pragma unroll
                for (int i = 0; i < NH / 64; i++) ok &= __hip_atomic_load((const u64*)(ws + WS_POST) + i * 64 + lane, __ATOMIC_RELAXED, __HIP_MEMORY_SCOPE_AGENT) >> 24 == (E1 << 24) >> 24;
                if (__builtin_amdgcn_ballot_w64(ok) == ~0ull) break;
                SLEEP(64);
            }
            if (lane == 0) __hip_atomic_store((u64*)(ws + WS_EPOCH), E1, __ATOMIC_RELAXED, __HIP_MEMORY_SCOPE_AGENT);
        }
        if (wave == NSW + 2) {
            const unsigned t16 = (unsigned)E1 & 0xffffu, t16b = ((unsigned)E1 + ((unsigned)E1 >> 16)) & 0xffffu;
            const __amdgpu_buffer_rsrc_t rsrc = __builtin_amdgcn_make_buffer_rsrc(ws + WS_SWAP + (blockIdx.x ^ 4) * 5120, 0, 5120, 0x00020000);
            const unsigned swp = lds_ld(&sm.swp);
            if (swp) {
                unsigned use = 0;
#pragma unroll
                for (int i = 0; i < 5; i++) use |= ((swp >> wunit(i * 64 + lane)) & 1) << i;
                const unsigned tg = tdue(PSG - 1) + (lds_ld(&sm.gap) & 0xffffu);
                while ((int)(NOW() - lds_ld(&sm.line) - tg) < 0) SLEEP(2)
                for (;;) {
                    v4u d[5]; bool ok = true;
#pragma unroll
                    for (int i = 0; i < 5; i++) d[i] = __builtin_amdgcn_raw_buffer_load_b128(rsrc, (use >> i) & 1 ? (i * 64 + lane) * 16 : 0x40000000, 0, SC1 | 2);
#pragma unroll
                    for (int i = 0; i < 5; i++) if ((use >> i) & 1) {
                        ok &= d[i].y >> 16 == t16 && d[i].w >> 16 == t16b;
                        unsigned* w = (unsigned*)&sm.wv[144 * NMIX + (i * 64 + lane) * 6];
                        w[0] = d[i].x; w[1] = (d[i].y & 0xffffu) | (d[i].z << 16); w[2] = (d[i].z >> 16) | (d[i].w << 16);
                    }
                    if (__builtin_amdgcn_ballot_w64(ok) == ~0ull) break;
                    SLEEP(32)
                }
                CBAR();
                LDS_OR(&sm.vrdy, swp)
            }
        }
    } else {
        const unsigned eu = lane >= NU ? lane - NU : lane, win = min(first_of(eu) >> 5, 15u), pair = QUARTER(eu) ? 2 * (eu - WQ) + (rank >> 2) : rank;
        const unsigned rot2 = ((const unsigned char*)ROT)[win * 8 + pair];
#define UENT(BIT) (win * 16 + 2 * pair + ((BIT) ^ side) | (QUARTER(eu) ? (rank - (eu - WQ)) * 8 & 31 : (rot2 >> ((BIT) ^ side) * 4 & 15) * 2) << 8)
        unsigned far = 0;
        const unsigned hp = (blockIdx.x & 1) * NH + (blockIdx.x >> 1) * 4 + 2;
        if (blockIdx.x < 144) {
            const char* pb = (blockIdx.x < 128 ? (const char*)(hp >= NH ? qv : qk) + (size_t)(hp & (NH - 1)) * (SKV * DH * 4) + SKV * DH * 2
                                               : (const char*)ws + WS_PART + (8u << 20) + (blockIdx.x & 15) * 65536) + ((NOW() * 0x9e3779b1u) >> 26) * 128;
            unsigned mn[2] = {~0u, ~0u};
#pragma unroll 1
            for (unsigned i = 1; i < 2 * NPRB + 2; i++) {
                u64 t0, t1; unsigned dm;
                asm volatile("s_memtime %0\n\ts_waitcnt lgkmcnt(0)\n\ts_load_dword %2, %3, 0x0\n\ts_waitcnt lgkmcnt(0)\n\ts_memtime %1\n\ts_waitcnt lgkmcnt(0)"
                             : "=&s"(t0), "=&s"(t1), "=&s"(dm) : "s"(pb + i * 8192) : "memory");
                if (i == 1) asm volatile("global_load_dword a0, %0, %1 nt" :: "v"(0), "s"(pb) : "a0");
                const unsigned t = (unsigned)(t1 - t0) < 330 || i == 1 ? ~0u : (unsigned)(t1 - t0);
                if (i & 1) mn[1] = min(mn[1], t); else mn[0] = min(mn[0], t);
            }
            far = mn[0] > mn[1];
        }
        if (lane < NMIX) lds_st(&sm.utab[lane], UENT(lane & 1));
        while (lds_ld(&sm.tag_rdy) == 0) SLEEP(1);
        CBAR();
        E1 = (u64)lds_ld(&sm.tag_lo) | ((u64)lds_ld(&sm.tag_hi) << 32);
        {
            const unsigned own = lds_ld(&sm.line), tg = (unsigned)E1;
            if (lane == 0) __hip_atomic_store((u64*)(ws + WS_POST) + blockIdx.x, E1 << 24 | min(lds_ld(&sm.slow), 4000u) << 1 | ((far ^ (blockIdx.x >> 2) ^ (blockIdx.x < 128 ? 0x96 >> (hp & 7) : 0)) & 1), __ATOMIC_RELAXED, __HIP_MEMORY_SCOPE_AGENT);
            const __amdgpu_buffer_rsrc_t nrs = __builtin_amdgcn_make_buffer_rsrc(ws + WS_NEWS, 0, 8 * 1280, 0x00020000);
            if (blockIdx.x == MASTER) {
                while ((int)(NOW() - own) < TMAP) SLEEP(2)
                const unsigned a = tg << 24, b = (unsigned)(E1 >> 8);
                v4u x; v2u z; u64 v0, v1;
#pragma unroll 1
                for (int i = 0; i < 8; i++) {
                    x = __builtin_amdgcn_raw_buffer_load_b128(__builtin_amdgcn_make_buffer_rsrc(ws + WS_POST, 0, 1024, 0x00020000), lane * 16, 0, SC1);
                    z = __builtin_amdgcn_raw_buffer_load_b64(__builtin_amdgcn_make_buffer_rsrc(ws + WS_POST + 1024, 0, 128, 0x00020000), lane * 8, 0, SC1);
                    v0 = __builtin_amdgcn_ballot_w64(((x.x ^ a) >> 24 | (x.y ^ b)) == 0); v1 = __builtin_amdgcn_ballot_w64(((x.z ^ a) >> 24 | (x.w ^ b)) == 0);
                    if (__builtin_popcountll(v0) + __builtin_popcountll(v1) >= MINV) break;
                    SLEEP(8)
                }
                const u64 m0 = __builtin_amdgcn_ballot_w64(x.x & 1) & v0, m1 = __builtin_amdgcn_ballot_w64(x.z & 1) & v1;
                const int s0 = lane < 43 ? min(max(lane - VW, 4 * NMIX), 42 - 2 * VW) : min(max(lane - VW, 43), 63 - 2 * VW);
#define PC(M) __builtin_popcount((unsigned)((M) >> s0) & ((2u << 2 * VW) - 1))
                const u64 g0 = __builtin_amdgcn_ballot_w64(2 * PC(m0) > PC(v0)), g1 = __builtin_amdgcn_ballot_w64(2 * PC(m1) > PC(v1));
                const float sl = wave_sum(((v0 >> lane & 1) ? (float)(x.x >> 1 & 4095u) : 0.f) + ((v1 >> lane & 1) ? (float)(x.z >> 1 & 4095u) : 0.f)) / ((8.f - PRE0) * (__builtin_popcountll(v0) + __builtin_popcountll(v1)) + 1.f);
                const unsigned al = lds_ld(&sm.aline), mean = (al ? al : own) + (int)fmaxf(sl - SL0, 0.f);
                const bool vz = lane < 16 && ((z.x ^ a) >> 24 | (z.y ^ b)) == 0;
                const unsigned gw = 2 * __builtin_popcountll(__builtin_amdgcn_ballot_w64(vz && (z.x & 1))) > __builtin_popcountll(__builtin_amdgcn_ballot_w64(vz));
                const unsigned k = lane & 3;
                if (lane < 32 && k < 3) __builtin_amdgcn_raw_buffer_store_b128((v4u){k == 0 ? (unsigned)g0 : k == 1 ? (unsigned)g1 : mean, tg, k == 0 ? (unsigned)(g0 >> 32) : k == 1 ? (unsigned)(g1 >> 32) : gw, tg},
                                                                              nrs, (lane >> 2) * 1280 + k * 16, 0, SC1);
            }
            while ((int)(NOW() - own) < TMAP + TRES) SLEEP(2)
            v4u y;
            for (;;) {
                y = __builtin_amdgcn_raw_buffer_load_b128(nrs, lane < 3 ? (blockIdx.x & 7) * 1280 + lane * 16 : 0x40000000, 0, SC1);
                if (((unsigned)__builtin_amdgcn_ballot_w64(y.y == tg && y.w == tg) & 7u) == 7u) break;
                SLEEP(8)
            }
            const u64 f0 = (unsigned)__builtin_amdgcn_readlane(y.x, 0) | (u64)(unsigned)__builtin_amdgcn_readlane(y.z, 0) << 32, f1 = (unsigned)__builtin_amdgcn_readlane(y.x, 1) | (u64)(unsigned)__builtin_amdgcn_readlane(y.z, 1) << 32;
            if (lane == 2) { sm.aline = y.x - own + 500 < 1000 ? y.x : own; sm.chead = (blockIdx.x & ~5u) | ((blockIdx.x >> 2 ^ y.z) & 1) | (blockIdx.x & 1) << 2; }
            unsigned bit = ((unsigned)((lane >= NU ? f1 : f0) >> (4 * win + (pair >> 1))) ^ (0x66 >> pair)) & 1;
            if (eu < NMIX) bit = eu & 1;
            const u64 bm = __builtin_amdgcn_ballot_w64(bit);
            const unsigned nsw = __builtin_popcountll((f0 ^ f1) >> (4 * NMIX));
            if (lane == 0) { sm.swp = (unsigned)(bm ^ (bm >> NU)) & ((1u << NU) - 1); lds_st(&sm.gap, nsw ? (TG0 + TG1 * nsw / 8) * 0x10001u + ((TH0 + TH1 * nsw / 8) << 16) : 0u); }
            CBAR();
            lds_st(&sm.utab[lane], UENT(bit));
        }
        const unsigned swp = lds_ld(&sm.swp);
        const unsigned T = lds_ld(&sm.tag_lo);
        const unsigned t15 = T & 0x7fffu;
        const __amdgpu_buffer_rsrc_t rsrc = __builtin_amdgcn_make_buffer_rsrc(ws + WS_PART + split * 512, 0, NRW * NSPLIT * 512 - split * 512, 0x00020000);
        v2u* recs = (v2u*)&sm.cs[0][0][0];
#pragma unroll
        for (int i = 0; i < NOUT * 3; i++) (&sm.acc[0][0])[i * 64 + lane] = 0.0;
        CBAR();
        unsigned vut = sm.utab[NU + (lane & 31)] & 255;
        if (QUARTER((unsigned)lane) && (rank & 3)) vut = NH - 16 + (rank & 3) * 16 + ((vut & 15) ^ 1);
        __builtin_amdgcn_s_setprio(3);
        unsigned po[NU / 2];
#pragma unroll
        for (int k = 0; k < NU / 2; k++) po[k] = __builtin_amdgcn_ds_bpermute((2 * k + (lane >> 5)) * 4, vut) * (NSPLIT * 512) + (lane & 31) * 16;
        const u64 rb_ = (u64)(ws + WS_PART + split * 512);
        const v4u rs_ = {(unsigned)rb_, (unsigned)(rb_ >> 32) & 0xffffu, NRW * NSPLIT * 512 - split * 512, 0x00020000u};
#pragma unroll 1
        for (unsigned u = 0; u < 2 * NU - 1; u++) {
            const unsigned e = u % NU, so = u % NOUT;
            if (u >= NU) while (!((lds_ld(&sm.vrdy) >> e) & 1)) SLEEP(4)
            CBAR();
            const v4f h = *(const v4f*)&sm.wv[went(e) + (QUARTER(e) ? 32 : 128)];
            while (lds_ld(&sm.cnt[so]) != (QUARTER(e) ? 8 : 32)) SLEEP(1)
            CBAR();
            const double a0 = sm.acc[so][lane], a1 = sm.acc[so][64 + lane], a2 = sm.acc[so][128 + lane];
            if (u < NU) {
                float2 lg = *(const float2*)&sm.ent[so][2 * lane];
                if (QUARTER(e) && lane >> 4 != (rank & 3)) lg = make_float2(-INFINITY, -INFINITY);
                const v4f zs = *(const v4f*)&sm.vz[u % NIN][2 * lane];
                const float m = wave_max(fmaxf(lg.x, lg.y));
                const float p0 = __expf(lg.x - m), p1 = __expf(lg.y - m);
                const float x0 = p0 * zs.y, x1 = p1 * zs.w;
                const unsigned eb = max(__float_as_uint(wave_max(fmaxf(fabsf(x0), fabsf(x1)))) >> 23, 14u);
                const float f = __uint_as_float((268u - eb) << 23);
                const float w0 = fminf(fmaxf(rintf(x0 * f), -32767.f), 32767.f), w1 = fminf(fmaxf(rintf(x1 * f), -32767.f), 32767.f);
                const float den = wave_sum(p0 + p1);
                const float bz = wave_sum(fmaf(w0, zs.x, w1 * zs.z));
                const float L = fmaf(m, 1.4426950408889634f, __builtin_amdgcn_logf(den));
                unsigned* wd = (unsigned*)(((swp >> u) & 1 ? sm.wo : sm.wv) + went(u));
                if (!QUARTER(u) || lane >> 4 == (rank & 3)) wd[QUARTER(u) ? lane & 15 : lane] = ((unsigned)(int)w0 & 0xffffu) | ((unsigned)(int)w1 << 16);
                if (lane == 0) *(v4f*)&wd[QUARTER(u) ? 16 : 64] = (v4f){ldexpf(1.f / den, (int)eb - 141), bz, L, 0.f};
                CBAR();
                if (!((swp >> u) & 1)) LDS_OR(&sm.vrdy, 1u << u)
            } else {
                const float n0 = (float)(a0 - (double)h.y) * h.x, n1 = (float)(a1 - (double)h.y) * h.x, n2 = (float)(a2 - (double)h.y) * h.x;
                const unsigned e2 = max(__float_as_uint(wave_maxv(fmaxf(fmaxf(fabsf(n0), fabsf(n1)), fabsf(n2)))) >> 23, 14u);
                const float f2 = __uint_as_float((268u - e2) << 23);
                const unsigned m01 = __builtin_bit_cast(unsigned, __builtin_amdgcn_cvt_pk_i16((int)rintf(n0 * f2), (int)rintf(n1 * f2)));
                const unsigned m2 = __builtin_bit_cast(unsigned, __builtin_amdgcn_cvt_pk_i16((int)rintf(n2 * f2), 0));
                const unsigned bit = ((lane & 1 ? e2 : __float_as_uint(h.z)) >> (lane >> 1)) & 1u;
                recs[e * 64 + lane] = (v2u){m01, m2 | bit << 16 | t15 << 17};
                CBAR();
                sm.acc[so][lane] = 0.0; sm.acc[so][64 + lane] = 0.0; sm.acc[so][128 + lane] = 0.0;
            }
            CBAR();
            if (lane == 0) { lds_st(&sm.cnt[so], 0u); CBAR(); lds_st(&sm.merged, u + 1); }
            if (u == NU - 1 && swp) {
                wave_sync();
                const __amdgpu_buffer_rsrc_t srs = __builtin_amdgcn_make_buffer_rsrc(ws + WS_SWAP + blockIdx.x * 5120, 0, 5120, 0x00020000);
                v4u d[5]; unsigned o[5];
#pragma unroll
                for (int i = 0; i < 5; i++) {
                    const unsigned* w = (const unsigned*)&sm.wo[144 * NMIX + (i * 64 + lane) * 6];
                    const unsigned d0 = w[0], d1 = w[1], d2 = w[2];
                    d[i] = (v4u){d0, (d1 & 0xffffu) | (T << 16), (d1 >> 16) | (d2 << 16), (d2 >> 16) | ((T + (T >> 16)) << 16)};
                    o[i] = (swp >> wunit(i * 64 + lane)) & 1 ? (i * 64 + lane) * 16 : 0x40000000;
                }
                while ((int)(NOW() - lds_ld(&sm.line)) < (int)tdue(PSW) + TWO) SLEEP(1)
#pragma unroll
                for (int i = 0; i < 5; i++) __builtin_amdgcn_raw_buffer_store_b128(d[i], srs, o[i], 0, SC1 | 2);
            }
        }
        wave_sync();
#pragma unroll
        for (int k = 0; k < NU / 2; k++) asm volatile("ds_read_b128 a[%c0:%c0+3], %1 offset:%2" :: "i"(16 + 4 * k), "v"(lds_addr(recs) + lane * 16), "i"(k * 1024) : "memory");
        asm volatile("s_waitcnt lgkmcnt(0)" ::: "memory");
        while (!((lds_ld(&sm.vrdy) >> (NU - 1)) & 1)) SLEEP(4)
        CBAR();
        const v2u hw_ = {*(const unsigned*)&sm.wv[went(NU - 1) + 132], T};
        const unsigned hrow = (NRW - GSZ + (__builtin_amdgcn_readlane(vut, NU - 1) & 15 ^ 1)) * (NSPLIT * 512);
        while (lds_ld(&sm.grab) < NPC - PUBP) SLEEP(1)
        if (lane < 16) __builtin_amdgcn_raw_buffer_store_b64(hw_, rsrc, lane * 8, hrow, SC1);
#define PUB(K) asm volatile("buffer_store_dwordx4 a[%c0:%c0+3], %1, %2, 0 offen sc1" :: "i"(16 + 4 * (K)), "v"(po[K]), "s"(rs_) : "memory");
        PUB(WQ / 2) PUB(WQ / 2 + 1)
        { const unsigned t_ = TLAST() + PDEL; GATE(t_) }
        PUB(0) PUB(1) PUB(2) PUB(3) PUB(4) PUB(7) PUB(8)
        return;
    }
    }
    __builtin_amdgcn_s_setprio(0);
    unsigned c;
#define ADDRTN(P) { u64 ex_; asm volatile("s_mov_b64 %0, exec\n\ts_mov_b64 exec, 1\n\tds_add_rtn_u32 a15, %1, a13\n\ts_mov_b64 exec, %0" : "=&s"(ex_) : "v"(lds_addr(P)) : "a15", "memory"); }
#define ADDED(V) { unsigned t_; asm volatile("s_waitcnt lgkmcnt(0)\n\tv_accvgpr_read_b32 %1, a15\n\ts_nop 0\n\tv_readfirstlane_b32 %0, %1\n\ts_nop 1" : "=s"(V), "=v"(t_)); }
    asm volatile("v_accvgpr_write_b32 a13, 1\n\ts_nop 1" ::: "a13");
    ADDRTN(&sm.cidx) ADDED(c)
    if (c < NSW) {
        while (lds_ld(&sm.grab) < NPC + 2) SLEEP(4)
        const unsigned head = lds_ld(&sm.chead), T = lds_ld(&sm.tag_lo), tl = TLAST();
        const u64 rb_ = (u64)(ws + WS_PART);
        const v4u rs_ = {(unsigned)rb_, (unsigned)(rb_ >> 32) & 0xffffu, NRW * NSPLIT * 512, 0x00020000u};
        const unsigned ro = head * (NSPLIT * 512) + c * 1024, rx = (NH + (head & 15 ^ 1)) * (NSPLIT * 512) + c * 1024;
#define POLL(S, O) asm volatile("buffer_load_dwordx4 a[%c0:%c0+3], %1, %2, %3 offen sc1" :: "i"(16 + 4 * (S)), "v"(lane * 16), "s"(rs_), "s"(O) : "memory");
#define FETCH(X, S, N) asm volatile("s_waitcnt vmcnt(%c5)\n\tv_accvgpr_read_b32 %0, a[%c4]\n\tv_accvgpr_read_b32 %1, a[%c4+1]\n\tv_accvgpr_read_b32 %2, a[%c4+2]\n\tv_accvgpr_read_b32 %3, a[%c4+3]" \
                                    : "=v"((X).x), "=v"((X).y), "=v"((X).z), "=v"((X).w) : "i"(16 + 4 * (S)), "i"(N));
        float o0, o1, o2;
        if (head >= NH - GSZ) {
            __builtin_amdgcn_s_setprio(3);
            if (c == 0) {
                v2u hd;
                GATE(tl + THD)
                for (;;) {
                    hd = __builtin_amdgcn_raw_buffer_load_b64(__builtin_amdgcn_make_buffer_rsrc(ws + WS_PART + (NRW - GSZ + (head & 15 ^ 1)) * (NSPLIT * 512), 0, NSPLIT * 512, 0x00020000), (lane & 15) * 512, 0, SC1);
                    if (__builtin_amdgcn_ballot_w64(hd.y == T) == ~0ull) break;
                    SLEEP(4)
                }
                const float w = __builtin_amdgcn_exp2f(__uint_as_float(hd.x) - row_max(__uint_as_float(hd.x)));
                if (lane < 16) sm.ent[0][lane] = w * __builtin_amdgcn_rcpf(row_sum(w));
                CBAR();
                if (lane == 0) lds_st(&sm.tabs, 0u);
            }
            while (lds_ld(&sm.tabs) || lds_ld(&sm.merged) <= (2 * NU - 1) / NOUT * NOUT) SLEEP(1)
            CBAR();
            const float cf = sm.ent[0][2 * c + (lane >> 5)];
            v4u x; bool got;
#define SEEN(J, N) { FETCH(x, J, N) got = __builtin_amdgcn_ballot_w64((((x.y ^ T << 24) | (x.w ^ T << 24)) >> 24) != 0) == 0; }
            GATE(tl + TCA) POLL(0, ro) GATE(tl + TCA + TCD) POLL(1, rx + 3 * GSZ * NSPLIT * 512) SEEN(0, 1)
            if (!got) SEEN(1, 0)
            while (!got) { POLL(2, rx + 4 * GSZ * NSPLIT * 512) SEEN(2, 0) SLEEP(0) }
            const double c0 = ldexpf(cf, (int)(x.y >> 16 & 255u) - 141), c1 = ldexpf(cf, (int)(x.w >> 16 & 255u) - 141);
#define SWADD(A, B) ({ const v2u a_ = __builtin_bit_cast(v2u, A), b_ = __builtin_bit_cast(v2u, B); \
                const auto l_ = __builtin_amdgcn_permlane32_swap(a_.x, b_.x, false, false); const auto h_ = __builtin_amdgcn_permlane32_swap(a_.y, b_.y, false, false); \
                __builtin_bit_cast(double, (v2u){l_[0], h_[0]}) + __builtin_bit_cast(double, (v2u){l_[1], h_[1]}); })
            const double s0 = SWADD(c0 * (double)(short)x.x, c1 * (double)(short)x.z), s1 = SWADD(c0 * (double)((int)x.x >> 16), c1 * (double)((int)x.z >> 16)),
                         s2 = SWADD(c0 * (double)(short)x.y, c1 * (double)(short)x.w);
            asm volatile("ds_add_f64 %0, %1\n\tds_add_f64 %0, %2 offset:512\n\tds_add_f64 %0, %3 offset:1024" :: "v"(lds_addr(&sm.acc[0][lane])), "v"(s0), "v"(s1), "v"(s2) : "memory");
            ADDRTN(&sm.cdone)
            const double a0 = sm.acc[0][lane], a1 = sm.acc[0][64 + lane], a2 = sm.acc[0][128 + lane];
            ADDED(c)
            o0 = a0; o1 = a1; o2 = a2;
        } else {
            const bool quart = head >> 4 == WQ;
            const unsigned t15 = T & 0x7fffu;
#define TAKE(K) { FETCH(x[K], K, 0) \
                bad |= (x[K].y >> 17 ^ t15) | (x[K].w >> 17 ^ t15); \
                const u64 bl = __builtin_amdgcn_ballot_w64((x[K].y & 0x10000u) != 0), be = __builtin_amdgcn_ballot_w64((x[K].w & 0x10000u) != 0); \
                L[K] = __uint_as_float(lane < 32 ? (unsigned)bl : (unsigned)(bl >> 32)); ex[K] = lane < 32 ? (unsigned)be : (unsigned)(be >> 32); \
                M = fmaxf(M, fmaxf(__uint_as_float((unsigned)bl), __uint_as_float((unsigned)(bl >> 32)))); }
#define SUMUP(K) { const float w_ = __builtin_amdgcn_exp2f(L[K] - M), c_ = ldexpf(w_, (int)(ex[K] & 255u) - 141); den += w_; \
                t[0] = fmaf(c_, (float)(short)x[K].x, t[0]); t[1] = fmaf(c_, (float)((int)x[K].x >> 16), t[1]); t[2] = fmaf(c_, (float)(short)x[K].y, t[2]); \
                t[3] = fmaf(c_, (float)(short)x[K].z, t[3]); t[4] = fmaf(c_, (float)((int)x[K].z >> 16), t[4]); t[5] = fmaf(c_, (float)(short)x[K].w, t[5]); }
            const unsigned tg = tl + (quart ? TRQ : TRD);
            GATE(tg)
            v4u x[4]; float L[4], M; unsigned ex[4];
            for (;;) {
                unsigned bad = 0; M = -3e38f;
                POLL(0, ro)
                if (quart) { POLL(1, rx) POLL(2, rx + GSZ * NSPLIT * 512) POLL(3, rx + 2 * GSZ * NSPLIT * 512) }
                __builtin_amdgcn_s_setprio(3);
                TAKE(0)
                if (quart) { TAKE(1) TAKE(2) TAKE(3) }
                if (__builtin_amdgcn_ballot_w64(bad != 0) == 0) break;
                SLEEP(2)
            }
            float t[6] = {0.f, 0.f, 0.f, 0.f, 0.f, 0.f}, den = 0.f;
            SUMUP(0)
            if (quart) { SUMUP(1) SUMUP(2) SUMUP(3) }
#define SWADF(A, B) ({ const auto l_ = __builtin_amdgcn_permlane32_swap(__float_as_uint(A), __float_as_uint(B), false, false); __uint_as_float(l_[0]) + __uint_as_float(l_[1]); })
            v4f* pb = (v4f*)((char*)&sm.cs[0][0][0] + 65536);
            float* pm = (float*)(pb + NSW * 64);
            pb[c * 64 + lane] = (v4f){SWADF(t[0], t[3]), SWADF(t[1], t[4]), SWADF(t[2], t[5]), SWADF(den, den)};
            if (lane == 0) pm[c] = M;
            CBAR();
            ADDRTN(&sm.cdone)
            ADDED(c)
            CBAR();
            if (c == NSW - 1) {
                const v4f ma = *(const v4f*)pm, mb = *(const v4f*)(pm + 4);
                const float mw[NSW] = {ma.x, ma.y, ma.z, ma.w, mb.x, mb.y, mb.z, mb.w};
                float Mx = mw[0];
#pragma unroll
                for (int w = 1; w < NSW; w++) Mx = fmaxf(Mx, mw[w]);
                v4f sum = {0.f, 0.f, 0.f, 0.f};
#pragma unroll
                for (int w = 0; w < NSW; w++) sum += pb[w * 64 + lane] * __builtin_amdgcn_exp2f(mw[w] - Mx);
                const float inv = __builtin_amdgcn_rcpf(sum.w);
                o0 = sum.x * inv; o1 = sum.y * inv; o2 = sum.z * inv;
            }
        }
        if (c == NSW - 1) {
            asm("" : "+v"(o0), "+v"(o1), "+v"(o2));
            __hip_bfloat16* o = out + (size_t)head * DH + (lane & 31) * 6 + (lane >> 5) * 3;
            o[0] = __float2bfloat16(o0); o[1] = __float2bfloat16(o1); o[2] = __float2bfloat16(o2);
        }
    }
}

extern "C" void kernel_launch(void* const* d_in, const int* in_sizes, int n_in,
                              void* d_out, int out_size, void* d_ws, size_t ws_size,
                              hipStream_t stream) {
    const unsigned short* q = (const unsigned short*)d_in[0];
    const int* qk = (const int*)d_in[1];
    const unsigned short* k_scales = (const unsigned short*)d_in[2];
    const unsigned short* k_zeros = (const unsigned short*)d_in[3];
    const int* qv = (const int*)d_in[4];
    const unsigned short* v_scales = (const unsigned short*)d_in[5];
    const unsigned short* v_zeros = (const unsigned short*)d_in[6];
    const unsigned short* q_cos = (const unsigned short*)d_in[7];
    const unsigned short* q_sin = (const unsigned short*)d_in[8];
    const unsigned short* k_cos = (const unsigned short*)d_in[9];
    const unsigned short* k_sin = (const unsigned short*)d_in[10];
    unsigned char* ws = (unsigned char*)d_ws;
    attn_kernel<<<NSPLIT * GSZ, NTHREADS, 0, stream>>>(q, qk, k_scales, k_zeros, qv, v_scales, v_zeros,
                                                       q_cos, q_sin, k_cos, k_sin, (__hip_bfloat16*)d_out, ws);
}
```

```cpp
#include <hip/hip_runtime.h>
#include <hip/hip_bf16.h>
#include <stdint.h>

#define DH 192
#define HALF 96
#define SKV 2048
#define NH 256
#define NSW 8
#define NWAVES (NSW + 4)
#define NTHREADS (64 * NWAVES)
#define BROWS 128
#define NSPLIT 16
#define GSZ 16
#define WQ 10
#define NU (NH / GSZ + 3)
#define NPC 1024
#define NRR (NH + 3 * GSZ)
#define NIN 8
#define NOUT 8
#define WALL (15 * 144 + 4 * 48)
#define SC1 16
#define NRND 48
#define RST 4352
#define TK 2
#define NMIX 3
#define TUNIT 311
#define T01 50
#define TMIX 7
#define TOFF 50
#define PSF 100
#define TGS 82
#define PSW (NPC / 2 + 28)
#define TG0 8
#define TG1 5
#define TWO -80
#define PSG (NPC / 2 + 60)
#define TH0 3
#define TH1 3
#define EARLY 10
#define LEADP 24
#define DPH 2
#define TA 210
#define TA2 70
#define CSH 2
#define NPRB 3
#define TMAP 485
#define MINV 96
#define VW 4
#define MASTER 203
#define TRES 175
#define TB1 1200
#define PER 300
#define RDD 100
#define LEAD 70
#define LHI 85
#define LOK 74
#define PSL0 124
#define PUBP -14
#define LJ 210
#define LOK2 75
#define NTRY 3
#define TFO 6
#define SL0 42.f
#define PST0 200
#define KC 4
#define TC0 1000
#define TCS 9
#define PRE0 3

#define KREL 12
#define PDEL 70
#define NCP 3
#define NRW (NRR + NCP * GSZ)
#define TRD 112
#define THD 55
#define TRQ 55
#define TCA 132
#define TCD 22
#define TSD 560
#define WS_EPOCH 0
#define WS_POST0 8192
#define WS_POST (WS_POST0 + ((unsigned)E1 & 15) * 2048)
#define WS_NEWS (49152 + 256)
#define WS_HELLO 59904
#define WS_BOARD 65536
#define WS_PART (2u << 20)
#define WS_SWAP (WS_PART + NRW * NSPLIT * 512)

typedef unsigned int v2u __attribute__((ext_vector_type(2)));
typedef unsigned int v16u __attribute__((ext_vector_type(16)));
typedef unsigned int v4u __attribute__((ext_vector_type(4)));
typedef int v4i __attribute__((ext_vector_type(4)));
typedef float v2f __attribute__((ext_vector_type(2)));
typedef float v4f __attribute__((ext_vector_type(4)));
typedef unsigned long long u64;

__device__ __forceinline__ float bflo(unsigned u) { return __uint_as_float(u << 16); }
__device__ __forceinline__ float bfhi(unsigned u) { return __uint_as_float(u & 0xffff0000u); }
__device__ __forceinline__ float bfr(float x) {
    __bf16 b = (__bf16)x;
    return __uint_as_float(((unsigned)__builtin_bit_cast(unsigned short, b)) << 16);
}

#define SLEEP(n) SLEEP_(n)
#define SLEEP_(n) asm volatile("s_sleep " #n "\n\tv_add_co_u32 %0, vcc, 1, %0\n\ts_cbranch_vccz 1f\n\ts_endpgm\n1:" : "+v"(spins) :: "vcc", "memory");
#define SPIN0 0xf0000000u
#define CBAR() asm volatile("" ::: "memory")

__device__ __forceinline__ void wave_sync() {
    __builtin_amdgcn_fence(__ATOMIC_RELEASE, "wavefront");
    __builtin_amdgcn_wave_barrier();
    __builtin_amdgcn_fence(__ATOMIC_ACQUIRE, "wavefront");
}
template <int CTRL>
__device__ __forceinline__ float dpp_add(float x) {
    int y = __builtin_amdgcn_update_dpp(0, __float_as_int(x), CTRL, 0xf, 0xf, true);
    return x + __int_as_float(y);
}
template <int CTRL>
__device__ __forceinline__ float dpp_max(float x) {
    int y = __builtin_amdgcn_update_dpp(__float_as_int(x), __float_as_int(x), CTRL, 0xf, 0xf, false);
    return fmaxf(x, __int_as_float(y));
}
__device__ __forceinline__ float row_sum(float x) { x = dpp_add<0x128>(x); x = dpp_add<0x124>(x); x = dpp_add<0x122>(x); return dpp_add<0x121>(x); }
__device__ __forceinline__ float row_max(float m) { m = dpp_max<0x128>(m); m = dpp_max<0x124>(m); m = dpp_max<0x122>(m); return dpp_max<0x121>(m); }
__device__ __forceinline__ float rdlane(float x, int l) { return __int_as_float(__builtin_amdgcn_readlane(__float_as_int(x), l)); }
__device__ __forceinline__ float wave_max(float m) { m = row_max(m); return fmaxf(fmaxf(rdlane(m, 0), rdlane(m, 16)), fmaxf(rdlane(m, 32), rdlane(m, 48))); }
__device__ __forceinline__ float wave_maxv(float m) {
    m = row_max(m);
    const auto a = __builtin_amdgcn_permlane16_swap(__float_as_uint(m), __float_as_uint(m), false, false);
    m = fmaxf(__uint_as_float(a[0]), __uint_as_float(a[1]));
    const auto b = __builtin_amdgcn_permlane32_swap(__float_as_uint(m), __float_as_uint(m), false, false);
    return fmaxf(__uint_as_float(b[0]), __uint_as_float(b[1]));
}
__device__ __forceinline__ float wave_sum(float x) { x = row_sum(x); return (rdlane(x, 0) + rdlane(x, 16)) + (rdlane(x, 32) + rdlane(x, 48)); }
__device__ __forceinline__ unsigned lds_ld(const unsigned* p) { return __builtin_amdgcn_readfirstlane(__hip_atomic_load(p, __ATOMIC_RELAXED, __HIP_MEMORY_SCOPE_WORKGROUP)); }
__device__ __forceinline__ unsigned lds_addr(const void* p) { return (unsigned)(size_t)(const __attribute__((address_space(3))) void*)p; }
#define DMA16(SRC, LDS) asm volatile("s_mov_b32 %0, m0\n\ts_mov_b32 m0, %2\n\ts_nop 0\n\tglobal_load_lds_dwordx4 %1, off\n\ts_mov_b32 m0, %0" : "=&s"(m0keep) : "v"(SRC), "s"(LDS) : "memory")
__device__ __forceinline__ void lds_st(unsigned* p, unsigned v) { __hip_atomic_store(p, v, __ATOMIC_RELAXED, __HIP_MEMORY_SCOPE_WORKGROUP); }
#define NOW() ((unsigned)__builtin_amdgcn_s_memrealtime())
__device__ __forceinline__ unsigned tdue(unsigned g) {
    return (g * TUNIT + min(g, 480u) * TK + min(g, 48u) * T01 + (min(g, 32u * NMIX) + min(max((int)g - NPC / 2, 0), 32 * NMIX)) * TMIX + (g >= PSF ? TGS * 32 : 0)) >> 5;
}

__device__ __forceinline__ unsigned unit_of(unsigned g) { const unsigned p = g & 511; return (g >> 9) * NU + (p >> 5) + min(max((int)p - 32 * WQ, 0) >> 3, 3); }
__device__ __forceinline__ unsigned first_of(unsigned e) { return 32 * e - 24 * min(max((int)e - WQ, 0), 4); }
#define QUARTER(E) ((E) - WQ < 4)
__device__ __forceinline__ unsigned went(unsigned e) { return 144 * e - 96 * min(max((int)e - WQ, 0), 4); }
__device__ __forceinline__ unsigned wunit(unsigned x) { x += 24 * NMIX; return x < 24 * WQ ? x / 24 : x < 24 * WQ + 32 ? WQ + (x - 24 * WQ) / 8 : (x + 64) / 24; }

#define ROTT 0xed8d2c3d2c35ed84ull, 0xba85aa165b03ac82ull, 0xee7efa489a38af71ull, 0xe932da1b0bab51d8ull, 0xfa96112d5fee971bull, 0x2c7e9ea5a544f071ull, 0x3d86093d5f0e810bull, 0xe932da1b0bab51d8ull, 0xfa96112d5fee971bull, 0xd834e62c5fa723e9ull, 0x1d7d1a3d2c061d88ull, 0xe932da1b0bab51d8ull, 0xfa96112d5fee971bull, 0xc834e12c0aaa4ec7ull, 0x3d86093d5f0e810bull, 0xe932da1b0bab51d8ull
static __device__ const u64 ROT[16] = {ROTT};

struct __attribute__((aligned(16))) Smem {
    unsigned cs[NSW][4][768];
    double acc[NOUT][DH];
    float ent[NOUT][BROWS];
    unsigned short wv[WALL];
    unsigned short wo[WALL];
    unsigned q[NIN][HALF];
    float2 kz[NIN][BROWS];
    float2 vz[NIN][BROWS];
    unsigned utab[64];
    unsigned line, gap;
    unsigned late, grab;
    unsigned rdy[NIN], cnt[NOUT];
    unsigned tabs, vrdy, swp;
    unsigned aline;
    unsigned tag_rdy, tag_lo, tag_hi, merged, cdone, cidx;
    unsigned slow;
    unsigned chead;
};

#define LDS_ADD(P, V) { u64 ex_; asm volatile("s_mov_b64 %0, exec\n\ts_mov_b64 exec, 1\n\tds_add_u32 %1, %2\n\ts_mov_b64 exec, %0" : "=&s"(ex_) : "v"(lds_addr(P)), "v"(V) : "memory"); }
#define LDS_OR(P, V) { u64 ex_; asm volatile("s_mov_b64 %0, exec\n\ts_mov_b64 exec, 1\n\tds_or_b32 %1, %2\n\ts_mov_b64 exec, %0" : "=&s"(ex_) : "v"(lds_addr(P)), "v"(V) : "memory"); }

__global__ __launch_bounds__(NTHREADS, 3) void attn_kernel(
    const unsigned short* __restrict__ q, const int* __restrict__ qk,
    const unsigned short* __restrict__ k_scales, const unsigned short* __restrict__ k_zeros,
    const int* __restrict__ qv,
    const unsigned short* __restrict__ v_scales, const unsigned short* __restrict__ v_zeros,
    const unsigned short* __restrict__ q_cos, const unsigned short* __restrict__ q_sin,
    const unsigned short* __restrict__ k_cos, const unsigned short* __restrict__ k_sin,
    __hip_bfloat16* __restrict__ out, unsigned char* __restrict__ ws)
{
    __shared__ Smem sm;
    const int tid = threadIdx.x;
    unsigned spins = SPIN0;
    const int lane = tid & 63;
    const int hw = __builtin_amdgcn_readfirstlane(tid >> 6);
    u64 t00; unsigned ka;
    asm volatile("s_memrealtime %0\n\ts_load_dword %1, %2, 0x40" : "=&s"(t00), "=&s"(ka) : "s"(__builtin_amdgcn_kernarg_segment_ptr()));
    if (hw == 0) {
        const char* pc0; asm volatile("s_getpc_b64 %0" : "=s"(pc0));
        asm volatile("global_load_dword a6, %0, %1" :: "v"(((blockIdx.x & ~7u) + (lane & 7)) * 128), "s"(pc0) : "a6", "memory");
    }
    const int wave = hw >= 4 ? hw - 4 : hw + NSW;
    const unsigned split = (blockIdx.x & 1) + (blockIdx.x & 2) * 4 + ((blockIdx.x >> 3) & 3) * 2;
    const unsigned side = (split ^ (blockIdx.x >> 2)) & 1, rank = blockIdx.x >> 5;
    const unsigned head0 = 2 * rank + side;
    const unsigned rf0 = (8 * split + 1) / 3, r3 = 8 * split + 1 - 3 * rf0;
    const unsigned koff = ((lane >> 5) * DH + 3 * (lane & 31)) * 4, koffb = koff + 384;
#define AR(S) (16 + (S) * 8)
#define GLD(A, OFF, BASE, IMM) asm volatile("global_load_dwordx3 a[%c0:%c1], %2, %3 offset:%4 nt" :: "i"(A), "i"((A) + 2), "v"(OFF), "s"(BASE), "i"(IMM))
#define ISSUE(S, J, BASE, OA, OB) { GLD(AR(S), OA, BASE, (J) * 1536); GLD(AR(S) + 4, OB, BASE, (J) * 1536); }
#define HEAD2 ((GSZ + 1 - 2 * side) * (SKV * DH * 4))
    const unsigned wo = (hw - 2) & 7;
    const bool pre = hw >= 4 && wo < PRE0;
    if (pre) {
        const char* a = (const char*)qk + (head0 * 512 + side * 256 + rf0 + wave + (wave >= 6 ? 80 : 0)) * 3072; const char* b = a + HEAD2;
        ISSUE(0, 0, a, koff, koffb) ISSUE(1, 1, a, koff, koffb) ISSUE(2, 0, b, koff, koffb) ISSUE(3, 1, b, koff, koffb)
    }
    asm volatile("s_waitcnt lgkmcnt(0)" : "+s"(t00), "+s"(ka));
    if (hw == 0) {
        constexpr u64 rot[16] = {ROTT};
        sm.utab[lane] = lane == 2 ? 2 * GSZ + head0 | ((unsigned)(rot[2] >> 4 * head0) & 15) << 9 : ~0u;
        unsigned l0 = (unsigned)t00 + TOFF;
        if (KREL) { const unsigned t1 = NOW() + KREL; if ((int)(t1 - l0) > 0) l0 = t1; }
        if (lane < 32) (&sm.line)[lane] = lane == 3 ? 64u : (lane & 29) == 0 ? l0 : 0u;
        asm volatile("s_waitcnt lgkmcnt(0)" ::: "memory");
    }
    __builtin_amdgcn_s_barrier();
    CBAR();
    const u64 rtab = r3 == 1 ? 0x5756552c2b020100ull : r3 == 0 ? 0x5756552c2b2a0100ull : 0x57562d2c2b020100ull;
#define R8(I) (rf0 + ((unsigned)(rtab >> (8 * (I))) & 255u))
#define GATE(DUE) { int d_ = (int)((DUE) - NOW()); if (d_ < 3000) while (d_ > 0) { __builtin_amdgcn_s_sleep(1); d_ = (int)((DUE) - NOW()); } }
#define TLAST() (lds_ld(&sm.line) + tdue(NPC - 2) + (lds_ld(&sm.gap) >> 16))
    if (__builtin_expect(wave < NSW, 1)) {
        const int cst = 2 * side;
        const unsigned r8w = R8((unsigned)wave);
        const char* cbase[3];
#pragma unroll
        for (int j = 0; j < 3; j++) { const int x = j * 64 + lane; cbase[j] = (x < 96 ? (const char*)k_cos + x * 16 : (const char*)k_sin + (x - 96) * 16) + r8w * (4 * DH * 2); }
        unsigned m0keep;
#define CS_DMA(W, C) { _Pragma("unroll") for (int j = 0; j < 3; j++) DMA16(cbase[j] + (C) * (512 * DH * 2), lds_addr(&sm.cs[0][0][0]) + ((W) * 4 + (C)) * 3072 + j * 1024); }
#define REPACK(W, C) { \
        unsigned* cw = &sm.cs[W][C][0]; \
        const unsigned* l = cw + rsel * (DH / 2) + qoff; \
        unsigned r[2][6]; \
        _Pragma("unroll") \
        for (int j = 0; j < 2; j++) { \
            unsigned e[4][2]; \
            _Pragma("unroll") \
            for (int k = 0; k < 4; k++) { \
                const unsigned* a = l + j * DH + (k & 1) * 48 + (k >> 1) * 384;       \
                unsigned x0 = a[0], x1 = a[1]; \
                e[k][0] = __builtin_amdgcn_alignbit(x1, x0, sh); \
                e[k][1] = x1 >> sh; \
            } \
            r[j][0] = (e[0][0] & 0xffffu) | (e[3][0] << 16); \
            r[j][1] = (e[0][0] >> 16) | (e[3][0] & 0xffff0000u); \
            r[j][2] = (e[0][1] & 0xffffu) | (e[3][1] << 16); \
            r[j][3] = (e[1][0] & 0xffffu) | (e[2][0] << 16); \
            r[j][4] = (e[1][0] >> 16) | (e[2][0] & 0xffff0000u); \
            r[j][5] = (e[1][1] & 0xffffu) | (e[2][1] << 16); \
        } \
        wave_sync(); \
        _Pragma("unroll") \
        for (int j = 0; j < 2; j++) { *(v4u*)&cw[j * 256 + lane * 4] = (v4u){r[j][0], r[j][1], r[j][2], r[j][3]}; *(v2u*)&cw[512 + j * 128 + lane * 2] = (v2u){r[j][4], r[j][5]}; } \
        wave_sync(); }
        const int sub = lane & 31;
        const int rsel = lane >> 5;
        const unsigned sh = (sub & 1) * 16;
        const int qoff = 3 * (sub >> 1) + (sub & 1);
        const unsigned voff = 3 * lane * 4;
        asm volatile("" ::: "a16", "a17", "a18", "a19", "a20", "a21", "a22", "a23", "a24", "a25", "a26", "a27", "a28", "a29", "a30", "a31",
                            "a32", "a33", "a34", "a35", "a36", "a37", "a38", "a39", "a40", "a41", "a42", "a43", "a44", "a45", "a46", "a47");
        asm volatile("" ::: "a48", "a49", "a50", "a51", "a52", "a53", "a54", "a55", "a56", "a57", "a58", "a59", "a60", "a61", "a62", "a63",
                            "a64", "a65", "a66", "a67", "a68", "a69", "a70", "a71", "a72", "a73", "a74", "a75", "a76", "a77", "a78", "a79");
        int ta[4][3], tb[4][3];
#define WAITT(P, S, N) asm volatile("s_waitcnt vmcnt(%6)\n\tv_accvgpr_read_b32 %0, a[%c7]\n\tv_accvgpr_read_b32 %1, a[%c8]\n\tv_accvgpr_read_b32 %2, a[%c9]\n\t" \
                              "v_accvgpr_read_b32 %3, a[%c10]\n\tv_accvgpr_read_b32 %4, a[%c11]\n\tv_accvgpr_read_b32 %5, a[%c12]" \
                              : "=v"(ta[P][0]), "=v"(ta[P][1]), "=v"(ta[P][2]), "=v"(tb[P][0]), "=v"(tb[P][1]), "=v"(tb[P][2]) \
                              : "i"(N), "i"(AR(S)), "i"(AR(S) + 1), "i"(AR(S) + 2), "i"(AR(S) + 4), "i"(AR(S) + 5), "i"(AR(S) + 6))
        const unsigned voffb = voff + 768;
        u64 ln = 0;
#define TDUE(G) (tdue(G) + ((G) >= PSW ? __builtin_amdgcn_readfirstlane((unsigned)(ln >> 32)) >> ((G) >= PSG ? 16 : 0) & 0xffffu : 0u))
#define REPORT() { const unsigned t_ = now - tdc; if (lane == 0) lds_st(&sm.late, t_); }
#define LINE() ln = LDV((u64*)&sm.line);
#define LDV(P) __hip_atomic_load(P, __ATOMIC_RELAXED, __HIP_MEMORY_SCOPE_WORKGROUP)
        unsigned u = 0, rg = cst * 8 + wave, np = 0;
        CS_DMA(wave, cst)

        const char* kb0 = (const char*)qk + (size_t)(head0 * 512 + r8w) * 3072;
#define KB(I) (kb0 + ((cst + (I)) & 3) * (128 * 3072))
        const char* kb; const char* kb2;
#define SISSUE(I) { kb = KB(I); kb2 = kb + HEAD2; const unsigned du_ = lds_ld(&sm.line) + tdue(16 * (I) + 2 * wo); GATE(du_) \
            ISSUE(((I) & 1) * 4, 0, kb, koff, koffb) ISSUE(((I) & 1) * 4 + 1, 1, kb, koff, koffb) ISSUE(((I) & 1) * 4 + 2, 0, kb2, koff, koffb) ISSUE(((I) & 1) * 4 + 3, 1, kb2, koff, koffb) }
        if (!pre) SISSUE(0)
        CS_DMA(wave, (cst + 1) & 3)
        if (pre) asm volatile("s_waitcnt vmcnt(3)" ::: "memory");
        asm volatile("s_waitcnt vmcnt(11)" ::: "memory");
        if (!pre) LDS_ADD(&sm.slow, NOW() - lds_ld(&sm.line) - tdue(2 * wo))
        REPACK(wave, cst)
        SISSUE(1)
#define TABDONE(I) LDS_OR(&sm.tabs, 1u << ((((cst + (I)) & 3) * 8) + wave))
        TABDONE(0)
        const float scale = 0.07216878364870322f;
        double acc[3] = {0.0, 0.0, 0.0};
        bool first = true, chg, none = false;
        unsigned Qp[3], Qn[3], Rp[3], Rn[3];
        float kz4[4], ksc, kres[4];
        v4u ca[4]; v2u cb[4];
        v2f qe[4][3], A[4];
        unsigned vut, nrg;
        const unsigned vr = ((lane >> 3) & 3) * 128 + R8((unsigned)lane & 7);
        unsigned g, mg;
        const unsigned grab_adr = lds_addr(&sm.grab);
        asm volatile("v_accvgpr_write_b32 a13, 1\n\tv_accvgpr_write_b32 a14, 2" ::: "a13", "a14", "a15");
#define GRAB(N) { u64 ex_; asm volatile("s_mov_b64 %0, exec\n\ts_mov_b64 exec, 1\n\tds_add_rtn_u32 a15, %1, a%c2\n\ts_mov_b64 exec, %0" : "=&s"(ex_) : "v"(grab_adr), "i"(12 + (N)) : "memory"); }
#define GRABBED() { unsigned t_; asm volatile("s_waitcnt lgkmcnt(0)\n\tv_accvgpr_read_b32 %1, a15\n\ts_nop 0\n\tv_readfirstlane_b32 %0, %1\n\ts_nop 1" : "=s"(g), "=v"(t_)); }
        unsigned oa, ob, un, tdn, tiss, tdc = 0;
#define PLAN() { \
            GRABBED() un = unit_of(g); chg = un != u; none = g >= NPC; \
            const bool nv = g >= NPC / 2; \
            unsigned hd = __builtin_amdgcn_readlane(vut, un); \
            while (hd == ~0u) { SLEEP(1) hd = lds_ld(&sm.utab[un]); } \
            nrg = (g + (hd >> 8)) & 31; hd &= 255; \
            unsigned boff, boff2; asm("s_mul_i32 %0, %1, 0xc00" : "=s"(boff) : "s"(hd * 512 + __builtin_amdgcn_readlane(vr, nrg))); \
            asm("s_mul_i32 %0, %1, 0xc00" : "=s"(boff2) : "s"(hd * 512 + __builtin_amdgcn_readlane(vr, nrg + 1))); \
            const char* b_ = nv ? (const char*)qv : (const char*)qk; \
            kb = b_ + boff; kb2 = b_ + boff2; \
            oa = nv ? voff : koff; ob = nv ? voffb : koffb; \
            tdn = TDUE(g); }
#define FIRE() { ISSUE(0, 0, kb, oa, ob) ISSUE(1, 1, kb, oa, ob) ISSUE(2, 0, kb2, oa, ob) ISSUE(3, 1, kb2, oa, ob) }
#define LAUNCH() { \
            LINE() \
            const unsigned du_ = __builtin_amdgcn_readfirstlane((unsigned)ln) + tdn; \
            GATE(du_) \
            ISSUE(0, 0, kb, oa, ob) ISSUE(1, 1, kb, oa, ob) \
            GATE(du_ + 10) \
            ISSUE(2, 0, kb2, oa, ob) ISSUE(3, 1, kb2, oa, ob) tiss = NOW(); }
#define ARRIVED() \
            const unsigned now = NOW(); \
            const bool stall = now - tiss > LJ && g - PST0 < NPC - 48 - PST0; \
            const bool late = !none && !stall && (int)(now + EARLY - __builtin_amdgcn_readfirstlane((unsigned)ln) - tdn) >= 0; \
            if (late) { FIRE() tiss = now; } \
            if (stall && lane == 0) __hip_atomic_fetch_max(&sm.line, (unsigned)ln + (now - tiss) - LOK2, __ATOMIC_RELAXED, __HIP_MEMORY_SCOPE_WORKGROUP); \
            REPORT()
#define ADVANCE(ISV) { \
            if (chg) FLUSH(ISV) \
            if (!none && !late) LAUNCH() \
            first = chg; u = un; rg = nrg; tdc = tdn + 10; }
#define K_TAB(MODE) { _Pragma("unroll") for (int p = 0; p < ((MODE) ? 2 : 4); p++) { \
            const unsigned* cw = &sm.cs[(rg & 7) + (p >> 1)][rg >> 3][0]; \
            ca[p] = *(const v4u*)&cw[(p & 1) * 256 + lane * 4]; cb[p] = *(const v2u*)&cw[512 + (p & 1) * 128 + lane * 2]; } }
#define K_PRE(MODE, P) { \
        float e0, e1, e2, f0, f1, f2; \
        const unsigned* qp_ = ((MODE) && (P) >= 2) ? Rp : Qp; const unsigned* qn_ = ((MODE) && (P) >= 2) ? Rn : Qn; \
        const int tp_ = (MODE) ? (P) & 1 : (P); \
        asm("v_dot2_f32_bf16 %0, %6, %12, 0\n\tv_dot2_f32_bf16 %3, %9, %15, 0\n\tv_dot2_f32_bf16 %1, %7, %13, 0\n\tv_dot2_f32_bf16 %4, %10, %16, 0\n\t" \
            "v_dot2_f32_bf16 %2, %8, %14, 0\n\tv_dot2_f32_bf16 %5, %11, %17, 0\n\ts_nop 2" \
            : "=&v"(e0), "=&v"(e1), "=&v"(e2), "=&v"(f0), "=&v"(f1), "=&v"(f2) \
            : "v"(qp_[0]), "v"(qp_[1]), "v"(qp_[2]), "v"(qn_[0]), "v"(qn_[1]), "v"(qn_[2]), \
              "v"(ca[tp_].x), "v"(ca[tp_].y), "v"(ca[tp_].z), "v"(ca[tp_].w), "v"(cb[tp_].x), "v"(cb[tp_].y)); \
        qe[P][0] = (v2f){e0, f0}; qe[P][1] = (v2f){e1, f1}; qe[P][2] = (v2f){e2, f2}; \
        A[P] = (qe[P][0] + qe[P][1] + qe[P][2]) * (v2f){-kz4[P], -kz4[P]}; }
#define K_WAIT(MODE, P) { \
        if ((MODE) == 0) { WAITT(P, P, 6 - 2 * (P)); } \
        else { WAITT(P, (((MODE) - 1) & 1) * 4 + (P), ((MODE) < 3 ? 20 : (MODE) == 3 ? 17 : 14) - 2 * (P)); } }
#define K_FMA(P) { \
        _Pragma("unroll") \
        for (int i = 0; i < 3; i++) { v2f x = {(float)ta[P][i], (float)tb[P][i]}; A[P] = qe[P][i] * x + A[P]; } \
        kres[P] = A[P].x + A[P].y; }
#define K_RED(MODE) { \
        const bool o1 = lane & 1, o2 = lane & 2; \
        float s01 = (o1 ? kres[1] : kres[0]) + __int_as_float(__builtin_amdgcn_update_dpp(0, __float_as_int(o1 ? kres[0] : kres[1]), 0xb1, 0xf, 0xf, true)); \
        float s23 = (o1 ? kres[3] : kres[2]) + __int_as_float(__builtin_amdgcn_update_dpp(0, __float_as_int(o1 ? kres[2] : kres[3]), 0xb1, 0xf, 0xf, true)); \
        float sx = (o2 ? s23 : s01) + __int_as_float(__builtin_amdgcn_update_dpp(0, __float_as_int(o2 ? s01 : s23), 0x4e, 0xf, 0xf, true)); \
        sx = dpp_add<0x124>(sx); sx = dpp_add<0x128>(sx); \
        const auto sw = __builtin_amdgcn_permlane16_swap(__float_as_uint(sx), __float_as_uint(sx), false, false); \
        sx = __uint_as_float(sw[0]) + __uint_as_float(sw[1]); \
        if ((lane & 28) == 0) { if (MODE) sm.ent[(lane >> 1) & 1][rg * 4 + 2 * (lane & 1) + rsel] = sx * ksc; else sm.ent[u % NOUT][rg * 4 + 2 * (lane & 3) + rsel] = sx * ksc; } }
#define K_PRE4(MODE) { K_PRE(MODE, 0) K_PRE(MODE, 1) K_PRE(MODE, 2) K_PRE(MODE, 3) asm volatile("" :: "v"(A[0]), "v"(A[1]), "v"(A[2]), "v"(A[3])); }
#define K_BODY(MODE) { K_PRE4(MODE) K_WAIT(MODE, 0) K_FMA(0) K_WAIT(MODE, 1) K_FMA(1) K_WAIT(MODE, 2) K_FMA(2) K_WAIT(MODE, 3) K_FMA(3) K_RED(MODE) }
#define V_FMA(J, W0, W1) { \
        _Pragma("unroll") \
        for (int i = 0; i < 3; i++) { \
            acc[i] = fma(W0, (double)ta[J][i], acc[i]); \
            acc[i] = fma(W1, (double)tb[J][i], acc[i]); \
        } }
#define V_PIN() asm volatile("" :: "v"(acc[0]), "v"(acc[1]), "v"(acc[2]));
#define RDYWAIT(SL, SEQ) { while (lds_ld(&sm.rdy[SL]) != (SEQ)) SLEEP(1); CBAR(); }
#define LOADZ() { _Pragma("unroll") for (int p = 0; p < 4; p++) kz4[p] = sm.kz[u % NIN][rg * 4 + 2 * p + rsel].x; ksc = sm.kz[u % NIN][rg * 4 + 2 * (lane & 3) + rsel].y * scale; }
#define LOADQ() { _Pragma("unroll") for (int i = 0; i < 3; i++) { Qp[i] = sm.q[u % NIN][3 * sub + i]; Qn[i] = __builtin_amdgcn_alignbit(Qp[i], Qp[i], 16) ^ 0x80000000u; } }
#define FLUSH(ISV) { \
            mg = __builtin_amdgcn_readfirstlane(mg); \
            while (u >= mg + NOUT) { SLEEP(1); mg = lds_ld(&sm.merged); } \
            CBAR(); \
            if (ISV) { const unsigned aa = lds_addr(&sm.acc[u % NOUT][lane]); \
                asm volatile("ds_add_f64 %0, %1\n\tds_add_f64 %0, %2 offset:512\n\tds_add_f64 %0, %3 offset:1024" :: "v"(aa), "v"(acc[0]), "v"(acc[1]), "v"(acc[2]) : "memory"); \
                acc[0] = 0.0; acc[1] = 0.0; acc[2] = 0.0; } \
            CBAR(); \
            LDS_ADD(&sm.cnt[u % NOUT], np) \
            CBAR(); \
            np = 0; }

#define LOADZ2() { _Pragma("unroll") for (int p = 0; p < 4; p++) kz4[p] = sm.kz[p >> 1][rg * 4 + 2 * (p & 1) + rsel].x; ksc = sm.kz[(lane >> 1) & 1][rg * 4 + 2 * (lane & 1) + rsel].y * scale; }
#define STATIC_STEP(I) { \
            rg = ((cst + (I)) & 3) * 8 + wave; \
            LOADZ2() \
            K_TAB((I) + 1) K_BODY((I) + 1) }
        CS_DMA(wave, (cst + 2) & 3)
        RDYWAIT(0, 1u) RDYWAIT(1, 2u)
        LOADQ()
#pragma unroll
        for (int i = 0; i < 3; i++) { Rp[i] = sm.q[1][3 * sub + i]; Rn[i] = __builtin_amdgcn_alignbit(Rp[i], Rp[i], 16) ^ 0x80000000u; }
        STATIC_STEP(0)
        asm volatile("s_waitcnt vmcnt(11)" ::: "memory");
        REPACK(wave, (cst + 1) & 3)
        TABDONE(1)
        SISSUE(2)
        CS_DMA(wave, (cst + 3) & 3)
        STATIC_STEP(1)
        asm volatile("s_waitcnt vmcnt(11)" ::: "memory");
        REPACK(wave, (cst + 2) & 3)
        TABDONE(2)
        SISSUE(3)
        asm volatile("s_waitcnt vmcnt(8)" ::: "memory");
        REPACK(wave, (cst + 3) & 3)
        TABDONE(3)
        STATIC_STEP(2)
        GRAB(2)
        vut = LDV(&sm.utab[lane]);
        rg = ((cst + 3) & 3) * 8 + wave;
        LOADZ2() K_TAB(4) K_PRE4(4)
        PLAN()
        LAUNCH()
        K_WAIT(4, 0) K_FMA(0) K_WAIT(4, 1) K_FMA(1) K_WAIT(4, 2) K_FMA(2) K_WAIT(4, 3) K_FMA(3) K_RED(4)
        CBAR();
        LDS_ADD(&sm.cnt[0], 4u) LDS_ADD(&sm.cnt[1], 4u)
        u = un; rg = nrg; tdc = tdn + 10;
        bool tabok = false;
        __builtin_amdgcn_s_setprio(2);
#pragma unroll 1
        while (u < NU) {
            GRAB(2)
            mg = LDV(&sm.merged);
            vut = LDV(&sm.utab[lane]);
            LINE()
            if (!tabok) {
                for (;;) { const unsigned tb_ = lds_ld(&sm.tabs); tabok = tb_ == ~0u; if (((tb_ >> rg) & 3u) == 3u) break; SLEEP(1) }
                CBAR();
            }
            K_TAB(0)
            if (first) {
                unsigned fl = LDV(&sm.rdy[u % NIN]);
                CBAR();
                LOADQ() LOADZ()
                if (__builtin_amdgcn_readfirstlane(fl) != u + 1) { RDYWAIT(u % NIN, u + 1) LOADQ() LOADZ() }
            } else LOADZ()
            K_PRE4(0)
            PLAN()
            K_WAIT(0, 0) K_FMA(0) K_WAIT(0, 1) K_FMA(1)
            asm volatile("" :: "v"(kres[0]), "v"(kres[1]));
            K_WAIT(0, 2) K_WAIT(0, 3)
            ARRIVED()
            K_FMA(2) K_FMA(3) K_RED(0)
            np += 2;
            ADVANCE(false)
        }
#pragma unroll 1
        while (u < 2 * NU) {
            GRAB(2)
            mg = LDV(&sm.merged);
            LINE()
            const unsigned vr_ = LDV(&sm.vrdy);
            CBAR();
            const unsigned short* wp = &sm.wv[went(u - NU) + (QUARTER(u - NU) ? rg & 7 : rg) * 4];
            v4i pw = *(const v4i*)wp;
            if (first && !((__builtin_amdgcn_readfirstlane(vr_) >> (u - NU)) & 1)) { while (!((lds_ld(&sm.vrdy) >> (u - NU)) & 1)) SLEEP(1); CBAR(); pw = *(const v4i*)wp; }
            const double w0 = (short)pw.x, w1 = pw.x >> 16, w2 = (short)pw.y, w3 = pw.y >> 16, w4 = (short)pw.z, w5 = pw.z >> 16, w6 = (short)pw.w, w7 = pw.w >> 16;
            asm volatile("" :: "v"(w0), "v"(w1), "v"(w2), "v"(w3), "v"(w4), "v"(w5), "v"(w6), "v"(w7));
            PLAN()
            v2f hl; unsigned tl8, rowl;
            if (none) { hl = *(const v2f*)&sm.wv[went(NU - 1) + 128]; tl8 = LDV(&sm.tag_lo) << 24; rowl = (lds_ld(&sm.utab[2 * NU - 1]) & 255) * (NSPLIT * 512); asm volatile("" : "+v"(hl), "+v"(tl8)); }
            WAITT(0, 0, 6); V_FMA(0, w0, w1) V_PIN()
            WAITT(1, 1, 4); V_FMA(1, w2, w3) V_PIN()
            WAITT(2, 2, 2); V_FMA(2, w4, w5) V_PIN()
            WAITT(3, 3, 0);
            np += 2;
            if (none) {
                V_FMA(3, w6, w7)
                if (u != 2 * NU - 1) { FLUSH(true) break; }
                mg = __builtin_amdgcn_readfirstlane(mg);
                while (u >= mg + NOUT) { SLEEP(1); mg = lds_ld(&sm.merged); }
                const unsigned aa = lds_addr(&sm.acc[(2 * NU - 1) % NOUT][lane]);
                double a0, a1, a2; unsigned cn; u64 ex_;
                asm volatile("ds_add_f64 %5, %6\n\tds_add_f64 %5, %7 offset:512\n\tds_add_f64 %5, %8 offset:1024\n\t"
                             "s_mov_b64 %4, exec\n\ts_mov_b64 exec, 1\n\tds_add_rtn_u32 %3, %9, %10\n\ts_mov_b64 exec, %4\n\t"
                             "ds_read_b64 %0, %5\n\tds_read_b64 %1, %5 offset:512\n\tds_read_b64 %2, %5 offset:1024\n\ts_waitcnt lgkmcnt(0)"
                             : "=&v"(a0), "=&v"(a1), "=&v"(a2), "=&v"(cn), "=&s"(ex_) : "v"(aa), "v"(acc[0]), "v"(acc[1]), "v"(acc[2]), "v"(lds_addr(&sm.cnt[(2 * NU - 1) % NOUT])), "v"(np) : "memory");
                const float n0 = (float)(a0 - (double)hl.y) * hl.x, n1 = (float)(a1 - (double)hl.y) * hl.x, n2 = (float)(a2 - (double)hl.y) * hl.x;
                const unsigned e2 = max(__float_as_uint(fmaxf(fmaxf(fabsf(n0), fabsf(n1)), fabsf(n2))) >> 23, 14u);
                const float f2 = __uint_as_float((268u - e2) << 23);
                const v2u r = {__builtin_bit_cast(unsigned, __builtin_amdgcn_cvt_pk_i16((int)rintf(n0 * f2), (int)rintf(n1 * f2))),
                               __builtin_bit_cast(unsigned, __builtin_amdgcn_cvt_pk_i16((int)rintf(n2 * f2), 0)) | e2 << 16 | tl8};
                const __amdgpu_buffer_rsrc_t rsrc = __builtin_amdgcn_make_buffer_rsrc(ws + WS_PART + split * 512, 0, NRW * NSPLIT * 512 - split * 512, 0x00020000);
                asm volatile("" :: "v"(r));
                if (__builtin_amdgcn_readfirstlane(cn + np) == 32) {
                    __builtin_amdgcn_raw_buffer_store_b64(r, rsrc, lane * 8, rowl, SC1);
#pragma unroll
                    for (int j = 0; j < NCP - 1; j++) __builtin_amdgcn_raw_buffer_store_b64(r, rsrc, lane * 8, (rowl & 15 * NSPLIT * 512 ^ NSPLIT * 512) + (NRR + j * GSZ) * NSPLIT * 512, SC1);
                }
                break;
            }
            ARRIVED()
            V_FMA(3, w6, w7)
            ADVANCE(true)
        }
    } else {
    u64 E1;
    if (wave == NSW) {
        unsigned ref;
        const unsigned sidx = (blockIdx.x & 7) | ((blockIdx.x >> 7) << 3);
#define SAMPLE(K) ((((blockIdx.x >> 3) - (blockIdx.x & 7) - (K) * 5) & 15) == 0)
#define BTAG(K) ((btag + (K)) & 0xfffffu)
#define POST(K, C) { if (lane == 0) __hip_atomic_store((u64*)(ws + (bd + (K) * RST + (sidx >> 3) * 256 + (sidx & 7) * 8)), (u64)(C) | ((u64)(((C) - ref) | BTAG(K) << 12) << 32), __ATOMIC_RELAXED, __HIP_MEMORY_SCOPE_AGENT); }
        const unsigned own = lds_ld(&sm.line);
        if (SAMPLE(0) && lane < NTRY) __hip_atomic_store((unsigned*)(ws + WS_HELLO + lane * 768) + sidx, own, __ATOMIC_RELAXED, __HIP_MEMORY_SCOPE_AGENT);
        const u64 E = __hip_atomic_load((const u64*)(ws + WS_EPOCH), __ATOMIC_RELAXED, __HIP_MEMORY_SCOPE_AGENT);
        E1 = E + 1;
        if (lane == 0) { sm.tag_lo = (unsigned)E1; sm.tag_hi = (unsigned)(E1 >> 32); CBAR(); lds_st(&sm.tag_rdy, 1u); }
        const unsigned btag = __builtin_amdgcn_readfirstlane((unsigned)E1) * 61u, bd = WS_BOARD + (btag & 7) * (NRND * RST);
        ref = own;
        int off = 0;
        {
            unsigned tries = 0, cur = own, tl = own;
            bool news = false;
#pragma unroll 1
            while (!news || cur != ref) {
                const unsigned now = NOW();
                if (tries < NTRY && (int)(now - own) >= (int)(TA + tries * TA2)) {
                    v16u x;
                    asm volatile("s_load_dwordx16 %0, %1, %2\n\ts_waitcnt lgkmcnt(0)" : "=&s"(x) : "s"(ws), "s"(__builtin_amdgcn_readfirstlane(WS_HELLO + tries * 768)) : "memory");
                    unsigned bad = 0; int sum = 0, dif = 0;
#pragma unroll
                    for (int i = 0; i < 16; i++) { bad |= x[i] - own + 512; sum += x[i] - x[0]; dif += i & 4 ? x[i] : -x[i]; }
                    tries++;
                    if (bad < 1024) { const unsigned com = x[0] + (sum >> 4) + abs(dif) * (8 - KC) / 128; ref = x[0] + ((sum + (blockIdx.x & 4 ? dif : -dif)) >> 4); off = ref - com;
                        tries = NTRY; tl = NOW(); if (lane == 0) lds_st(&sm.aline, com); }
                    continue;
                }
                if (!news && lds_ld(&sm.utab[31]) != ~0u) { news = true; CBAR(); if (cur == ref) tl = now; ref = lds_ld(&sm.aline) + off; tries = NTRY; }
                const int d = (now - tl) >> CSH;
                if (cur != ref && d) { tl += d << CSH; cur += min(max((int)(ref - cur), -d), d); if (lane == 0) lds_st(&sm.line, cur); }
                SLEEP(2)
            }
        }
        ref -= off;
        const unsigned ref0 = ref;
        const unsigned lead = (blockIdx.x >> 3) == 9 ? 0 : LEAD;
        unsigned posted = ref, lagf = ref, tpost = ref + TB1, tpoll = tpost + RDD + lead, kw = 0, kr = 0, cur = ref + off, khold = 0;
        int o = off; bool held = false;
#pragma unroll 1
        for (;;) {
            const unsigned p = lds_ld(&sm.grab);
            if (p >= NPC) break;
            lagf += (int)(lds_ld(&sm.late) - lagf) >> 1;
            { const unsigned l = lds_ld(&sm.line); if (l != cur) { cur = l; khold = kw; held = true; } }
            const unsigned now = NOW();
            if (o) { o = off * min(max((int)(ref0 + TC0 + (1 << TCS) - now), 0), 1 << TCS) >> TCS; if (!held && cur != ref + o) { cur = ref + o; if (lane == 0) lds_st(&sm.line, cur); } }
            if (kw < NRND && (int)(now - tpost) >= 0) {
                const unsigned c = ref + min((p >= PSL0 && (int)(lagf - cur) > LHI ? lagf - LOK : cur) - o - ref, 4000u);
                if (SAMPLE(kw) && (c != ref || (ref != posted && (sidx & 7) == (kw & 7)))) POST(kw, c)
                posted = ref;
                tpost += PER; kw++;
            }
            if (kr < NRND && (int)(now - tpoll) >= 0) {
                v16u xa, xb;
                asm volatile("s_load_dwordx16 %0, %2, %3\n\ts_load_dwordx16 %1, %2, %4\n\ts_waitcnt lgkmcnt(0)" : "=&s"(xa), "=&s"(xb)
                             : "s"(ws), "s"(__builtin_amdgcn_readfirstlane(bd + kr * RST)), "s"(__builtin_amdgcn_readfirstlane(bd + kr * RST + 256)) : "memory");
                int nb = 0, sg = 0;
#pragma unroll
                for (int i = 0; i < 16; i++) {
                    const unsigned lo = i < 8 ? xa[2 * i & 15] : xb[2 * i & 15], hi = i < 8 ? xa[(2 * i + 1) & 15] : xb[(2 * i + 1) & 15];
                    if (hi >> 12 == BTAG(kr) && lo - ref0 < 8000) { nb = max(nb, (int)(lo - (hi & 4095u) - ref)); sg += hi & 4095u; }
                }
                ref += nb + (sg >> 4);
                if (cur != ref + o && (kr >= khold || (int)(ref + o - cur) > 0)) { cur = ref + o; held = false; if (lane == 0) lds_st(&sm.line, cur); }
                tpoll += PER; kr++;
            }
            SLEEP(2)
        }
    } else if (wave == NSW + 1 || wave == NSW + 2) {
        const int ql = lane < 48 ? lane : 47;
        unsigned c1, c2, s1, s2;
#define QT_LOADS() { int qo_ = ql; asm volatile("" : "+v"(qo_)); \
                     c1 = ((const unsigned*)q_cos)[qo_]; c2 = ((const unsigned*)q_cos)[48 + qo_]; s1 = ((const unsigned*)q_sin)[qo_]; s2 = ((const unsigned*)q_sin)[48 + qo_]; }
        unsigned q1, q2, lkz, lks, lvz, lvs;
        const unsigned sofs = R8(((unsigned)lane >> 1) & 7) * 2 + (lane & 1);
#define UNIT_LOADS(HEAD, GRP) { \
        q1 = ((const unsigned*)(q + (size_t)(HEAD) * DH))[ql]; q2 = ((const unsigned*)(q + (size_t)(HEAD) * DH))[48 + ql]; \
        const size_t so = (size_t)(HEAD) * (SKV / 2) + (GRP) * 256 + sofs; \
        lkz = *((const unsigned*)k_zeros + so); lks = *((const unsigned*)k_scales + so); \
        lvz = *((const unsigned*)v_zeros + so); lvs = *((const unsigned*)v_scales + so); }
#define UNIT_FILL(SL, U) { \
            v4f qq; \
            qq.x = bfr(bfr(bflo(q1) * bflo(c1)) + bfr(-bflo(q2) * bflo(s1))); \
            qq.y = bfr(bfr(bflo(q2) * bflo(c2)) + bfr(bflo(q1) * bflo(s2))); \
            qq.z = bfr(bfr(bfhi(q1) * bfhi(c1)) + bfr(-bfhi(q2) * bfhi(s1))); \
            qq.w = bfr(bfr(bfhi(q2) * bfhi(c2)) + bfr(bfhi(q1) * bfhi(s2))); \
            *(v2u*)&sm.q[SL][2 * ql] = (v2u){(__float_as_uint(qq.x) >> 16) | __float_as_uint(qq.y), (__float_as_uint(qq.z) >> 16) | __float_as_uint(qq.w)}; \
            *(v4f*)&sm.kz[SL][2 * lane] = (v4f){bflo(lkz), bflo(lks), bfhi(lkz), bfhi(lks)}; \
            *(v4f*)&sm.vz[SL][2 * lane] = (v4f){bflo(lvz), bflo(lvs), bfhi(lvz), bfhi(lvs)}; \
            CBAR(); \
            if (lane == 0) lds_st(&sm.rdy[SL], (U) + 1); \
            CBAR(); }
        const unsigned dw = NSW + 2 - wave;
        QT_LOADS()
        UNIT_LOADS(head0 + dw * (GSZ + 1 - 2 * side), lane >> 4)
#pragma unroll 1
        for (unsigned u = dw;;) {
            while (u >= lds_ld(&sm.merged) + NIN) SLEEP(4);
            CBAR();
            UNIT_FILL(u % NIN, u)
            if (u - 1 < 12) {
                const unsigned x_ = (blockIdx.x >> 3) + 32 * u - 8; unsigned dm;
                const char* p_ = x_ < 384 ? (const char*)(x_ < 192 ? qk : qv) + (size_t)(x_ < 192 ? x_ : x_ - 192) * (2u << 20) + (x_ * 8 + (blockIdx.x & 7)) * 640 : (const char*)out + (x_ - 384) * 4096;
                while ((int)(NOW() - lds_ld(&sm.line)) < TSD) SLEEP(2)
                asm volatile("s_load_dword %0, %1, 0x0\n\ts_waitcnt lgkmcnt(0)" : "=&s"(dm) : "s"(p_) : "memory");
            }
            u += 2;
            if (u >= NU) break;
            if (u == 4) {
                const unsigned tpf = tdue(PSF - 1) + TFO;
                { int d; while ((d = (int)(lds_ld(&sm.line) + tpf - NOW())) > 0 && d < 3000) SLEEP(1) }
#define PF3(P) asm volatile("global_load_dwordx4 a[0:3], %0, %1\n\tglobal_load_dwordx4 a[0:3], %0, %1 offset:1024\n\tglobal_load_dwordx4 a[0:3], %0, %1 offset:2048" \
                            :: "v"(64 * SKV * 2 + blockIdx.x * 3072 + lane * 16), "s"(P) : "a0", "a1", "a2", "a3")
                PF3(k_zeros); PF3(k_scales); PF3(v_zeros); PF3(v_scales);
                asm volatile("global_load_dword a0, %0, %1" :: "v"(blockIdx.x * (DH * 2) + ql * 8), "s"(q) : "a0");
            }
            const unsigned tin = tdue(first_of(u) - (QUARTER(u) ? 24 : 0) - LEADP + (split & 7) * DPH);
            { int d; while ((d = (int)(lds_ld(&sm.line) + tin - NOW())) > 0 && d < 3000) SLEEP(4) }
            unsigned hd;
            while ((hd = lds_ld(&sm.utab[u])) == ~0u) SLEEP(2)
            UNIT_LOADS(hd & 255, QUARTER(u) ? rank & 3 : lane >> 4)
        }
        while (lds_ld(&sm.tag_rdy) == 0) SLEEP(1);
        CBAR();
        E1 = (u64)lds_ld(&sm.tag_lo) | ((u64)lds_ld(&sm.tag_hi) << 32);
        if (wave == NSW + 1 && blockIdx.x == 0) {
            for (;;) {
                bool ok = true;
#pragma unroll
                for (int i = 0; i < NH / 64; i++) ok &= __hip_atomic_load((const u64*)(ws + WS_POST) + i * 64 + lane, __ATOMIC_RELAXED, __HIP_MEMORY_SCOPE_AGENT) >> 24 == (E1 << 24) >> 24;
                if (__builtin_amdgcn_ballot_w64(ok) == ~0ull) break;
                SLEEP(64);
            }
            if (lane == 0) __hip_atomic_store((u64*)(ws + WS_EPOCH), E1, __ATOMIC_RELAXED, __HIP_MEMORY_SCOPE_AGENT);
        }
        if (wave == NSW + 2) {
            const unsigned t16 = (unsigned)E1 & 0xffffu, t16b = ((unsigned)E1 + ((unsigned)E1 >> 16)) & 0xffffu;
            const __amdgpu_buffer_rsrc_t rsrc = __builtin_amdgcn_make_buffer_rsrc(ws + WS_SWAP + (blockIdx.x ^ 4) * 5120, 0, 5120, 0x00020000);
            const unsigned swp = lds_ld(&sm.swp);
            if (swp) {
                unsigned use = 0;
#pragma unroll
                for (int i = 0; i < 5; i++) use |= ((swp >> wunit(i * 64 + lane)) & 1) << i;
                const unsigned tg = tdue(PSG - 1) + (lds_ld(&sm.gap) & 0xffffu);
                while ((int)(NOW() - lds_ld(&sm.line) - tg) < 0) SLEEP(2)
                for (;;) {
                    v4u d[5]; bool ok = true;
#pragma unroll
                    for (int i = 0; i < 5; i++) d[i] = __builtin_amdgcn_raw_buffer_load_b128(rsrc, (use >> i) & 1 ? (i * 64 + lane) * 16 : 0x40000000, 0, SC1 | 2);
#pragma unroll
                    for (int i = 0; i < 5; i++) if ((use >> i) & 1) {
                        ok &= d[i].y >> 16 == t16 && d[i].w >> 16 == t16b;
                        unsigned* w = (unsigned*)&sm.wv[144 * NMIX + (i * 64 + lane) * 6];
                        w[0] = d[i].x; w[1] = (d[i].y & 0xffffu) | (d[i].z << 16); w[2] = (d[i].z >> 16) | (d[i].w << 16);
                    }
                    if (__builtin_amdgcn_ballot_w64(ok) == ~0ull) break;
                    SLEEP(32)
                }
                CBAR();
                LDS_OR(&sm.vrdy, swp)
            }
        }
    } else {
        const unsigned eu = lane >= NU ? lane - NU : lane, win = min(first_of(eu) >> 5, 15u), pair = QUARTER(eu) ? 2 * (eu - WQ) + (rank >> 2) : rank;
        const unsigned rot2 = ((const unsigned char*)ROT)[win * 8 + pair];
#define UENT(BIT) (win * 16 + 2 * pair + ((BIT) ^ side) | (QUARTER(eu) ? (rank - (eu - WQ)) * 8 & 31 : (rot2 >> ((BIT) ^ side) * 4 & 15) * 2) << 8)
        unsigned far = 0;
        const unsigned hp = (blockIdx.x & 1) * NH + (blockIdx.x >> 1) * 4 + 2;
        if (blockIdx.x < 144) {
            const char* pb = (blockIdx.x < 128 ? (const char*)(hp >= NH ? qv : qk) + (size_t)(hp & (NH - 1)) * (SKV * DH * 4) + SKV * DH * 2
                                               : (const char*)ws + WS_PART + (8u << 20) + (blockIdx.x & 15) * 65536) + ((NOW() * 0x9e3779b1u) >> 26) * 128;
            unsigned mn[2] = {~0u, ~0u};
#pragma unroll 1
            for (unsigned i = 1; i < 2 * NPRB + 2; i++) {
                u64 t0, t1; unsigned dm;
                asm volatile("s_memtime %0\n\ts_waitcnt lgkmcnt(0)\n\ts_load_dword %2, %3, 0x0\n\ts_waitcnt lgkmcnt(0)\n\ts_memtime %1\n\ts_waitcnt lgkmcnt(0)"
                             : "=&s"(t0), "=&s"(t1), "=&s"(dm) : "s"(pb + i * 8192) : "memory");
                if (i == 1) asm volatile("global_load_dword a0, %0, %1 nt" :: "v"(0), "s"(pb) : "a0");
                const unsigned t = (unsigned)(t1 - t0) < 330 || i == 1 ? ~0u : (unsigned)(t1 - t0);
                if (i & 1) mn[1] = min(mn[1], t); else mn[0] = min(mn[0], t);
            }
            far = mn[0] > mn[1];
        }
        while (lds_ld(&sm.tag_rdy) == 0) SLEEP(1);
        CBAR();
        E1 = (u64)lds_ld(&sm.tag_lo) | ((u64)lds_ld(&sm.tag_hi) << 32);
        {
            const unsigned own = lds_ld(&sm.line), tg = (unsigned)E1;
            if (lane == 0) __hip_atomic_store((u64*)(ws + WS_POST) + blockIdx.x, E1 << 24 | min(lds_ld(&sm.slow), 4000u) << 1 | ((far ^ (blockIdx.x >> 2) ^ (blockIdx.x < 128 ? 0x96 >> (hp & 7) : 0)) & 1), __ATOMIC_RELAXED, __HIP_MEMORY_SCOPE_AGENT);
            const __amdgpu_buffer_rsrc_t nrs = __builtin_amdgcn_make_buffer_rsrc(ws + WS_NEWS, 0, 8 * 1280, 0x00020000);
            if (blockIdx.x == MASTER) {
                while ((int)(NOW() - own) < TMAP) SLEEP(2)
                const unsigned a = tg << 24, b = (unsigned)(E1 >> 8);
                v4u x; v2u z; u64 v0, v1;
#pragma unroll 1
                for (int i = 0; i < 8; i++) {
                    x = __builtin_amdgcn_raw_buffer_load_b128(__builtin_amdgcn_make_buffer_rsrc(ws + WS_POST, 0, 1024, 0x00020000), lane * 16, 0, SC1);
                    z = __builtin_amdgcn_raw_buffer_load_b64(__builtin_amdgcn_make_buffer_rsrc(ws + WS_POST + 1024, 0, 128, 0x00020000), lane * 8, 0, SC1);
                    v0 = __builtin_amdgcn_ballot_w64(((x.x ^ a) >> 24 | (x.y ^ b)) == 0); v1 = __builtin_amdgcn_ballot_w64(((x.z ^ a) >> 24 | (x.w ^ b)) == 0);
                    if (__builtin_popcountll(v0) + __builtin_popcountll(v1) >= MINV) break;
                    SLEEP(8)
                }
                const u64 m0 = __builtin_amdgcn_ballot_w64(x.x & 1) & v0, m1 = __builtin_amdgcn_ballot_w64(x.z & 1) & v1;
                const int s0 = lane < 43 ? min(max(lane - VW, 4 * NMIX), 42 - 2 * VW) : min(max(lane - VW, 43), 63 - 2 * VW);
#define PC(M) __builtin_popcount((unsigned)((M) >> s0) & ((2u << 2 * VW) - 1))
                const u64 g0 = __builtin_amdgcn_ballot_w64(2 * PC(m0) > PC(v0)), g1 = __builtin_amdgcn_ballot_w64(2 * PC(m1) > PC(v1));
                const float sl = wave_sum(((v0 >> lane & 1) ? (float)(x.x >> 1 & 4095u) : 0.f) + ((v1 >> lane & 1) ? (float)(x.z >> 1 & 4095u) : 0.f)) / ((8.f - PRE0) * (__builtin_popcountll(v0) + __builtin_popcountll(v1)) + 1.f);
                const unsigned al = lds_ld(&sm.aline), mean = (al ? al : own) + (int)fmaxf(sl - SL0, 0.f);
                const bool vz = lane < 16 && ((z.x ^ a) >> 24 | (z.y ^ b)) == 0;
                const unsigned gw = 2 * __builtin_popcountll(__builtin_amdgcn_ballot_w64(vz && (z.x & 1))) > __builtin_popcountll(__builtin_amdgcn_ballot_w64(vz));
                const unsigned k = lane & 3;
                if (lane < 32 && k < 3) __builtin_amdgcn_raw_buffer_store_b128((v4u){k == 0 ? (unsigned)g0 : k == 1 ? (unsigned)g1 : mean, tg, k == 0 ? (unsigned)(g0 >> 32) : k == 1 ? (unsigned)(g1 >> 32) : gw, tg},
                                                                              nrs, (lane >> 2) * 1280 + k * 16, 0, SC1);
            }
            while ((int)(NOW() - own) < TMAP + TRES) SLEEP(2)
            v4u y;
            for (;;) {
                y = __builtin_amdgcn_raw_buffer_load_b128(nrs, lane < 3 ? (blockIdx.x & 7) * 1280 + lane * 16 : 0x40000000, 0, SC1);
                if (((unsigned)__builtin_amdgcn_ballot_w64(y.y == tg && y.w == tg) & 7u) == 7u) break;
                SLEEP(8)
            }
            const u64 f0 = (unsigned)__builtin_amdgcn_readlane(y.x, 0) | (u64)(unsigned)__builtin_amdgcn_readlane(y.z, 0) << 32, f1 = (unsigned)__builtin_amdgcn_readlane(y.x, 1) | (u64)(unsigned)__builtin_amdgcn_readlane(y.z, 1) << 32;
            if (lane == 2) { sm.aline = y.x - own + 500 < 1000 ? y.x : own; sm.chead = (blockIdx.x & ~5u) | ((blockIdx.x >> 2 ^ y.z) & 1) | (blockIdx.x & 1) << 2; }
            unsigned bit = ((unsigned)((lane >= NU ? f1 : f0) >> (4 * win + (pair >> 1))) ^ (0x66 >> pair)) & 1;
            if (eu < NMIX) bit = eu & 1;
            const u64 bm = __builtin_amdgcn_ballot_w64(bit);
            const unsigned nsw = __builtin_popcountll((f0 ^ f1) >> (4 * NMIX));
            if (lane == 0) { sm.swp = (unsigned)(bm ^ (bm >> NU)) & ((1u << NU) - 1); lds_st(&sm.gap, nsw ? (TG0 + TG1 * nsw / 8) * 0x10001u + ((TH0 + TH1 * nsw / 8) << 16) : 0u); }
            CBAR();
            lds_st(&sm.utab[lane], UENT(bit));
        }
        const unsigned swp = lds_ld(&sm.swp);
        const unsigned T = lds_ld(&sm.tag_lo);
        const unsigned t15 = T & 0x7fffu;
        const __amdgpu_buffer_rsrc_t rsrc = __builtin_amdgcn_make_buffer_rsrc(ws + WS_PART + split * 512, 0, NRW * NSPLIT * 512 - split * 512, 0x00020000);
        v2u* recs = (v2u*)&sm.cs[0][0][0];
#pragma unroll
        for (int i = 0; i < NOUT * 3; i++) (&sm.acc[0][0])[i * 64 + lane] = 0.0;
        CBAR();
        unsigned vut = sm.utab[NU + (lane & 31)] & 255;
        if (QUARTER((unsigned)lane) && (rank & 3)) vut = NH - 16 + (rank & 3) * 16 + ((vut & 15) ^ 1);
        __builtin_amdgcn_s_setprio(3);
        unsigned po[NU / 2];
#pragma unroll
        for (int k = 0; k < NU / 2; k++) po[k] = __builtin_amdgcn_ds_bpermute((2 * k + (lane >> 5)) * 4, vut) * (NSPLIT * 512) + (lane & 31) * 16;
        const u64 rb_ = (u64)(ws + WS_PART + split * 512);
        const v4u rs_ = {(unsigned)rb_, (unsigned)(rb_ >> 32) & 0xffffu, NRW * NSPLIT * 512 - split * 512, 0x00020000u};
#pragma unroll 1
        for (unsigned u = 0; u < 2 * NU - 1; u++) {
            const unsigned e = u % NU, so = u % NOUT;
            if (u >= NU) while (!((lds_ld(&sm.vrdy) >> e) & 1)) SLEEP(4)
            CBAR();
            const v4f h = *(const v4f*)&sm.wv[went(e) + (QUARTER(e) ? 32 : 128)];
            while (lds_ld(&sm.cnt[so]) != (QUARTER(e) ? 8 : 32)) SLEEP(1)
            CBAR();
            const double a0 = sm.acc[so][lane], a1 = sm.acc[so][64 + lane], a2 = sm.acc[so][128 + lane];
            if (u < NU) {
                float2 lg = *(const float2*)&sm.ent[so][2 * lane];
                if (QUARTER(e) && lane >> 4 != (rank & 3)) lg = make_float2(-INFINITY, -INFINITY);
                const v4f zs = *(const v4f*)&sm.vz[u % NIN][2 * lane];
                const float m = wave_max(fmaxf(lg.x, lg.y));
                const float p0 = __expf(lg.x - m), p1 = __expf(lg.y - m);
                const float x0 = p0 * zs.y, x1 = p1 * zs.w;
                const unsigned eb = max(__float_as_uint(wave_max(fmaxf(fabsf(x0), fabsf(x1)))) >> 23, 14u);
                const float f = __uint_as_float((268u - eb) << 23);
                const float w0 = fminf(fmaxf(rintf(x0 * f), -32767.f), 32767.f), w1 = fminf(fmaxf(rintf(x1 * f), -32767.f), 32767.f);
                const float den = wave_sum(p0 + p1);
                const float bz = wave_sum(fmaf(w0, zs.x, w1 * zs.z));
                const float L = fmaf(m, 1.4426950408889634f, __builtin_amdgcn_logf(den));
                unsigned* wd = (unsigned*)(((swp >> u) & 1 ? sm.wo : sm.wv) + went(u));
                if (!QUARTER(u) || lane >> 4 == (rank & 3)) wd[QUARTER(u) ? lane & 15 : lane] = ((unsigned)(int)w0 & 0xffffu) | ((unsigned)(int)w1 << 16);
                if (lane == 0) *(v4f*)&wd[QUARTER(u) ? 16 : 64] = (v4f){ldexpf(1.f / den, (int)eb - 141), bz, L, 0.f};
                CBAR();
                if (!((swp >> u) & 1)) LDS_OR(&sm.vrdy, 1u << u)
            } else {
                const float n0 = (float)(a0 - (double)h.y) * h.x, n1 = (float)(a1 - (double)h.y) * h.x, n2 = (float)(a2 - (double)h.y) * h.x;
                const unsigned e2 = max(__float_as_uint(wave_maxv(fmaxf(fmaxf(fabsf(n0), fabsf(n1)), fabsf(n2)))) >> 23, 14u);
                const float f2 = __uint_as_float((268u - e2) << 23);
                const unsigned m01 = __builtin_bit_cast(unsigned, __builtin_amdgcn_cvt_pk_i16((int)rintf(n0 * f2), (int)rintf(n1 * f2)));
                const unsigned m2 = __builtin_bit_cast(unsigned, __builtin_amdgcn_cvt_pk_i16((int)rintf(n2 * f2), 0));
                const unsigned bit = ((lane & 1 ? e2 : __float_as_uint(h.z)) >> (lane >> 1)) & 1u;
                recs[e * 64 + lane] = (v2u){m01, m2 | bit << 16 | t15 << 17};
                CBAR();
                sm.acc[so][lane] = 0.0; sm.acc[so][64 + lane] = 0.0; sm.acc[so][128 + lane] = 0.0;
            }
            CBAR();
            if (lane == 0) { lds_st(&sm.cnt[so], 0u); CBAR(); lds_st(&sm.merged, u + 1); }
            if (u == NU - 1 && swp) {
                wave_sync();
                const __amdgpu_buffer_rsrc_t srs = __builtin_amdgcn_make_buffer_rsrc(ws + WS_SWAP + blockIdx.x * 5120, 0, 5120, 0x00020000);
                v4u d[5]; unsigned o[5];
#pragma unroll
                for (int i = 0; i < 5; i++) {
                    const unsigned* w = (const unsigned*)&sm.wo[144 * NMIX + (i * 64 + lane) * 6];
                    const unsigned d0 = w[0], d1 = w[1], d2 = w[2];
                    d[i] = (v4u){d0, (d1 & 0xffffu) | (T << 16), (d1 >> 16) | (d2 << 16), (d2 >> 16) | ((T + (T >> 16)) << 16)};
                    o[i] = (swp >> wunit(i * 64 + lane)) & 1 ? (i * 64 + lane) * 16 : 0x40000000;
                }
                while ((int)(NOW() - lds_ld(&sm.line)) < (int)tdue(PSW) + TWO) SLEEP(1)
#pragma unroll
                for (int i = 0; i < 5; i++) __builtin_amdgcn_raw_buffer_store_b128(d[i], srs, o[i], 0, SC1 | 2);
            }
        }
        wave_sync();
#pragma unroll
        for (int k = 0; k < NU / 2; k++) asm volatile("ds_read_b128 a[%c0:%c0+3], %1 offset:%2" :: "i"(16 + 4 * k), "v"(lds_addr(recs) + lane * 16), "i"(k * 1024) : "memory");
        asm volatile("s_waitcnt lgkmcnt(0)" ::: "memory");
        while (!((lds_ld(&sm.vrdy) >> (NU - 1)) & 1)) SLEEP(4)
        CBAR();
        const v2u hw_ = {*(const unsigned*)&sm.wv[went(NU - 1) + 132], T};
        const unsigned hrow = (NRW - GSZ + (__builtin_amdgcn_readlane(vut, NU - 1) & 15 ^ 1)) * (NSPLIT * 512);
        while (lds_ld(&sm.grab) < NPC - PUBP) SLEEP(1)
        if (lane < 16) __builtin_amdgcn_raw_buffer_store_b64(hw_, rsrc, lane * 8, hrow, SC1);
#define PUB(K) asm volatile("buffer_store_dwordx4 a[%c0:%c0+3], %1, %2, 0 offen sc1" :: "i"(16 + 4 * (K)), "v"(po[K]), "s"(rs_) : "memory");
        PUB(WQ / 2) PUB(WQ / 2 + 1)
        { const unsigned t_ = TLAST() + PDEL; GATE(t_) }
        PUB(0) PUB(1) PUB(2) PUB(3) PUB(4) PUB(7) PUB(8)
        return;
    }
    }
    __builtin_amdgcn_s_setprio(0);
    unsigned c;
#define ADDRTN(P) { u64 ex_; asm volatile("s_mov_b64 %0, exec\n\ts_mov_b64 exec, 1\n\tds_add_rtn_u32 a15, %1, a13\n\ts_mov_b64 exec, %0" : "=&s"(ex_) : "v"(lds_addr(P)) : "a15", "memory"); }
#define ADDED(V) { unsigned t_; asm volatile("s_waitcnt lgkmcnt(0)\n\tv_accvgpr_read_b32 %1, a15\n\ts_nop 0\n\tv_readfirstlane_b32 %0, %1\n\ts_nop 1" : "=s"(V), "=v"(t_)); }
    asm volatile("v_accvgpr_write_b32 a13, 1\n\ts_nop 1" ::: "a13");
    ADDRTN(&sm.cidx) ADDED(c)
    if (c < NSW) {
        while (lds_ld(&sm.grab) < NPC + 2) SLEEP(4)
        const unsigned head = lds_ld(&sm.chead), T = lds_ld(&sm.tag_lo), tl = TLAST();
        const u64 rb_ = (u64)(ws + WS_PART);
        const v4u rs_ = {(unsigned)rb_, (unsigned)(rb_ >> 32) & 0xffffu, NRW * NSPLIT * 512, 0x00020000u};
        const unsigned ro = head * (NSPLIT * 512) + c * 1024, rx = (NH + (head & 15 ^ 1)) * (NSPLIT * 512) + c * 1024;
#define POLL(S, O) asm volatile("buffer_load_dwordx4 a[%c0:%c0+3], %1, %2, %3 offen sc1" :: "i"(16 + 4 * (S)), "v"(lane * 16), "s"(rs_), "s"(O) : "memory");
#define FETCH(X, S, N) asm volatile("s_waitcnt vmcnt(%c5)\n\tv_accvgpr_read_b32 %0, a[%c4]\n\tv_accvgpr_read_b32 %1, a[%c4+1]\n\tv_accvgpr_read_b32 %2, a[%c4+2]\n\tv_accvgpr_read_b32 %3, a[%c4+3]" \
                                    : "=v"((X).x), "=v"((X).y), "=v"((X).z), "=v"((X).w) : "i"(16 + 4 * (S)), "i"(N));
        float o0, o1, o2;
        if (head >= NH - GSZ) {
            __builtin_amdgcn_s_setprio(3);
            if (c == 0) {
                v2u hd;
                GATE(tl + THD)
                for (;;) {
                    hd = __builtin_amdgcn_raw_buffer_load_b64(__builtin_amdgcn_make_buffer_rsrc(ws + WS_PART + (NRW - GSZ + (head & 15 ^ 1)) * (NSPLIT * 512), 0, NSPLIT * 512, 0x00020000), (lane & 15) * 512, 0, SC1);
                    if (__builtin_amdgcn_ballot_w64(hd.y == T) == ~0ull) break;
                    SLEEP(4)
                }
                const float w = __builtin_amdgcn_exp2f(__uint_as_float(hd.x) - row_max(__uint_as_float(hd.x)));
                if (lane < 16) sm.ent[0][lane] = w * __builtin_amdgcn_rcpf(row_sum(w));
                CBAR();
                if (lane == 0) lds_st(&sm.tabs, 0u);
            }
            while (lds_ld(&sm.tabs) || lds_ld(&sm.merged) <= (2 * NU - 1) / NOUT * NOUT) SLEEP(1)
            CBAR();
            const float cf = sm.ent[0][2 * c + (lane >> 5)];
            v4u x; bool got;
#define SEEN(J, N) { FETCH(x, J, N) got = __builtin_amdgcn_ballot_w64((((x.y ^ T << 24) | (x.w ^ T << 24)) >> 24) != 0) == 0; }
            GATE(tl + TCA) POLL(0, ro) GATE(tl + TCA + TCD) POLL(1, rx + 3 * GSZ * NSPLIT * 512) SEEN(0, 1)
            if (!got) SEEN(1, 0)
            while (!got) { POLL(2, rx + 4 * GSZ * NSPLIT * 512) SEEN(2, 0) SLEEP(0) }
            const double c0 = ldexpf(cf, (int)(x.y >> 16 & 255u) - 141), c1 = ldexpf(cf, (int)(x.w >> 16 & 255u) - 141);
#define SWADD(A, B) ({ const v2u a_ = __builtin_bit_cast(v2u, A), b_ = __builtin_bit_cast(v2u, B); \
                const auto l_ = __builtin_amdgcn_permlane32_swap(a_.x, b_.x, false, false); const auto h_ = __builtin_amdgcn_permlane32_swap(a_.y, b_.y, false, false); \
                __builtin_bit_cast(double, (v2u){l_[0], h_[0]}) + __builtin_bit_cast(double, (v2u){l_[1], h_[1]}); })
            const double s0 = SWADD(c0 * (double)(short)x.x, c1 * (double)(short)x.z), s1 = SWADD(c0 * (double)((int)x.x >> 16), c1 * (double)((int)x.z >> 16)),
                         s2 = SWADD(c0 * (double)(short)x.y, c1 * (double)(short)x.w);
            asm volatile("ds_add_f64 %0, %1\n\tds_add_f64 %0, %2 offset:512\n\tds_add_f64 %0, %3 offset:1024" :: "v"(lds_addr(&sm.acc[0][lane])), "v"(s0), "v"(s1), "v"(s2) : "memory");
            ADDRTN(&sm.cdone)
            const double a0 = sm.acc[0][lane], a1 = sm.acc[0][64 + lane], a2 = sm.acc[0][128 + lane];
            ADDED(c)
            o0 = a0; o1 = a1; o2 = a2;
        } else {
            const bool quart = head >> 4 == WQ;
            const unsigned t15 = T & 0x7fffu;
#define TAKE(K) { FETCH(x[K], K, 0) \
                bad |= (x[K].y >> 17 ^ t15) | (x[K].w >> 17 ^ t15); \
                const u64 bl = __builtin_amdgcn_ballot_w64((x[K].y & 0x10000u) != 0), be = __builtin_amdgcn_ballot_w64((x[K].w & 0x10000u) != 0); \
                L[K] = __uint_as_float(lane < 32 ? (unsigned)bl : (unsigned)(bl >> 32)); ex[K] = lane < 32 ? (unsigned)be : (unsigned)(be >> 32); \
                M = fmaxf(M, fmaxf(__uint_as_float((unsigned)bl), __uint_as_float((unsigned)(bl >> 32)))); }
#define SUMUP(K) { const float w_ = __builtin_amdgcn_exp2f(L[K] - M), c_ = ldexpf(w_, (int)(ex[K] & 255u) - 141); den += w_; \
                t[0] = fmaf(c_, (float)(short)x[K].x, t[0]); t[1] = fmaf(c_, (float)((int)x[K].x >> 16), t[1]); t[2] = fmaf(c_, (float)(short)x[K].y, t[2]); \
                t[3] = fmaf(c_, (float)(short)x[K].z, t[3]); t[4] = fmaf(c_, (float)((int)x[K].z >> 16), t[4]); t[5] = fmaf(c_, (float)(short)x[K].w, t[5]); }
            const unsigned tg = tl + (quart ? TRQ : TRD);
            GATE(tg)
            v4u x[4]; float L[4], M; unsigned ex[4];
            for (;;) {
                unsigned bad = 0; M = -3e38f;
                POLL(0, ro)
                if (quart) { POLL(1, rx) POLL(2, rx + GSZ * NSPLIT * 512) POLL(3, rx + 2 * GSZ * NSPLIT * 512) }
                __builtin_amdgcn_s_setprio(3);
                TAKE(0)
                if (quart) { TAKE(1) TAKE(2) TAKE(3) }
                if (__builtin_amdgcn_ballot_w64(bad != 0) == 0) break;
                SLEEP(2)
            }
            float t[6] = {0.f, 0.f, 0.f, 0.f, 0.f, 0.f}, den = 0.f;
            SUMUP(0)
            if (quart) { SUMUP(1) SUMUP(2) SUMUP(3) }
#define SWADF(A, B) ({ const auto l_ = __builtin_amdgcn_permlane32_swap(__float_as_uint(A), __float_as_uint(B), false, false); __uint_as_float(l_[0]) + __uint_as_float(l_[1]); })
            v4f* pb = (v4f*)((char*)&sm.cs[0][0][0] + 65536);
            float* pm = (float*)(pb + NSW * 64);
            pb[c * 64 + lane] = (v4f){SWADF(t[0], t[3]), SWADF(t[1], t[4]), SWADF(t[2], t[5]), SWADF(den, den)};
            if (lane == 0) pm[c] = M;
            CBAR();
            ADDRTN(&sm.cdone)
            ADDED(c)
            CBAR();
            if (c == NSW - 1) {
                const v4f ma = *(const v4f*)pm, mb = *(const v4f*)(pm + 4);
                const float mw[NSW] = {ma.x, ma.y, ma.z, ma.w, mb.x, mb.y, mb.z, mb.w};
                float Mx = mw[0];
#pragma unroll
                for (int w = 1; w < NSW; w++) Mx = fmaxf(Mx, mw[w]);
                v4f sum = {0.f, 0.f, 0.f, 0.f};
#pragma unroll
                for (int w = 0; w < NSW; w++) sum += pb[w * 64 + lane] * __builtin_amdgcn_exp2f(mw[w] - Mx);
                const float inv = __builtin_amdgcn_rcpf(sum.w);
                o0 = sum.x * inv; o1 = sum.y * inv; o2 = sum.z * inv;
            }
        }
        if (c == NSW - 1) {
            asm("" : "+v"(o0), "+v"(o1), "+v"(o2));
            __hip_bfloat16* o = out + (size_t)head * DH + (lane & 31) * 6 + (lane >> 5) * 3;
            o[0] = __float2bfloat16(o0); o[1] = __float2bfloat16(o1); o[2] = __float2bfloat16(o2);
        }
    }
}

extern "C" void kernel_launch(void* const* d_in, const int* in_sizes, int n_in,
                              void* d_out, int out_size, void* d_ws, size_t ws_size,
                              hipStream_t stream) {
    const unsigned short* q = (const unsigned short*)d_in[0];
    const int* qk = (const int*)d_in[1];
    const unsigned short* k_scales = (const unsigned short*)d_in[2];
    const unsigned short* k_zeros = (const unsigned short*)d_in[3];
    const int* qv = (const int*)d_in[4];
    const unsigned short* v_scales = (const unsigned short*)d_in[5];
    const unsigned short* v_zeros = (const unsigned short*)d_in[6];
    const unsigned short* q_cos = (const unsigned short*)d_in[7];
    const unsigned short* q_sin = (const unsigned short*)d_in[8];
    const unsigned short* k_cos = (const unsigned short*)d_in[9];
    const unsigned short* k_sin = (const unsigned short*)d_in[10];
    unsigned char* ws = (unsigned char*)d_ws;
    attn_kernel<<<NSPLIT * GSZ, NTHREADS, 0, stream>>>(q, qk, k_scales, k_zeros, qv, v_scales, v_zeros,
                                                       q_cos, q_sin, k_cos, k_sin, (__hip_bfloat16*)d_out, ws);
}
```

```cpp
#include <hip/hip_runtime.h>
#include <hip/hip_bf16.h>
#include <stdint.h>

#define DH 192
#define HALF 96
#define SKV 2048
#define NH 256
#define NSW 8
#define NWAVES (NSW + 4)
#define NTHREADS (64 * NWAVES)
#define BROWS 128
#define NSPLIT 16
#define GSZ 16
#define WQ 10
#define NU (NH / GSZ + 3)
#define NPC 1024
#define NRR (NH + 3 * GSZ)
#define NIN 8
#define NOUT 8
#define WALL (15 * 144 + 4 * 48)
#define SC1 16
#define NRND 48
#define RST 4352
#define TK 2
#define NMIX 3
#define TUNIT 311
#define T01 50
#define TMIX 7
#define TOFF 50
#define PSF 100
#define TGS 82
#define PSW (NPC / 2 + 28)
#define TG0 8
#define TG1 5
#define TWO -80
#define PSG (NPC / 2 + 60)
#define TH0 3
#define TH1 3
#define EARLY 10
#define LEADP 24
#define DPH 2
#define TA 210
#define TA2 70
#define CSH 2
#define NPRB 3
#define TMAP 485
#define MINV 96
#define VW 4
#define MASTER 203
#define TRES 175
#define TB1 1200
#define PER 300
#define RDD 100
#define LEAD 70
#define LHI 85
#define LOK 74
#define PSL0 124
#define PUBP -12
#define LJ 210
#define LOK2 75
#define NTRY 3
#define TFO 6
#define SL0 42.f
#define PST0 200
#define KC 4
#define TC0 1000
#define TCS 9
#define PRE0 3

#define KREL 12
#define PDEL 70
#define NCP 3
#define NRW (NRR + NCP * GSZ)
#define TRD 116
#define THD 55
#define TRQ 55
#define TCA 138
#define TCD 22
#define TSD 560
#define WS_EPOCH 0
#define WS_POST0 8192
#define WS_POST (WS_POST0 + ((unsigned)E1 & 15) * 2048)
#define WS_NEWS (49152 + 256)
#define WS_HELLO 59904
#define WS_BOARD 65536
#define WS_PART (2u << 20)
#define WS_SWAP (WS_PART + NRW * NSPLIT * 512)

typedef unsigned int v2u __attribute__((ext_vector_type(2)));
typedef unsigned int v16u __attribute__((ext_vector_type(16)));
typedef unsigned int v4u __attribute__((ext_vector_type(4)));
typedef int v4i __attribute__((ext_vector_type(4)));
typedef float v2f __attribute__((ext_vector_type(2)));
typedef float v4f __attribute__((ext_vector_type(4)));
typedef unsigned long long u64;

__device__ __forceinline__ float bflo(unsigned u) { return __uint_as_float(u << 16); }
__device__ __forceinline__ float bfhi(unsigned u) { return __uint_as_float(u & 0xffff0000u); }
__device__ __forceinline__ float bfr(float x) {
    __bf16 b = (__bf16)x;
    return __uint_as_float(((unsigned)__builtin_bit_cast(unsigned short, b)) << 16);
}

#define SLEEP(n) SLEEP_(n)
#define SLEEP_(n) asm volatile("s_sleep " #n "\n\tv_add_co_u32 %0, vcc, 1, %0\n\ts_cbranch_vccz 1f\n\ts_endpgm\n1:" : "+v"(spins) :: "vcc", "memory");
#define SPIN0 0xf0000000u
#define CBAR() asm volatile("" ::: "memory")

__device__ __forceinline__ void wave_sync() {
    __builtin_amdgcn_fence(__ATOMIC_RELEASE, "wavefront");
    __builtin_amdgcn_wave_barrier();
    __builtin_amdgcn_fence(__ATOMIC_ACQUIRE, "wavefront");
}
template <int CTRL>
__device__ __forceinline__ float dpp_add(float x) {
    int y = __builtin_amdgcn_update_dpp(0, __float_as_int(x), CTRL, 0xf, 0xf, true);
    return x + __int_as_float(y);
}
template <int CTRL>
__device__ __forceinline__ float dpp_max(float x) {
    int y = __builtin_amdgcn_update_dpp(__float_as_int(x), __float_as_int(x), CTRL, 0xf, 0xf, false);
    return fmaxf(x, __int_as_float(y));
}
__device__ __forceinline__ float row_sum(float x) { x = dpp_add<0x128>(x); x = dpp_add<0x124>(x); x = dpp_add<0x122>(x); return dpp_add<0x121>(x); }
__device__ __forceinline__ float row_max(float m) { m = dpp_max<0x128>(m); m = dpp_max<0x124>(m); m = dpp_max<0x122>(m); return dpp_max<0x121>(m); }
__device__ __forceinline__ float rdlane(float x, int l) { return __int_as_float(__builtin_amdgcn_readlane(__float_as_int(x), l)); }
__device__ __forceinline__ float wave_max(float m) { m = row_max(m); return fmaxf(fmaxf(rdlane(m, 0), rdlane(m, 16)), fmaxf(rdlane(m, 32), rdlane(m, 48))); }
__device__ __forceinline__ float wave_maxv(float m) {
    m = row_max(m);
    const auto a = __builtin_amdgcn_permlane16_swap(__float_as_uint(m), __float_as_uint(m), false, false);
    m = fmaxf(__uint_as_float(a[0]), __uint_as_float(a[1]));
    const auto b = __builtin_amdgcn_permlane32_swap(__float_as_uint(m), __float_as_uint(m), false, false);
    return fmaxf(__uint_as_float(b[0]), __uint_as_float(b[1]));
}
__device__ __forceinline__ float wave_sum(float x) { x = row_sum(x); return (rdlane(x, 0) + rdlane(x, 16)) + (rdlane(x, 32) + rdlane(x, 48)); }
__device__ __forceinline__ unsigned lds_ld(const unsigned* p) { return __builtin_amdgcn_readfirstlane(__hip_atomic_load(p, __ATOMIC_RELAXED, __HIP_MEMORY_SCOPE_WORKGROUP)); }
__device__ __forceinline__ unsigned lds_addr(const void* p) { return (unsigned)(size_t)(const __attribute__((address_space(3))) void*)p; }
#define DMA16(SRC, LDS) asm volatile("s_mov_b32 %0, m0\n\ts_mov_b32 m0, %2\n\ts_nop 0\n\tglobal_load_lds_dwordx4 %1, off\n\ts_mov_b32 m0, %0" : "=&s"(m0keep) : "v"(SRC), "s"(LDS) : "memory")
__device__ __forceinline__ void lds_st(unsigned* p, unsigned v) { __hip_atomic_store(p, v, __ATOMIC_RELAXED, __HIP_MEMORY_SCOPE_WORKGROUP); }
#define NOW() ((unsigned)__builtin_amdgcn_s_memrealtime())
__device__ __forceinline__ unsigned tdue(unsigned g) {
    return (g * TUNIT + min(g, 480u) * TK + min(g, 48u) * T01 + (min(g, 32u * NMIX) + min(max((int)g - NPC / 2, 0), 32 * NMIX)) * TMIX + (g >= PSF ? TGS * 32 : 0)) >> 5;
}

__device__ __forceinline__ unsigned unit_of(unsigned g) { const unsigned p = g & 511; return (g >> 9) * NU + (p >> 5) + min(max((int)p - 32 * WQ, 0) >> 3, 3); }
__device__ __forceinline__ unsigned first_of(unsigned e) { return 32 * e - 24 * min(max((int)e - WQ, 0), 4); }
#define QUARTER(E) ((E) - WQ < 4)
__device__ __forceinline__ unsigned went(unsigned e) { return 144 * e - 96 * min(max((int)e - WQ, 0), 4); }
__device__ __forceinline__ unsigned wunit(unsigned x) { x += 24 * NMIX; return x < 24 * WQ ? x / 24 : x < 24 * WQ + 32 ? WQ + (x - 24 * WQ) / 8 : (x + 64) / 24; }

#define ROTT 0xed8d2c3d2c35ed84ull, 0xba85aa165b03ac82ull, 0xee7efa489a38af71ull, 0xe932da1b0bab51d8ull, 0xfa96112d5fee971bull, 0x2c7e9ea5a544f071ull, 0x3d86093d5f0e810bull, 0xe932da1b0bab51d8ull, 0xfa96112d5fee971bull, 0xd834e62c5fa723e9ull, 0x1d7d1a3d2c061d88ull, 0xe932da1b0bab51d8ull, 0xfa96112d5fee971bull, 0xc834e12c0aaa4ec7ull, 0x3d86093d5f0e810bull, 0xe932da1b0bab51d8ull
static __device__ const u64 ROT[16] = {ROTT};

struct __attribute__((aligned(16))) Smem {
    unsigned cs[NSW][4][768];
    double acc[NOUT][DH];
    float ent[NOUT][BROWS];
    unsigned short wv[WALL];
    unsigned short wo[WALL];
    unsigned q[NIN][HALF];
    float2 kz[NIN][BROWS];
    float2 vz[NIN][BROWS];
    unsigned utab[64];
    unsigned line, gap;
    unsigned late, grab;
    unsigned rdy[NIN], cnt[NOUT];
    unsigned tabs, vrdy, swp;
    unsigned aline;
    unsigned tag_rdy, tag_lo, tag_hi, merged, cdone, cidx;
    unsigned slow;
    unsigned chead;
};

#define LDS_ADD(P, V) { u64 ex_; asm volatile("s_mov_b64 %0, exec\n\ts_mov_b64 exec, 1\n\tds_add_u32 %1, %2\n\ts_mov_b64 exec, %0" : "=&s"(ex_) : "v"(lds_addr(P)), "v"(V) : "memory"); }
#define LDS_OR(P, V) { u64 ex_; asm volatile("s_mov_b64 %0, exec\n\ts_mov_b64 exec, 1\n\tds_or_b32 %1, %2\n\ts_mov_b64 exec, %0" : "=&s"(ex_) : "v"(lds_addr(P)), "v"(V) : "memory"); }

__global__ __launch_bounds__(NTHREADS, 3) void attn_kernel(
    const unsigned short* __restrict__ q, const int* __restrict__ qk,
    const unsigned short* __restrict__ k_scales, const unsigned short* __restrict__ k_zeros,
    const int* __restrict__ qv,
    const unsigned short* __restrict__ v_scales, const unsigned short* __restrict__ v_zeros,
    const unsigned short* __restrict__ q_cos, const unsigned short* __restrict__ q_sin,
    const unsigned short* __restrict__ k_cos, const unsigned short* __restrict__ k_sin,
    __hip_bfloat16* __restrict__ out, unsigned char* __restrict__ ws)
{
    __shared__ Smem sm;
    const int tid = threadIdx.x;
    unsigned spins = SPIN0;
    const int lane = tid & 63;
    const int hw = __builtin_amdgcn_readfirstlane(tid >> 6);
    u64 t00; unsigned ka;
    asm volatile("s_memrealtime %0\n\ts_load_dword %1, %2, 0x40" : "=&s"(t00), "=&s"(ka) : "s"(__builtin_amdgcn_kernarg_segment_ptr()));
    if (hw == 0) {
        const char* pc0; asm volatile("s_getpc_b64 %0" : "=s"(pc0));
        asm volatile("global_load_dword a6, %0, %1" :: "v"(((blockIdx.x & ~7u) + (lane & 7)) * 128), "s"(pc0) : "a6", "memory");
    }
    const int wave = hw >= 4 ? hw - 4 : hw + NSW;
    const unsigned split = (blockIdx.x & 1) + (blockIdx.x & 2) * 4 + ((blockIdx.x >> 3) & 3) * 2;
    const unsigned side = (split ^ (blockIdx.x >> 2)) & 1, rank = blockIdx.x >> 5;
    const unsigned head0 = 2 * rank + side;
    const unsigned rf0 = (8 * split + 1) / 3, r3 = 8 * split + 1 - 3 * rf0;
    const unsigned koff = ((lane >> 5) * DH + 3 * (lane & 31)) * 4, koffb = koff + 384;
#define AR(S) (16 + (S) * 8)
#define GLD(A, OFF, BASE, IMM) asm volatile("global_load_dwordx3 a[%c0:%c1], %2, %3 offset:%4 nt" :: "i"(A), "i"((A) + 2), "v"(OFF), "s"(BASE), "i"(IMM))
#define ISSUE(S, J, BASE, OA, OB) { GLD(AR(S), OA, BASE, (J) * 1536); GLD(AR(S) + 4, OB, BASE, (J) * 1536); }
#define HEAD2 ((GSZ + 1 - 2 * side) * (SKV * DH * 4))
    const unsigned wo = (hw - 2) & 7;
    const bool pre = hw >= 4 && wo < PRE0;
    if (pre) {
        const char* a = (const char*)qk + (head0 * 512 + side * 256 + rf0 + wave + (wave >= 6 ? 80 : 0)) * 3072; const char* b = a + HEAD2;
        ISSUE(0, 0, a, koff, koffb) ISSUE(1, 1, a, koff, koffb) ISSUE(2, 0, b, koff, koffb) ISSUE(3, 1, b, koff, koffb)
    }
    asm volatile("s_waitcnt lgkmcnt(0)" : "+s"(t00), "+s"(ka));
    if (hw == 0) {
        constexpr u64 rot[16] = {ROTT};
        sm.utab[lane] = lane == 2 ? 2 * GSZ + head0 | ((unsigned)(rot[2] >> 4 * head0) & 15) << 9 : ~0u;
        unsigned l0 = (unsigned)t00 + TOFF;
        if (KREL) { const unsigned t1 = NOW() + KREL; if ((int)(t1 - l0) > 0) l0 = t1; }
        if (lane < 32) (&sm.line)[lane] = lane == 3 ? 64u : (lane & 29) == 0 ? l0 : 0u;
        asm volatile("s_waitcnt lgkmcnt(0)" ::: "memory");
    }
    __builtin_amdgcn_s_barrier();
    CBAR();
    const u64 rtab = r3 == 1 ? 0x5756552c2b020100ull : r3 == 0 ? 0x5756552c2b2a0100ull : 0x57562d2c2b020100ull;
#define R8(I) (rf0 + ((unsigned)(rtab >> (8 * (I))) & 255u))
#define GATE(DUE) { int d_ = (int)((DUE) - NOW()); if (d_ < 3000) while (d_ > 0) { __builtin_amdgcn_s_sleep(1); d_ = (int)((DUE) - NOW()); } }
#define TLAST() (lds_ld(&sm.line) + tdue(NPC - 2) + (lds_ld(&sm.gap) >> 16))
    if (__builtin_expect(wave < NSW, 1)) {
        const int cst = 2 * side;
        const unsigned r8w = R8((unsigned)wave);
        const char* cbase[3];
#pragma unroll
        for (int j = 0; j < 3; j++) { const int x = j * 64 + lane; cbase[j] = (x < 96 ? (const char*)k_cos + x * 16 : (const char*)k_sin + (x - 96) * 16) + r8w * (4 * DH * 2); }
        unsigned m0keep;
#define CS_DMA(W, C) { _Pragma("unroll") for (int j = 0; j < 3; j++) DMA16(cbase[j] + (C) * (512 * DH * 2), lds_addr(&sm.cs[0][0][0]) + ((W) * 4 + (C)) * 3072 + j * 1024); }
#define REPACK(W, C) { \
        unsigned* cw = &sm.cs[W][C][0]; \
        const unsigned* l = cw + rsel * (DH / 2) + qoff; \
        unsigned r[2][6]; \
        _Pragma("unroll") \
        for (int j = 0; j < 2; j++) { \
            unsigned e[4][2]; \
            _Pragma("unroll") \
            for (int k = 0; k < 4; k++) { \
                const unsigned* a = l + j * DH + (k & 1) * 48 + (k >> 1) * 384;       \
                unsigned x0 = a[0], x1 = a[1]; \
                e[k][0] = __builtin_amdgcn_alignbit(x1, x0, sh); \
                e[k][1] = x1 >> sh; \
            } \
            r[j][0] = (e[0][0] & 0xffffu) | (e[3][0] << 16); \
            r[j][1] = (e[0][0] >> 16) | (e[3][0] & 0xffff0000u); \
            r[j][2] = (e[0][1] & 0xffffu) | (e[3][1] << 16); \
            r[j][3] = (e[1][0] & 0xffffu) | (e[2][0] << 16); \
            r[j][4] = (e[1][0] >> 16) | (e[2][0] & 0xffff0000u); \
            r[j][5] = (e[1][1] & 0xffffu) | (e[2][1] << 16); \
        } \
        wave_sync(); \
        _Pragma("unroll") \
        for (int j = 0; j < 2; j++) { *(v4u*)&cw[j * 256 + lane * 4] = (v4u){r[j][0], r[j][1], r[j][2], r[j][3]}; *(v2u*)&cw[512 + j * 128 + lane * 2] = (v2u){r[j][4], r[j][5]}; } \
        wave_sync(); }
        const int sub = lane & 31;
        const int rsel = lane >> 5;
        const unsigned sh = (sub & 1) * 16;
        const int qoff = 3 * (sub >> 1) + (sub & 1);
        const unsigned voff = 3 * lane * 4;
        asm volatile("" ::: "a16", "a17", "a18", "a19", "a20", "a21", "a22", "a23", "a24", "a25", "a26", "a27", "a28", "a29", "a30", "a31",
                            "a32", "a33", "a34", "a35", "a36", "a37", "a38", "a39", "a40", "a41", "a42", "a43", "a44", "a45", "a46", "a47");
        asm volatile("" ::: "a48", "a49", "a50", "a51", "a52", "a53", "a54", "a55", "a56", "a57", "a58", "a59", "a60", "a61", "a62", "a63",
                            "a64", "a65", "a66", "a67", "a68", "a69", "a70", "a71", "a72", "a73", "a74", "a75", "a76", "a77", "a78", "a79");
        int ta[4][3], tb[4][3];
#define WAITT(P, S, N) asm volatile("s_waitcnt vmcnt(%6)\n\tv_accvgpr_read_b32 %0, a[%c7]\n\tv_accvgpr_read_b32 %1, a[%c8]\n\tv_accvgpr_read_b32 %2, a[%c9]\n\t" \
                              "v_accvgpr_read_b32 %3, a[%c10]\n\tv_accvgpr_read_b32 %4, a[%c11]\n\tv_accvgpr_read_b32 %5, a[%c12]" \
                              : "=v"(ta[P][0]), "=v"(ta[P][1]), "=v"(ta[P][2]), "=v"(tb[P][0]), "=v"(tb[P][1]), "=v"(tb[P][2]) \
                              : "i"(N), "i"(AR(S)), "i"(AR(S) + 1), "i"(AR(S) + 2), "i"(AR(S) + 4), "i"(AR(S) + 5), "i"(AR(S) + 6))
        const unsigned voffb = voff + 768;
        u64 ln = 0;
#define TDUE(G) (tdue(G) + ((G) >= PSW ? __builtin_amdgcn_readfirstlane((unsigned)(ln >> 32)) >> ((G) >= PSG ? 16 : 0) & 0xffffu : 0u))
#define REPORT() { const unsigned t_ = now - tdc; if (lane == 0) lds_st(&sm.late, t_); }
#define LINE() ln = LDV((u64*)&sm.line);
#define LDV(P) __hip_atomic_load(P, __ATOMIC_RELAXED, __HIP_MEMORY_SCOPE_WORKGROUP)
        unsigned u = 0, rg = cst * 8 + wave, np = 0;
        CS_DMA(wave, cst)

        const char* kb0 = (const char*)qk + (size_t)(head0 * 512 + r8w) * 3072;
#define KB(I) (kb0 + ((cst + (I)) & 3) * (128 * 3072))
        const char* kb; const char* kb2;
#define SISSUE(I) { kb = KB(I); kb2 = kb + HEAD2; const unsigned du_ = lds_ld(&sm.line) + tdue(16 * (I) + 2 * wo); GATE(du_) \
            ISSUE(((I) & 1) * 4, 0, kb, koff, koffb) ISSUE(((I) & 1) * 4 + 1, 1, kb, koff, koffb) ISSUE(((I) & 1) * 4 + 2, 0, kb2, koff, koffb) ISSUE(((I) & 1) * 4 + 3, 1, kb2, koff, koffb) }
        if (!pre) SISSUE(0)
        CS_DMA(wave, (cst + 1) & 3)
        if (pre) asm volatile("s_waitcnt vmcnt(3)" ::: "memory");
        asm volatile("s_waitcnt vmcnt(11)" ::: "memory");
        if (!pre) LDS_ADD(&sm.slow, NOW() - lds_ld(&sm.line) - tdue(2 * wo))
        REPACK(wave, cst)
        SISSUE(1)
#define TABDONE(I) LDS_OR(&sm.tabs, 1u << ((((cst + (I)) & 3) * 8) + wave))
        TABDONE(0)
        const float scale = 0.07216878364870322f;
        double acc[3] = {0.0, 0.0, 0.0};
        bool first = true, chg, none = false;
        unsigned Qp[3], Qn[3], Rp[3], Rn[3];
        float kz4[4], ksc, kres[4];
        v4u ca[4]; v2u cb[4];
        v2f qe[4][3], A[4];
        unsigned vut, nrg;
        const unsigned vr = ((lane >> 3) & 3) * 128 + R8((unsigned)lane & 7);
        unsigned g, mg;
        const unsigned grab_adr = lds_addr(&sm.grab);
        asm volatile("v_accvgpr_write_b32 a13, 1\n\tv_accvgpr_write_b32 a14, 2" ::: "a13", "a14", "a15");
#define GRAB(N) { u64 ex_; asm volatile("s_mov_b64 %0, exec\n\ts_mov_b64 exec, 1\n\tds_add_rtn_u32 a15, %1, a%c2\n\ts_mov_b64 exec, %0" : "=&s"(ex_) : "v"(grab_adr), "i"(12 + (N)) : "memory"); }
#define GRABBED() { unsigned t_; asm volatile("s_waitcnt lgkmcnt(0)\n\tv_accvgpr_read_b32 %1, a15\n\ts_nop 0\n\tv_readfirstlane_b32 %0, %1\n\ts_nop 1" : "=s"(g), "=v"(t_)); }
        unsigned oa, ob, un, tdn, tiss, tdc = 0;
#define PLAN() { \
            GRABBED() un = unit_of(g); chg = un != u; none = g >= NPC; \
            const bool nv = g >= NPC / 2; \
            unsigned hd = __builtin_amdgcn_readlane(vut, un); \
            while (hd == ~0u) { SLEEP(1) hd = lds_ld(&sm.utab[un]); } \
            nrg = (g + (hd >> 8)) & 31; hd &= 255; \
            unsigned boff, boff2; asm("s_mul_i32 %0, %1, 0xc00" : "=s"(boff) : "s"(hd * 512 + __builtin_amdgcn_readlane(vr, nrg))); \
            asm("s_mul_i32 %0, %1, 0xc00" : "=s"(boff2) : "s"(hd * 512 + __builtin_amdgcn_readlane(vr, nrg + 1))); \
            const char* b_ = nv ? (const char*)qv : (const char*)qk; \
            kb = b_ + boff; kb2 = b_ + boff2; \
            oa = nv ? voff : koff; ob = nv ? voffb : koffb; \
            tdn = TDUE(g); }
#define FIRE() { ISSUE(0, 0, kb, oa, ob) ISSUE(1, 1, kb, oa, ob) ISSUE(2, 0, kb2, oa, ob) ISSUE(3, 1, kb2, oa, ob) }
#define LAUNCH() { \
            LINE() \
            const unsigned du_ = __builtin_amdgcn_readfirstlane((unsigned)ln) + tdn; \
            GATE(du_) \
            ISSUE(0, 0, kb, oa, ob) ISSUE(1, 1, kb, oa, ob) \
            GATE(du_ + 10) \
            ISSUE(2, 0, kb2, oa, ob) ISSUE(3, 1, kb2, oa, ob) tiss = NOW(); }
#define ARRIVED() \
            const unsigned now = NOW(); \
            const bool stall = now - tiss > LJ && g - PST0 < NPC - 48 - PST0; \
            const bool late = !none && !stall && (int)(now + EARLY - __builtin_amdgcn_readfirstlane((unsigned)ln) - tdn) >= 0; \
            if (late) { FIRE() tiss = now; } \
            if (stall && lane == 0) __hip_atomic_fetch_max(&sm.line, (unsigned)ln + (now - tiss) - LOK2, __ATOMIC_RELAXED, __HIP_MEMORY_SCOPE_WORKGROUP); \
            REPORT()
#define ADVANCE(ISV) { \
            if (chg) FLUSH(ISV) \
            if (!none && !late) LAUNCH() \
            first = chg; u = un; rg = nrg; tdc = tdn + 10; }
#define K_TAB(MODE) { _Pragma("unroll") for (int p = 0; p < ((MODE) ? 2 : 4); p++) { \
            const unsigned* cw = &sm.cs[(rg & 7) + (p >> 1)][rg >> 3][0]; \
            ca[p] = *(const v4u*)&cw[(p & 1) * 256 + lane * 4]; cb[p] = *(const v2u*)&cw[512 + (p & 1) * 128 + lane * 2]; } }
#define K_PRE(MODE, P) { \
        float e0, e1, e2, f0, f1, f2; \
        const unsigned* qp_ = ((MODE) && (P) >= 2) ? Rp : Qp; const unsigned* qn_ = ((MODE) && (P) >= 2) ? Rn : Qn; \
        const int tp_ = (MODE) ? (P) & 1 : (P); \
        asm("v_dot2_f32_bf16 %0, %6, %12, 0\n\tv_dot2_f32_bf16 %3, %9, %15, 0\n\tv_dot2_f32_bf16 %1, %7, %13, 0\n\tv_dot2_f32_bf16 %4, %10, %16, 0\n\t" \
            "v_dot2_f32_bf16 %2, %8, %14, 0\n\tv_dot2_f32_bf16 %5, %11, %17, 0\n\ts_nop 2" \
            : "=&v"(e0), "=&v"(e1), "=&v"(e2), "=&v"(f0), "=&v"(f1), "=&v"(f2) \
            : "v"(qp_[0]), "v"(qp_[1]), "v"(qp_[2]), "v"(qn_[0]), "v"(qn_[1]), "v"(qn_[2]), \
              "v"(ca[tp_].x), "v"(ca[tp_].y), "v"(ca[tp_].z), "v"(ca[tp_].w), "v"(cb[tp_].x), "v"(cb[tp_].y)); \
        qe[P][0] = (v2f){e0, f0}; qe[P][1] = (v2f){e1, f1}; qe[P][2] = (v2f){e2, f2}; \
        A[P] = (qe[P][0] + qe[P][1] + qe[P][2]) * (v2f){-kz4[P], -kz4[P]}; }
#define K_WAIT(MODE, P) { \
        if ((MODE) == 0) { WAITT(P, P, 6 - 2 * (P)); } \
        else { WAITT(P, (((MODE) - 1) & 1) * 4 + (P), ((MODE) < 3 ? 20 : (MODE) == 3 ? 17 : 14) - 2 * (P)); } }
#define K_FMA(P) { \
        _Pragma("unroll") \
        for (int i = 0; i < 3; i++) { v2f x = {(float)ta[P][i], (float)tb[P][i]}; A[P] = qe[P][i] * x + A[P]; } \
        kres[P] = A[P].x + A[P].y; }
#define K_RED(MODE) { \
        const bool o1 = lane & 1, o2 = lane & 2; \
        float s01 = (o1 ? kres[1] : kres[0]) + __int_as_float(__builtin_amdgcn_update_dpp(0, __float_as_int(o1 ? kres[0] : kres[1]), 0xb1, 0xf, 0xf, true)); \
        float s23 = (o1 ? kres[3] : kres[2]) + __int_as_float(__builtin_amdgcn_update_dpp(0, __float_as_int(o1 ? kres[2] : kres[3]), 0xb1, 0xf, 0xf, true)); \
        float sx = (o2 ? s23 : s01) + __int_as_float(__builtin_amdgcn_update_dpp(0, __float_as_int(o2 ? s01 : s23), 0x4e, 0xf, 0xf, true)); \
        sx = dpp_add<0x124>(sx); sx = dpp_add<0x128>(sx); \
        const auto sw = __builtin_amdgcn_permlane16_swap(__float_as_uint(sx), __float_as_uint(sx), false, false); \
        sx = __uint_as_float(sw[0]) + __uint_as_float(sw[1]); \
        if ((lane & 28) == 0) { if (MODE) sm.ent[(lane >> 1) & 1][rg * 4 + 2 * (lane & 1) + rsel] = sx * ksc; else sm.ent[u % NOUT][rg * 4 + 2 * (lane & 3) + rsel] = sx * ksc; } }
#define K_PRE4(MODE) { K_PRE(MODE, 0) K_PRE(MODE, 1) K_PRE(MODE, 2) K_PRE(MODE, 3) asm volatile("" :: "v"(A[0]), "v"(A[1]), "v"(A[2]), "v"(A[3])); }
#define K_BODY(MODE) { K_PRE4(MODE) K_WAIT(MODE, 0) K_FMA(0) K_WAIT(MODE, 1) K_FMA(1) K_WAIT(MODE, 2) K_FMA(2) K_WAIT(MODE, 3) K_FMA(3) K_RED(MODE) }
#define V_FMA(J, W0, W1) { \
        _Pragma("unroll") \
        for (int i = 0; i < 3; i++) { \
            acc[i] = fma(W0, (double)ta[J][i], acc[i]); \
            acc[i] = fma(W1, (double)tb[J][i], acc[i]); \
        } }
#define V_PIN() asm volatile("" :: "v"(acc[0]), "v"(acc[1]), "v"(acc[2]));
#define RDYWAIT(SL, SEQ) { while (lds_ld(&sm.rdy[SL]) != (SEQ)) SLEEP(1); CBAR(); }
#define LOADZ() { _Pragma("unroll") for (int p = 0; p < 4; p++) kz4[p] = sm.kz[u % NIN][rg * 4 + 2 * p + rsel].x; ksc = sm.kz[u % NIN][rg * 4 + 2 * (lane & 3) + rsel].y * scale; }
#define LOADQ() { _Pragma("unroll") for (int i = 0; i < 3; i++) { Qp[i] = sm.q[u % NIN][3 * sub + i]; Qn[i] = __builtin_amdgcn_alignbit(Qp[i], Qp[i], 16) ^ 0x80000000u; } }
#define FLUSH(ISV) { \
            mg = __builtin_amdgcn_readfirstlane(mg); \
            while (u >= mg + NOUT) { SLEEP(1); mg = lds_ld(&sm.merged); } \
            CBAR(); \
            if (ISV) { const unsigned aa = lds_addr(&sm.acc[u % NOUT][lane]); \
                asm volatile("ds_add_f64 %0, %1\n\tds_add_f64 %0, %2 offset:512\n\tds_add_f64 %0, %3 offset:1024" :: "v"(aa), "v"(acc[0]), "v"(acc[1]), "v"(acc[2]) : "memory"); \
                acc[0] = 0.0; acc[1] = 0.0; acc[2] = 0.0; } \
            CBAR(); \
            LDS_ADD(&sm.cnt[u % NOUT], np) \
            CBAR(); \
            np = 0; }

#define LOADZ2() { _Pragma("unroll") for (int p = 0; p < 4; p++) kz4[p] = sm.kz[p >> 1][rg * 4 + 2 * (p & 1) + rsel].x; ksc = sm.kz[(lane >> 1) & 1][rg * 4 + 2 * (lane & 1) + rsel].y * scale; }
#define STATIC_STEP(I) { \
            rg = ((cst + (I)) & 3) * 8 + wave; \
            LOADZ2() \
            K_TAB((I) + 1) K_BODY((I) + 1) }
        CS_DMA(wave, (cst + 2) & 3)
        RDYWAIT(0, 1u) RDYWAIT(1, 2u)
        LOADQ()
#pragma unroll
        for (int i = 0; i < 3; i++) { Rp[i] = sm.q[1][3 * sub + i]; Rn[i] = __builtin_amdgcn_alignbit(Rp[i], Rp[i], 16) ^ 0x80000000u; }
        STATIC_STEP(0)
        asm volatile("s_waitcnt vmcnt(11)" ::: "memory");
        REPACK(wave, (cst + 1) & 3)
        TABDONE(1)
        SISSUE(2)
        CS_DMA(wave, (cst + 3) & 3)
        STATIC_STEP(1)
        asm volatile("s_waitcnt vmcnt(11)" ::: "memory");
        REPACK(wave, (cst + 2) & 3)
        TABDONE(2)
        SISSUE(3)
        asm volatile("s_waitcnt vmcnt(8)" ::: "memory");
        REPACK(wave, (cst + 3) & 3)
        TABDONE(3)
        STATIC_STEP(2)
        GRAB(2)
        vut = LDV(&sm.utab[lane]);
        rg = ((cst + 3) & 3) * 8 + wave;
        LOADZ2() K_TAB(4) K_PRE4(4)
        PLAN()
        LAUNCH()
        K_WAIT(4, 0) K_FMA(0) K_WAIT(4, 1) K_FMA(1) K_WAIT(4, 2) K_FMA(2) K_WAIT(4, 3) K_FMA(3) K_RED(4)
        CBAR();
        LDS_ADD(&sm.cnt[0], 4u) LDS_ADD(&sm.cnt[1], 4u)
        u = un; rg = nrg; tdc = tdn + 10;
        bool tabok = false;
        __builtin_amdgcn_s_setprio(2);
#pragma unroll 1
        while (u < NU) {
            GRAB(2)
            mg = LDV(&sm.merged);
            vut = LDV(&sm.utab[lane]);
            LINE()
            if (!tabok) {
                for (;;) { const unsigned tb_ = lds_ld(&sm.tabs); tabok = tb_ == ~0u; if (((tb_ >> rg) & 3u) == 3u) break; SLEEP(1) }
                CBAR();
            }
            K_TAB(0)
            if (first) {
                unsigned fl = LDV(&sm.rdy[u % NIN]);
                CBAR();
                LOADQ() LOADZ()
                if (__builtin_amdgcn_readfirstlane(fl) != u + 1) { RDYWAIT(u % NIN, u + 1) LOADQ() LOADZ() }
            } else LOADZ()
            K_PRE4(0)
            PLAN()
            K_WAIT(0, 0) K_FMA(0) K_WAIT(0, 1) K_FMA(1)
            asm volatile("" :: "v"(kres[0]), "v"(kres[1]));
            K_WAIT(0, 2) K_WAIT(0, 3)
            ARRIVED()
            K_FMA(2) K_FMA(3) K_RED(0)
            np += 2;
            ADVANCE(false)
        }
#pragma unroll 1
        while (u < 2 * NU) {
            GRAB(2)
            mg = LDV(&sm.merged);
            LINE()
            const unsigned vr_ = LDV(&sm.vrdy);
            CBAR();
            const unsigned short* wp = &sm.wv[went(u - NU) + (QUARTER(u - NU) ? rg & 7 : rg) * 4];
            v4i pw = *(const v4i*)wp;
            if (first && !((__builtin_amdgcn_readfirstlane(vr_) >> (u - NU)) & 1)) { while (!((lds_ld(&sm.vrdy) >> (u - NU)) & 1)) SLEEP(1); CBAR(); pw = *(const v4i*)wp; }
            const double w0 = (short)pw.x, w1 = pw.x >> 16, w2 = (short)pw.y, w3 = pw.y >> 16, w4 = (short)pw.z, w5 = pw.z >> 16, w6 = (short)pw.w, w7 = pw.w >> 16;
            asm volatile("" :: "v"(w0), "v"(w1), "v"(w2), "v"(w3), "v"(w4), "v"(w5), "v"(w6), "v"(w7));
            PLAN()
            v2f hl; unsigned tl8, rowl;
            if (none) { hl = *(const v2f*)&sm.wv[went(NU - 1) + 128]; tl8 = LDV(&sm.tag_lo) << 24; rowl = (lds_ld(&sm.utab[2 * NU - 1]) & 255) * (NSPLIT * 512); asm volatile("" : "+v"(hl), "+v"(tl8)); }
            WAITT(0, 0, 6); V_FMA(0, w0, w1) V_PIN()
            WAITT(1, 1, 4); V_FMA(1, w2, w3) V_PIN()
            WAITT(2, 2, 2); V_FMA(2, w4, w5) V_PIN()
            WAITT(3, 3, 0);
            np += 2;
            if (none) {
                V_FMA(3, w6, w7)
                if (u != 2 * NU - 1) { FLUSH(true) break; }
                mg = __builtin_amdgcn_readfirstlane(mg);
                while (u >= mg + NOUT) { SLEEP(1); mg = lds_ld(&sm.merged); }
                const unsigned aa = lds_addr(&sm.acc[(2 * NU - 1) % NOUT][lane]);
                double a0, a1, a2; unsigned cn; u64 ex_;
                asm volatile("ds_add_f64 %5, %6\n\tds_add_f64 %5, %7 offset:512\n\tds_add_f64 %5, %8 offset:1024\n\t"
                             "s_mov_b64 %4, exec\n\ts_mov_b64 exec, 1\n\tds_add_rtn_u32 %3, %9, %10\n\ts_mov_b64 exec, %4\n\t"
                             "ds_read_b64 %0, %5\n\tds_read_b64 %1, %5 offset:512\n\tds_read_b64 %2, %5 offset:1024\n\ts_waitcnt lgkmcnt(0)"
                             : "=&v"(a0), "=&v"(a1), "=&v"(a2), "=&v"(cn), "=&s"(ex_) : "v"(aa), "v"(acc[0]), "v"(acc[1]), "v"(acc[2]), "v"(lds_addr(&sm.cnt[(2 * NU - 1) % NOUT])), "v"(np) : "memory");
                const float n0 = (float)(a0 - (double)hl.y) * hl.x, n1 = (float)(a1 - (double)hl.y) * hl.x, n2 = (float)(a2 - (double)hl.y) * hl.x;
                const unsigned e2 = max(__float_as_uint(fmaxf(fmaxf(fabsf(n0), fabsf(n1)), fabsf(n2))) >> 23, 14u);
                const float f2 = __uint_as_float((268u - e2) << 23);
                const v2u r = {__builtin_bit_cast(unsigned, __builtin_amdgcn_cvt_pk_i16((int)rintf(n0 * f2), (int)rintf(n1 * f2))),
                               __builtin_bit_cast(unsigned, __builtin_amdgcn_cvt_pk_i16((int)rintf(n2 * f2), 0)) | e2 << 16 | tl8};
                const __amdgpu_buffer_rsrc_t rsrc = __builtin_amdgcn_make_buffer_rsrc(ws + WS_PART + split * 512, 0, NRW * NSPLIT * 512 - split * 512, 0x00020000);
                asm volatile("" :: "v"(r));
                if (__builtin_amdgcn_readfirstlane(cn + np) == 32) {
                    __builtin_amdgcn_raw_buffer_store_b64(r, rsrc, lane * 8, rowl, SC1);
#pragma unroll
                    for (int j = 0; j < NCP - 1; j++) __builtin_amdgcn_raw_buffer_store_b64(r, rsrc, lane * 8, (rowl & 15 * NSPLIT * 512 ^ NSPLIT * 512) + (NRR + j * GSZ) * NSPLIT * 512, SC1);
                }
                break;
            }
            ARRIVED()
            V_FMA(3, w6, w7)
            ADVANCE(true)
        }
    } else {
    u64 E1;
    if (wave == NSW) {
        unsigned ref;
        const unsigned sidx = (blockIdx.x & 7) | ((blockIdx.x >> 7) << 3);
#define SAMPLE(K) ((((blockIdx.x >> 3) - (blockIdx.x & 7) - (K) * 5) & 15) == 0)
#define BTAG(K) ((btag + (K)) & 0xfffffu)
#define POST(K, C) { if (lane == 0) __hip_atomic_store((u64*)(ws + (bd + (K) * RST + (sidx >> 3) * 256 + (sidx & 7) * 8)), (u64)(C) | ((u64)(((C) - ref) | BTAG(K) << 12) << 32), __ATOMIC_RELAXED, __HIP_MEMORY_SCOPE_AGENT); }
        const unsigned own = lds_ld(&sm.line);
        if (SAMPLE(0) && lane < NTRY) __hip_atomic_store((unsigned*)(ws + WS_HELLO + lane * 768) + sidx, own, __ATOMIC_RELAXED, __HIP_MEMORY_SCOPE_AGENT);
        const u64 E = __hip_atomic_load((const u64*)(ws + WS_EPOCH), __ATOMIC_RELAXED, __HIP_MEMORY_SCOPE_AGENT);
        E1 = E + 1;
        if (lane == 0) { sm.tag_lo = (unsigned)E1; sm.tag_hi = (unsigned)(E1 >> 32); CBAR(); lds_st(&sm.tag_rdy, 1u); }
        const unsigned btag = __builtin_amdgcn_readfirstlane((unsigned)E1) * 61u, bd = WS_BOARD + (btag & 7) * (NRND * RST);
        ref = own;
        int off = 0;
        {
            unsigned tries = 0, cur = own, tl = own;
            bool news = false;
#pragma unroll 1
            while (!news || cur != ref) {
                const unsigned now = NOW();
                if (tries < NTRY && (int)(now - own) >= (int)(TA + tries * TA2)) {
                    v16u x;
                    asm volatile("s_load_dwordx16 %0, %1, %2\n\ts_waitcnt lgkmcnt(0)" : "=&s"(x) : "s"(ws), "s"(__builtin_amdgcn_readfirstlane(WS_HELLO + tries * 768)) : "memory");
                    unsigned bad = 0; int sum = 0, dif = 0;
#pragma unroll
                    for (int i = 0; i < 16; i++) { bad |= x[i] - own + 512; sum += x[i] - x[0]; dif += i & 4 ? x[i] : -x[i]; }
                    tries++;
                    if (bad < 1024) { const unsigned com = x[0] + (sum >> 4) + abs(dif) * (8 - KC) / 128; ref = x[0] + ((sum + (blockIdx.x & 4 ? dif : -dif)) >> 4); off = ref - com;
                        tries = NTRY; tl = NOW(); if (lane == 0) lds_st(&sm.aline, com); }
                    continue;
                }
                if (!news && lds_ld(&sm.utab[31]) != ~0u) { news = true; CBAR(); if (cur == ref) tl = now; ref = lds_ld(&sm.aline) + off; tries = NTRY; }
                const int d = (now - tl) >> CSH;
                if (cur != ref && d) { tl += d << CSH; cur += min(max((int)(ref - cur), -d), d); if (lane == 0) lds_st(&sm.line, cur); }
                SLEEP(2)
            }
        }
        ref -= off;
        const unsigned ref0 = ref;
        const unsigned lead = (blockIdx.x >> 3) == 9 ? 0 : LEAD;
        unsigned posted = ref, lagf = ref, tpost = ref + TB1, tpoll = tpost + RDD + lead, kw = 0, kr = 0, cur = ref + off, khold = 0;
        int o = off; bool held = false;
#pragma unroll 1
        for (;;) {
            const unsigned p = lds_ld(&sm.grab);
            if (p >= NPC) break;
            lagf += (int)(lds_ld(&sm.late) - lagf) >> 1;
            { const unsigned l = lds_ld(&sm.line); if (l != cur) { cur = l; khold = kw; held = true; } }
            const unsigned now = NOW();
            if (o) { o = off * min(max((int)(ref0 + TC0 + (1 << TCS) - now), 0), 1 << TCS) >> TCS; if (!held && cur != ref + o) { cur = ref + o; if (lane == 0) lds_st(&sm.line, cur); } }
            if (kw < NRND && (int)(now - tpost) >= 0) {
                const unsigned c = ref + min((p >= PSL0 && (int)(lagf - cur) > LHI ? lagf - LOK : cur) - o - ref, 4000u);
                if (SAMPLE(kw) && (c != ref || (ref != posted && (sidx & 7) == (kw & 7)))) POST(kw, c)
                posted = ref;
                tpost += PER; kw++;
            }
            if (kr < NRND && (int)(now - tpoll) >= 0) {
                v16u xa, xb;
                asm volatile("s_load_dwordx16 %0, %2, %3\n\ts_load_dwordx16 %1, %2, %4\n\ts_waitcnt lgkmcnt(0)" : "=&s"(xa), "=&s"(xb)
                             : "s"(ws), "s"(__builtin_amdgcn_readfirstlane(bd + kr * RST)), "s"(__builtin_amdgcn_readfirstlane(bd + kr * RST + 256)) : "memory");
                int nb = 0, sg = 0;
#pragma unroll
                for (int i = 0; i < 16; i++) {
                    const unsigned lo = i < 8 ? xa[2 * i & 15] : xb[2 * i & 15], hi = i < 8 ? xa[(2 * i + 1) & 15] : xb[(2 * i + 1) & 15];
                    if (hi >> 12 == BTAG(kr) && lo - ref0 < 8000) { nb = max(nb, (int)(lo - (hi & 4095u) - ref)); sg += hi & 4095u; }
                }
                ref += nb + (sg >> 4);
                if (cur != ref + o && (kr >= khold || (int)(ref + o - cur) > 0)) { cur = ref + o; held = false; if (lane == 0) lds_st(&sm.line, cur); }
                tpoll += PER; kr++;
            }
            SLEEP(2)
        }
    } else if (wave == NSW + 1 || wave == NSW + 2) {
        const int ql = lane < 48 ? lane : 47;
        unsigned c1, c2, s1, s2;
#define QT_LOADS() { int qo_ = ql; asm volatile("" : "+v"(qo_)); \
                     c1 = ((const unsigned*)q_cos)[qo_]; c2 = ((const unsigned*)q_cos)[48 + qo_]; s1 = ((const unsigned*)q_sin)[qo_]; s2 = ((const unsigned*)q_sin)[48 + qo_]; }
        unsigned q1, q2, lkz, lks, lvz, lvs;
        const unsigned sofs = R8(((unsigned)lane >> 1) & 7) * 2 + (lane & 1);
#define UNIT_LOADS(HEAD, GRP) { \
        q1 = ((const unsigned*)(q + (size_t)(HEAD) * DH))[ql]; q2 = ((const unsigned*)(q + (size_t)(HEAD) * DH))[48 + ql]; \
        const size_t so = (size_t)(HEAD) * (SKV / 2) + (GRP) * 256 + sofs; \
        lkz = *((const unsigned*)k_zeros + so); lks = *((const unsigned*)k_scales + so); \
        lvz = *((const unsigned*)v_zeros + so); lvs = *((const unsigned*)v_scales + so); }
#define UNIT_FILL(SL, U) { \
            v4f qq; \
            qq.x = bfr(bfr(bflo(q1) * bflo(c1)) + bfr(-bflo(q2) * bflo(s1))); \
            qq.y = bfr(bfr(bflo(q2) * bflo(c2)) + bfr(bflo(q1) * bflo(s2))); \
            qq.z = bfr(bfr(bfhi(q1) * bfhi(c1)) + bfr(-bfhi(q2) * bfhi(s1))); \
            qq.w = bfr(bfr(bfhi(q2) * bfhi(c2)) + bfr(bfhi(q1) * bfhi(s2))); \
            *(v2u*)&sm.q[SL][2 * ql] = (v2u){(__float_as_uint(qq.x) >> 16) | __float_as_uint(qq.y), (__float_as_uint(qq.z) >> 16) | __float_as_uint(qq.w)}; \
            *(v4f*)&sm.kz[SL][2 * lane] = (v4f){bflo(lkz), bflo(lks), bfhi(lkz), bfhi(lks)}; \
            *(v4f*)&sm.vz[SL][2 * lane] = (v4f){bflo(lvz), bflo(lvs), bfhi(lvz), bfhi(lvs)}; \
            CBAR(); \
            if (lane == 0) lds_st(&sm.rdy[SL], (U) + 1); \
            CBAR(); }
        const unsigned dw = NSW + 2 - wave;
        QT_LOADS()
        UNIT_LOADS(head0 + dw * (GSZ + 1 - 2 * side), lane >> 4)
#pragma unroll 1
        for (unsigned u = dw;;) {
            while (u >= lds_ld(&sm.merged) + NIN) SLEEP(4);
            CBAR();
            UNIT_FILL(u % NIN, u)
            if (u - 1 < 12) {
                const unsigned x_ = (blockIdx.x >> 3) + 32 * u - 8; unsigned dm;
                const char* p_ = x_ < 384 ? (const char*)(x_ < 192 ? qk : qv) + (size_t)(x_ < 192 ? x_ : x_ - 192) * (2u << 20) + (x_ * 8 + (blockIdx.x & 7)) * 640 : (const char*)out + (x_ - 384) * 4096;
                while ((int)(NOW() - lds_ld(&sm.line)) < TSD) SLEEP(2)
                asm volatile("s_load_dword %0, %1, 0x0\n\ts_waitcnt lgkmcnt(0)" : "=&s"(dm) : "s"(p_) : "memory");
            }
            u += 2;
            if (u >= NU) break;
            if (u == 4) {
                const unsigned tpf = tdue(PSF - 1) + TFO;
                { int d; while ((d = (int)(lds_ld(&sm.line) + tpf - NOW())) > 0 && d < 3000) SLEEP(1) }
#define PF3(P) asm volatile("global_load_dwordx4 a[0:3], %0, %1\n\tglobal_load_dwordx4 a[0:3], %0, %1 offset:1024\n\tglobal_load_dwordx4 a[0:3], %0, %1 offset:2048" \
                            :: "v"(64 * SKV * 2 + blockIdx.x * 3072 + lane * 16), "s"(P) : "a0", "a1", "a2", "a3")
                PF3(k_zeros); PF3(k_scales); PF3(v_zeros); PF3(v_scales);
                asm volatile("global_load_dword a0, %0, %1" :: "v"(blockIdx.x * (DH * 2) + ql * 8), "s"(q) : "a0");
            }
            const unsigned tin = tdue(first_of(u) - (QUARTER(u) ? 24 : 0) - LEADP + (split & 7) * DPH);
            { int d; while ((d = (int)(lds_ld(&sm.line) + tin - NOW())) > 0 && d < 3000) SLEEP(4) }
            unsigned hd;
            while ((hd = lds_ld(&sm.utab[u])) == ~0u) SLEEP(2)
            UNIT_LOADS(hd & 255, QUARTER(u) ? rank & 3 : lane >> 4)
        }
        while (lds_ld(&sm.tag_rdy) == 0) SLEEP(1);
        CBAR();
        E1 = (u64)lds_ld(&sm.tag_lo) | ((u64)lds_ld(&sm.tag_hi) << 32);
        if (wave == NSW + 1 && blockIdx.x == 0) {
            for (;;) {
                bool ok = true;
#pragma unroll
                for (int i = 0; i < NH / 64; i++) ok &= __hip_atomic_load((const u64*)(ws + WS_POST) + i * 64 + lane, __ATOMIC_RELAXED, __HIP_MEMORY_SCOPE_AGENT) >> 24 == (E1 << 24) >> 24;
                if (__builtin_amdgcn_ballot_w64(ok) == ~0ull) break;
                SLEEP(64);
            }
            if (lane == 0) __hip_atomic_store((u64*)(ws + WS_EPOCH), E1, __ATOMIC_RELAXED, __HIP_MEMORY_SCOPE_AGENT);
        }
        if (wave == NSW + 2) {
            const unsigned t16 = (unsigned)E1 & 0xffffu, t16b = ((unsigned)E1 + ((unsigned)E1 >> 16)) & 0xffffu;
            const __amdgpu_buffer_rsrc_t rsrc = __builtin_amdgcn_make_buffer_rsrc(ws + WS_SWAP + (blockIdx.x ^ 4) * 5120, 0, 5120, 0x00020000);
            const unsigned swp = lds_ld(&sm.swp);
            if (swp) {
                unsigned use = 0;
#pragma unroll
                for (int i = 0; i < 5; i++) use |= ((swp >> wunit(i * 64 + lane)) & 1) << i;
                const unsigned tg = tdue(PSG - 1) + (lds_ld(&sm.gap) & 0xffffu);
                while ((int)(NOW() - lds_ld(&sm.line) - tg) < 0) SLEEP(2)
                for (;;) {
                    v4u d[5]; bool ok = true;
#pragma unroll
                    for (int i = 0; i < 5; i++) d[i] = __builtin_amdgcn_raw_buffer_load_b128(rsrc, (use >> i) & 1 ? (i * 64 + lane) * 16 : 0x40000000, 0, SC1 | 2);
#pragma unroll
                    for (int i = 0; i < 5; i++) if ((use >> i) & 1) {
                        ok &= d[i].y >> 16 == t16 && d[i].w >> 16 == t16b;
                        unsigned* w = (unsigned*)&sm.wv[144 * NMIX + (i * 64 + lane) * 6];
                        w[0] = d[i].x; w[1] = (d[i].y & 0xffffu) | (d[i].z << 16); w[2] = (d[i].z >> 16) | (d[i].w << 16);
                    }
                    if (__builtin_amdgcn_ballot_w64(ok) == ~0ull) break;
                    SLEEP(32)
                }
                CBAR();
                LDS_OR(&sm.vrdy, swp)
            }
        }
    } else {
        const unsigned eu = lane >= NU ? lane - NU : lane, win = min(first_of(eu) >> 5, 15u), pair = QUARTER(eu) ? 2 * (eu - WQ) + (rank >> 2) : rank;
        const unsigned rot2 = ((const unsigned char*)ROT)[win * 8 + pair];
#define UENT(BIT) (win * 16 + 2 * pair + ((BIT) ^ side) | (QUARTER(eu) ? (rank - (eu - WQ)) * 8 & 31 : (rot2 >> ((BIT) ^ side) * 4 & 15) * 2) << 8)
        unsigned far = 0;
        const unsigned hp = (blockIdx.x & 1) * NH + (blockIdx.x >> 1) * 4 + 2;
        if (blockIdx.x < 144) {
            const char* pb = (blockIdx.x < 128 ? (const char*)(hp >= NH ? qv : qk) + (size_t)(hp & (NH - 1)) * (SKV * DH * 4) + SKV * DH * 2
                                               : (const char*)ws + WS_PART + (8u << 20) + (blockIdx.x & 15) * 65536) + ((NOW() * 0x9e3779b1u) >> 26) * 128;
            unsigned mn[2] = {~0u, ~0u};
#pragma unroll 1
            for (unsigned i = 1; i < 2 * NPRB + 2; i++) {
                u64 t0, t1; unsigned dm;
                asm volatile("s_memtime %0\n\ts_waitcnt lgkmcnt(0)\n\ts_load_dword %2, %3, 0x0\n\ts_waitcnt lgkmcnt(0)\n\ts_memtime %1\n\ts_waitcnt lgkmcnt(0)"
                             : "=&s"(t0), "=&s"(t1), "=&s"(dm) : "s"(pb + i * 8192) : "memory");
                if (i == 1) asm volatile("global_load_dword a0, %0, %1 nt" :: "v"(0), "s"(pb) : "a0");
                const unsigned t = (unsigned)(t1 - t0) < 330 || i == 1 ? ~0u : (unsigned)(t1 - t0);
                if (i & 1) mn[1] = min(mn[1], t); else mn[0] = min(mn[0], t);
            }
            far = mn[0] > mn[1];
        }
        while (lds_ld(&sm.tag_rdy) == 0) SLEEP(1);
        CBAR();
        E1 = (u64)lds_ld(&sm.tag_lo) | ((u64)lds_ld(&sm.tag_hi) << 32);
        {
            const unsigned own = lds_ld(&sm.line), tg = (unsigned)E1;
            if (lane == 0) __hip_atomic_store((u64*)(ws + WS_POST) + blockIdx.x, E1 << 24 | min(lds_ld(&sm.slow), 4000u) << 1 | ((far ^ (blockIdx.x >> 2) ^ (blockIdx.x < 128 ? 0x96 >> (hp & 7) : 0)) & 1), __ATOMIC_RELAXED, __HIP_MEMORY_SCOPE_AGENT);
            const __amdgpu_buffer_rsrc_t nrs = __builtin_amdgcn_make_buffer_rsrc(ws + WS_NEWS, 0, 8 * 1280, 0x00020000);
            if (blockIdx.x == MASTER) {
                while ((int)(NOW() - own) < TMAP) SLEEP(2)
                const unsigned a = tg << 24, b = (unsigned)(E1 >> 8);
                v4u x; v2u z; u64 v0, v1;
#pragma unroll 1
                for (int i = 0; i < 8; i++) {
                    x = __builtin_amdgcn_raw_buffer_load_b128(__builtin_amdgcn_make_buffer_rsrc(ws + WS_POST, 0, 1024, 0x00020000), lane * 16, 0, SC1);
                    z = __builtin_amdgcn_raw_buffer_load_b64(__builtin_amdgcn_make_buffer_rsrc(ws + WS_POST + 1024, 0, 128, 0x00020000), lane * 8, 0, SC1);
                    v0 = __builtin_amdgcn_ballot_w64(((x.x ^ a) >> 24 | (x.y ^ b)) == 0); v1 = __builtin_amdgcn_ballot_w64(((x.z ^ a) >> 24 | (x.w ^ b)) == 0);
                    if (__builtin_popcountll(v0) + __builtin_popcountll(v1) >= MINV) break;
                    SLEEP(8)
                }
                const u64 m0 = __builtin_amdgcn_ballot_w64(x.x & 1) & v0, m1 = __builtin_amdgcn_ballot_w64(x.z & 1) & v1;
                const int s0 = lane < 43 ? min(max(lane - VW, 4 * NMIX), 42 - 2 * VW) : min(max(lane - VW, 43), 63 - 2 * VW);
#define PC(M) __builtin_popcount((unsigned)((M) >> s0) & ((2u << 2 * VW) - 1))
                const u64 g0 = __builtin_amdgcn_ballot_w64(2 * PC(m0) > PC(v0)), g1 = __builtin_amdgcn_ballot_w64(2 * PC(m1) > PC(v1));
                const float sl = wave_sum(((v0 >> lane & 1) ? (float)(x.x >> 1 & 4095u) : 0.f) + ((v1 >> lane & 1) ? (float)(x.z >> 1 & 4095u) : 0.f)) / ((8.f - PRE0) * (__builtin_popcountll(v0) + __builtin_popcountll(v1)) + 1.f);
                const unsigned al = lds_ld(&sm.aline), mean = (al ? al : own) + (int)fmaxf(sl - SL0, 0.f);
                const bool vz = lane < 16 && ((z.x ^ a) >> 24 | (z.y ^ b)) == 0;
                const unsigned gw = 2 * __builtin_popcountll(__builtin_amdgcn_ballot_w64(vz && (z.x & 1))) > __builtin_popcountll(__builtin_amdgcn_ballot_w64(vz));
                const unsigned k = lane & 3;
                if (lane < 32 && k < 3) __builtin_amdgcn_raw_buffer_store_b128((v4u){k == 0 ? (unsigned)g0 : k == 1 ? (unsigned)g1 : mean, tg, k == 0 ? (unsigned)(g0 >> 32) : k == 1 ? (unsigned)(g1 >> 32) : gw, tg},
                                                                              nrs, (lane >> 2) * 1280 + k * 16, 0, SC1);
            }
            while ((int)(NOW() - own) < TMAP + TRES) SLEEP(2)
            v4u y;
            for (;;) {
                y = __builtin_amdgcn_raw_buffer_load_b128(nrs, lane < 3 ? (blockIdx.x & 7) * 1280 + lane * 16 : 0x40000000, 0, SC1);
                if (((unsigned)__builtin_amdgcn_ballot_w64(y.y == tg && y.w == tg) & 7u) == 7u) break;
                SLEEP(8)
            }
            const u64 f0 = (unsigned)__builtin_amdgcn_readlane(y.x, 0) | (u64)(unsigned)__builtin_amdgcn_readlane(y.z, 0) << 32, f1 = (unsigned)__builtin_amdgcn_readlane(y.x, 1) | (u64)(unsigned)__builtin_amdgcn_readlane(y.z, 1) << 32;
            if (lane == 2) { sm.aline = y.x - own + 500 < 1000 ? y.x : own; sm.chead = (blockIdx.x & ~5u) | ((blockIdx.x >> 2 ^ y.z) & 1) | (blockIdx.x & 1) << 2; }
            unsigned bit = ((unsigned)((lane >= NU ? f1 : f0) >> (4 * win + (pair >> 1))) ^ (0x66 >> pair)) & 1;
            if (eu < NMIX) bit = eu & 1;
            const u64 bm = __builtin_amdgcn_ballot_w64(bit);
            const unsigned nsw = __builtin_popcountll((f0 ^ f1) >> (4 * NMIX));
            if (lane == 0) { sm.swp = (unsigned)(bm ^ (bm >> NU)) & ((1u << NU) - 1); lds_st(&sm.gap, nsw ? (TG0 + TG1 * nsw / 8) * 0x10001u + ((TH0 + TH1 * nsw / 8) << 16) : 0u); }
            CBAR();
            lds_st(&sm.utab[lane], UENT(bit));
        }
        const unsigned swp = lds_ld(&sm.swp);
        const unsigned T = lds_ld(&sm.tag_lo);
        const unsigned t15 = T & 0x7fffu;
        const __amdgpu_buffer_rsrc_t rsrc = __builtin_amdgcn_make_buffer_rsrc(ws + WS_PART + split * 512, 0, NRW * NSPLIT * 512 - split * 512, 0x00020000);
        v2u* recs = (v2u*)&sm.cs[0][0][0];
#pragma unroll
        for (int i = 0; i < NOUT * 3; i++) (&sm.acc[0][0])[i * 64 + lane] = 0.0;
        CBAR();
        unsigned vut = sm.utab[NU + (lane & 31)] & 255;
        if (QUARTER((unsigned)lane) && (rank & 3)) vut = NH - 16 + (rank & 3) * 16 + ((vut & 15) ^ 1);
        __builtin_amdgcn_s_setprio(3);
        unsigned po[NU / 2];
#pragma unroll
        for (int k = 0; k < NU / 2; k++) po[k] = __builtin_amdgcn_ds_bpermute((2 * k + (lane >> 5)) * 4, vut) * (NSPLIT * 512) + (lane & 31) * 16;
        const u64 rb_ = (u64)(ws + WS_PART + split * 512);
        const v4u rs_ = {(unsigned)rb_, (unsigned)(rb_ >> 32) & 0xffffu, NRW * NSPLIT * 512 - split * 512, 0x00020000u};
#pragma unroll 1
        for (unsigned u = 0; u < 2 * NU - 1; u++) {
            const unsigned e = u % NU, so = u % NOUT;
            if (u >= NU) while (!((lds_ld(&sm.vrdy) >> e) & 1)) SLEEP(4)
            CBAR();
            const v4f h = *(const v4f*)&sm.wv[went(e) + (QUARTER(e) ? 32 : 128)];
            while (lds_ld(&sm.cnt[so]) != (QUARTER(e) ? 8 : 32)) SLEEP(1)
            CBAR();
            const double a0 = sm.acc[so][lane], a1 = sm.acc[so][64 + lane], a2 = sm.acc[so][128 + lane];
            if (u < NU) {
                float2 lg = *(const float2*)&sm.ent[so][2 * lane];
                if (QUARTER(e) && lane >> 4 != (rank & 3)) lg = make_float2(-INFINITY, -INFINITY);
                const v4f zs = *(const v4f*)&sm.vz[u % NIN][2 * lane];
                const float m = wave_max(fmaxf(lg.x, lg.y));
                const float p0 = __expf(lg.x - m), p1 = __expf(lg.y - m);
                const float x0 = p0 * zs.y, x1 = p1 * zs.w;
                const unsigned eb = max(__float_as_uint(wave_max(fmaxf(fabsf(x0), fabsf(x1)))) >> 23, 14u);
                const float f = __uint_as_float((268u - eb) << 23);
                const float w0 = fminf(fmaxf(rintf(x0 * f), -32767.f), 32767.f), w1 = fminf(fmaxf(rintf(x1 * f), -32767.f), 32767.f);
                const float den = wave_sum(p0 + p1);
                const float bz = wave_sum(fmaf(w0, zs.x, w1 * zs.z));
                const float L = fmaf(m, 1.4426950408889634f, __builtin_amdgcn_logf(den));
                unsigned* wd = (unsigned*)(((swp >> u) & 1 ? sm.wo : sm.wv) + went(u));
                if (!QUARTER(u) || lane >> 4 == (rank & 3)) wd[QUARTER(u) ? lane & 15 : lane] = ((unsigned)(int)w0 & 0xffffu) | ((unsigned)(int)w1 << 16);
                if (lane == 0) *(v4f*)&wd[QUARTER(u) ? 16 : 64] = (v4f){ldexpf(1.f / den, (int)eb - 141), bz, L, 0.f};
                CBAR();
                if (!((swp >> u) & 1)) LDS_OR(&sm.vrdy, 1u << u)
            } else {
                const float n0 = (float)(a0 - (double)h.y) * h.x, n1 = (float)(a1 - (double)h.y) * h.x, n2 = (float)(a2 - (double)h.y) * h.x;
                const unsigned e2 = max(__float_as_uint(wave_maxv(fmaxf(fmaxf(fabsf(n0), fabsf(n1)), fabsf(n2)))) >> 23, 14u);
                const float f2 = __uint_as_float((268u - e2) << 23);
                const unsigned m01 = __builtin_bit_cast(unsigned, __builtin_amdgcn_cvt_pk_i16((int)rintf(n0 * f2), (int)rintf(n1 * f2)));
                const unsigned m2 = __builtin_bit_cast(unsigned, __builtin_amdgcn_cvt_pk_i16((int)rintf(n2 * f2), 0));
                const unsigned bit = ((lane & 1 ? e2 : __float_as_uint(h.z)) >> (lane >> 1)) & 1u;
                recs[e * 64 + lane] = (v2u){m01, m2 | bit << 16 | t15 << 17};
                CBAR();
                sm.acc[so][lane] = 0.0; sm.acc[so][64 + lane] = 0.0; sm.acc[so][128 + lane] = 0.0;
            }
            CBAR();
            if (lane == 0) { lds_st(&sm.cnt[so], 0u); CBAR(); lds_st(&sm.merged, u + 1); }
            if (u == NU - 1 && swp) {
                wave_sync();
                const __amdgpu_buffer_rsrc_t srs = __builtin_amdgcn_make_buffer_rsrc(ws + WS_SWAP + blockIdx.x * 5120, 0, 5120, 0x00020000);
                v4u d[5]; unsigned o[5];
#pragma unroll
                for (int i = 0; i < 5; i++) {
                    const unsigned* w = (const unsigned*)&sm.wo[144 * NMIX + (i * 64 + lane) * 6];
                    const unsigned d0 = w[0], d1 = w[1], d2 = w[2];
                    d[i] = (v4u){d0, (d1 & 0xffffu) | (T << 16), (d1 >> 16) | (d2 << 16), (d2 >> 16) | ((T + (T >> 16)) << 16)};
                    o[i] = (swp >> wunit(i * 64 + lane)) & 1 ? (i * 64 + lane) * 16 : 0x40000000;
                }
                while ((int)(NOW() - lds_ld(&sm.line)) < (int)tdue(PSW) + TWO) SLEEP(1)
#pragma unroll
                for (int i = 0; i < 5; i++) __builtin_amdgcn_raw_buffer_store_b128(d[i], srs, o[i], 0, SC1 | 2);
            }
        }
        wave_sync();
#pragma unroll
        for (int k = 0; k < NU / 2; k++) asm volatile("ds_read_b128 a[%c0:%c0+3], %1 offset:%2" :: "i"(16 + 4 * k), "v"(lds_addr(recs) + lane * 16), "i"(k * 1024) : "memory");
        asm volatile("s_waitcnt lgkmcnt(0)" ::: "memory");
        while (!((lds_ld(&sm.vrdy) >> (NU - 1)) & 1)) SLEEP(4)
        CBAR();
        const v2u hw_ = {*(const unsigned*)&sm.wv[went(NU - 1) + 132], T};
        const unsigned hrow = (NRW - GSZ + (__builtin_amdgcn_readlane(vut, NU - 1) & 15 ^ 1)) * (NSPLIT * 512);
        while (lds_ld(&sm.grab) < NPC - PUBP) SLEEP(1)
        if (lane < 16) __builtin_amdgcn_raw_buffer_store_b64(hw_, rsrc, lane * 8, hrow, SC1);
#define PUB(K) asm volatile("buffer_store_dwordx4 a[%c0:%c0+3], %1, %2, 0 offen sc1" :: "i"(16 + 4 * (K)), "v"(po[K]), "s"(rs_) : "memory");
        PUB(WQ / 2) PUB(WQ / 2 + 1)
        { const unsigned t_ = TLAST() + PDEL; GATE(t_) }
        PUB(0) PUB(1) PUB(2) PUB(3) PUB(4) PUB(7) PUB(8)
        return;
    }
    }
    __builtin_amdgcn_s_setprio(0);
    unsigned c;
#define ADDRTN(P) { u64 ex_; asm volatile("s_mov_b64 %0, exec\n\ts_mov_b64 exec, 1\n\tds_add_rtn_u32 a15, %1, a13\n\ts_mov_b64 exec, %0" : "=&s"(ex_) : "v"(lds_addr(P)) : "a15", "memory"); }
#define ADDED(V) { unsigned t_; asm volatile("s_waitcnt lgkmcnt(0)\n\tv_accvgpr_read_b32 %1, a15\n\ts_nop 0\n\tv_readfirstlane_b32 %0, %1\n\ts_nop 1" : "=s"(V), "=v"(t_)); }
    asm volatile("v_accvgpr_write_b32 a13, 1\n\ts_nop 1" ::: "a13");
    ADDRTN(&sm.cidx) ADDED(c)
    if (c < NSW) {
        while (lds_ld(&sm.grab) < NPC + 2) SLEEP(4)
        const unsigned head = lds_ld(&sm.chead), T = lds_ld(&sm.tag_lo), tl = TLAST();
        const u64 rb_ = (u64)(ws + WS_PART);
        const v4u rs_ = {(unsigned)rb_, (unsigned)(rb_ >> 32) & 0xffffu, NRW * NSPLIT * 512, 0x00020000u};
        const unsigned ro = head * (NSPLIT * 512) + c * 1024, rx = (NH + (head & 15 ^ 1)) * (NSPLIT * 512) + c * 1024;
#define POLL(S, O) asm volatile("buffer_load_dwordx4 a[%c0:%c0+3], %1, %2, %3 offen sc1" :: "i"(16 + 4 * (S)), "v"(lane * 16), "s"(rs_), "s"(O) : "memory");
#define FETCH(X, S, N) asm volatile("s_waitcnt vmcnt(%c5)\n\tv_accvgpr_read_b32 %0, a[%c4]\n\tv_accvgpr_read_b32 %1, a[%c4+1]\n\tv_accvgpr_read_b32 %2, a[%c4+2]\n\tv_accvgpr_read_b32 %3, a[%c4+3]" \
                                    : "=v"((X).x), "=v"((X).y), "=v"((X).z), "=v"((X).w) : "i"(16 + 4 * (S)), "i"(N));
        float o0, o1, o2;
        if (head >= NH - GSZ) {
            __builtin_amdgcn_s_setprio(3);
            if (c == 0) {
                v2u hd;
                GATE(tl + THD)
                for (;;) {
                    hd = __builtin_amdgcn_raw_buffer_load_b64(__builtin_amdgcn_make_buffer_rsrc(ws + WS_PART + (NRW - GSZ + (head & 15 ^ 1)) * (NSPLIT * 512), 0, NSPLIT * 512, 0x00020000), (lane & 15) * 512, 0, SC1);
                    if (__builtin_amdgcn_ballot_w64(hd.y == T) == ~0ull) break;
                    SLEEP(4)
                }
                const float w = __builtin_amdgcn_exp2f(__uint_as_float(hd.x) - row_max(__uint_as_float(hd.x)));
                if (lane < 16) sm.ent[0][lane] = w * __builtin_amdgcn_rcpf(row_sum(w));
                CBAR();
                if (lane == 0) lds_st(&sm.tabs, 0u);
            }
            while (lds_ld(&sm.tabs) || lds_ld(&sm.merged) <= (2 * NU - 1) / NOUT * NOUT) SLEEP(1)
            CBAR();
            const float cf = sm.ent[0][2 * c + (lane >> 5)];
            v4u x; bool got;
#define SEEN(J, N) { FETCH(x, J, N) got = __builtin_amdgcn_ballot_w64((((x.y ^ T << 24) | (x.w ^ T << 24)) >> 24) != 0) == 0; }
            GATE(tl + TCA) POLL(0, ro) GATE(tl + TCA + TCD) POLL(1, rx + 3 * GSZ * NSPLIT * 512) SEEN(0, 1)
            if (!got) SEEN(1, 0)
            while (!got) { POLL(2, rx + 4 * GSZ * NSPLIT * 512) SEEN(2, 0) SLEEP(0) }
            const double c0 = ldexpf(cf, (int)(x.y >> 16 & 255u) - 141), c1 = ldexpf(cf, (int)(x.w >> 16 & 255u) - 141);
#define SWADD(A, B) ({ const v2u a_ = __builtin_bit_cast(v2u, A), b_ = __builtin_bit_cast(v2u, B); \
                const auto l_ = __builtin_amdgcn_permlane32_swap(a_.x, b_.x, false, false); const auto h_ = __builtin_amdgcn_permlane32_swap(a_.y, b_.y, false, false); \
                __builtin_bit_cast(double, (v2u){l_[0], h_[0]}) + __builtin_bit_cast(double, (v2u){l_[1], h_[1]}); })
            const double s0 = SWADD(c0 * (double)(short)x.x, c1 * (double)(short)x.z), s1 = SWADD(c0 * (double)((int)x.x >> 16), c1 * (double)((int)x.z >> 16)),
                         s2 = SWADD(c0 * (double)(short)x.y, c1 * (double)(short)x.w);
            asm volatile("ds_add_f64 %0, %1\n\tds_add_f64 %0, %2 offset:512\n\tds_add_f64 %0, %3 offset:1024" :: "v"(lds_addr(&sm.acc[0][lane])), "v"(s0), "v"(s1), "v"(s2) : "memory");
            ADDRTN(&sm.cdone)
            const double a0 = sm.acc[0][lane], a1 = sm.acc[0][64 + lane], a2 = sm.acc[0][128 + lane];
            ADDED(c)
            o0 = a0; o1 = a1; o2 = a2;
        } else {
            const bool quart = head >> 4 == WQ;
            const unsigned t15 = T & 0x7fffu;
#define TAKE(K) { FETCH(x[K], K, 0) \
                bad |= (x[K].y >> 17 ^ t15) | (x[K].w >> 17 ^ t15); \
                const u64 bl = __builtin_amdgcn_ballot_w64((x[K].y & 0x10000u) != 0), be = __builtin_amdgcn_ballot_w64((x[K].w & 0x10000u) != 0); \
                L[K] = __uint_as_float(lane < 32 ? (unsigned)bl : (unsigned)(bl >> 32)); ex[K] = lane < 32 ? (unsigned)be : (unsigned)(be >> 32); \
                M = fmaxf(M, fmaxf(__uint_as_float((unsigned)bl), __uint_as_float((unsigned)(bl >> 32)))); }
#define SUMUP(K) { const float w_ = __builtin_amdgcn_exp2f(L[K] - M), c_ = ldexpf(w_, (int)(ex[K] & 255u) - 141); den += w_; \
                t[0] = fmaf(c_, (float)(short)x[K].x, t[0]); t[1] = fmaf(c_, (float)((int)x[K].x >> 16), t[1]); t[2] = fmaf(c_, (float)(short)x[K].y, t[2]); \
                t[3] = fmaf(c_, (float)(short)x[K].z, t[3]); t[4] = fmaf(c_, (float)((int)x[K].z >> 16), t[4]); t[5] = fmaf(c_, (float)(short)x[K].w, t[5]); }
            const unsigned tg = tl + (quart ? TRQ : TRD);
            GATE(tg)
            v4u x[4]; float L[4], M; unsigned ex[4];
            for (;;) {
                unsigned bad = 0; M = -3e38f;
                POLL(0, ro)
                if (quart) { POLL(1, rx) POLL(2, rx + GSZ * NSPLIT * 512) POLL(3, rx + 2 * GSZ * NSPLIT * 512) }
                __builtin_amdgcn_s_setprio(3);
                TAKE(0)
                if (quart) { TAKE(1) TAKE(2) TAKE(3) }
                if (__builtin_amdgcn_ballot_w64(bad != 0) == 0) break;
                SLEEP(2)
            }
            float t[6] = {0.f, 0.f, 0.f, 0.f, 0.f, 0.f}, den = 0.f;
            SUMUP(0)
            if (quart) { SUMUP(1) SUMUP(2) SUMUP(3) }
#define SWADF(A, B) ({ const auto l_ = __builtin_amdgcn_permlane32_swap(__float_as_uint(A), __float_as_uint(B), false, false); __uint_as_float(l_[0]) + __uint_as_float(l_[1]); })
            v4f* pb = (v4f*)((char*)&sm.cs[0][0][0] + 65536);
            float* pm = (float*)(pb + NSW * 64);
            pb[c * 64 + lane] = (v4f){SWADF(t[0], t[3]), SWADF(t[1], t[4]), SWADF(t[2], t[5]), SWADF(den, den)};
            if (lane == 0) pm[c] = M;
            CBAR();
            ADDRTN(&sm.cdone)
            ADDED(c)
            CBAR();
            if (c == NSW - 1) {
                const v4f ma = *(const v4f*)pm, mb = *(const v4f*)(pm + 4);
                const float mw[NSW] = {ma.x, ma.y, ma.z, ma.w, mb.x, mb.y, mb.z, mb.w};
                float Mx = mw[0];
#pragma unroll
                for (int w = 1; w < NSW; w++) Mx = fmaxf(Mx, mw[w]);
                v4f sum = {0.f, 0.f, 0.f, 0.f};
#pragma unroll
                for (int w = 0; w < NSW; w++) sum += pb[w * 64 + lane] * __builtin_amdgcn_exp2f(mw[w] - Mx);
                const float inv = __builtin_amdgcn_rcpf(sum.w);
                o0 = sum.x * inv; o1 = sum.y * inv; o2 = sum.z * inv;
            }
        }
        if (c == NSW - 1) {
            asm("" : "+v"(o0), "+v"(o1), "+v"(o2));
            typedef __bf16 v2b __attribute__((ext_vector_type(2)));
            typedef unsigned v3u __attribute__((ext_vector_type(3)));
#define PK(A, B) __builtin_bit_cast(unsigned, (v2b){(__bf16)(A), (__bf16)(B)})
            const unsigned p12 = PK(o1, o2);
            const v3u ov = {PK(o0, o1), PK(o2, __uint_as_float(__builtin_amdgcn_permlane32_swap(__float_as_uint(o0), __float_as_uint(o0), false, false)[1])), __builtin_amdgcn_permlane32_swap(p12, p12, false, false)[1]};
            if (lane < 32) __builtin_amdgcn_raw_buffer_store_b96(ov, __builtin_amdgcn_make_buffer_rsrc(out + (size_t)head * DH, 0, DH * 2, 0x00020000), lane * 12, 0, 0);
        }
    }
}

extern "C" void kernel_launch(void* const* d_in, const int* in_sizes, int n_in,
                              void* d_out, int out_size, void* d_ws, size_t ws_size,
                              hipStream_t stream) {
    const unsigned short* q = (const unsigned short*)d_in[0];
    const int* qk = (const int*)d_in[1];
    const unsigned short* k_scales = (const unsigned short*)d_in[2];
    const unsigned short* k_zeros = (const unsigned short*)d_in[3];
    const int* qv = (const int*)d_in[4];
    const unsigned short* v_scales = (const unsigned short*)d_in[5];
    const unsigned short* v_zeros = (const unsigned short*)d_in[6];
    const unsigned short* q_cos = (const unsigned short*)d_in[7];
    const unsigned short* q_sin = (const unsigned short*)d_in[8];
    const unsigned short* k_cos = (const unsigned short*)d_in[9];
    const unsigned short* k_sin = (const unsigned short*)d_in[10];
    unsigned char* ws = (unsigned char*)d_ws;
    attn_kernel<<<NSPLIT * GSZ, NTHREADS, 0, stream>>>(q, qk, k_scales, k_zeros, qv, v_scales, v_zeros,
                                                       q_cos, q_sin, k_cos, k_sin, (__hip_bfloat16*)d_out, ws);
}
```

```cpp
#define DIAG 44
#include <hip/hip_runtime.h>
#include <hip/hip_bf16.h>
#include <stdint.h>

#define DH 192
#define HALF 96
#define SKV 2048
#define NH 256
#define NSW 8
#define NWAVES (NSW + 4)
#define NTHREADS (64 * NWAVES)
#define BROWS 128
#define NSPLIT 16
#define GSZ 16
#define WQ 10
#define NU (NH / GSZ + 3)
#define NPC 1024
#define NRR (NH + 3 * GSZ)
#define NIN 8
#define NOUT 8
#define WALL (15 * 144 + 4 * 48)
#define SC1 16
#define NRND 48
#define RST 4352
#define TK 2
#define NMIX 3
#define TUNIT 311
#define T01 50
#define TMIX 7
#define TOFF 50
#define PSF 100
#define TGS 82
#define PSW (NPC / 2 + 28)
#define TG0 8
#define TG1 5
#define TWO -80
#define PSG (NPC / 2 + 60)
#define TH0 3
#define TH1 3
#define EARLY 10
#define LEADP 24
#define DPH 2
#define TA 210
#define TA2 70
#define CSH 2
#define NPRB 3
#define TMAP 485
#define MINV 96
#define VW 4
#define MASTER 203
#define TRES 175
#define TB1 1200
#define PER 300
#define RDD 100
#define LEAD 70
#define LHI 85
#define LOK 74
#define PSL0 124
#define PUBP -12
#define LJ 210
#define LOK2 75
#define NTRY 6
#define TFO 6
#define SL0 42.f
#define PST0 200
#define KC 4
#define TC0 1000
#define TCS 9
#define PRE0 3

#define KREL 12
#define PDEL 70
#define NCP 3
#define NRW (NRR + NCP * GSZ)
#define TRD 116
#define THD 55
#define TRQ 55
#define TCA 138
#define TCD 22
#define TSD 560
#define WS_EPOCH 0
#define WS_POST0 8192
#define WS_POST (WS_POST0 + ((unsigned)E1 & 15) * 2048)
#define WS_NEWS (49152 + 256)
#define WS_HELLO 59904
#define WS_BOARD 65536
#define WS_PART (2u << 20)
#define WS_SWAP (WS_PART + NRW * NSPLIT * 512)

typedef unsigned int v2u __attribute__((ext_vector_type(2)));
typedef unsigned int v16u __attribute__((ext_vector_type(16)));
typedef unsigned int v4u __attribute__((ext_vector_type(4)));
typedef int v4i __attribute__((ext_vector_type(4)));
typedef float v2f __attribute__((ext_vector_type(2)));
typedef float v4f __attribute__((ext_vector_type(4)));
typedef unsigned long long u64;

__device__ __forceinline__ float bflo(unsigned u) { return __uint_as_float(u << 16); }
__device__ __forceinline__ float bfhi(unsigned u) { return __uint_as_float(u & 0xffff0000u); }
__device__ __forceinline__ float bfr(float x) {
    __bf16 b = (__bf16)x;
    return __uint_as_float(((unsigned)__builtin_bit_cast(unsigned short, b)) << 16);
}

#define SLEEP(n) SLEEP_(n)
#define SLEEP_(n) asm volatile("s_sleep " #n "\n\tv_add_co_u32 %0, vcc, 1, %0\n\ts_cbranch_vccz 1f\n\ts_endpgm\n1:" : "+v"(spins) :: "vcc", "memory");
#define SPIN0 0xf0000000u
#define CBAR() asm volatile("" ::: "memory")

__device__ __forceinline__ void wave_sync() {
    __builtin_amdgcn_fence(__ATOMIC_RELEASE, "wavefront");
    __builtin_amdgcn_wave_barrier();
    __builtin_amdgcn_fence(__ATOMIC_ACQUIRE, "wavefront");
}
template <int CTRL>
__device__ __forceinline__ float dpp_add(float x) {
    int y = __builtin_amdgcn_update_dpp(0, __float_as_int(x), CTRL, 0xf, 0xf, true);
    return x + __int_as_float(y);
}
template <int CTRL>
__device__ __forceinline__ float dpp_max(float x) {
    int y = __builtin_amdgcn_update_dpp(__float_as_int(x), __float_as_int(x), CTRL, 0xf, 0xf, false);
    return fmaxf(x, __int_as_float(y));
}
__device__ __forceinline__ float row_sum(float x) { x = dpp_add<0x128>(x); x = dpp_add<0x124>(x); x = dpp_add<0x122>(x); return dpp_add<0x121>(x); }
__device__ __forceinline__ float row_max(float m) { m = dpp_max<0x128>(m); m = dpp_max<0x124>(m); m = dpp_max<0x122>(m); return dpp_max<0x121>(m); }
__device__ __forceinline__ float rdlane(float x, int l) { return __int_as_float(__builtin_amdgcn_readlane(__float_as_int(x), l)); }
__device__ __forceinline__ float wave_max(float m) { m = row_max(m); return fmaxf(fmaxf(rdlane(m, 0), rdlane(m, 16)), fmaxf(rdlane(m, 32), rdlane(m, 48))); }
__device__ __forceinline__ float wave_maxv(float m) {
    m = row_max(m);
    const auto a = __builtin_amdgcn_permlane16_swap(__float_as_uint(m), __float_as_uint(m), false, false);
    m = fmaxf(__uint_as_float(a[0]), __uint_as_float(a[1]));
    const auto b = __builtin_amdgcn_permlane32_swap(__float_as_uint(m), __float_as_uint(m), false, false);
    return fmaxf(__uint_as_float(b[0]), __uint_as_float(b[1]));
}
__device__ __forceinline__ float wave_sum(float x) { x = row_sum(x); return (rdlane(x, 0) + rdlane(x, 16)) + (rdlane(x, 32) + rdlane(x, 48)); }
__device__ __forceinline__ unsigned lds_ld(const unsigned* p) { return __builtin_amdgcn_readfirstlane(__hip_atomic_load(p, __ATOMIC_RELAXED, __HIP_MEMORY_SCOPE_WORKGROUP)); }
__device__ __forceinline__ unsigned lds_addr(const void* p) { return (unsigned)(size_t)(const __attribute__((address_space(3))) void*)p; }
#define DMA16(SRC, LDS) asm volatile("s_mov_b32 %0, m0\n\ts_mov_b32 m0, %2\n\ts_nop 0\n\tglobal_load_lds_dwordx4 %1, off\n\ts_mov_b32 m0, %0" : "=&s"(m0keep) : "v"(SRC), "s"(LDS) : "memory")
__device__ __forceinline__ void lds_st(unsigned* p, unsigned v) { __hip_atomic_store(p, v, __ATOMIC_RELAXED, __HIP_MEMORY_SCOPE_WORKGROUP); }
#define NOW() ((unsigned)__builtin_amdgcn_s_memrealtime())
__device__ __forceinline__ unsigned tdue(unsigned g) {
    return (g * TUNIT + min(g, 480u) * TK + min(g, 48u) * T01 + (min(g, 32u * NMIX) + min(max((int)g - NPC / 2, 0), 32 * NMIX)) * TMIX + (g >= PSF ? TGS * 32 : 0)) >> 5;
}

__device__ __forceinline__ unsigned unit_of(unsigned g) { const unsigned p = g & 511; return (g >> 9) * NU + (p >> 5) + min(max((int)p - 32 * WQ, 0) >> 3, 3); }
__device__ __forceinline__ unsigned first_of(unsigned e) { return 32 * e - 24 * min(max((int)e - WQ, 0), 4); }
#define QUARTER(E) ((E) - WQ < 4)
__device__ __forceinline__ unsigned went(unsigned e) { return 144 * e - 96 * min(max((int)e - WQ, 0), 4); }
__device__ __forceinline__ unsigned wunit(unsigned x) { x += 24 * NMIX; return x < 24 * WQ ? x / 24 : x < 24 * WQ + 32 ? WQ + (x - 24 * WQ) / 8 : (x + 64) / 24; }

#define ROTT 0xed8d2c3d2c35ed84ull, 0xba85aa165b03ac82ull, 0xee7efa489a38af71ull, 0xe932da1b0bab51d8ull, 0xfa96112d5fee971bull, 0x2c7e9ea5a544f071ull, 0x3d86093d5f0e810bull, 0xe932da1b0bab51d8ull, 0xfa96112d5fee971bull, 0xd834e62c5fa723e9ull, 0x1d7d1a3d2c061d88ull, 0xe932da1b0bab51d8ull, 0xfa96112d5fee971bull, 0xc834e12c0aaa4ec7ull, 0x3d86093d5f0e810bull, 0xe932da1b0bab51d8ull
static __device__ const u64 ROT[16] = {ROTT};

struct __attribute__((aligned(16))) Smem {
    unsigned cs[NSW][4][768];
    double acc[NOUT][DH];
    float ent[NOUT][BROWS];
    unsigned short wv[WALL];
    unsigned short wo[WALL];
    unsigned q[NIN][HALF];
    float2 kz[NIN][BROWS];
    float2 vz[NIN][BROWS];
    unsigned utab[64];
    unsigned line, gap;
    unsigned late, grab;
    unsigned rdy[NIN], cnt[NOUT];
    unsigned tabs, vrdy, swp;
    unsigned aline;
    unsigned tag_rdy, tag_lo, tag_hi, merged, cdone, cidx;
    unsigned slow;
    unsigned chead, dg;
};

#define LDS_ADD(P, V) { u64 ex_; asm volatile("s_mov_b64 %0, exec\n\ts_mov_b64 exec, 1\n\tds_add_u32 %1, %2\n\ts_mov_b64 exec, %0" : "=&s"(ex_) : "v"(lds_addr(P)), "v"(V) : "memory"); }
#define LDS_OR(P, V) { u64 ex_; asm volatile("s_mov_b64 %0, exec\n\ts_mov_b64 exec, 1\n\tds_or_b32 %1, %2\n\ts_mov_b64 exec, %0" : "=&s"(ex_) : "v"(lds_addr(P)), "v"(V) : "memory"); }

__global__ __launch_bounds__(NTHREADS, 3) void attn_kernel(
    const unsigned short* __restrict__ q, const int* __restrict__ qk,
    const unsigned short* __restrict__ k_scales, const unsigned short* __restrict__ k_zeros,
    const int* __restrict__ qv,
    const unsigned short* __restrict__ v_scales, const unsigned short* __restrict__ v_zeros,
    const unsigned short* __restrict__ q_cos, const unsigned short* __restrict__ q_sin,
    const unsigned short* __restrict__ k_cos, const unsigned short* __restrict__ k_sin,
    __hip_bfloat16* __restrict__ out, unsigned char* __restrict__ ws)
{
    __shared__ Smem sm;
    const int tid = threadIdx.x;
    unsigned spins = SPIN0;
    const int lane = tid & 63;
    const int hw = __builtin_amdgcn_readfirstlane(tid >> 6);
    u64 t00; unsigned ka;
    asm volatile("s_memrealtime %0\n\ts_load_dword %1, %2, 0x40" : "=&s"(t00), "=&s"(ka) : "s"(__builtin_amdgcn_kernarg_segment_ptr()));
    if (hw == 0) {
        const char* pc0; asm volatile("s_getpc_b64 %0" : "=s"(pc0));
        asm volatile("global_load_dword a6, %0, %1" :: "v"(((blockIdx.x & ~7u) + (lane & 7)) * 128), "s"(pc0) : "a6", "memory");
    }
    const int wave = hw >= 4 ? hw - 4 : hw + NSW;
    const unsigned split = (blockIdx.x & 1) + (blockIdx.x & 2) * 4 + ((blockIdx.x >> 3) & 3) * 2;
    const unsigned side = (split ^ (blockIdx.x >> 2)) & 1, rank = blockIdx.x >> 5;
    const unsigned head0 = 2 * rank + side;
    const unsigned rf0 = (8 * split + 1) / 3, r3 = 8 * split + 1 - 3 * rf0;
    const unsigned koff = ((lane >> 5) * DH + 3 * (lane & 31)) * 4, koffb = koff + 384;
#define AR(S) (16 + (S) * 8)
#define GLD(A, OFF, BASE, IMM) asm volatile("global_load_dwordx3 a[%c0:%c1], %2, %3 offset:%4 nt" :: "i"(A), "i"((A) + 2), "v"(OFF), "s"(BASE), "i"(IMM))
#define ISSUE(S, J, BASE, OA, OB) { GLD(AR(S), OA, BASE, (J) * 1536); GLD(AR(S) + 4, OB, BASE, (J) * 1536); }
#define HEAD2 ((GSZ + 1 - 2 * side) * (SKV * DH * 4))
    const unsigned wo = (hw - 2) & 7;
    const bool pre = hw >= 4 && wo < PRE0;
    if (pre) {
        const char* a = (const char*)qk + (head0 * 512 + side * 256 + rf0 + wave + (wave >= 6 ? 80 : 0)) * 3072; const char* b = a + HEAD2;
        ISSUE(0, 0, a, koff, koffb) ISSUE(1, 1, a, koff, koffb) ISSUE(2, 0, b, koff, koffb) ISSUE(3, 1, b, koff, koffb)
    }
    asm volatile("s_waitcnt lgkmcnt(0)" : "+s"(t00), "+s"(ka));
    if (hw == 0) {
        if (DIAG == 30 || DIAG == 39 || DIAG == 38) sm.dg = NOW() - (unsigned)t00;
        constexpr u64 rot[16] = {ROTT};
        sm.utab[lane] = lane == 2 ? 2 * GSZ + head0 | ((unsigned)(rot[2] >> 4 * head0) & 15) << 9 : ~0u;
        unsigned l0 = (unsigned)t00 + TOFF;
        if (KREL) { const unsigned t1 = NOW() + KREL; if ((int)(t1 - l0) > 0) l0 = t1; }
        if (lane < 32) (&sm.line)[lane] = lane == 3 ? 64u : (lane & 29) == 0 ? l0 : 0u;
        asm volatile("s_waitcnt lgkmcnt(0)" ::: "memory");
    }
    __builtin_amdgcn_s_barrier();
    CBAR();
    const u64 rtab = r3 == 1 ? 0x5756552c2b020100ull : r3 == 0 ? 0x5756552c2b2a0100ull : 0x57562d2c2b020100ull;
#define R8(I) (rf0 + ((unsigned)(rtab >> (8 * (I))) & 255u))
#define GATE(DUE) { int d_ = (int)((DUE) - NOW()); if (d_ < 3000) while (d_ > 0) { __builtin_amdgcn_s_sleep(1); d_ = (int)((DUE) - NOW()); } }
#define TLAST() (lds_ld(&sm.line) + tdue(NPC - 2) + (lds_ld(&sm.gap) >> 16))
    if (__builtin_expect(wave < NSW, 1)) {
        const int cst = 2 * side;
        const unsigned r8w = R8((unsigned)wave);
        const char* cbase[3];
#pragma unroll
        for (int j = 0; j < 3; j++) { const int x = j * 64 + lane; cbase[j] = (x < 96 ? (const char*)k_cos + x * 16 : (const char*)k_sin + (x - 96) * 16) + r8w * (4 * DH * 2); }
        unsigned m0keep;
#define CS_DMA(W, C) { _Pragma("unroll") for (int j = 0; j < 3; j++) DMA16(cbase[j] + (C) * (512 * DH * 2), lds_addr(&sm.cs[0][0][0]) + ((W) * 4 + (C)) * 3072 + j * 1024); }
#define REPACK(W, C) { \
        unsigned* cw = &sm.cs[W][C][0]; \
        const unsigned* l = cw + rsel * (DH / 2) + qoff; \
        unsigned r[2][6]; \
        _Pragma("unroll") \
        for (int j = 0; j < 2; j++) { \
            unsigned e[4][2]; \
            _Pragma("unroll") \
            for (int k = 0; k < 4; k++) { \
                const unsigned* a = l + j * DH + (k & 1) * 48 + (k >> 1) * 384;       \
                unsigned x0 = a[0], x1 = a[1]; \
                e[k][0] = __builtin_amdgcn_alignbit(x1, x0, sh); \
                e[k][1] = x1 >> sh; \
            } \
            r[j][0] = (e[0][0] & 0xffffu) | (e[3][0] << 16); \
            r[j][1] = (e[0][0] >> 16) | (e[3][0] & 0xffff0000u); \
            r[j][2] = (e[0][1] & 0xffffu) | (e[3][1] << 16); \
            r[j][3] = (e[1][0] & 0xffffu) | (e[2][0] << 16); \
            r[j][4] = (e[1][0] >> 16) | (e[2][0] & 0xffff0000u); \
            r[j][5] = (e[1][1] & 0xffffu) | (e[2][1] << 16); \
        } \
        wave_sync(); \
        _Pragma("unroll") \
        for (int j = 0; j < 2; j++) { *(v4u*)&cw[j * 256 + lane * 4] = (v4u){r[j][0], r[j][1], r[j][2], r[j][3]}; *(v2u*)&cw[512 + j * 128 + lane * 2] = (v2u){r[j][4], r[j][5]}; } \
        wave_sync(); }
        const int sub = lane & 31;
        const int rsel = lane >> 5;
        const unsigned sh = (sub & 1) * 16;
        const int qoff = 3 * (sub >> 1) + (sub & 1);
        const unsigned voff = 3 * lane * 4;
        asm volatile("" ::: "a16", "a17", "a18", "a19", "a20", "a21", "a22", "a23", "a24", "a25", "a26", "a27", "a28", "a29", "a30", "a31",
                            "a32", "a33", "a34", "a35", "a36", "a37", "a38", "a39", "a40", "a41", "a42", "a43", "a44", "a45", "a46", "a47");
        asm volatile("" ::: "a48", "a49", "a50", "a51", "a52", "a53", "a54", "a55", "a56", "a57", "a58", "a59", "a60", "a61", "a62", "a63",
                            "a64", "a65", "a66", "a67", "a68", "a69", "a70", "a71", "a72", "a73", "a74", "a75", "a76", "a77", "a78", "a79");
        int ta[4][3], tb[4][3];
#define WAITT(P, S, N) asm volatile("s_waitcnt vmcnt(%6)\n\tv_accvgpr_read_b32 %0, a[%c7]\n\tv_accvgpr_read_b32 %1, a[%c8]\n\tv_accvgpr_read_b32 %2, a[%c9]\n\t" \
                              "v_accvgpr_read_b32 %3, a[%c10]\n\tv_accvgpr_read_b32 %4, a[%c11]\n\tv_accvgpr_read_b32 %5, a[%c12]" \
                              : "=v"(ta[P][0]), "=v"(ta[P][1]), "=v"(ta[P][2]), "=v"(tb[P][0]), "=v"(tb[P][1]), "=v"(tb[P][2]) \
                              : "i"(N), "i"(AR(S)), "i"(AR(S) + 1), "i"(AR(S) + 2), "i"(AR(S) + 4), "i"(AR(S) + 5), "i"(AR(S) + 6))
        const unsigned voffb = voff + 768;
        u64 ln = 0;
#define TDUE(G) (tdue(G) + ((G) >= PSW ? __builtin_amdgcn_readfirstlane((unsigned)(ln >> 32)) >> ((G) >= PSG ? 16 : 0) & 0xffffu : 0u))
#define REPORT() { const unsigned t_ = now - tdc; if (lane == 0) lds_st(&sm.late, t_); }
#define LINE() ln = LDV((u64*)&sm.line);
#define LDV(P) __hip_atomic_load(P, __ATOMIC_RELAXED, __HIP_MEMORY_SCOPE_WORKGROUP)
        unsigned u = 0, rg = cst * 8 + wave, np = 0;
        CS_DMA(wave, cst)

        const char* kb0 = (const char*)qk + (size_t)(head0 * 512 + r8w) * 3072;
#define KB(I) (kb0 + ((cst + (I)) & 3) * (128 * 3072))
        const char* kb; const char* kb2;
#define SISSUE(I) { kb = KB(I); kb2 = kb + HEAD2; const unsigned du_ = lds_ld(&sm.line) + tdue(16 * (I) + 2 * wo); GATE(du_) \
            ISSUE(((I) & 1) * 4, 0, kb, koff, koffb) ISSUE(((I) & 1) * 4 + 1, 1, kb, koff, koffb) ISSUE(((I) & 1) * 4 + 2, 0, kb2, koff, koffb) ISSUE(((I) & 1) * 4 + 3, 1, kb2, koff, koffb) }
        if (!pre) SISSUE(0)
        CS_DMA(wave, (cst + 1) & 3)
        if (pre) asm volatile("s_waitcnt vmcnt(3)" ::: "memory");
        if (DIAG == 32 && wave == 0 && lane == 0) sm.dg = NOW() - (unsigned)t00;
        if (DIAG == 33 && wave == 1 && lane == 0) sm.dg = NOW() - (unsigned)t00;
        asm volatile("s_waitcnt vmcnt(11)" ::: "memory");
        if (!pre) LDS_ADD(&sm.slow, NOW() - lds_ld(&sm.line) - tdue(2 * wo))
        REPACK(wave, cst)
        SISSUE(1)
#define TABDONE(I) LDS_OR(&sm.tabs, 1u << ((((cst + (I)) & 3) * 8) + wave))
        TABDONE(0)
        const float scale = 0.07216878364870322f;
        double acc[3] = {0.0, 0.0, 0.0};
        bool first = true, chg, none = false;
        unsigned Qp[3], Qn[3], Rp[3], Rn[3];
        float kz4[4], ksc, kres[4];
        v4u ca[4]; v2u cb[4];
        v2f qe[4][3], A[4];
        unsigned vut, nrg;
        const unsigned vr = ((lane >> 3) & 3) * 128 + R8((unsigned)lane & 7);
        unsigned g, mg;
        const unsigned grab_adr = lds_addr(&sm.grab);
        asm volatile("v_accvgpr_write_b32 a13, 1\n\tv_accvgpr_write_b32 a14, 2" ::: "a13", "a14", "a15");
#define GRAB(N) { u64 ex_; asm volatile("s_mov_b64 %0, exec\n\ts_mov_b64 exec, 1\n\tds_add_rtn_u32 a15, %1, a%c2\n\ts_mov_b64 exec, %0" : "=&s"(ex_) : "v"(grab_adr), "i"(12 + (N)) : "memory"); }
#define GRABBED() { unsigned t_; asm volatile("s_waitcnt lgkmcnt(0)\n\tv_accvgpr_read_b32 %1, a15\n\ts_nop 0\n\tv_readfirstlane_b32 %0, %1\n\ts_nop 1" : "=s"(g), "=v"(t_)); }
        unsigned oa, ob, un, tdn, tiss, tdc = 0;
#define PLAN() { \
            GRABBED() un = unit_of(g); chg = un != u; none = g >= NPC; \
            const bool nv = g >= NPC / 2; \
            unsigned hd = __builtin_amdgcn_readlane(vut, un); \
            while (hd == ~0u) { SLEEP(1) hd = lds_ld(&sm.utab[un]); } \
            nrg = (g + (hd >> 8)) & 31; hd &= 255; \
            unsigned boff, boff2; asm("s_mul_i32 %0, %1, 0xc00" : "=s"(boff) : "s"(hd * 512 + __builtin_amdgcn_readlane(vr, nrg))); \
            asm("s_mul_i32 %0, %1, 0xc00" : "=s"(boff2) : "s"(hd * 512 + __builtin_amdgcn_readlane(vr, nrg + 1))); \
            const char* b_ = nv ? (const char*)qv : (const char*)qk; \
            kb = b_ + boff; kb2 = b_ + boff2; \
            oa = nv ? voff : koff; ob = nv ? voffb : koffb; \
            tdn = TDUE(g); }
#define FIRE() { ISSUE(0, 0, kb, oa, ob) ISSUE(1, 1, kb, oa, ob) ISSUE(2, 0, kb2, oa, ob) ISSUE(3, 1, kb2, oa, ob) }
#define LAUNCH() { \
            LINE() \
            const unsigned du_ = __builtin_amdgcn_readfirstlane((unsigned)ln) + tdn; \
            GATE(du_) \
            ISSUE(0, 0, kb, oa, ob) ISSUE(1, 1, kb, oa, ob) \
            GATE(du_ + 10) \
            ISSUE(2, 0, kb2, oa, ob) ISSUE(3, 1, kb2, oa, ob) tiss = NOW(); }
#define ARRIVED() \
            const unsigned now = NOW(); \
            const bool stall = now - tiss > LJ && g - PST0 < NPC - 48 - PST0; \
            const bool late = !none && !stall && (int)(now + EARLY - __builtin_amdgcn_readfirstlane((unsigned)ln) - tdn) >= 0; \
            if (late) { FIRE() tiss = now; } \
            if (stall && lane == 0) __hip_atomic_fetch_max(&sm.line, (unsigned)ln + (now - tiss) - LOK2, __ATOMIC_RELAXED, __HIP_MEMORY_SCOPE_WORKGROUP); \
            REPORT()
#define ADVANCE(ISV) { \
            if (chg) FLUSH(ISV) \
            if (!none && !late) LAUNCH() \
            first = chg; u = un; rg = nrg; tdc = tdn + 10; }
#define K_TAB(MODE) { _Pragma("unroll") for (int p = 0; p < ((MODE) ? 2 : 4); p++) { \
            const unsigned* cw = &sm.cs[(rg & 7) + (p >> 1)][rg >> 3][0]; \
            ca[p] = *(const v4u*)&cw[(p & 1) * 256 + lane * 4]; cb[p] = *(const v2u*)&cw[512 + (p & 1) * 128 + lane * 2]; } }
#define K_PRE(MODE, P) { \
        float e0, e1, e2, f0, f1, f2; \
        const unsigned* qp_ = ((MODE) && (P) >= 2) ? Rp : Qp; const unsigned* qn_ = ((MODE) && (P) >= 2) ? Rn : Qn; \
        const int tp_ = (MODE) ? (P) & 1 : (P); \
        asm("v_dot2_f32_bf16 %0, %6, %12, 0\n\tv_dot2_f32_bf16 %3, %9, %15, 0\n\tv_dot2_f32_bf16 %1, %7, %13, 0\n\tv_dot2_f32_bf16 %4, %10, %16, 0\n\t" \
            "v_dot2_f32_bf16 %2, %8, %14, 0\n\tv_dot2_f32_bf16 %5, %11, %17, 0\n\ts_nop 2" \
            : "=&v"(e0), "=&v"(e1), "=&v"(e2), "=&v"(f0), "=&v"(f1), "=&v"(f2) \
            : "v"(qp_[0]), "v"(qp_[1]), "v"(qp_[2]), "v"(qn_[0]), "v"(qn_[1]), "v"(qn_[2]), \
              "v"(ca[tp_].x), "v"(ca[tp_].y), "v"(ca[tp_].z), "v"(ca[tp_].w), "v"(cb[tp_].x), "v"(cb[tp_].y)); \
        qe[P][0] = (v2f){e0, f0}; qe[P][1] = (v2f){e1, f1}; qe[P][2] = (v2f){e2, f2}; \
        A[P] = (qe[P][0] + qe[P][1] + qe[P][2]) * (v2f){-kz4[P], -kz4[P]}; }
#define K_WAIT(MODE, P) { \
        if ((MODE) == 0) { WAITT(P, P, 6 - 2 * (P)); } \
        else { WAITT(P, (((MODE) - 1) & 1) * 4 + (P), ((MODE) < 3 ? 20 : (MODE) == 3 ? 17 : 14) - 2 * (P)); } }
#define K_FMA(P) { \
        _Pragma("unroll") \
        for (int i = 0; i < 3; i++) { v2f x = {(float)ta[P][i], (float)tb[P][i]}; A[P] = qe[P][i] * x + A[P]; } \
        kres[P] = A[P].x + A[P].y; }
#define K_RED(MODE) { \
        const bool o1 = lane & 1, o2 = lane & 2; \
        float s01 = (o1 ? kres[1] : kres[0]) + __int_as_float(__builtin_amdgcn_update_dpp(0, __float_as_int(o1 ? kres[0] : kres[1]), 0xb1, 0xf, 0xf, true)); \
        float s23 = (o1 ? kres[3] : kres[2]) + __int_as_float(__builtin_amdgcn_update_dpp(0, __float_as_int(o1 ? kres[2] : kres[3]), 0xb1, 0xf, 0xf, true)); \
        float sx = (o2 ? s23 : s01) + __int_as_float(__builtin_amdgcn_update_dpp(0, __float_as_int(o2 ? s01 : s23), 0x4e, 0xf, 0xf, true)); \
        sx = dpp_add<0x124>(sx); sx = dpp_add<0x128>(sx); \
        const auto sw = __builtin_amdgcn_permlane16_swap(__float_as_uint(sx), __float_as_uint(sx), false, false); \
        sx = __uint_as_float(sw[0]) + __uint_as_float(sw[1]); \
        if ((lane & 28) == 0) { if (MODE) sm.ent[(lane >> 1) & 1][rg * 4 + 2 * (lane & 1) + rsel] = sx * ksc; else sm.ent[u % NOUT][rg * 4 + 2 * (lane & 3) + rsel] = sx * ksc; } }
#define K_PRE4(MODE) { K_PRE(MODE, 0) K_PRE(MODE, 1) K_PRE(MODE, 2) K_PRE(MODE, 3) asm volatile("" :: "v"(A[0]), "v"(A[1]), "v"(A[2]), "v"(A[3])); }
#define K_BODY(MODE) { K_PRE4(MODE) K_WAIT(MODE, 0) K_FMA(0) K_WAIT(MODE, 1) K_FMA(1) K_WAIT(MODE, 2) K_FMA(2) K_WAIT(MODE, 3) K_FMA(3) K_RED(MODE) }
#define V_FMA(J, W0, W1) { \
        _Pragma("unroll") \
        for (int i = 0; i < 3; i++) { \
            acc[i] = fma(W0, (double)ta[J][i], acc[i]); \
            acc[i] = fma(W1, (double)tb[J][i], acc[i]); \
        } }
#define V_PIN() asm volatile("" :: "v"(acc[0]), "v"(acc[1]), "v"(acc[2]));
#define RDYWAIT(SL, SEQ) { while (lds_ld(&sm.rdy[SL]) != (SEQ)) SLEEP(1); CBAR(); }
#define LOADZ() { _Pragma("unroll") for (int p = 0; p < 4; p++) kz4[p] = sm.kz[u % NIN][rg * 4 + 2 * p + rsel].x; ksc = sm.kz[u % NIN][rg * 4 + 2 * (lane & 3) + rsel].y * scale; }
#define LOADQ() { _Pragma("unroll") for (int i = 0; i < 3; i++) { Qp[i] = sm.q[u % NIN][3 * sub + i]; Qn[i] = __builtin_amdgcn_alignbit(Qp[i], Qp[i], 16) ^ 0x80000000u; } }
#define FLUSH(ISV) { \
            mg = __builtin_amdgcn_readfirstlane(mg); \
            while (u >= mg + NOUT) { SLEEP(1); mg = lds_ld(&sm.merged); } \
            CBAR(); \
            if (ISV) { const unsigned aa = lds_addr(&sm.acc[u % NOUT][lane]); \
                asm volatile("ds_add_f64 %0, %1\n\tds_add_f64 %0, %2 offset:512\n\tds_add_f64 %0, %3 offset:1024" :: "v"(aa), "v"(acc[0]), "v"(acc[1]), "v"(acc[2]) : "memory"); \
                acc[0] = 0.0; acc[1] = 0.0; acc[2] = 0.0; } \
            CBAR(); \
            LDS_ADD(&sm.cnt[u % NOUT], np) \
            CBAR(); \
            np = 0; }

#define LOADZ2() { _Pragma("unroll") for (int p = 0; p < 4; p++) kz4[p] = sm.kz[p >> 1][rg * 4 + 2 * (p & 1) + rsel].x; ksc = sm.kz[(lane >> 1) & 1][rg * 4 + 2 * (lane & 1) + rsel].y * scale; }
#define STATIC_STEP(I) { \
            rg = ((cst + (I)) & 3) * 8 + wave; \
            LOADZ2() \
            K_TAB((I) + 1) K_BODY((I) + 1) }
        CS_DMA(wave, (cst + 2) & 3)
        RDYWAIT(0, 1u) RDYWAIT(1, 2u)
        LOADQ()
#pragma unroll
        for (int i = 0; i < 3; i++) { Rp[i] = sm.q[1][3 * sub + i]; Rn[i] = __builtin_amdgcn_alignbit(Rp[i], Rp[i], 16) ^ 0x80000000u; }
        STATIC_STEP(0)
        asm volatile("s_waitcnt vmcnt(11)" ::: "memory");
        REPACK(wave, (cst + 1) & 3)
        TABDONE(1)
        SISSUE(2)
        CS_DMA(wave, (cst + 3) & 3)
        STATIC_STEP(1)
        asm volatile("s_waitcnt vmcnt(11)" ::: "memory");
        REPACK(wave, (cst + 2) & 3)
        TABDONE(2)
        SISSUE(3)
        asm volatile("s_waitcnt vmcnt(8)" ::: "memory");
        REPACK(wave, (cst + 3) & 3)
        TABDONE(3)
        STATIC_STEP(2)
        GRAB(2)
        vut = LDV(&sm.utab[lane]);
        rg = ((cst + 3) & 3) * 8 + wave;
        LOADZ2() K_TAB(4) K_PRE4(4)
        PLAN()
        LAUNCH()
        K_WAIT(4, 0) K_FMA(0) K_WAIT(4, 1) K_FMA(1) K_WAIT(4, 2) K_FMA(2) K_WAIT(4, 3) K_FMA(3) K_RED(4)
        CBAR();
        LDS_ADD(&sm.cnt[0], 4u) LDS_ADD(&sm.cnt[1], 4u)
        u = un; rg = nrg; tdc = tdn + 10;
        bool tabok = false;
        __builtin_amdgcn_s_setprio(2);
#pragma unroll 1
        while (u < NU) {
            GRAB(2)
            mg = LDV(&sm.merged);
            vut = LDV(&sm.utab[lane]);
            LINE()
            if (!tabok) {
                for (;;) { const unsigned tb_ = lds_ld(&sm.tabs); tabok = tb_ == ~0u; if (((tb_ >> rg) & 3u) == 3u) break; SLEEP(1) }
                CBAR();
            }
            K_TAB(0)
            if (first) {
                unsigned fl = LDV(&sm.rdy[u % NIN]);
                CBAR();
                LOADQ() LOADZ()
                if (__builtin_amdgcn_readfirstlane(fl) != u + 1) { RDYWAIT(u % NIN, u + 1) LOADQ() LOADZ() }
            } else LOADZ()
            K_PRE4(0)
            PLAN()
            K_WAIT(0, 0) K_FMA(0) K_WAIT(0, 1) K_FMA(1)
            asm volatile("" :: "v"(kres[0]), "v"(kres[1]));
            K_WAIT(0, 2) K_WAIT(0, 3)
            ARRIVED()
            K_FMA(2) K_FMA(3) K_RED(0)
            np += 2;
            ADVANCE(false)
        }
#pragma unroll 1
        while (u < 2 * NU) {
            GRAB(2)
            mg = LDV(&sm.merged);
            LINE()
            const unsigned vr_ = LDV(&sm.vrdy);
            CBAR();
            const unsigned short* wp = &sm.wv[went(u - NU) + (QUARTER(u - NU) ? rg & 7 : rg) * 4];
            v4i pw = *(const v4i*)wp;
            if (first && !((__builtin_amdgcn_readfirstlane(vr_) >> (u - NU)) & 1)) { while (!((lds_ld(&sm.vrdy) >> (u - NU)) & 1)) SLEEP(1); CBAR(); pw = *(const v4i*)wp; }
            const double w0 = (short)pw.x, w1 = pw.x >> 16, w2 = (short)pw.y, w3 = pw.y >> 16, w4 = (short)pw.z, w5 = pw.z >> 16, w6 = (short)pw.w, w7 = pw.w >> 16;
            asm volatile("" :: "v"(w0), "v"(w1), "v"(w2), "v"(w3), "v"(w4), "v"(w5), "v"(w6), "v"(w7));
            PLAN()
            v2f hl; unsigned tl8, rowl;
            if (none) { hl = *(const v2f*)&sm.wv[went(NU - 1) + 128]; tl8 = LDV(&sm.tag_lo) << 24; rowl = (lds_ld(&sm.utab[2 * NU - 1]) & 255) * (NSPLIT * 512); asm volatile("" : "+v"(hl), "+v"(tl8)); }
            WAITT(0, 0, 6); V_FMA(0, w0, w1) V_PIN()
            WAITT(1, 1, 4); V_FMA(1, w2, w3) V_PIN()
            WAITT(2, 2, 2); V_FMA(2, w4, w5) V_PIN()
            WAITT(3, 3, 0);
            np += 2;
            if (none) {
                V_FMA(3, w6, w7)
                if (u != 2 * NU - 1) { FLUSH(true) break; }
                mg = __builtin_amdgcn_readfirstlane(mg);
                while (u >= mg + NOUT) { SLEEP(1); mg = lds_ld(&sm.merged); }
                const unsigned aa = lds_addr(&sm.acc[(2 * NU - 1) % NOUT][lane]);
                double a0, a1, a2; unsigned cn; u64 ex_;
                asm volatile("ds_add_f64 %5, %6\n\tds_add_f64 %5, %7 offset:512\n\tds_add_f64 %5, %8 offset:1024\n\t"
                             "s_mov_b64 %4, exec\n\ts_mov_b64 exec, 1\n\tds_add_rtn_u32 %3, %9, %10\n\ts_mov_b64 exec, %4\n\t"
                             "ds_read_b64 %0, %5\n\tds_read_b64 %1, %5 offset:512\n\tds_read_b64 %2, %5 offset:1024\n\ts_waitcnt lgkmcnt(0)"
                             : "=&v"(a0), "=&v"(a1), "=&v"(a2), "=&v"(cn), "=&s"(ex_) : "v"(aa), "v"(acc[0]), "v"(acc[1]), "v"(acc[2]), "v"(lds_addr(&sm.cnt[(2 * NU - 1) % NOUT])), "v"(np) : "memory");
                const float n0 = (float)(a0 - (double)hl.y) * hl.x, n1 = (float)(a1 - (double)hl.y) * hl.x, n2 = (float)(a2 - (double)hl.y) * hl.x;
                const unsigned e2 = max(__float_as_uint(fmaxf(fmaxf(fabsf(n0), fabsf(n1)), fabsf(n2))) >> 23, 14u);
                const float f2 = __uint_as_float((268u - e2) << 23);
                const v2u r = {__builtin_bit_cast(unsigned, __builtin_amdgcn_cvt_pk_i16((int)rintf(n0 * f2), (int)rintf(n1 * f2))),
                               __builtin_bit_cast(unsigned, __builtin_amdgcn_cvt_pk_i16((int)rintf(n2 * f2), 0)) | e2 << 16 | tl8};
                const __amdgpu_buffer_rsrc_t rsrc = __builtin_amdgcn_make_buffer_rsrc(ws + WS_PART + split * 512, 0, NRW * NSPLIT * 512 - split * 512, 0x00020000);
                asm volatile("" :: "v"(r));
                if (__builtin_amdgcn_readfirstlane(cn + np) == 32) {
                    __builtin_amdgcn_raw_buffer_store_b64(r, rsrc, lane * 8, rowl, SC1);
#pragma unroll
                    for (int j = 0; j < NCP - 1; j++) __builtin_amdgcn_raw_buffer_store_b64(r, rsrc, lane * 8, (rowl & 15 * NSPLIT * 512 ^ NSPLIT * 512) + (NRR + j * GSZ) * NSPLIT * 512, SC1);
                }
                break;
            }
            ARRIVED()
            V_FMA(3, w6, w7)
            ADVANCE(true)
        }
    } else {
    u64 E1;
    if (wave == NSW) {
        unsigned ref;
        const unsigned sidx = (blockIdx.x & 7) | ((blockIdx.x >> 7) << 3);
#define SAMPLE(K) ((((blockIdx.x >> 3) - (blockIdx.x & 7) - (K) * 5) & 15) == 0)
#define BTAG(K) ((btag + (K)) & 0xfffffu)
#define POST(K, C) { if (lane == 0) __hip_atomic_store((u64*)(ws + (bd + (K) * RST + (sidx >> 3) * 256 + (sidx & 7) * 8)), (u64)(C) | ((u64)(((C) - ref) | BTAG(K) << 12) << 32), __ATOMIC_RELAXED, __HIP_MEMORY_SCOPE_AGENT); }
        const unsigned own = lds_ld(&sm.line);
        if (SAMPLE(0) && lane < NTRY) __hip_atomic_store((unsigned*)(ws + WS_HELLO + lane * 768) + sidx, own, __ATOMIC_RELAXED, __HIP_MEMORY_SCOPE_AGENT);
        const u64 E = __hip_atomic_load((const u64*)(ws + WS_EPOCH), __ATOMIC_RELAXED, __HIP_MEMORY_SCOPE_AGENT);
        E1 = E + 1;
        if (lane == 0) { sm.tag_lo = (unsigned)E1; sm.tag_hi = (unsigned)(E1 >> 32); CBAR(); lds_st(&sm.tag_rdy, 1u); }
        const unsigned btag = __builtin_amdgcn_readfirstlane((unsigned)E1) * 61u, bd = WS_BOARD + (btag & 7) * (NRND * RST);
        ref = own; unsigned hmin = own, tnews = 0, ntry = 9; unsigned hmean = own; int nl = 20, iod = 0, hmx = 500;
        int off = 0;
        {
            unsigned tries = 0, cur = own, tl = own;
            bool news = false;
#pragma unroll 1
            while (!news || cur != ref) {
                const unsigned now = NOW();
                if (tries < NTRY && (int)(now - own) >= (int)(TA + tries * TA2)) {
                    v16u x;
                    asm volatile("s_load_dwordx16 %0, %1, %2\n\ts_waitcnt lgkmcnt(0)" : "=&s"(x) : "s"(ws), "s"(__builtin_amdgcn_readfirstlane(WS_HELLO + tries * 768)) : "memory");
                    unsigned bad = 0; int sum = 0, dif = 0;
#pragma unroll
                    for (int i = 0; i < 16; i++) { bad |= x[i] - own + 512; sum += x[i] - x[0]; dif += i & 4 ? x[i] : -x[i]; }
                    tries++;
                    if (bad < 1024) { ntry = tries; hmean = x[0] + (sum >> 4); for (int i = 0; i < 16; i++) if ((int)(x[i] - hmin) < 0) hmin = x[i]; nl = 0; hmx = 0; for (int i = 0; i < 16; i++) { nl += x[i] - hmin > 80; iod += (i & 4) ? (int)(x[i] - hmin) : -(int)(x[i] - hmin); hmx = max(hmx, (int)(x[i] - hmin)); } const unsigned com = x[0] + (sum >> 4) + abs(dif) * (8 - KC) / 128; ref = x[0] + ((sum + (blockIdx.x & 4 ? dif : -dif)) >> 4); off = ref - com;
                        tries = NTRY; tl = NOW(); if (lane == 0) lds_st(&sm.aline, com); }
                    continue;
                }
                if (!news && lds_ld(&sm.utab[31]) != ~0u) { news = true; tnews = now; CBAR(); if (cur == ref) tl = now; ref = lds_ld(&sm.aline) + off; tries = NTRY; }
                const int d = (now - tl) >> CSH;
                if (cur != ref && d) { tl += d << CSH; cur += min(max((int)(ref - cur), -d), d); if (lane == 0) lds_st(&sm.line, cur); }
                SLEEP(2)
            }
        }
        ref -= off;
        const unsigned ref0 = ref;
        unsigned dg[2] = {0, 0}; int mxj = 0, nsh = 0; unsigned lsum = 0, lcnt = 0;
        const unsigned lead = (blockIdx.x >> 3) == 9 ? 0 : LEAD;
        unsigned posted = ref, lagf = ref, tpost = ref + TB1, tpoll = tpost + RDD + lead, kw = 0, kr = 0, cur = ref + off, khold = 0;
        int o = off; bool held = false;
#pragma unroll 1
        for (;;) {
            const unsigned p = lds_ld(&sm.grab);
            if (p >= NPC) break;
            if (p < (DIAG == 44 ? 720 : 260)) dg[0] = ref; if (p < NPC / 2) dg[1] = ref;
            lagf += (int)(lds_ld(&sm.late) - lagf) >> 1;
            { const unsigned l = lds_ld(&sm.line); if (l != cur) { cur = l; khold = kw; held = true; } }
            const unsigned now = NOW();
            if (o) { o = off * min(max((int)(ref0 + TC0 + (1 << TCS) - now), 0), 1 << TCS) >> TCS; if (!held && cur != ref + o) { cur = ref + o; if (lane == 0) lds_st(&sm.line, cur); } }
            if (kw < NRND && (int)(now - tpost) >= 0) {
                const unsigned c = ref + min((p >= PSL0 && (int)(lagf - cur) > LHI ? lagf - LOK : cur) - o - ref, 4000u);
                if (SAMPLE(kw) && (c != ref || (ref != posted && (sidx & 7) == (kw & 7)))) POST(kw, c)
                posted = ref;
                tpost += PER; kw++;
            }
            if (kr < NRND && (int)(now - tpoll) >= 0) {
                v16u xa, xb;
                asm volatile("s_load_dwordx16 %0, %2, %3\n\ts_load_dwordx16 %1, %2, %4\n\ts_waitcnt lgkmcnt(0)" : "=&s"(xa), "=&s"(xb)
                             : "s"(ws), "s"(__builtin_amdgcn_readfirstlane(bd + kr * RST)), "s"(__builtin_amdgcn_readfirstlane(bd + kr * RST + 256)) : "memory");
                int nb = 0, sg = 0;
#pragma unroll
                for (int i = 0; i < 16; i++) {
                    const unsigned lo = i < 8 ? xa[2 * i & 15] : xb[2 * i & 15], hi = i < 8 ? xa[(2 * i + 1) & 15] : xb[(2 * i + 1) & 15];
                    if (hi >> 12 == BTAG(kr) && lo - ref0 < 8000) { nb = max(nb, (int)(lo - (hi & 4095u) - ref)); sg += hi & 4095u; }
                }
                ref += nb + (sg >> 4); mxj = max(mxj, nb + (sg >> 4)); nsh += (nb + (sg >> 4)) > 0;
                if (cur != ref + o && (kr >= khold || (int)(ref + o - cur) > 0)) { cur = ref + o; held = false; if (lane == 0) lds_st(&sm.line, cur); }
                tpoll += PER; kr++;
            }
            SLEEP(2)
        }
        if (blockIdx.x == 0) {
            unsigned X = 0;
            if (DIAG == 2) X = (ref - ref0) * 8;
            if (DIAG == 6) X = (dg[1] - ref0) * 8;
            if (DIAG == 8) X = (dg[0] - ref0) * 8;
            if (DIAG == 17) X = hmx * 8;
            if (DIAG == 22) X = (ref0 - hmean) * 8;
            if (DIAG == 24) X = (hmean - hmin) * 8;
            if (DIAG == 25) X = (ref - hmin) * 4;
            if (DIAG == 27) X = __builtin_popcount(lds_ld(&sm.swp)) * 100;
            if (DIAG == 20) X = lds_ld(&sm.slow) * 2;
            if (DIAG >= 30 && DIAG < 40) X = lds_ld(&sm.dg) * 8;
            if (DIAG == 11) X = mxj * 8;
            X = min(X, 6000u);
            if (DIAG == 39) { const unsigned f3 = min((ref - hmin) / 40u, 11u), f2 = mxj < 40 ? 0 : mxj < 120 ? 1 : mxj < 220 ? 2 : 3, ka_ = lds_ld(&sm.dg), f1 = ka_ < 50 ? 0 : ka_ < 85 ? 1 : 2, f0 = hmx < 50 ? 0 : hmx < 80 ? 1 : hmx < 110 ? 2 : 3;
                X = (((f3 * 4 + f2) * 3 + f1) * 4 + f0) * 400; }
            if (DIAG == 41) { hmin = (unsigned)t00; X = min((dg[0] - ref0) / 4u, 99u) * 400; }
            if (DIAG == 44) { hmin = (unsigned)t00; const unsigned n_ = __builtin_popcount(lds_ld(&sm.swp)); X = (min((dg[0] - dg[1]) / 4u, 19u) + 20 * (n_ == 0 ? 0 : n_ < 9 ? 1 : n_ < 13 ? 2 : 3)) * 400; }
            if (DIAG == 38) { hmin = (unsigned)t00; X = (min((ref - hmin) / 10u, 49u) * 2 + (lds_ld(&sm.dg) >= 60)) * 400; }
            while (NOW() - hmin < 15000 + X) SLEEP(1)
        }
    } else if (wave == NSW + 1 || wave == NSW + 2) {
        const int ql = lane < 48 ? lane : 47;
        unsigned c1, c2, s1, s2;
#define QT_LOADS() { int qo_ = ql; asm volatile("" : "+v"(qo_)); \
                     c1 = ((const unsigned*)q_cos)[qo_]; c2 = ((const unsigned*)q_cos)[48 + qo_]; s1 = ((const unsigned*)q_sin)[qo_]; s2 = ((const unsigned*)q_sin)[48 + qo_]; }
        unsigned q1, q2, lkz, lks, lvz, lvs;
        const unsigned sofs = R8(((unsigned)lane >> 1) & 7) * 2 + (lane & 1);
#define UNIT_LOADS(HEAD, GRP) { \
        q1 = ((const unsigned*)(q + (size_t)(HEAD) * DH))[ql]; q2 = ((const unsigned*)(q + (size_t)(HEAD) * DH))[48 + ql]; \
        const size_t so = (size_t)(HEAD) * (SKV / 2) + (GRP) * 256 + sofs; \
        lkz = *((const unsigned*)k_zeros + so); lks = *((const unsigned*)k_scales + so); \
        lvz = *((const unsigned*)v_zeros + so); lvs = *((const unsigned*)v_scales + so); }
#define UNIT_FILL(SL, U) { \
            v4f qq; \
            qq.x = bfr(bfr(bflo(q1) * bflo(c1)) + bfr(-bflo(q2) * bflo(s1))); \
            qq.y = bfr(bfr(bflo(q2) * bflo(c2)) + bfr(bflo(q1) * bflo(s2))); \
            qq.z = bfr(bfr(bfhi(q1) * bfhi(c1)) + bfr(-bfhi(q2) * bfhi(s1))); \
            qq.w = bfr(bfr(bfhi(q2) * bfhi(c2)) + bfr(bfhi(q1) * bfhi(s2))); \
            *(v2u*)&sm.q[SL][2 * ql] = (v2u){(__float_as_uint(qq.x) >> 16) | __float_as_uint(qq.y), (__float_as_uint(qq.z) >> 16) | __float_as_uint(qq.w)}; \
            *(v4f*)&sm.kz[SL][2 * lane] = (v4f){bflo(lkz), bflo(lks), bfhi(lkz), bfhi(lks)}; \
            *(v4f*)&sm.vz[SL][2 * lane] = (v4f){bflo(lvz), bflo(lvs), bfhi(lvz), bfhi(lvs)}; \
            CBAR(); \
            if (lane == 0) lds_st(&sm.rdy[SL], (U) + 1); \
            CBAR(); }
        const unsigned dw = NSW + 2 - wave;
        QT_LOADS()
        UNIT_LOADS(head0 + dw * (GSZ + 1 - 2 * side), lane >> 4)
#pragma unroll 1
        for (unsigned u = dw;;) {
            while (u >= lds_ld(&sm.merged) + NIN) SLEEP(4);
            CBAR();
            UNIT_FILL(u % NIN, u)
            if (u - 1 < 12) {
                const unsigned x_ = (blockIdx.x >> 3) + 32 * u - 8; unsigned dm;
                const char* p_ = x_ < 384 ? (const char*)(x_ < 192 ? qk : qv) + (size_t)(x_ < 192 ? x_ : x_ - 192) * (2u << 20) + (x_ * 8 + (blockIdx.x & 7)) * 640 : (const char*)out + (x_ - 384) * 4096;
                while ((int)(NOW() - lds_ld(&sm.line)) < TSD) SLEEP(2)
                asm volatile("s_load_dword %0, %1, 0x0\n\ts_waitcnt lgkmcnt(0)" : "=&s"(dm) : "s"(p_) : "memory");
            }
            u += 2;
            if (u >= NU) break;
            if (u == 4) {
                const unsigned tpf = tdue(PSF - 1) + TFO;
                { int d; while ((d = (int)(lds_ld(&sm.line) + tpf - NOW())) > 0 && d < 3000) SLEEP(1) }
#define PF3(P) asm volatile("global_load_dwordx4 a[0:3], %0, %1\n\tglobal_load_dwordx4 a[0:3], %0, %1 offset:1024\n\tglobal_load_dwordx4 a[0:3], %0, %1 offset:2048" \
                            :: "v"(64 * SKV * 2 + blockIdx.x * 3072 + lane * 16), "s"(P) : "a0", "a1", "a2", "a3")
                PF3(k_zeros); PF3(k_scales); PF3(v_zeros); PF3(v_scales);
                asm volatile("global_load_dword a0, %0, %1" :: "v"(blockIdx.x * (DH * 2) + ql * 8), "s"(q) : "a0");
            }
            const unsigned tin = tdue(first_of(u) - (QUARTER(u) ? 24 : 0) - LEADP + (split & 7) * DPH);
            { int d; while ((d = (int)(lds_ld(&sm.line) + tin - NOW())) > 0 && d < 3000) SLEEP(4) }
            unsigned hd;
            while ((hd = lds_ld(&sm.utab[u])) == ~0u) SLEEP(2)
            UNIT_LOADS(hd & 255, QUARTER(u) ? rank & 3 : lane >> 4)
        }
        while (lds_ld(&sm.tag_rdy) == 0) SLEEP(1);
        CBAR();
        E1 = (u64)lds_ld(&sm.tag_lo) | ((u64)lds_ld(&sm.tag_hi) << 32);
        if (wave == NSW + 1 && blockIdx.x == 0) {
            for (;;) {
                bool ok = true;
#pragma unroll
                for (int i = 0; i < NH / 64; i++) ok &= __hip_atomic_load((const u64*)(ws + WS_POST) + i * 64 + lane, __ATOMIC_RELAXED, __HIP_MEMORY_SCOPE_AGENT) >> 24 == (E1 << 24) >> 24;
                if (__builtin_amdgcn_ballot_w64(ok) == ~0ull) break;
                SLEEP(64);
            }
            if (lane == 0) __hip_atomic_store((u64*)(ws + WS_EPOCH), E1, __ATOMIC_RELAXED, __HIP_MEMORY_SCOPE_AGENT);
        }
        if (wave == NSW + 2) {
            const unsigned t16 = (unsigned)E1 & 0xffffu, t16b = ((unsigned)E1 + ((unsigned)E1 >> 16)) & 0xffffu;
            const __amdgpu_buffer_rsrc_t rsrc = __builtin_amdgcn_make_buffer_rsrc(ws + WS_SWAP + (blockIdx.x ^ 4) * 5120, 0, 5120, 0x00020000);
            const unsigned swp = lds_ld(&sm.swp);
            if (swp) {
                unsigned use = 0;
#pragma unroll
                for (int i = 0; i < 5; i++) use |= ((swp >> wunit(i * 64 + lane)) & 1) << i;
                const unsigned tg = tdue(PSG - 1) + (lds_ld(&sm.gap) & 0xffffu);
                while ((int)(NOW() - lds_ld(&sm.line) - tg) < 0) SLEEP(2)
                for (;;) {
                    v4u d[5]; bool ok = true;
#pragma unroll
                    for (int i = 0; i < 5; i++) d[i] = __builtin_amdgcn_raw_buffer_load_b128(rsrc, (use >> i) & 1 ? (i * 64 + lane) * 16 : 0x40000000, 0, SC1 | 2);
#pragma unroll
                    for (int i = 0; i < 5; i++) if ((use >> i) & 1) {
                        ok &= d[i].y >> 16 == t16 && d[i].w >> 16 == t16b;
                        unsigned* w = (unsigned*)&sm.wv[144 * NMIX + (i * 64 + lane) * 6];
                        w[0] = d[i].x; w[1] = (d[i].y & 0xffffu) | (d[i].z << 16); w[2] = (d[i].z >> 16) | (d[i].w << 16);
                    }
                    if (__builtin_amdgcn_ballot_w64(ok) == ~0ull) break;
                    SLEEP(32)
                }
                CBAR();
                LDS_OR(&sm.vrdy, swp)
            }
        }
    } else {
        const unsigned eu = lane >= NU ? lane - NU : lane, win = min(first_of(eu) >> 5, 15u), pair = QUARTER(eu) ? 2 * (eu - WQ) + (rank >> 2) : rank;
        const unsigned rot2 = ((const unsigned char*)ROT)[win * 8 + pair];
#define UENT(BIT) (win * 16 + 2 * pair + ((BIT) ^ side) | (QUARTER(eu) ? (rank - (eu - WQ)) * 8 & 31 : (rot2 >> ((BIT) ^ side) * 4 & 15) * 2) << 8)
        unsigned far = 0;
        const unsigned hp = (blockIdx.x & 1) * NH + (blockIdx.x >> 1) * 4 + 2;
        if (blockIdx.x < 144) {
            const char* pb = (blockIdx.x < 128 ? (const char*)(hp >= NH ? qv : qk) + (size_t)(hp & (NH - 1)) * (SKV * DH * 4) + SKV * DH * 2
                                               : (const char*)ws + WS_PART + (8u << 20) + (blockIdx.x & 15) * 65536) + ((NOW() * 0x9e3779b1u) >> 26) * 128;
            unsigned mn[2] = {~0u, ~0u};
#pragma unroll 1
            for (unsigned i = 1; i < 2 * NPRB + 2; i++) {
                u64 t0, t1; unsigned dm;
                asm volatile("s_memtime %0\n\ts_waitcnt lgkmcnt(0)\n\ts_load_dword %2, %3, 0x0\n\ts_waitcnt lgkmcnt(0)\n\ts_memtime %1\n\ts_waitcnt lgkmcnt(0)"
                             : "=&s"(t0), "=&s"(t1), "=&s"(dm) : "s"(pb + i * 8192) : "memory");
                if (i == 1) asm volatile("global_load_dword a0, %0, %1 nt" :: "v"(0), "s"(pb) : "a0");
                const unsigned t = (unsigned)(t1 - t0) < 330 || i == 1 ? ~0u : (unsigned)(t1 - t0);
                if (i & 1) mn[1] = min(mn[1], t); else mn[0] = min(mn[0], t);
            }
            far = mn[0] > mn[1];
        }
        while (lds_ld(&sm.tag_rdy) == 0) SLEEP(1);
        CBAR();
        E1 = (u64)lds_ld(&sm.tag_lo) | ((u64)lds_ld(&sm.tag_hi) << 32);
        {
            const unsigned own = lds_ld(&sm.line), tg = (unsigned)E1;
            if (lane == 0) __hip_atomic_store((u64*)(ws + WS_POST) + blockIdx.x, E1 << 24 | min(lds_ld(&sm.slow), 4000u) << 1 | ((far ^ (blockIdx.x >> 2) ^ (blockIdx.x < 128 ? 0x96 >> (hp & 7) : 0)) & 1), __ATOMIC_RELAXED, __HIP_MEMORY_SCOPE_AGENT);
            const __amdgpu_buffer_rsrc_t nrs = __builtin_amdgcn_make_buffer_rsrc(ws + WS_NEWS, 0, 8 * 1280, 0x00020000);
            if (blockIdx.x == MASTER) {
                while ((int)(NOW() - own) < TMAP) SLEEP(2)
                const unsigned a = tg << 24, b = (unsigned)(E1 >> 8);
                v4u x; v2u z; u64 v0, v1;
#pragma unroll 1
                for (int i = 0; i < 8; i++) {
                    x = __builtin_amdgcn_raw_buffer_load_b128(__builtin_amdgcn_make_buffer_rsrc(ws + WS_POST, 0, 1024, 0x00020000), lane * 16, 0, SC1);
                    z = __builtin_amdgcn_raw_buffer_load_b64(__builtin_amdgcn_make_buffer_rsrc(ws + WS_POST + 1024, 0, 128, 0x00020000), lane * 8, 0, SC1);
                    v0 = __builtin_amdgcn_ballot_w64(((x.x ^ a) >> 24 | (x.y ^ b)) == 0); v1 = __builtin_amdgcn_ballot_w64(((x.z ^ a) >> 24 | (x.w ^ b)) == 0);
                    if (__builtin_popcountll(v0) + __builtin_popcountll(v1) >= MINV) break;
                    SLEEP(8)
                }
                const u64 m0 = __builtin_amdgcn_ballot_w64(x.x & 1) & v0, m1 = __builtin_amdgcn_ballot_w64(x.z & 1) & v1;
                const int s0 = lane < 43 ? min(max(lane - VW, 4 * NMIX), 42 - 2 * VW) : min(max(lane - VW, 43), 63 - 2 * VW);
#define PC(M) __builtin_popcount((unsigned)((M) >> s0) & ((2u << 2 * VW) - 1))
                const u64 g0 = __builtin_amdgcn_ballot_w64(2 * PC(m0) > PC(v0)), g1 = __builtin_amdgcn_ballot_w64(2 * PC(m1) > PC(v1));
                const float sl = wave_sum(((v0 >> lane & 1) ? (float)(x.x >> 1 & 4095u) : 0.f) + ((v1 >> lane & 1) ? (float)(x.z >> 1 & 4095u) : 0.f)) / ((8.f - PRE0) * (__builtin_popcountll(v0) + __builtin_popcountll(v1)) + 1.f);
                const unsigned al = lds_ld(&sm.aline), mean = (al ? al : own) + (int)fmaxf(sl - SL0, 0.f);
                const bool vz = lane < 16 && ((z.x ^ a) >> 24 | (z.y ^ b)) == 0;
                const unsigned gw = 2 * __builtin_popcountll(__builtin_amdgcn_ballot_w64(vz && (z.x & 1))) > __builtin_popcountll(__builtin_amdgcn_ballot_w64(vz));
                const unsigned k = lane & 3;
                if (lane < 32 && k < 3) __builtin_amdgcn_raw_buffer_store_b128((v4u){k == 0 ? (unsigned)g0 : k == 1 ? (unsigned)g1 : mean, tg, k == 0 ? (unsigned)(g0 >> 32) : k == 1 ? (unsigned)(g1 >> 32) : gw, tg},
                                                                              nrs, (lane >> 2) * 1280 + k * 16, 0, SC1);
            }
            while ((int)(NOW() - own) < TMAP + TRES) SLEEP(2)
            v4u y;
            for (;;) {
                y = __builtin_amdgcn_raw_buffer_load_b128(nrs, lane < 3 ? (blockIdx.x & 7) * 1280 + lane * 16 : 0x40000000, 0, SC1);
                if (((unsigned)__builtin_amdgcn_ballot_w64(y.y == tg && y.w == tg) & 7u) == 7u) break;
                SLEEP(8)
            }
            const u64 f0 = (unsigned)__builtin_amdgcn_readlane(y.x, 0) | (u64)(unsigned)__builtin_amdgcn_readlane(y.z, 0) << 32, f1 = (unsigned)__builtin_amdgcn_readlane(y.x, 1) | (u64)(unsigned)__builtin_amdgcn_readlane(y.z, 1) << 32;
            if (lane == 2) { sm.aline = y.x - own + 500 < 1000 ? y.x : own; sm.chead = (blockIdx.x & ~5u) | ((blockIdx.x >> 2 ^ y.z) & 1) | (blockIdx.x & 1) << 2; }
            unsigned bit = ((unsigned)((lane >= NU ? f1 : f0) >> (4 * win + (pair >> 1))) ^ (0x66 >> pair)) & 1;
            if (eu < NMIX) bit = eu & 1;
            const u64 bm = __builtin_amdgcn_ballot_w64(bit);
            const unsigned nsw = __builtin_popcountll((f0 ^ f1) >> (4 * NMIX));
            if (lane == 0) { sm.swp = (unsigned)(bm ^ (bm >> NU)) & ((1u << NU) - 1); lds_st(&sm.gap, nsw ? (TG0 + TG1 * nsw / 8) * 0x10001u + ((TH0 + TH1 * nsw / 8) << 16) : 0u); }
            CBAR();
            lds_st(&sm.utab[lane], UENT(bit));
        }
        const unsigned swp = lds_ld(&sm.swp);
        const unsigned T = lds_ld(&sm.tag_lo);
        const unsigned t15 = T & 0x7fffu;
        const __amdgpu_buffer_rsrc_t rsrc = __builtin_amdgcn_make_buffer_rsrc(ws + WS_PART + split * 512, 0, NRW * NSPLIT * 512 - split * 512, 0x00020000);
        v2u* recs = (v2u*)&sm.cs[0][0][0];
#pragma unroll
        for (int i = 0; i < NOUT * 3; i++) (&sm.acc[0][0])[i * 64 + lane] = 0.0;
        CBAR();
        unsigned vut = sm.utab[NU + (lane & 31)] & 255;
        if (QUARTER((unsigned)lane) && (rank & 3)) vut = NH - 16 + (rank & 3) * 16 + ((vut & 15) ^ 1);
        __builtin_amdgcn_s_setprio(3);
        unsigned po[NU / 2];
#pragma unroll
        for (int k = 0; k < NU / 2; k++) po[k] = __builtin_amdgcn_ds_bpermute((2 * k + (lane >> 5)) * 4, vut) * (NSPLIT * 512) + (lane & 31) * 16;
        const u64 rb_ = (u64)(ws + WS_PART + split * 512);
        const v4u rs_ = {(unsigned)rb_, (unsigned)(rb_ >> 32) & 0xffffu, NRW * NSPLIT * 512 - split * 512, 0x00020000u};
#pragma unroll 1
        for (unsigned u = 0; u < 2 * NU - 1; u++) {
            const unsigned e = u % NU, so = u % NOUT;
            if (u >= NU) while (!((lds_ld(&sm.vrdy) >> e) & 1)) SLEEP(4)
            CBAR();
            const v4f h = *(const v4f*)&sm.wv[went(e) + (QUARTER(e) ? 32 : 128)];
            while (lds_ld(&sm.cnt[so]) != (QUARTER(e) ? 8 : 32)) SLEEP(1)
            CBAR();
            const double a0 = sm.acc[so][lane], a1 = sm.acc[so][64 + lane], a2 = sm.acc[so][128 + lane];
            if (u < NU) {
                float2 lg = *(const float2*)&sm.ent[so][2 * lane];
                if (QUARTER(e) && lane >> 4 != (rank & 3)) lg = make_float2(-INFINITY, -INFINITY);
                const v4f zs = *(const v4f*)&sm.vz[u % NIN][2 * lane];
                const float m = wave_max(fmaxf(lg.x, lg.y));
                const float p0 = __expf(lg.x - m), p1 = __expf(lg.y - m);
                const float x0 = p0 * zs.y, x1 = p1 * zs.w;
                const unsigned eb = max(__float_as_uint(wave_max(fmaxf(fabsf(x0), fabsf(x1)))) >> 23, 14u);
                const float f = __uint_as_float((268u - eb) << 23);
                const float w0 = fminf(fmaxf(rintf(x0 * f), -32767.f), 32767.f), w1 = fminf(fmaxf(rintf(x1 * f), -32767.f), 32767.f);
                const float den = wave_sum(p0 + p1);
                const float bz = wave_sum(fmaf(w0, zs.x, w1 * zs.z));
                const float L = fmaf(m, 1.4426950408889634f, __builtin_amdgcn_logf(den));
                unsigned* wd = (unsigned*)(((swp >> u) & 1 ? sm.wo : sm.wv) + went(u));
                if (!QUARTER(u) || lane >> 4 == (rank & 3)) wd[QUARTER(u) ? lane & 15 : lane] = ((unsigned)(int)w0 & 0xffffu) | ((unsigned)(int)w1 << 16);
                if (lane == 0) *(v4f*)&wd[QUARTER(u) ? 16 : 64] = (v4f){ldexpf(1.f / den, (int)eb - 141), bz, L, 0.f};
                CBAR();
                if (!((swp >> u) & 1)) LDS_OR(&sm.vrdy, 1u << u)
            } else {
                const float n0 = (float)(a0 - (double)h.y) * h.x, n1 = (float)(a1 - (double)h.y) * h.x, n2 = (float)(a2 - (double)h.y) * h.x;
                const unsigned e2 = max(__float_as_uint(wave_maxv(fmaxf(fmaxf(fabsf(n0), fabsf(n1)), fabsf(n2)))) >> 23, 14u);
                const float f2 = __uint_as_float((268u - e2) << 23);
                const unsigned m01 = __builtin_bit_cast(unsigned, __builtin_amdgcn_cvt_pk_i16((int)rintf(n0 * f2), (int)rintf(n1 * f2)));
                const unsigned m2 = __builtin_bit_cast(unsigned, __builtin_amdgcn_cvt_pk_i16((int)rintf(n2 * f2), 0));
                const unsigned bit = ((lane & 1 ? e2 : __float_as_uint(h.z)) >> (lane >> 1)) & 1u;
                recs[e * 64 + lane] = (v2u){m01, m2 | bit << 16 | t15 << 17};
                CBAR();
                sm.acc[so][lane] = 0.0; sm.acc[so][64 + lane] = 0.0; sm.acc[so][128 + lane] = 0.0;
            }
            CBAR();
            if (lane == 0) { lds_st(&sm.cnt[so], 0u); CBAR(); lds_st(&sm.merged, u + 1); }
            if (u == NU - 1 && swp) {
                wave_sync();
                const __amdgpu_buffer_rsrc_t srs = __builtin_amdgcn_make_buffer_rsrc(ws + WS_SWAP + blockIdx.x * 5120, 0, 5120, 0x00020000);
                v4u d[5]; unsigned o[5];
#pragma unroll
                for (int i = 0; i < 5; i++) {
                    const unsigned* w = (const unsigned*)&sm.wo[144 * NMIX + (i * 64 + lane) * 6];
                    const unsigned d0 = w[0], d1 = w[1], d2 = w[2];
                    d[i] = (v4u){d0, (d1 & 0xffffu) | (T << 16), (d1 >> 16) | (d2 << 16), (d2 >> 16) | ((T + (T >> 16)) << 16)};
                    o[i] = (swp >> wunit(i * 64 + lane)) & 1 ? (i * 64 + lane) * 16 : 0x40000000;
                }
                while ((int)(NOW() - lds_ld(&sm.line)) < (int)tdue(PSW) + TWO) SLEEP(1)
#pragma unroll
                for (int i = 0; i < 5; i++) __builtin_amdgcn_raw_buffer_store_b128(d[i], srs, o[i], 0, SC1 | 2);
            }
        }
        wave_sync();
#pragma unroll
        for (int k = 0; k < NU / 2; k++) asm volatile("ds_read_b128 a[%c0:%c0+3], %1 offset:%2" :: "i"(16 + 4 * k), "v"(lds_addr(recs) + lane * 16), "i"(k * 1024) : "memory");
        asm volatile("s_waitcnt lgkmcnt(0)" ::: "memory");
        while (!((lds_ld(&sm.vrdy) >> (NU - 1)) & 1)) SLEEP(4)
        CBAR();
        const v2u hw_ = {*(const unsigned*)&sm.wv[went(NU - 1) + 132], T};
        const unsigned hrow = (NRW - GSZ + (__builtin_amdgcn_readlane(vut, NU - 1) & 15 ^ 1)) * (NSPLIT * 512);
        while (lds_ld(&sm.grab) < NPC - PUBP) SLEEP(1)
        if (lane < 16) __builtin_amdgcn_raw_buffer_store_b64(hw_, rsrc, lane * 8, hrow, SC1);
#define PUB(K) asm volatile("buffer_store_dwordx4 a[%c0:%c0+3], %1, %2, 0 offen sc1" :: "i"(16 + 4 * (K)), "v"(po[K]), "s"(rs_) : "memory");
        PUB(WQ / 2) PUB(WQ / 2 + 1)
        { const unsigned t_ = TLAST() + PDEL; GATE(t_) }
        PUB(0) PUB(1) PUB(2) PUB(3) PUB(4) PUB(7) PUB(8)
        return;
    }
    }
    __builtin_amdgcn_s_setprio(0);
    unsigned c;
#define ADDRTN(P) { u64 ex_; asm volatile("s_mov_b64 %0, exec\n\ts_mov_b64 exec, 1\n\tds_add_rtn_u32 a15, %1, a13\n\ts_mov_b64 exec, %0" : "=&s"(ex_) : "v"(lds_addr(P)) : "a15", "memory"); }
#define ADDED(V) { unsigned t_; asm volatile("s_waitcnt lgkmcnt(0)\n\tv_accvgpr_read_b32 %1, a15\n\ts_nop 0\n\tv_readfirstlane_b32 %0, %1\n\ts_nop 1" : "=s"(V), "=v"(t_)); }
    asm volatile("v_accvgpr_write_b32 a13, 1\n\ts_nop 1" ::: "a13");
    ADDRTN(&sm.cidx) ADDED(c)
    if (c < NSW) {
        while (lds_ld(&sm.grab) < NPC + 2) SLEEP(4)
        const unsigned head = lds_ld(&sm.chead), T = lds_ld(&sm.tag_lo), tl = TLAST();
        const u64 rb_ = (u64)(ws + WS_PART);
        const v4u rs_ = {(unsigned)rb_, (unsigned)(rb_ >> 32) & 0xffffu, NRW * NSPLIT * 512, 0x00020000u};
        const unsigned ro = head * (NSPLIT * 512) + c * 1024, rx = (NH + (head & 15 ^ 1)) * (NSPLIT * 512) + c * 1024;
#define POLL(S, O) asm volatile("buffer_load_dwordx4 a[%c0:%c0+3], %1, %2, %3 offen sc1" :: "i"(16 + 4 * (S)), "v"(lane * 16), "s"(rs_), "s"(O) : "memory");
#define FETCH(X, S, N) asm volatile("s_waitcnt vmcnt(%c5)\n\tv_accvgpr_read_b32 %0, a[%c4]\n\tv_accvgpr_read_b32 %1, a[%c4+1]\n\tv_accvgpr_read_b32 %2, a[%c4+2]\n\tv_accvgpr_read_b32 %3, a[%c4+3]" \
                                    : "=v"((X).x), "=v"((X).y), "=v"((X).z), "=v"((X).w) : "i"(16 + 4 * (S)), "i"(N));
        float o0, o1, o2;
        if (head >= NH - GSZ) {
            __builtin_amdgcn_s_setprio(3);
            if (c == 0) {
                v2u hd;
                GATE(tl + THD)
                for (;;) {
                    hd = __builtin_amdgcn_raw_buffer_load_b64(__builtin_amdgcn_make_buffer_rsrc(ws + WS_PART + (NRW - GSZ + (head & 15 ^ 1)) * (NSPLIT * 512), 0, NSPLIT * 512, 0x00020000), (lane & 15) * 512, 0, SC1);
                    if (__builtin_amdgcn_ballot_w64(hd.y == T) == ~0ull) break;
                    SLEEP(4)
                }
                const float w = __builtin_amdgcn_exp2f(__uint_as_float(hd.x) - row_max(__uint_as_float(hd.x)));
                if (lane < 16) sm.ent[0][lane] = w * __builtin_amdgcn_rcpf(row_sum(w));
                CBAR();
                if (lane == 0) lds_st(&sm.tabs, 0u);
            }
            while (lds_ld(&sm.tabs) || lds_ld(&sm.merged) <= (2 * NU - 1) / NOUT * NOUT) SLEEP(1)
            CBAR();
            const float cf = sm.ent[0][2 * c + (lane >> 5)];
            v4u x; bool got;
#define SEEN(J, N) { FETCH(x, J, N) got = __builtin_amdgcn_ballot_w64((((x.y ^ T << 24) | (x.w ^ T << 24)) >> 24) != 0) == 0; }
            GATE(tl + TCA) POLL(0, ro) GATE(tl + TCA + TCD) POLL(1, rx + 3 * GSZ * NSPLIT * 512) SEEN(0, 1)
            if (!got) SEEN(1, 0)
            while (!got) { POLL(2, rx + 4 * GSZ * NSPLIT * 512) SEEN(2, 0) SLEEP(0) }
            const double c0 = ldexpf(cf, (int)(x.y >> 16 & 255u) - 141), c1 = ldexpf(cf, (int)(x.w >> 16 & 255u) - 141);
#define SWADD(A, B) ({ const v2u a_ = __builtin_bit_cast(v2u, A), b_ = __builtin_bit_cast(v2u, B); \
                const auto l_ = __builtin_amdgcn_permlane32_swap(a_.x, b_.x, false, false); const auto h_ = __builtin_amdgcn_permlane32_swap(a_.y, b_.y, false, false); \
                __builtin_bit_cast(double, (v2u){l_[0], h_[0]}) + __builtin_bit_cast(double, (v2u){l_[1], h_[1]}); })
            const double s0 = SWADD(c0 * (double)(short)x.x, c1 * (double)(short)x.z), s1 = SWADD(c0 * (double)((int)x.x >> 16), c1 * (double)((int)x.z >> 16)),
                         s2 = SWADD(c0 * (double)(short)x.y, c1 * (double)(short)x.w);
            asm volatile("ds_add_f64 %0, %1\n\tds_add_f64 %0, %2 offset:512\n\tds_add_f64 %0, %3 offset:1024" :: "v"(lds_addr(&sm.acc[0][lane])), "v"(s0), "v"(s1), "v"(s2) : "memory");
            ADDRTN(&sm.cdone)
            const double a0 = sm.acc[0][lane], a1 = sm.acc[0][64 + lane], a2 = sm.acc[0][128 + lane];
            ADDED(c)
            o0 = a0; o1 = a1; o2 = a2;
        } else {
            const bool quart = head >> 4 == WQ;
            const unsigned t15 = T & 0x7fffu;
#define TAKE(K) { FETCH(x[K], K, 0) \
                bad |= (x[K].y >> 17 ^ t15) | (x[K].w >> 17 ^ t15); \
                const u64 bl = __builtin_amdgcn_ballot_w64((x[K].y & 0x10000u) != 0), be = __builtin_amdgcn_ballot_w64((x[K].w & 0x10000u) != 0); \
                L[K] = __uint_as_float(lane < 32 ? (unsigned)bl : (unsigned)(bl >> 32)); ex[K] = lane < 32 ? (unsigned)be : (unsigned)(be >> 32); \
                M = fmaxf(M, fmaxf(__uint_as_float((unsigned)bl), __uint_as_float((unsigned)(bl >> 32)))); }
#define SUMUP(K) { const float w_ = __builtin_amdgcn_exp2f(L[K] - M), c_ = ldexpf(w_, (int)(ex[K] & 255u) - 141); den += w_; \
                t[0] = fmaf(c_, (float)(short)x[K].x, t[0]); t[1] = fmaf(c_, (float)((int)x[K].x >> 16), t[1]); t[2] = fmaf(c_, (float)(short)x[K].y, t[2]); \
                t[3] = fmaf(c_, (float)(short)x[K].z, t[3]); t[4] = fmaf(c_, (float)((int)x[K].z >> 16), t[4]); t[5] = fmaf(c_, (float)(short)x[K].w, t[5]); }
            const unsigned tg = tl + (quart ? TRQ : TRD);
            GATE(tg)
            v4u x[4]; float L[4], M; unsigned ex[4];
            for (;;) {
                unsigned bad = 0; M = -3e38f;
                POLL(0, ro)
                if (quart) { POLL(1, rx) POLL(2, rx + GSZ * NSPLIT * 512) POLL(3, rx + 2 * GSZ * NSPLIT * 512) }
                __builtin_amdgcn_s_setprio(3);
                TAKE(0)
                if (quart) { TAKE(1) TAKE(2) TAKE(3) }
                if (__builtin_amdgcn_ballot_w64(bad != 0) == 0) break;
                SLEEP(2)
            }
            float t[6] = {0.f, 0.f, 0.f, 0.f, 0.f, 0.f}, den = 0.f;
            SUMUP(0)
            if (quart) { SUMUP(1) SUMUP(2) SUMUP(3) }
#define SWADF(A, B) ({ const auto l_ = __builtin_amdgcn_permlane32_swap(__float_as_uint(A), __float_as_uint(B), false, false); __uint_as_float(l_[0]) + __uint_as_float(l_[1]); })
            v4f* pb = (v4f*)((char*)&sm.cs[0][0][0] + 65536);
            float* pm = (float*)(pb + NSW * 64);
            pb[c * 64 + lane] = (v4f){SWADF(t[0], t[3]), SWADF(t[1], t[4]), SWADF(t[2], t[5]), SWADF(den, den)};
            if (lane == 0) pm[c] = M;
            CBAR();
            ADDRTN(&sm.cdone)
            ADDED(c)
            CBAR();
            if (c == NSW - 1) {
                const v4f ma = *(const v4f*)pm, mb = *(const v4f*)(pm + 4);
                const float mw[NSW] = {ma.x, ma.y, ma.z, ma.w, mb.x, mb.y, mb.z, mb.w};
                float Mx = mw[0];
#pragma unroll
                for (int w = 1; w < NSW; w++) Mx = fmaxf(Mx, mw[w]);
                v4f sum = {0.f, 0.f, 0.f, 0.f};
#pragma unroll
                for (int w = 0; w < NSW; w++) sum += pb[w * 64 + lane] * __builtin_amdgcn_exp2f(mw[w] - Mx);
                const float inv = __builtin_amdgcn_rcpf(sum.w);
                o0 = sum.x * inv; o1 = sum.y * inv; o2 = sum.z * inv;
            }
        }
        if (c == NSW - 1) {
            asm("" : "+v"(o0), "+v"(o1), "+v"(o2));
            typedef __bf16 v2b __attribute__((ext_vector_type(2)));
            typedef unsigned v3u __attribute__((ext_vector_type(3)));
#define PK(A, B) __builtin_bit_cast(unsigned, (v2b){(__bf16)(A), (__bf16)(B)})
            const unsigned p12 = PK(o1, o2);
            const v3u ov = {PK(o0, o1), PK(o2, __uint_as_float(__builtin_amdgcn_permlane32_swap(__float_as_uint(o0), __float_as_uint(o0), false, false)[1])), __builtin_amdgcn_permlane32_swap(p12, p12, false, false)[1]};
            if (lane < 32) __builtin_amdgcn_raw_buffer_store_b96(ov, __builtin_amdgcn_make_buffer_rsrc(out + (size_t)head * DH, 0, DH * 2, 0x00020000), lane * 12, 0, 0);
        }
    }
}

extern "C" void kernel_launch(void* const* d_in, const int* in_sizes, int n_in,
                              void* d_out, int out_size, void* d_ws, size_t ws_size,
                              hipStream_t stream) {
    const unsigned short* q = (const unsigned short*)d_in[0];
    const int* qk = (const int*)d_in[1];
    const unsigned short* k_scales = (const unsigned short*)d_in[2];
    const unsigned short* k_zeros = (const unsigned short*)d_in[3];
    const int* qv = (const int*)d_in[4];
    const unsigned short* v_scales = (const unsigned short*)d_in[5];
    const unsigned short* v_zeros = (const unsigned short*)d_in[6];
    const unsigned short* q_cos = (const unsigned short*)d_in[7];
    const unsigned short* q_sin = (const unsigned short*)d_in[8];
    const unsigned short* k_cos = (const unsigned short*)d_in[9];
    const unsigned short* k_sin = (const unsigned short*)d_in[10];
    unsigned char* ws = (unsigned char*)d_ws;
    attn_kernel<<<NSPLIT * GSZ, NTHREADS, 0, stream>>>(q, qk, k_scales, k_zeros, qv, v_scales, v_zeros,
                                                       q_cos, q_sin, k_cos, k_sin, (__hip_bfloat16*)d_out, ws);
}
```

```cpp
#include <hip/hip_runtime.h>
#include <hip/hip_bf16.h>
#include <stdint.h>

#define DH 192
#define HALF 96
#define SKV 2048
#define NH 256
#define NSW 8
#define NWAVES (NSW + 4)
#define NTHREADS (64 * NWAVES)
#define BROWS 128
#define NSPLIT 16
#define GSZ 16
#define WQ 10
#define NU (NH / GSZ + 3)
#define NPC 1024
#define NRR (NH + 3 * GSZ)
#define NIN 8
#define NOUT 8
#define WALL (15 * 144 + 4 * 48)
#define SC1 16
#define NRND 48
#define RST 4352
#define TK 2
#define NMIX 3
#define TUNIT 311
#define T01 50
#define TMIX 7
#define TOFF 50
#define PSF 100
#define TGS 82
#define PSW (NPC / 2 + 28)
#define TG0 8
#define TG1 5
#define TWO -80
#define PSG (NPC / 2 + 60)
#define TH0 3
#define TH1 3
#define EARLY 10
#define LEADP 24
#define DPH 2
#define TA 210
#define TA2 70
#define CSH 2
#define NPRB 3
#define TMAP 485
#define MINV 96
#define VW 4
#define MASTER 203
#define TRES 175
#define TB1 1200
#define PER 300
#define RDD 100
#define LEAD 70
#define LHI 85
#define LOK 74
#define PSL0 124
#define PUBP -12
#define LJ 210
#define LOK2 75
#define NTRY 3
#define TFO 6
#define SL0 42.f
#define PST0 200
#define KC 4
#define TC0 1000
#define TCS 9
#define PRE0 3

#define KREL 12
#define PDEL 70
#define NCP 3
#define NRW (NRR + NCP * GSZ)
#define TRD 116
#define THD 55
#define TRQ 55
#define TCA 138
#define TCD 22
#define TSD 560
#define WS_EPOCH 0
#define WS_POST0 8192
#define WS_POST (WS_POST0 + ((unsigned)E1 & 15) * 2048)
#define WS_NEWS (49152 + 256)
#define WS_HELLO 59904
#define WS_BOARD 65536
#define WS_PART (2u << 20)
#define WS_SWAP (WS_PART + NRW * NSPLIT * 512)

typedef unsigned int v2u __attribute__((ext_vector_type(2)));
typedef unsigned int v16u __attribute__((ext_vector_type(16)));
typedef unsigned int v4u __attribute__((ext_vector_type(4)));
typedef int v4i __attribute__((ext_vector_type(4)));
typedef float v2f __attribute__((ext_vector_type(2)));
typedef float v4f __attribute__((ext_vector_type(4)));
typedef unsigned long long u64;

__device__ __forceinline__ float bflo(unsigned u) { return __uint_as_float(u << 16); }
__device__ __forceinline__ float bfhi(unsigned u) { return __uint_as_float(u & 0xffff0000u); }
__device__ __forceinline__ float bfr(float x) {
    __bf16 b = (__bf16)x;
    return __uint_as_float(((unsigned)__builtin_bit_cast(unsigned short, b)) << 16);
}

#define SLEEP(n) SLEEP_(n)
#define SLEEP_(n) asm volatile("s_sleep " #n "\n\tv_add_co_u32 %0, vcc, 1, %0\n\ts_cbranch_vccz 1f\n\ts_endpgm\n1:" : "+v"(spins) :: "vcc", "memory");
#define SPIN0 0xf0000000u
#define CBAR() asm volatile("" ::: "memory")

__device__ __forceinline__ void wave_sync() {
    __builtin_amdgcn_fence(__ATOMIC_RELEASE, "wavefront");
    __builtin_amdgcn_wave_barrier();
    __builtin_amdgcn_fence(__ATOMIC_ACQUIRE, "wavefront");
}
template <int CTRL>
__device__ __forceinline__ float dpp_add(float x) {
    int y = __builtin_amdgcn_update_dpp(0, __float_as_int(x), CTRL, 0xf, 0xf, true);
    return x + __int_as_float(y);
}
template <int CTRL>
__device__ __forceinline__ float dpp_max(float x) {
    int y = __builtin_amdgcn_update_dpp(__float_as_int(x), __float_as_int(x), CTRL, 0xf, 0xf, false);
    return fmaxf(x, __int_as_float(y));
}
__device__ __forceinline__ float row_sum(float x) { x = dpp_add<0x128>(x); x = dpp_add<0x124>(x); x = dpp_add<0x122>(x); return dpp_add<0x121>(x); }
__device__ __forceinline__ float row_max(float m) { m = dpp_max<0x128>(m); m = dpp_max<0x124>(m); m = dpp_max<0x122>(m); return dpp_max<0x121>(m); }
__device__ __forceinline__ float rdlane(float x, int l) { return __int_as_float(__builtin_amdgcn_readlane(__float_as_int(x), l)); }
__device__ __forceinline__ float wave_max(float m) { m = row_max(m); return fmaxf(fmaxf(rdlane(m, 0), rdlane(m, 16)), fmaxf(rdlane(m, 32), rdlane(m, 48))); }
__device__ __forceinline__ float wave_maxv(float m) {
    m = row_max(m);
    const auto a = __builtin_amdgcn_permlane16_swap(__float_as_uint(m), __float_as_uint(m), false, false);
    m = fmaxf(__uint_as_float(a[0]), __uint_as_float(a[1]));
    const auto b = __builtin_amdgcn_permlane32_swap(__float_as_uint(m), __float_as_uint(m), false, false);
    return fmaxf(__uint_as_float(b[0]), __uint_as_float(b[1]));
}
__device__ __forceinline__ float wave_sum(float x) { x = row_sum(x); return (rdlane(x, 0) + rdlane(x, 16)) + (rdlane(x, 32) + rdlane(x, 48)); }
__device__ __forceinline__ unsigned lds_ld(const unsigned* p) { return __builtin_amdgcn_readfirstlane(__hip_atomic_load(p, __ATOMIC_RELAXED, __HIP_MEMORY_SCOPE_WORKGROUP)); }
__device__ __forceinline__ unsigned lds_addr(const void* p) { return (unsigned)(size_t)(const __attribute__((address_space(3))) void*)p; }
#define DMA16(SRC, LDS) asm volatile("s_mov_b32 %0, m0\n\ts_mov_b32 m0, %2\n\ts_nop 0\n\tglobal_load_lds_dwordx4 %1, off\n\ts_mov_b32 m0, %0" : "=&s"(m0keep) : "v"(SRC), "s"(LDS) : "memory")
__device__ __forceinline__ void lds_st(unsigned* p, unsigned v) { __hip_atomic_store(p, v, __ATOMIC_RELAXED, __HIP_MEMORY_SCOPE_WORKGROUP); }
#define NOW() ((unsigned)__builtin_amdgcn_s_memrealtime())
__device__ __forceinline__ unsigned tdue(unsigned g) {
    return (g * TUNIT + min(g, 480u) * TK + min(g, 48u) * T01 + min(g, 32u * NMIX) * TMIX + (g >= PSF ? TGS * 32 : 0)) >> 5;
}
__device__ __forceinline__ unsigned tvx(unsigned g, unsigned gap) {
    return (g >= PSW ? gap >> (g >= PSG ? 12 : 0) & 0xfffu : 0u) + (min(g - NPC / 2, 32u * NMIX) * (gap >> 24) >> 5);
}

__device__ __forceinline__ unsigned unit_of(unsigned g) { const unsigned p = g & 511; return (g >> 9) * NU + (p >> 5) + min(max((int)p - 32 * WQ, 0) >> 3, 3); }
__device__ __forceinline__ unsigned first_of(unsigned e) { return 32 * e - 24 * min(max((int)e - WQ, 0), 4); }
#define QUARTER(E) ((E) - WQ < 4)
__device__ __forceinline__ unsigned went(unsigned e) { return 144 * e - 96 * min(max((int)e - WQ, 0), 4); }
__device__ __forceinline__ unsigned wunit(unsigned x) { x += 24 * NMIX; return x < 24 * WQ ? x / 24 : x < 24 * WQ + 32 ? WQ + (x - 24 * WQ) / 8 : (x + 64) / 24; }

#define ROTT 0xed8d2c3d2c35ed84ull, 0xba85aa165b03ac82ull, 0xee7efa489a38af71ull, 0xe932da1b0bab51d8ull, 0xfa96112d5fee971bull, 0x2c7e9ea5a544f071ull, 0x3d86093d5f0e810bull, 0xe932da1b0bab51d8ull, 0xfa96112d5fee971bull, 0xd834e62c5fa723e9ull, 0xa4cd778c2ca4aaa5ull, 0xe932da1b0bab51d8ull, 0xfa96112d5fee971bull, 0xc834e12c0aaa4ec7ull, 0x3d86093d5f0e810bull, 0xe932da1b0bab51d8ull
static __device__ const u64 ROT[16] = {ROTT};

struct __attribute__((aligned(16))) Smem {
    unsigned cs[NSW][4][768];
    double acc[NOUT][DH];
    float ent[NOUT][BROWS];
    unsigned short wv[WALL];
    unsigned short wo[WALL];
    unsigned q[NIN][HALF];
    float2 kz[NIN][BROWS];
    float2 vz[NIN][BROWS];
    unsigned utab[64];
    unsigned line, gap;
    unsigned late, grab;
    unsigned rdy[NIN], cnt[NOUT];
    unsigned tabs, vrdy, swp;
    unsigned aline;
    unsigned tag_rdy, tag_lo, tag_hi, merged, cdone, cidx;
    unsigned slow;
    unsigned chead;
};

#define LDS_ADD(P, V) { u64 ex_; asm volatile("s_mov_b64 %0, exec\n\ts_mov_b64 exec, 1\n\tds_add_u32 %1, %2\n\ts_mov_b64 exec, %0" : "=&s"(ex_) : "v"(lds_addr(P)), "v"(V) : "memory"); }
#define LDS_OR(P, V) { u64 ex_; asm volatile("s_mov_b64 %0, exec\n\ts_mov_b64 exec, 1\n\tds_or_b32 %1, %2\n\ts_mov_b64 exec, %0" : "=&s"(ex_) : "v"(lds_addr(P)), "v"(V) : "memory"); }

__global__ __launch_bounds__(NTHREADS, 3) void attn_kernel(
    const unsigned short* __restrict__ q, const int* __restrict__ qk,
    const unsigned short* __restrict__ k_scales, const unsigned short* __restrict__ k_zeros,
    const int* __restrict__ qv,
    const unsigned short* __restrict__ v_scales, const unsigned short* __restrict__ v_zeros,
    const unsigned short* __restrict__ q_cos, const unsigned short* __restrict__ q_sin,
    const unsigned short* __restrict__ k_cos, const unsigned short* __restrict__ k_sin,
    __hip_bfloat16* __restrict__ out, unsigned char* __restrict__ ws)
{
    __shared__ Smem sm;
    const int tid = threadIdx.x;
    unsigned spins = SPIN0;
    const int lane = tid & 63;
    const int hw = __builtin_amdgcn_readfirstlane(tid >> 6);
    u64 t00; unsigned ka;
    asm volatile("s_memrealtime %0\n\ts_load_dword %1, %2, 0x40" : "=&s"(t00), "=&s"(ka) : "s"(__builtin_amdgcn_kernarg_segment_ptr()));
    if (hw == 0) {
        const char* pc0; asm volatile("s_getpc_b64 %0" : "=s"(pc0));
        asm volatile("global_load_dword a6, %0, %1" :: "v"(((blockIdx.x & ~7u) + (lane & 7)) * 128), "s"(pc0) : "a6", "memory");
    }
    const int wave = hw >= 4 ? hw - 4 : hw + NSW;
    const unsigned split = (blockIdx.x & 1) + (blockIdx.x & 2) * 4 + ((blockIdx.x >> 3) & 3) * 2;
    const unsigned side = (split ^ (blockIdx.x >> 2)) & 1, rank = blockIdx.x >> 5;
    const unsigned head0 = 2 * rank + side;
    const unsigned rf0 = (8 * split + 1) / 3, r3 = 8 * split + 1 - 3 * rf0;
    const unsigned koff = ((lane >> 5) * DH + 3 * (lane & 31)) * 4, koffb = koff + 384;
#define AR(S) (16 + (S) * 8)
#define GLD(A, OFF, BASE, IMM) asm volatile("global_load_dwordx3 a[%c0:%c1], %2, %3 offset:%4 nt" :: "i"(A), "i"((A) + 2), "v"(OFF), "s"(BASE), "i"(IMM))
#define ISSUE(S, J, BASE, OA, OB) { GLD(AR(S), OA, BASE, (J) * 1536); GLD(AR(S) + 4, OB, BASE, (J) * 1536); }
#define HEAD2 ((GSZ + 1 - 2 * side) * (SKV * DH * 4))
    const unsigned wo = (hw - 2) & 7;
    const bool pre = hw >= 4 && wo < PRE0;
    if (pre) {
        const char* a = (const char*)qk + (head0 * 512 + side * 256 + rf0 + wave + (wave >= 6 ? 80 : 0)) * 3072; const char* b = a + HEAD2;
        ISSUE(0, 0, a, koff, koffb) ISSUE(1, 1, a, koff, koffb) ISSUE(2, 0, b, koff, koffb) ISSUE(3, 1, b, koff, koffb)
    }
    asm volatile("s_waitcnt lgkmcnt(0)" : "+s"(t00), "+s"(ka));
    if (hw == 0) {
        constexpr u64 rot[16] = {ROTT};
        sm.utab[lane] = lane == 2 ? 2 * GSZ + head0 | ((unsigned)(rot[2] >> 4 * head0) & 15) << 9 : ~0u;
        unsigned l0 = (unsigned)t00 + TOFF;
        if (KREL) { const unsigned t1 = NOW() + KREL; if ((int)(t1 - l0) > 0) l0 = t1; }
        if (lane < 32) (&sm.line)[lane] = lane == 3 ? 64u : (lane & 29) == 0 ? l0 : 0u;
        asm volatile("s_waitcnt lgkmcnt(0)" ::: "memory");
    }
    __builtin_amdgcn_s_barrier();
    CBAR();
    const u64 rtab = r3 == 1 ? 0x5756552c2b020100ull : r3 == 0 ? 0x5756552c2b2a0100ull : 0x57562d2c2b020100ull;
#define R8(I) (rf0 + ((unsigned)(rtab >> (8 * (I))) & 255u))
#define GATE(DUE) { int d_ = (int)((DUE) - NOW()); if (d_ < 3000) while (d_ > 0) { __builtin_amdgcn_s_sleep(1); d_ = (int)((DUE) - NOW()); } }
#define TLAST() (lds_ld(&sm.line) + tdue(NPC - 2) + tvx(NPC - 2, lds_ld(&sm.gap)))
    if (__builtin_expect(wave < NSW, 1)) {
        const int cst = 2 * side;
        const unsigned r8w = R8((unsigned)wave);
        const char* cbase[3];
#pragma unroll
        for (int j = 0; j < 3; j++) { const int x = j * 64 + lane; cbase[j] = (x < 96 ? (const char*)k_cos + x * 16 : (const char*)k_sin + (x - 96) * 16) + r8w * (4 * DH * 2); }
        unsigned m0keep;
#define CS_DMA(W, C) { _Pragma("unroll") for (int j = 0; j < 3; j++) DMA16(cbase[j] + (C) * (512 * DH * 2), lds_addr(&sm.cs[0][0][0]) + ((W) * 4 + (C)) * 3072 + j * 1024); }
#define REPACK(W, C) { \
        unsigned* cw = &sm.cs[W][C][0]; \
        const unsigned* l = cw + rsel * (DH / 2) + qoff; \
        unsigned r[2][6]; \
        _Pragma("unroll") \
        for (int j = 0; j < 2; j++) { \
            unsigned e[4][2]; \
            _Pragma("unroll") \
            for (int k = 0; k < 4; k++) { \
                const unsigned* a = l + j * DH + (k & 1) * 48 + (k >> 1) * 384;       \
                unsigned x0 = a[0], x1 = a[1]; \
                e[k][0] = __builtin_amdgcn_alignbit(x1, x0, sh); \
                e[k][1] = x1 >> sh; \
            } \
            r[j][0] = (e[0][0] & 0xffffu) | (e[3][0] << 16); \
            r[j][1] = (e[0][0] >> 16) | (e[3][0] & 0xffff0000u); \
            r[j][2] = (e[0][1] & 0xffffu) | (e[3][1] << 16); \
            r[j][3] = (e[1][0] & 0xffffu) | (e[2][0] << 16); \
            r[j][4] = (e[1][0] >> 16) | (e[2][0] & 0xffff0000u); \
            r[j][5] = (e[1][1] & 0xffffu) | (e[2][1] << 16); \
        } \
        wave_sync(); \
        _Pragma("unroll") \
        for (int j = 0; j < 2; j++) { *(v4u*)&cw[j * 256 + lane * 4] = (v4u){r[j][0], r[j][1], r[j][2], r[j][3]}; *(v2u*)&cw[512 + j * 128 + lane * 2] = (v2u){r[j][4], r[j][5]}; } \
        wave_sync(); }
        const int sub = lane & 31;
        const int rsel = lane >> 5;
        const unsigned sh = (sub & 1) * 16;
        const int qoff = 3 * (sub >> 1) + (sub & 1);
        const unsigned voff = 3 * lane * 4;
        asm volatile("" ::: "a16", "a17", "a18", "a19", "a20", "a21", "a22", "a23", "a24", "a25", "a26", "a27", "a28", "a29", "a30", "a31",
                            "a32", "a33", "a34", "a35", "a36", "a37", "a38", "a39", "a40", "a41", "a42", "a43", "a44", "a45", "a46", "a47");
        asm volatile("" ::: "a48", "a49", "a50", "a51", "a52", "a53", "a54", "a55", "a56", "a57", "a58", "a59", "a60", "a61", "a62", "a63",
                            "a64", "a65", "a66", "a67", "a68", "a69", "a70", "a71", "a72", "a73", "a74", "a75", "a76", "a77", "a78", "a79");
        int ta[4][3], tb[4][3];
#define WAITT(P, S, N) asm volatile("s_waitcnt vmcnt(%6)\n\tv_accvgpr_read_b32 %0, a[%c7]\n\tv_accvgpr_read_b32 %1, a[%c8]\n\tv_accvgpr_read_b32 %2, a[%c9]\n\t" \
                              "v_accvgpr_read_b32 %3, a[%c10]\n\tv_accvgpr_read_b32 %4, a[%c11]\n\tv_accvgpr_read_b32 %5, a[%c12]" \
                              : "=v"(ta[P][0]), "=v"(ta[P][1]), "=v"(ta[P][2]), "=v"(tb[P][0]), "=v"(tb[P][1]), "=v"(tb[P][2]) \
                              : "i"(N), "i"(AR(S)), "i"(AR(S) + 1), "i"(AR(S) + 2), "i"(AR(S) + 4), "i"(AR(S) + 5), "i"(AR(S) + 6))
        const unsigned voffb = voff + 768;
        u64 ln = 0;
#define TDUE(G) (tdue(G) + ((G) >= NPC / 2 ? tvx(G, __builtin_amdgcn_readfirstlane((unsigned)(ln >> 32))) : 0u))
#define REPORT() { const unsigned t_ = now - tdc; if (lane == 0) lds_st(&sm.late, t_); }
#define LINE() ln = LDV((u64*)&sm.line);
#define LDV(P) __hip_atomic_load(P, __ATOMIC_RELAXED, __HIP_MEMORY_SCOPE_WORKGROUP)
        unsigned u = 0, rg = cst * 8 + wave, np = 0;
        CS_DMA(wave, cst)

        const char* kb0 = (const char*)qk + (size_t)(head0 * 512 + r8w) * 3072;
#define KB(I) (kb0 + ((cst + (I)) & 3) * (128 * 3072))
        const char* kb; const char* kb2;
#define SISSUE(I) { kb = KB(I); kb2 = kb + HEAD2; const unsigned du_ = lds_ld(&sm.line) + tdue(16 * (I) + 2 * wo); GATE(du_) \
            ISSUE(((I) & 1) * 4, 0, kb, koff, koffb) ISSUE(((I) & 1) * 4 + 1, 1, kb, koff, koffb) ISSUE(((I) & 1) * 4 + 2, 0, kb2, koff, koffb) ISSUE(((I) & 1) * 4 + 3, 1, kb2, koff, koffb) }
        if (!pre) SISSUE(0)
        CS_DMA(wave, (cst + 1) & 3)
        if (pre) asm volatile("s_waitcnt vmcnt(3)" ::: "memory");
        asm volatile("s_waitcnt vmcnt(11)" ::: "memory");
        if (!pre) LDS_ADD(&sm.slow, NOW() - lds_ld(&sm.line) - tdue(2 * wo))
        REPACK(wave, cst)
        SISSUE(1)
#define TABDONE(I) LDS_OR(&sm.tabs, 1u << ((((cst + (I)) & 3) * 8) + wave))
        TABDONE(0)
        const float scale = 0.07216878364870322f;
        double acc[3] = {0.0, 0.0, 0.0};
        bool first = true, chg, none = false;
        unsigned Qp[3], Qn[3], Rp[3], Rn[3];
        float kz4[4], ksc, kres[4];
        v4u ca[4]; v2u cb[4];
        v2f qe[4][3], A[4];
        unsigned vut, nrg;
        const unsigned vr = ((lane >> 3) & 3) * 128 + R8((unsigned)lane & 7);
        unsigned g, mg;
        const unsigned grab_adr = lds_addr(&sm.grab);
        asm volatile("v_accvgpr_write_b32 a13, 1\n\tv_accvgpr_write_b32 a14, 2" ::: "a13", "a14", "a15");
#define GRAB(N) { u64 ex_; asm volatile("s_mov_b64 %0, exec\n\ts_mov_b64 exec, 1\n\tds_add_rtn_u32 a15, %1, a%c2\n\ts_mov_b64 exec, %0" : "=&s"(ex_) : "v"(grab_adr), "i"(12 + (N)) : "memory"); }
#define GRABBED() { unsigned t_; asm volatile("s_waitcnt lgkmcnt(0)\n\tv_accvgpr_read_b32 %1, a15\n\ts_nop 0\n\tv_readfirstlane_b32 %0, %1\n\ts_nop 1" : "=s"(g), "=v"(t_)); }
        unsigned oa, ob, un, tdn, tiss, tdc = 0;
#define PLAN() { \
            GRABBED() un = unit_of(g); chg = un != u; none = g >= NPC; \
            const bool nv = g >= NPC / 2; \
            unsigned hd = __builtin_amdgcn_readlane(vut, un); \
            while (hd == ~0u) { SLEEP(1) hd = lds_ld(&sm.utab[un]); } \
            nrg = (g + (hd >> 8)) & 31; hd &= 255; \
            unsigned boff, boff2; asm("s_mul_i32 %0, %1, 0xc00" : "=s"(boff) : "s"(hd * 512 + __builtin_amdgcn_readlane(vr, nrg))); \
            asm("s_mul_i32 %0, %1, 0xc00" : "=s"(boff2) : "s"(hd * 512 + __builtin_amdgcn_readlane(vr, nrg + 1))); \
            const char* b_ = nv ? (const char*)qv : (const char*)qk; \
            kb = b_ + boff; kb2 = b_ + boff2; \
            oa = nv ? voff : koff; ob = nv ? voffb : koffb; \
            tdn = TDUE(g); }
#define FIRE() { ISSUE(0, 0, kb, oa, ob) ISSUE(1, 1, kb, oa, ob) ISSUE(2, 0, kb2, oa, ob) ISSUE(3, 1, kb2, oa, ob) }
#define LAUNCH() { \
            LINE() \
            const unsigned du_ = __builtin_amdgcn_readfirstlane((unsigned)ln) + tdn; \
            GATE(du_) \
            ISSUE(0, 0, kb, oa, ob) ISSUE(1, 1, kb, oa, ob) \
            GATE(du_ + 10) \
            ISSUE(2, 0, kb2, oa, ob) ISSUE(3, 1, kb2, oa, ob) tiss = NOW(); }
#define ARRIVED() \
            const unsigned now = NOW(); \
            const bool stall = now - tiss > LJ && g - PST0 < NPC - 48 - PST0; \
            const bool late = !none && !stall && (int)(now + EARLY - __builtin_amdgcn_readfirstlane((unsigned)ln) - tdn) >= 0; \
            if (late) { FIRE() tiss = now; } \
            if (stall && lane == 0) __hip_atomic_fetch_max(&sm.line, (unsigned)ln + (now - tiss) - LOK2, __ATOMIC_RELAXED, __HIP_MEMORY_SCOPE_WORKGROUP); \
            REPORT()
#define ADVANCE(ISV) { \
            if (chg) FLUSH(ISV) \
            if (!none && !late) LAUNCH() \
            first = chg; u = un; rg = nrg; tdc = tdn + 10; }
#define K_TAB(MODE) { _Pragma("unroll") for (int p = 0; p < ((MODE) ? 2 : 4); p++) { \
            const unsigned* cw = &sm.cs[(rg & 7) + (p >> 1)][rg >> 3][0]; \
            ca[p] = *(const v4u*)&cw[(p & 1) * 256 + lane * 4]; cb[p] = *(const v2u*)&cw[512 + (p & 1) * 128 + lane * 2]; } }
#define K_PRE(MODE, P) { \
        float e0, e1, e2, f0, f1, f2; \
        const unsigned* qp_ = ((MODE) && (P) >= 2) ? Rp : Qp; const unsigned* qn_ = ((MODE) && (P) >= 2) ? Rn : Qn; \
        const int tp_ = (MODE) ? (P) & 1 : (P); \
        asm("v_dot2_f32_bf16 %0, %6, %12, 0\n\tv_dot2_f32_bf16 %3, %9, %15, 0\n\tv_dot2_f32_bf16 %1, %7, %13, 0\n\tv_dot2_f32_bf16 %4, %10, %16, 0\n\t" \
            "v_dot2_f32_bf16 %2, %8, %14, 0\n\tv_dot2_f32_bf16 %5, %11, %17, 0\n\ts_nop 2" \
            : "=&v"(e0), "=&v"(e1), "=&v"(e2), "=&v"(f0), "=&v"(f1), "=&v"(f2) \
            : "v"(qp_[0]), "v"(qp_[1]), "v"(qp_[2]), "v"(qn_[0]), "v"(qn_[1]), "v"(qn_[2]), \
              "v"(ca[tp_].x), "v"(ca[tp_].y), "v"(ca[tp_].z), "v"(ca[tp_].w), "v"(cb[tp_].x), "v"(cb[tp_].y)); \
        qe[P][0] = (v2f){e0, f0}; qe[P][1] = (v2f){e1, f1}; qe[P][2] = (v2f){e2, f2}; \
        A[P] = (qe[P][0] + qe[P][1] + qe[P][2]) * (v2f){-kz4[P], -kz4[P]}; }
#define K_WAIT(MODE, P) { \
        if ((MODE) == 0) { WAITT(P, P, 6 - 2 * (P)); } \
        else { WAITT(P, (((MODE) - 1) & 1) * 4 + (P), ((MODE) < 3 ? 20 : (MODE) == 3 ? 17 : 14) - 2 * (P)); } }
#define K_FMA(P) { \
        _Pragma("unroll") \
        for (int i = 0; i < 3; i++) { v2f x = {(float)ta[P][i], (float)tb[P][i]}; A[P] = qe[P][i] * x + A[P]; } \
        kres[P] = A[P].x + A[P].y; }
#define K_RED(MODE) { \
        const bool o1 = lane & 1, o2 = lane & 2; \
        float s01 = (o1 ? kres[1] : kres[0]) + __int_as_float(__builtin_amdgcn_update_dpp(0, __float_as_int(o1 ? kres[0] : kres[1]), 0xb1, 0xf, 0xf, true)); \
        float s23 = (o1 ? kres[3] : kres[2]) + __int_as_float(__builtin_amdgcn_update_dpp(0, __float_as_int(o1 ? kres[2] : kres[3]), 0xb1, 0xf, 0xf, true)); \
        float sx = (o2 ? s23 : s01) + __int_as_float(__builtin_amdgcn_update_dpp(0, __float_as_int(o2 ? s01 : s23), 0x4e, 0xf, 0xf, true)); \
        sx = dpp_add<0x124>(sx); sx = dpp_add<0x128>(sx); \
        const auto sw = __builtin_amdgcn_permlane16_swap(__float_as_uint(sx), __float_as_uint(sx), false, false); \
        sx = __uint_as_float(sw[0]) + __uint_as_float(sw[1]); \
        if ((lane & 28) == 0) { if (MODE) sm.ent[(lane >> 1) & 1][rg * 4 + 2 * (lane & 1) + rsel] = sx * ksc; else sm.ent[u % NOUT][rg * 4 + 2 * (lane & 3) + rsel] = sx * ksc; } }
#define K_PRE4(MODE) { K_PRE(MODE, 0) K_PRE(MODE, 1) K_PRE(MODE, 2) K_PRE(MODE, 3) asm volatile("" :: "v"(A[0]), "v"(A[1]), "v"(A[2]), "v"(A[3])); }
#define K_BODY(MODE) { K_PRE4(MODE) K_WAIT(MODE, 0) K_FMA(0) K_WAIT(MODE, 1) K_FMA(1) K_WAIT(MODE, 2) K_FMA(2) K_WAIT(MODE, 3) K_FMA(3) K_RED(MODE) }
#define V_FMA(J, W0, W1) { \
        _Pragma("unroll") \
        for (int i = 0; i < 3; i++) { \
            acc[i] = fma(W0, (double)ta[J][i], acc[i]); \
            acc[i] = fma(W1, (double)tb[J][i], acc[i]); \
        } }
#define V_PIN() asm volatile("" :: "v"(acc[0]), "v"(acc[1]), "v"(acc[2]));
#define RDYWAIT(SL, SEQ) { while (lds_ld(&sm.rdy[SL]) != (SEQ)) SLEEP(1); CBAR(); }
#define LOADZ() { _Pragma("unroll") for (int p = 0; p < 4; p++) kz4[p] = sm.kz[u % NIN][rg * 4 + 2 * p + rsel].x; ksc = sm.kz[u % NIN][rg * 4 + 2 * (lane & 3) + rsel].y * scale; }
#define LOADQ() { _Pragma("unroll") for (int i = 0; i < 3; i++) { Qp[i] = sm.q[u % NIN][3 * sub + i]; Qn[i] = __builtin_amdgcn_alignbit(Qp[i], Qp[i], 16) ^ 0x80000000u; } }
#define FLUSH(ISV) { \
            mg = __builtin_amdgcn_readfirstlane(mg); \
            while (u >= mg + NOUT) { SLEEP(1); mg = lds_ld(&sm.merged); } \
            CBAR(); \
            if (ISV) { const unsigned aa = lds_addr(&sm.acc[u % NOUT][lane]); \
                asm volatile("ds_add_f64 %0, %1\n\tds_add_f64 %0, %2 offset:512\n\tds_add_f64 %0, %3 offset:1024" :: "v"(aa), "v"(acc[0]), "v"(acc[1]), "v"(acc[2]) : "memory"); \
                acc[0] = 0.0; acc[1] = 0.0; acc[2] = 0.0; } \
            CBAR(); \
            LDS_ADD(&sm.cnt[u % NOUT], np) \
            CBAR(); \
            np = 0; }

#define LOADZ2() { _Pragma("unroll") for (int p = 0; p < 4; p++) kz4[p] = sm.kz[p >> 1][rg * 4 + 2 * (p & 1) + rsel].x; ksc = sm.kz[(lane >> 1) & 1][rg * 4 + 2 * (lane & 1) + rsel].y * scale; }
#define STATIC_STEP(I) { \
            rg = ((cst + (I)) & 3) * 8 + wave; \
            LOADZ2() \
            K_TAB((I) + 1) K_BODY((I) + 1) }
        CS_DMA(wave, (cst + 2) & 3)
        RDYWAIT(0, 1u) RDYWAIT(1, 2u)
        LOADQ()
#pragma unroll
        for (int i = 0; i < 3; i++) { Rp[i] = sm.q[1][3 * sub + i]; Rn[i] = __builtin_amdgcn_alignbit(Rp[i], Rp[i], 16) ^ 0x80000000u; }
        STATIC_STEP(0)
        asm volatile("s_waitcnt vmcnt(11)" ::: "memory");
        REPACK(wave, (cst + 1) & 3)
        TABDONE(1)
        SISSUE(2)
        CS_DMA(wave, (cst + 3) & 3)
        STATIC_STEP(1)
        asm volatile("s_waitcnt vmcnt(11)" ::: "memory");
        REPACK(wave, (cst + 2) & 3)
        TABDONE(2)
        SISSUE(3)
        asm volatile("s_waitcnt vmcnt(8)" ::: "memory");
        REPACK(wave, (cst + 3) & 3)
        TABDONE(3)
        STATIC_STEP(2)
        GRAB(2)
        vut = LDV(&sm.utab[lane]);
        rg = ((cst + 3) & 3) * 8 + wave;
        LOADZ2() K_TAB(4) K_PRE4(4)
        PLAN()
        LAUNCH()
        K_WAIT(4, 0) K_FMA(0) K_WAIT(4, 1) K_FMA(1) K_WAIT(4, 2) K_FMA(2) K_WAIT(4, 3) K_FMA(3) K_RED(4)
        CBAR();
        LDS_ADD(&sm.cnt[0], 4u) LDS_ADD(&sm.cnt[1], 4u)
        u = un; rg = nrg; tdc = tdn + 10;
        bool tabok = false;
        __builtin_amdgcn_s_setprio(2);
#pragma unroll 1
        while (u < NU) {
            GRAB(2)
            mg = LDV(&sm.merged);
            vut = LDV(&sm.utab[lane]);
            LINE()
            if (!tabok) {
                for (;;) { const unsigned tb_ = lds_ld(&sm.tabs); tabok = tb_ == ~0u; if (((tb_ >> rg) & 3u) == 3u) break; SLEEP(1) }
                CBAR();
            }
            K_TAB(0)
            if (first) {
                unsigned fl = LDV(&sm.rdy[u % NIN]);
                CBAR();
                LOADQ() LOADZ()
                if (__builtin_amdgcn_readfirstlane(fl) != u + 1) { RDYWAIT(u % NIN, u + 1) LOADQ() LOADZ() }
            } else LOADZ()
            K_PRE4(0)
            PLAN()
            K_WAIT(0, 0) K_FMA(0) K_WAIT(0, 1) K_FMA(1)
            asm volatile("" :: "v"(kres[0]), "v"(kres[1]));
            K_WAIT(0, 2) K_WAIT(0, 3)
            ARRIVED()
            K_FMA(2) K_FMA(3) K_RED(0)
            np += 2;
            ADVANCE(false)
        }
#pragma unroll 1
        while (u < 2 * NU) {
            GRAB(2)
            mg = LDV(&sm.merged);
            LINE()
            const unsigned vr_ = LDV(&sm.vrdy);
            CBAR();
            const unsigned short* wp = &sm.wv[went(u - NU) + (QUARTER(u - NU) ? rg & 7 : rg) * 4];
            v4i pw = *(const v4i*)wp;
            if (first && !((__builtin_amdgcn_readfirstlane(vr_) >> (u - NU)) & 1)) { while (!((lds_ld(&sm.vrdy) >> (u - NU)) & 1)) SLEEP(1); CBAR(); pw = *(const v4i*)wp; }
            const double w0 = (short)pw.x, w1 = pw.x >> 16, w2 = (short)pw.y, w3 = pw.y >> 16, w4 = (short)pw.z, w5 = pw.z >> 16, w6 = (short)pw.w, w7 = pw.w >> 16;
            asm volatile("" :: "v"(w0), "v"(w1), "v"(w2), "v"(w3), "v"(w4), "v"(w5), "v"(w6), "v"(w7));
            PLAN()
            v2f hl; unsigned tl8, rowl;
            if (none) { hl = *(const v2f*)&sm.wv[went(NU - 1) + 128]; tl8 = LDV(&sm.tag_lo) << 24; rowl = (lds_ld(&sm.utab[2 * NU - 1]) & 255) * (NSPLIT * 512); asm volatile("" : "+v"(hl), "+v"(tl8)); }
            WAITT(0, 0, 6); V_FMA(0, w0, w1) V_PIN()
            WAITT(1, 1, 4); V_FMA(1, w2, w3) V_PIN()
            WAITT(2, 2, 2); V_FMA(2, w4, w5) V_PIN()
            WAITT(3, 3, 0);
            np += 2;
            if (none) {
                V_FMA(3, w6, w7)
                if (u != 2 * NU - 1) { FLUSH(true) break; }
                mg = __builtin_amdgcn_readfirstlane(mg);
                while (u >= mg + NOUT) { SLEEP(1); mg = lds_ld(&sm.merged); }
                const unsigned aa = lds_addr(&sm.acc[(2 * NU - 1) % NOUT][lane]);
                double a0, a1, a2; unsigned cn; u64 ex_;
                asm volatile("ds_add_f64 %5, %6\n\tds_add_f64 %5, %7 offset:512\n\tds_add_f64 %5, %8 offset:1024\n\t"
                             "s_mov_b64 %4, exec\n\ts_mov_b64 exec, 1\n\tds_add_rtn_u32 %3, %9, %10\n\ts_mov_b64 exec, %4\n\t"
                             "ds_read_b64 %0, %5\n\tds_read_b64 %1, %5 offset:512\n\tds_read_b64 %2, %5 offset:1024\n\ts_waitcnt lgkmcnt(0)"
                             : "=&v"(a0), "=&v"(a1), "=&v"(a2), "=&v"(cn), "=&s"(ex_) : "v"(aa), "v"(acc[0]), "v"(acc[1]), "v"(acc[2]), "v"(lds_addr(&sm.cnt[(2 * NU - 1) % NOUT])), "v"(np) : "memory");
                const float n0 = (float)(a0 - (double)hl.y) * hl.x, n1 = (float)(a1 - (double)hl.y) * hl.x, n2 = (float)(a2 - (double)hl.y) * hl.x;
                const unsigned e2 = max(__float_as_uint(fmaxf(fmaxf(fabsf(n0), fabsf(n1)), fabsf(n2))) >> 23, 14u);
                const float f2 = __uint_as_float((268u - e2) << 23);
                const v2u r = {__builtin_bit_cast(unsigned, __builtin_amdgcn_cvt_pk_i16((int)rintf(n0 * f2), (int)rintf(n1 * f2))),
                               __builtin_bit_cast(unsigned, __builtin_amdgcn_cvt_pk_i16((int)rintf(n2 * f2), 0)) | e2 << 16 | tl8};
                const __amdgpu_buffer_rsrc_t rsrc = __builtin_amdgcn_make_buffer_rsrc(ws + WS_PART + split * 512, 0, NRW * NSPLIT * 512 - split * 512, 0x00020000);
                asm volatile("" :: "v"(r));
                if (__builtin_amdgcn_readfirstlane(cn + np) == 32) {
                    __builtin_amdgcn_raw_buffer_store_b64(r, rsrc, lane * 8, rowl, SC1);
#pragma unroll
                    for (int j = 0; j < NCP - 1; j++) __builtin_amdgcn_raw_buffer_store_b64(r, rsrc, lane * 8, (rowl & 15 * NSPLIT * 512 ^ NSPLIT * 512) + (NRR + j * GSZ) * NSPLIT * 512, SC1);
                }
                break;
            }
            ARRIVED()
            V_FMA(3, w6, w7)
            ADVANCE(true)
        }
    } else {
    u64 E1;
    if (wave == NSW) {
        unsigned ref;
        const unsigned sidx = (blockIdx.x & 7) | ((blockIdx.x >> 7) << 3);
#define SAMPLE(K) ((((blockIdx.x >> 3) - (blockIdx.x & 7) - (K) * 5) & 15) == 0)
#define BTAG(K) ((btag + (K)) & 0xfffffu)
#define POST(K, C) { if (lane == 0) __hip_atomic_store((u64*)(ws + (bd + (K) * RST + (sidx >> 3) * 256 + (sidx & 7) * 8)), (u64)(C) | ((u64)(((C) - ref) | BTAG(K) << 12) << 32), __ATOMIC_RELAXED, __HIP_MEMORY_SCOPE_AGENT); }
        const unsigned own = lds_ld(&sm.line);
        if (SAMPLE(0) && lane < NTRY) __hip_atomic_store((unsigned*)(ws + WS_HELLO + lane * 768) + sidx, own, __ATOMIC_RELAXED, __HIP_MEMORY_SCOPE_AGENT);
        const u64 E = __hip_atomic_load((const u64*)(ws + WS_EPOCH), __ATOMIC_RELAXED, __HIP_MEMORY_SCOPE_AGENT);
        E1 = E + 1;
        if (lane == 0) { sm.tag_lo = (unsigned)E1; sm.tag_hi = (unsigned)(E1 >> 32); CBAR(); lds_st(&sm.tag_rdy, 1u); }
        const unsigned btag = __builtin_amdgcn_readfirstlane((unsigned)E1) * 61u, bd = WS_BOARD + (btag & 7) * (NRND * RST);
        ref = own;
        int off = 0;
        {
            unsigned tries = 0, cur = own, tl = own;
            bool news = false;
#pragma unroll 1
            while (!news || cur != ref) {
                const unsigned now = NOW();
                if (tries < NTRY && (int)(now - own) >= (int)(TA + tries * TA2)) {
                    v16u x;
                    asm volatile("s_load_dwordx16 %0, %1, %2\n\ts_waitcnt lgkmcnt(0)" : "=&s"(x) : "s"(ws), "s"(__builtin_amdgcn_readfirstlane(WS_HELLO + tries * 768)) : "memory");
                    unsigned bad = 0; int sum = 0, dif = 0;
#pragma unroll
                    for (int i = 0; i < 16; i++) { bad |= x[i] - own + 512; sum += x[i] - x[0]; dif += i & 4 ? x[i] : -x[i]; }
                    tries++;
                    if (bad < 1024) { const unsigned com = x[0] + (sum >> 4) + abs(dif) * (8 - KC) / 128; ref = x[0] + ((sum + (blockIdx.x & 4 ? dif : -dif)) >> 4); off = ref - com;
                        tries = NTRY; tl = NOW(); if (lane == 0) lds_st(&sm.aline, com); }
                    continue;
                }
                if (!news && lds_ld(&sm.utab[31]) != ~0u) { news = true; CBAR(); if (cur == ref) tl = now; ref = lds_ld(&sm.aline) + off; tries = NTRY; }
                const int d = (now - tl) >> CSH;
                if (cur != ref && d) { tl += d << CSH; cur += min(max((int)(ref - cur), -d), d); if (lane == 0) lds_st(&sm.line, cur); }
                SLEEP(2)
            }
        }
        ref -= off;
        const unsigned ref0 = ref;
        const unsigned lead = (blockIdx.x >> 3) == 9 ? 0 : LEAD;
        unsigned posted = ref, lagf = ref, tpost = ref + TB1, tpoll = tpost + RDD + lead, kw = 0, kr = 0, cur = ref + off, khold = 0;
        int o = off; bool held = false;
#pragma unroll 1
        for (;;) {
            const unsigned p = lds_ld(&sm.grab);
            if (p >= NPC) break;
            lagf += (int)(lds_ld(&sm.late) - lagf) >> 1;
            { const unsigned l = lds_ld(&sm.line); if (l != cur) { cur = l; khold = kw; held = true; } }
            const unsigned now = NOW();
            if (o) { o = off * min(max((int)(ref0 + TC0 + (1 << TCS) - now), 0), 1 << TCS) >> TCS; if (!held && cur != ref + o) { cur = ref + o; if (lane == 0) lds_st(&sm.line, cur); } }
            if (kw < NRND && (int)(now - tpost) >= 0) {
                const unsigned c = ref + min((p >= PSL0 && (int)(lagf - cur) > LHI ? lagf - LOK : cur) - o - ref, 4000u);
                if (SAMPLE(kw) && (c != ref || (ref != posted && (sidx & 7) == (kw & 7)))) POST(kw, c)
                posted = ref;
                tpost += PER; kw++;
            }
            if (kr < NRND && (int)(now - tpoll) >= 0) {
                v16u xa, xb;
                asm volatile("s_load_dwordx16 %0, %2, %3\n\ts_load_dwordx16 %1, %2, %4\n\ts_waitcnt lgkmcnt(0)" : "=&s"(xa), "=&s"(xb)
                             : "s"(ws), "s"(__builtin_amdgcn_readfirstlane(bd + kr * RST)), "s"(__builtin_amdgcn_readfirstlane(bd + kr * RST + 256)) : "memory");
                int nb = 0, sg = 0;
#pragma unroll
                for (int i = 0; i < 16; i++) {
                    const unsigned lo = i < 8 ? xa[2 * i & 15] : xb[2 * i & 15], hi = i < 8 ? xa[(2 * i + 1) & 15] : xb[(2 * i + 1) & 15];
                    if (hi >> 12 == BTAG(kr) && lo - ref0 < 8000) { nb = max(nb, (int)(lo - (hi & 4095u) - ref)); sg += hi & 4095u; }
                }
                ref += nb + (sg >> 4);
                if (cur != ref + o && (kr >= khold || (int)(ref + o - cur) > 0)) { cur = ref + o; held = false; if (lane == 0) lds_st(&sm.line, cur); }
                tpoll += PER; kr++;
            }
            SLEEP(2)
        }
    } else if (wave == NSW + 1 || wave == NSW + 2) {
        const int ql = lane < 48 ? lane : 47;
        unsigned c1, c2, s1, s2;
#define QT_LOADS() { int qo_ = ql; asm volatile("" : "+v"(qo_)); \
                     c1 = ((const unsigned*)q_cos)[qo_]; c2 = ((const unsigned*)q_cos)[48 + qo_]; s1 = ((const unsigned*)q_sin)[qo_]; s2 = ((const unsigned*)q_sin)[48 + qo_]; }
        unsigned q1, q2, lkz, lks, lvz, lvs;
        const unsigned sofs = R8(((unsigned)lane >> 1) & 7) * 2 + (lane & 1);
#define UNIT_LOADS(HEAD, GRP) { \
        q1 = ((const unsigned*)(q + (size_t)(HEAD) * DH))[ql]; q2 = ((const unsigned*)(q + (size_t)(HEAD) * DH))[48 + ql]; \
        const size_t so = (size_t)(HEAD) * (SKV / 2) + (GRP) * 256 + sofs; \
        lkz = *((const unsigned*)k_zeros + so); lks = *((const unsigned*)k_scales + so); \
        lvz = *((const unsigned*)v_zeros + so); lvs = *((const unsigned*)v_scales + so); }
#define UNIT_FILL(SL, U) { \
            v4f qq; \
            qq.x = bfr(bfr(bflo(q1) * bflo(c1)) + bfr(-bflo(q2) * bflo(s1))); \
            qq.y = bfr(bfr(bflo(q2) * bflo(c2)) + bfr(bflo(q1) * bflo(s2))); \
            qq.z = bfr(bfr(bfhi(q1) * bfhi(c1)) + bfr(-bfhi(q2) * bfhi(s1))); \
            qq.w = bfr(bfr(bfhi(q2) * bfhi(c2)) + bfr(bfhi(q1) * bfhi(s2))); \
            *(v2u*)&sm.q[SL][2 * ql] = (v2u){(__float_as_uint(qq.x) >> 16) | __float_as_uint(qq.y), (__float_as_uint(qq.z) >> 16) | __float_as_uint(qq.w)}; \
            *(v4f*)&sm.kz[SL][2 * lane] = (v4f){bflo(lkz), bflo(lks), bfhi(lkz), bfhi(lks)}; \
            *(v4f*)&sm.vz[SL][2 * lane] = (v4f){bflo(lvz), bflo(lvs), bfhi(lvz), bfhi(lvs)}; \
            CBAR(); \
            if (lane == 0) lds_st(&sm.rdy[SL], (U) + 1); \
            CBAR(); }
        const unsigned dw = NSW + 2 - wave;
        QT_LOADS()
        UNIT_LOADS(head0 + dw * (GSZ + 1 - 2 * side), lane >> 4)
#pragma unroll 1
        for (unsigned u = dw;;) {
            while (u >= lds_ld(&sm.merged) + NIN) SLEEP(4);
            CBAR();
            UNIT_FILL(u % NIN, u)
            if (u - 1 < 12) {
                const unsigned x_ = (blockIdx.x >> 3) + 32 * u - 8; unsigned dm;
                const char* p_ = x_ < 384 ? (const char*)(x_ < 192 ? qk : qv) + (size_t)(x_ < 192 ? x_ : x_ - 192) * (2u << 20) + (x_ * 8 + (blockIdx.x & 7)) * 640 : (const char*)out + (x_ - 384) * 4096;
                while ((int)(NOW() - lds_ld(&sm.line)) < TSD) SLEEP(2)
                asm volatile("s_load_dword %0, %1, 0x0\n\ts_waitcnt lgkmcnt(0)" : "=&s"(dm) : "s"(p_) : "memory");
            }
            u += 2;
            if (u >= NU) break;
            if (u == 4) {
                const unsigned tpf = tdue(PSF - 1) + TFO;
                { int d; while ((d = (int)(lds_ld(&sm.line) + tpf - NOW())) > 0 && d < 3000) SLEEP(1) }
#define PF3(P) asm volatile("global_load_dwordx4 a[0:3], %0, %1\n\tglobal_load_dwordx4 a[0:3], %0, %1 offset:1024\n\tglobal_load_dwordx4 a[0:3], %0, %1 offset:2048" \
                            :: "v"(64 * SKV * 2 + blockIdx.x * 3072 + lane * 16), "s"(P) : "a0", "a1", "a2", "a3")
                PF3(k_zeros); PF3(k_scales); PF3(v_zeros); PF3(v_scales);
                asm volatile("global_load_dword a0, %0, %1" :: "v"(blockIdx.x * (DH * 2) + ql * 8), "s"(q) : "a0");
            }
            const unsigned tin = tdue(first_of(u) - (QUARTER(u) ? 24 : 0) - LEADP + (split & 7) * DPH);
            { int d; while ((d = (int)(lds_ld(&sm.line) + tin - NOW())) > 0 && d < 3000) SLEEP(4) }
            unsigned hd;
            while ((hd = lds_ld(&sm.utab[u])) == ~0u) SLEEP(2)
            UNIT_LOADS(hd & 255, QUARTER(u) ? rank & 3 : lane >> 4)
        }
        while (lds_ld(&sm.tag_rdy) == 0) SLEEP(1);
        CBAR();
        E1 = (u64)lds_ld(&sm.tag_lo) | ((u64)lds_ld(&sm.tag_hi) << 32);
        if (wave == NSW + 1 && blockIdx.x == 0) {
            for (;;) {
                bool ok = true;
#pragma unroll
                for (int i = 0; i < NH / 64; i++) ok &= __hip_atomic_load((const u64*)(ws + WS_POST) + i * 64 + lane, __ATOMIC_RELAXED, __HIP_MEMORY_SCOPE_AGENT) >> 24 == (E1 << 24) >> 24;
                if (__builtin_amdgcn_ballot_w64(ok) == ~0ull) break;
                SLEEP(64);
            }
            if (lane == 0) __hip_atomic_store((u64*)(ws + WS_EPOCH), E1, __ATOMIC_RELAXED, __HIP_MEMORY_SCOPE_AGENT);
        }
        if (wave == NSW + 2) {
            const unsigned t16 = (unsigned)E1 & 0xffffu, t16b = ((unsigned)E1 + ((unsigned)E1 >> 16)) & 0xffffu;
            const __amdgpu_buffer_rsrc_t rsrc = __builtin_amdgcn_make_buffer_rsrc(ws + WS_SWAP + (blockIdx.x ^ 4) * 5120, 0, 5120, 0x00020000);
            const unsigned swp = lds_ld(&sm.swp);
            if (swp) {
                unsigned use = 0;
#pragma unroll
                for (int i = 0; i < 5; i++) use |= ((swp >> wunit(i * 64 + lane)) & 1) << i;
                const unsigned tg = tdue(PSG - 1) + tvx(PSG - 1, lds_ld(&sm.gap));
                while ((int)(NOW() - lds_ld(&sm.line) - tg) < 0) SLEEP(2)
                for (;;) {
                    v4u d[5]; bool ok = true;
#pragma unroll
                    for (int i = 0; i < 5; i++) d[i] = __builtin_amdgcn_raw_buffer_load_b128(rsrc, (use >> i) & 1 ? (i * 64 + lane) * 16 : 0x40000000, 0, SC1 | 2);
#pragma unroll
                    for (int i = 0; i < 5; i++) if ((use >> i) & 1) {
                        ok &= d[i].y >> 16 == t16 && d[i].w >> 16 == t16b;
                        unsigned* w = (unsigned*)&sm.wv[144 * NMIX + (i * 64 + lane) * 6];
                        w[0] = d[i].x; w[1] = (d[i].y & 0xffffu) | (d[i].z << 16); w[2] = (d[i].z >> 16) | (d[i].w << 16);
                    }
                    if (__builtin_amdgcn_ballot_w64(ok) == ~0ull) break;
                    SLEEP(32)
                }
                CBAR();
                LDS_OR(&sm.vrdy, swp)
            }
        }
    } else {
        const unsigned eu = lane >= NU ? lane - NU : lane, win = min(first_of(eu) >> 5, 15u), pair = QUARTER(eu) ? 2 * (eu - WQ) + (rank >> 2) : rank;
        unsigned rot2 = ((const unsigned char*)ROT)[win * 8 + pair];
        const unsigned rotn = ((const unsigned char*)ROT)[(win == 0 ? 4 : win == 1 ? 9 : WQ) * 8 + pair];
#define UENT(BIT) (win * 16 + 2 * pair + ((BIT) ^ side) | (QUARTER(eu) ? (rank - (eu - WQ)) * 8 & 31 : (rot2 >> ((BIT) ^ side) * 4 & 15) * 2) << 8)
        unsigned far = 0, vs;
        const unsigned hp = (blockIdx.x & 1) * NH + (blockIdx.x >> 1) * 4 + 2;
        if (blockIdx.x < 144) {
            const char* pb = (blockIdx.x < 128 ? (const char*)(hp >= NH ? qv : qk) + (size_t)(hp & (NH - 1)) * (SKV * DH * 4) + SKV * DH * 2
                                               : (const char*)ws + WS_PART + (8u << 20) + (blockIdx.x & 15) * 65536) + ((NOW() * 0x9e3779b1u) >> 26) * 128;
            unsigned mn[2] = {~0u, ~0u};
#pragma unroll 1
            for (unsigned i = 1; i < 2 * NPRB + 2; i++) {
                u64 t0, t1; unsigned dm;
                asm volatile("s_memtime %0\n\ts_waitcnt lgkmcnt(0)\n\ts_load_dword %2, %3, 0x0\n\ts_waitcnt lgkmcnt(0)\n\ts_memtime %1\n\ts_waitcnt lgkmcnt(0)"
                             : "=&s"(t0), "=&s"(t1), "=&s"(dm) : "s"(pb + i * 8192) : "memory");
                if (i == 1) asm volatile("global_load_dword a0, %0, %1 nt" :: "v"(0), "s"(pb) : "a0");
                const unsigned t = (unsigned)(t1 - t0) < 330 || i == 1 ? ~0u : (unsigned)(t1 - t0);
                if (i & 1) mn[1] = min(mn[1], t); else mn[0] = min(mn[0], t);
            }
            far = mn[0] > mn[1];
        }
        while (lds_ld(&sm.tag_rdy) == 0) SLEEP(1);
        CBAR();
        E1 = (u64)lds_ld(&sm.tag_lo) | ((u64)lds_ld(&sm.tag_hi) << 32);
        {
            const unsigned own = lds_ld(&sm.line), tg = (unsigned)E1;
            if (lane == 0) __hip_atomic_store((u64*)(ws + WS_POST) + blockIdx.x, E1 << 24 | min(lds_ld(&sm.slow), 4000u) << 1 | ((far ^ (blockIdx.x >> 2) ^ (blockIdx.x < 128 ? 0x96 >> (hp & 7) : 0)) & 1), __ATOMIC_RELAXED, __HIP_MEMORY_SCOPE_AGENT);
            const __amdgpu_buffer_rsrc_t nrs = __builtin_amdgcn_make_buffer_rsrc(ws + WS_NEWS, 0, 8 * 1280, 0x00020000);
            if (blockIdx.x == MASTER) {
                while ((int)(NOW() - own) < TMAP) SLEEP(2)
                const unsigned a = tg << 24, b = (unsigned)(E1 >> 8);
                v4u x; v2u z; u64 v0, v1;
#pragma unroll 1
                for (int i = 0; i < 8; i++) {
                    x = __builtin_amdgcn_raw_buffer_load_b128(__builtin_amdgcn_make_buffer_rsrc(ws + WS_POST, 0, 1024, 0x00020000), lane * 16, 0, SC1);
                    z = __builtin_amdgcn_raw_buffer_load_b64(__builtin_amdgcn_make_buffer_rsrc(ws + WS_POST + 1024, 0, 128, 0x00020000), lane * 8, 0, SC1);
                    v0 = __builtin_amdgcn_ballot_w64(((x.x ^ a) >> 24 | (x.y ^ b)) == 0); v1 = __builtin_amdgcn_ballot_w64(((x.z ^ a) >> 24 | (x.w ^ b)) == 0);
                    if (__builtin_popcountll(v0) + __builtin_popcountll(v1) >= MINV) break;
                    SLEEP(8)
                }
                const u64 m0 = __builtin_amdgcn_ballot_w64(x.x & 1) & v0, m1 = __builtin_amdgcn_ballot_w64(x.z & 1) & v1;
                const int s0 = lane < 43 ? min(max(lane - VW, 4 * NMIX), 42 - 2 * VW) : min(max(lane - VW, 43), 63 - 2 * VW);
#define PC(M) __builtin_popcount((unsigned)((M) >> s0) & ((2u << 2 * VW) - 1))
                const u64 g0 = __builtin_amdgcn_ballot_w64(2 * PC(m0) > PC(v0)), g1 = __builtin_amdgcn_ballot_w64(2 * PC(m1) > PC(v1));
                const float sl = wave_sum(((v0 >> lane & 1) ? (float)(x.x >> 1 & 4095u) : 0.f) + ((v1 >> lane & 1) ? (float)(x.z >> 1 & 4095u) : 0.f)) / ((8.f - PRE0) * (__builtin_popcountll(v0) + __builtin_popcountll(v1)) + 1.f);
                const unsigned al = lds_ld(&sm.aline), mean = (al ? al : own) + (int)fmaxf(sl - SL0, 0.f);
                const bool vz = lane < 16 && ((z.x ^ a) >> 24 | (z.y ^ b)) == 0;
                const unsigned gw = 2 * __builtin_popcountll(__builtin_amdgcn_ballot_w64(vz && (z.x & 1))) > __builtin_popcountll(__builtin_amdgcn_ballot_w64(vz));
                const unsigned k = lane & 3;
                if (lane < 32 && k < 3) __builtin_amdgcn_raw_buffer_store_b128((v4u){k == 0 ? (unsigned)g0 : k == 1 ? (unsigned)g1 : mean, tg, k == 0 ? (unsigned)(g0 >> 32) : k == 1 ? (unsigned)(g1 >> 32) : gw, tg},
                                                                              nrs, (lane >> 2) * 1280 + k * 16, 0, SC1);
            }
            while ((int)(NOW() - own) < TMAP + TRES) SLEEP(2)
            v4u y;
            for (;;) {
                y = __builtin_amdgcn_raw_buffer_load_b128(nrs, lane < 3 ? (blockIdx.x & 7) * 1280 + lane * 16 : 0x40000000, 0, SC1);
                if (((unsigned)__builtin_amdgcn_ballot_w64(y.y == tg && y.w == tg) & 7u) == 7u) break;
                SLEEP(8)
            }
            const u64 f0 = (unsigned)__builtin_amdgcn_readlane(y.x, 0) | (u64)(unsigned)__builtin_amdgcn_readlane(y.z, 0) << 32, f1 = (unsigned)__builtin_amdgcn_readlane(y.x, 1) | (u64)(unsigned)__builtin_amdgcn_readlane(y.z, 1) << 32;
            if (lane == 2) { sm.aline = y.x - own + 500 < 1000 ? y.x : own; sm.chead = (blockIdx.x & ~5u) | ((blockIdx.x >> 2 ^ y.z) & 1) | (blockIdx.x & 1) << 2; }
            unsigned bit = ((unsigned)((lane >= NU ? f1 : f0) >> (4 * win + (pair >> 1))) ^ (0x66 >> pair)) & 1;
            vs = !((f0 ^ f1) >> 4 * NMIX) ? 0 : !((f0 ^ f1) >> 4 * NMIX & 4095) ? NMIX : !((f0 ^ f1) >> 4 * (WQ + 1) & 4095) ? WQ + 4 : 0;
            if (eu < NMIX) { if (lane < NU || !vs) bit = eu & 1; else rot2 = rotn; }
            const u64 bm = __builtin_amdgcn_ballot_w64(bit);
            const unsigned nsw = __builtin_popcountll((f0 ^ f1) >> (4 * NMIX)) + (vs ? 2 * NMIX : 0);
            const unsigned sw = (unsigned)(bm ^ (bm >> NU)) & ((1u << NU) - 1), sx = (sw >> vs ^ sw) & 7;
            if (lane == 0) { sm.swp = sw ^ sx ^ sx << vs; lds_st(&sm.gap, (nsw ? (TG0 + TG1 * nsw / 8) * 0x1001u + ((TH0 + TH1 * nsw / 8) << 12) : 0u) | (vs ? 0u : TMIX << 24)); }
            CBAR();
#define VPOS(E) ((E) < NMIX ? (E) + vs : (E) - vs < NMIX ? (E) - vs : (E))
            lds_st(&sm.utab[lane >= NU ? NU + VPOS(eu) : lane], UENT(bit));
        }
        const unsigned swp = lds_ld(&sm.swp);
        const unsigned T = lds_ld(&sm.tag_lo);
        const unsigned t15 = T & 0x7fffu;
        const __amdgpu_buffer_rsrc_t rsrc = __builtin_amdgcn_make_buffer_rsrc(ws + WS_PART + split * 512, 0, NRW * NSPLIT * 512 - split * 512, 0x00020000);
        v2u* recs = (v2u*)&sm.cs[0][0][0];
#pragma unroll
        for (int i = 0; i < NOUT * 3; i++) (&sm.acc[0][0])[i * 64 + lane] = 0.0;
        CBAR();
        unsigned vut = sm.utab[NU + (lane & 31)] & 255;
        if (QUARTER((unsigned)lane) && (rank & 3)) vut = NH - 16 + (rank & 3) * 16 + ((vut & 15) ^ 1);
        __builtin_amdgcn_s_setprio(3);
        unsigned po[NU / 2];
#pragma unroll
        for (int k = 0; k < NU / 2; k++) po[k] = __builtin_amdgcn_ds_bpermute((2 * k + (lane >> 5)) * 4, vut) * (NSPLIT * 512) + (lane & 31) * 16;
        const u64 rb_ = (u64)(ws + WS_PART + split * 512);
        const v4u rs_ = {(unsigned)rb_, (unsigned)(rb_ >> 32) & 0xffffu, NRW * NSPLIT * 512 - split * 512, 0x00020000u};
#pragma unroll 1
        for (unsigned u = 0; u < 2 * NU - 1; u++) {
            const unsigned e = u % NU, so = u % NOUT;
            if (u >= NU) while (!((lds_ld(&sm.vrdy) >> e) & 1)) SLEEP(4)
            CBAR();
            const v4f h = *(const v4f*)&sm.wv[went(e) + (QUARTER(e) ? 32 : 128)];
            while (lds_ld(&sm.cnt[so]) != (QUARTER(e) ? 8 : 32)) SLEEP(1)
            CBAR();
            const double a0 = sm.acc[so][lane], a1 = sm.acc[so][64 + lane], a2 = sm.acc[so][128 + lane];
            if (u < NU) {
                float2 lg = *(const float2*)&sm.ent[so][2 * lane];
                if (QUARTER(e) && lane >> 4 != (rank & 3)) lg = make_float2(-INFINITY, -INFINITY);
                const v4f zs = *(const v4f*)&sm.vz[u % NIN][2 * lane];
                const float m = wave_max(fmaxf(lg.x, lg.y));
                const float p0 = __expf(lg.x - m), p1 = __expf(lg.y - m);
                const float x0 = p0 * zs.y, x1 = p1 * zs.w;
                const unsigned eb = max(__float_as_uint(wave_max(fmaxf(fabsf(x0), fabsf(x1)))) >> 23, 14u);
                const float f = __uint_as_float((268u - eb) << 23);
                const float w0 = fminf(fmaxf(rintf(x0 * f), -32767.f), 32767.f), w1 = fminf(fmaxf(rintf(x1 * f), -32767.f), 32767.f);
                const float den = wave_sum(p0 + p1);
                const float bz = wave_sum(fmaf(w0, zs.x, w1 * zs.z));
                const float L = fmaf(m, 1.4426950408889634f, __builtin_amdgcn_logf(den));
                const unsigned vp = VPOS(u);
                unsigned* wd = (unsigned*)(((swp >> vp) & 1 ? sm.wo : sm.wv) + went(vp));
                if (!QUARTER(u) || lane >> 4 == (rank & 3)) wd[QUARTER(u) ? lane & 15 : lane] = ((unsigned)(int)w0 & 0xffffu) | ((unsigned)(int)w1 << 16);
                if (lane == 0) *(v4f*)&wd[QUARTER(u) ? 16 : 64] = (v4f){ldexpf(1.f / den, (int)eb - 141), bz, L, 0.f};
                CBAR();
                if (!((swp >> vp) & 1)) LDS_OR(&sm.vrdy, 1u << vp)
            } else {
                const float n0 = (float)(a0 - (double)h.y) * h.x, n1 = (float)(a1 - (double)h.y) * h.x, n2 = (float)(a2 - (double)h.y) * h.x;
                const unsigned e2 = max(__float_as_uint(wave_maxv(fmaxf(fmaxf(fabsf(n0), fabsf(n1)), fabsf(n2)))) >> 23, 14u);
                const float f2 = __uint_as_float((268u - e2) << 23);
                const unsigned m01 = __builtin_bit_cast(unsigned, __builtin_amdgcn_cvt_pk_i16((int)rintf(n0 * f2), (int)rintf(n1 * f2)));
                const unsigned m2 = __builtin_bit_cast(unsigned, __builtin_amdgcn_cvt_pk_i16((int)rintf(n2 * f2), 0));
                const unsigned bit = ((lane & 1 ? e2 : __float_as_uint(h.z)) >> (lane >> 1)) & 1u;
                recs[e * 64 + lane] = (v2u){m01, m2 | bit << 16 | t15 << 17};
                CBAR();
                sm.acc[so][lane] = 0.0; sm.acc[so][64 + lane] = 0.0; sm.acc[so][128 + lane] = 0.0;
            }
            CBAR();
            if (lane == 0) { lds_st(&sm.cnt[so], 0u); CBAR(); lds_st(&sm.merged, u + 1); }
            if (u == NU - 1 && swp) {
                wave_sync();
                const __amdgpu_buffer_rsrc_t srs = __builtin_amdgcn_make_buffer_rsrc(ws + WS_SWAP + blockIdx.x * 5120, 0, 5120, 0x00020000);
                v4u d[5]; unsigned o[5];
#pragma unroll
                for (int i = 0; i < 5; i++) {
                    const unsigned* w = (const unsigned*)&sm.wo[144 * NMIX + (i * 64 + lane) * 6];
                    const unsigned d0 = w[0], d1 = w[1], d2 = w[2];
                    d[i] = (v4u){d0, (d1 & 0xffffu) | (T << 16), (d1 >> 16) | (d2 << 16), (d2 >> 16) | ((T + (T >> 16)) << 16)};
                    o[i] = (swp >> wunit(i * 64 + lane)) & 1 ? (i * 64 + lane) * 16 : 0x40000000;
                }
                while ((int)(NOW() - lds_ld(&sm.line)) < (int)(tdue(PSW - 1) + tvx(PSW - 1, lds_ld(&sm.gap))) + 10 + TWO) SLEEP(1)
#pragma unroll
                for (int i = 0; i < 5; i++) __builtin_amdgcn_raw_buffer_store_b128(d[i], srs, o[i], 0, SC1 | 2);
            }
        }
        wave_sync();
#pragma unroll
        for (int k = 0; k < NU / 2; k++) asm volatile("ds_read_b128 a[%c0:%c0+3], %1 offset:%2" :: "i"(16 + 4 * k), "v"(lds_addr(recs) + lane * 16), "i"(k * 1024) : "memory");
        asm volatile("s_waitcnt lgkmcnt(0)" ::: "memory");
        while (!((lds_ld(&sm.vrdy) >> (NU - 1)) & 1)) SLEEP(4)
        CBAR();
        const v2u hw_ = {*(const unsigned*)&sm.wv[went(NU - 1) + 132], T};
        const unsigned hrow = (NRW - GSZ + (__builtin_amdgcn_readlane(vut, NU - 1) & 15 ^ 1)) * (NSPLIT * 512);
        while (lds_ld(&sm.grab) < NPC - PUBP) SLEEP(1)
        if (lane < 16) __builtin_amdgcn_raw_buffer_store_b64(hw_, rsrc, lane * 8, hrow, SC1);
#define PUB(K) asm volatile("buffer_store_dwordx4 a[%c0:%c0+3], %1, %2, 0 offen sc1" :: "i"(16 + 4 * (K)), "v"(po[K]), "s"(rs_) : "memory");
        PUB(WQ / 2) PUB(WQ / 2 + 1)
        { const unsigned t_ = TLAST() + PDEL; GATE(t_) }
        PUB(0) PUB(1) PUB(2) PUB(3) PUB(4) PUB(7) PUB(8)
        return;
    }
    }
    __builtin_amdgcn_s_setprio(0);
    unsigned c;
#define ADDRTN(P) { u64 ex_; asm volatile("s_mov_b64 %0, exec\n\ts_mov_b64 exec, 1\n\tds_add_rtn_u32 a15, %1, a13\n\ts_mov_b64 exec, %0" : "=&s"(ex_) : "v"(lds_addr(P)) : "a15", "memory"); }
#define ADDED(V) { unsigned t_; asm volatile("s_waitcnt lgkmcnt(0)\n\tv_accvgpr_read_b32 %1, a15\n\ts_nop 0\n\tv_readfirstlane_b32 %0, %1\n\ts_nop 1" : "=s"(V), "=v"(t_)); }
    asm volatile("v_accvgpr_write_b32 a13, 1\n\ts_nop 1" ::: "a13");
    ADDRTN(&sm.cidx) ADDED(c)
    if (c < NSW) {
        while (lds_ld(&sm.grab) < NPC + 2) SLEEP(4)
        const unsigned head = lds_ld(&sm.chead), T = lds_ld(&sm.tag_lo), tl = TLAST();
        const u64 rb_ = (u64)(ws + WS_PART);
        const v4u rs_ = {(unsigned)rb_, (unsigned)(rb_ >> 32) & 0xffffu, NRW * NSPLIT * 512, 0x00020000u};
        const unsigned ro = head * (NSPLIT * 512) + c * 1024, rx = (NH + (head & 15 ^ 1)) * (NSPLIT * 512) + c * 1024;
#define POLL(S, O) asm volatile("buffer_load_dwordx4 a[%c0:%c0+3], %1, %2, %3 offen sc1" :: "i"(16 + 4 * (S)), "v"(lane * 16), "s"(rs_), "s"(O) : "memory");
#define FETCH(X, S, N) asm volatile("s_waitcnt vmcnt(%c5)\n\tv_accvgpr_read_b32 %0, a[%c4]\n\tv_accvgpr_read_b32 %1, a[%c4+1]\n\tv_accvgpr_read_b32 %2, a[%c4+2]\n\tv_accvgpr_read_b32 %3, a[%c4+3]" \
                                    : "=v"((X).x), "=v"((X).y), "=v"((X).z), "=v"((X).w) : "i"(16 + 4 * (S)), "i"(N));
        float o0, o1, o2;
        if (head >= NH - GSZ) {
            __builtin_amdgcn_s_setprio(3);
            if (c == 0) {
                v2u hd;
                GATE(tl + THD)
                for (;;) {
                    hd = __builtin_amdgcn_raw_buffer_load_b64(__builtin_amdgcn_make_buffer_rsrc(ws + WS_PART + (NRW - GSZ + (head & 15 ^ 1)) * (NSPLIT * 512), 0, NSPLIT * 512, 0x00020000), (lane & 15) * 512, 0, SC1);
                    if (__builtin_amdgcn_ballot_w64(hd.y == T) == ~0ull) break;
                    SLEEP(4)
                }
                const float w = __builtin_amdgcn_exp2f(__uint_as_float(hd.x) - row_max(__uint_as_float(hd.x)));
                if (lane < 16) sm.ent[0][lane] = w * __builtin_amdgcn_rcpf(row_sum(w));
                CBAR();
                if (lane == 0) lds_st(&sm.tabs, 0u);
            }
            while (lds_ld(&sm.tabs) || lds_ld(&sm.merged) <= (2 * NU - 1) / NOUT * NOUT) SLEEP(1)
            CBAR();
            const float cf = sm.ent[0][2 * c + (lane >> 5)];
            v4u x; bool got;
#define SEEN(J, N) { FETCH(x, J, N) got = __builtin_amdgcn_ballot_w64((((x.y ^ T << 24) | (x.w ^ T << 24)) >> 24) != 0) == 0; }
            GATE(tl + TCA) POLL(0, ro) GATE(tl + TCA + TCD) POLL(1, rx + 3 * GSZ * NSPLIT * 512) SEEN(0, 1)
            if (!got) SEEN(1, 0)
            while (!got) { POLL(2, rx + 4 * GSZ * NSPLIT * 512) SEEN(2, 0) SLEEP(0) }
            const double c0 = ldexpf(cf, (int)(x.y >> 16 & 255u) - 141), c1 = ldexpf(cf, (int)(x.w >> 16 & 255u) - 141);
#define SWADD(A, B) ({ const v2u a_ = __builtin_bit_cast(v2u, A), b_ = __builtin_bit_cast(v2u, B); \
                const auto l_ = __builtin_amdgcn_permlane32_swap(a_.x, b_.x, false, false); const auto h_ = __builtin_amdgcn_permlane32_swap(a_.y, b_.y, false, false); \
                __builtin_bit_cast(double, (v2u){l_[0], h_[0]}) + __builtin_bit_cast(double, (v2u){l_[1], h_[1]}); })
            const double s0 = SWADD(c0 * (double)(short)x.x, c1 * (double)(short)x.z), s1 = SWADD(c0 * (double)((int)x.x >> 16), c1 * (double)((int)x.z >> 16)),
                         s2 = SWADD(c0 * (double)(short)x.y, c1 * (double)(short)x.w);
            asm volatile("ds_add_f64 %0, %1\n\tds_add_f64 %0, %2 offset:512\n\tds_add_f64 %0, %3 offset:1024" :: "v"(lds_addr(&sm.acc[0][lane])), "v"(s0), "v"(s1), "v"(s2) : "memory");
            ADDRTN(&sm.cdone)
            const double a0 = sm.acc[0][lane], a1 = sm.acc[0][64 + lane], a2 = sm.acc[0][128 + lane];
            ADDED(c)
            o0 = a0; o1 = a1; o2 = a2;
        } else {
            const bool quart = head >> 4 == WQ;
            const unsigned t15 = T & 0x7fffu;
#define TAKE(K) { FETCH(x[K], K, 0) \
                bad |= (x[K].y >> 17 ^ t15) | (x[K].w >> 17 ^ t15); \
                const u64 bl = __builtin_amdgcn_ballot_w64((x[K].y & 0x10000u) != 0), be = __builtin_amdgcn_ballot_w64((x[K].w & 0x10000u) != 0); \
                L[K] = __uint_as_float(lane < 32 ? (unsigned)bl : (unsigned)(bl >> 32)); ex[K] = lane < 32 ? (unsigned)be : (unsigned)(be >> 32); \
                M = fmaxf(M, fmaxf(__uint_as_float((unsigned)bl), __uint_as_float((unsigned)(bl >> 32)))); }
#define SUMUP(K) { const float w_ = __builtin_amdgcn_exp2f(L[K] - M), c_ = ldexpf(w_, (int)(ex[K] & 255u) - 141); den += w_; \
                t[0] = fmaf(c_, (float)(short)x[K].x, t[0]); t[1] = fmaf(c_, (float)((int)x[K].x >> 16), t[1]); t[2] = fmaf(c_, (float)(short)x[K].y, t[2]); \
                t[3] = fmaf(c_, (float)(short)x[K].z, t[3]); t[4] = fmaf(c_, (float)((int)x[K].z >> 16), t[4]); t[5] = fmaf(c_, (float)(short)x[K].w, t[5]); }
            const unsigned tg = tl + (quart ? TRQ : TRD);
            GATE(tg)
            v4u x[4]; float L[4], M; unsigned ex[4];
            for (;;) {
                unsigned bad = 0; M = -3e38f;
                POLL(0, ro)
                if (quart) { POLL(1, rx) POLL(2, rx + GSZ * NSPLIT * 512) POLL(3, rx + 2 * GSZ * NSPLIT * 512) }
                __builtin_amdgcn_s_setprio(3);
                TAKE(0)
                if (quart) { TAKE(1) TAKE(2) TAKE(3) }
                if (__builtin_amdgcn_ballot_w64(bad != 0) == 0) break;
                SLEEP(2)
            }
            float t[6] = {0.f, 0.f, 0.f, 0.f, 0.f, 0.f}, den = 0.f;
            SUMUP(0)
            if (quart) { SUMUP(1) SUMUP(2) SUMUP(3) }
#define SWADF(A, B) ({ const auto l_ = __builtin_amdgcn_permlane32_swap(__float_as_uint(A), __float_as_uint(B), false, false); __uint_as_float(l_[0]) + __uint_as_float(l_[1]); })
            v4f* pb = (v4f*)((char*)&sm.cs[0][0][0] + 65536);
            float* pm = (float*)(pb + NSW * 64);
            pb[c * 64 + lane] = (v4f){SWADF(t[0], t[3]), SWADF(t[1], t[4]), SWADF(t[2], t[5]), SWADF(den, den)};
            if (lane == 0) pm[c] = M;
            CBAR();
            ADDRTN(&sm.cdone)
            ADDED(c)
            CBAR();
            if (c == NSW - 1) {
                const v4f ma = *(const v4f*)pm, mb = *(const v4f*)(pm + 4);
                const float mw[NSW] = {ma.x, ma.y, ma.z, ma.w, mb.x, mb.y, mb.z, mb.w};
                float Mx = mw[0];
#pragma unroll
                for (int w = 1; w < NSW; w++) Mx = fmaxf(Mx, mw[w]);
                v4f sum = {0.f, 0.f, 0.f, 0.f};
#pragma unroll
                for (int w = 0; w < NSW; w++) sum += pb[w * 64 + lane] * __builtin_amdgcn_exp2f(mw[w] - Mx);
                const float inv = __builtin_amdgcn_rcpf(sum.w);
                o0 = sum.x * inv; o1 = sum.y * inv; o2 = sum.z * inv;
            }
        }
        if (c == NSW - 1) {
            asm("" : "+v"(o0), "+v"(o1), "+v"(o2));
            typedef __bf16 v2b __attribute__((ext_vector_type(2)));
            typedef unsigned v3u __attribute__((ext_vector_type(3)));
#define PK(A, B) __builtin_bit_cast(unsigned, (v2b){(__bf16)(A), (__bf16)(B)})
            const unsigned p12 = PK(o1, o2);
            const v3u ov = {PK(o0, o1), PK(o2, __uint_as_float(__builtin_amdgcn_permlane32_swap(__float_as_uint(o0), __float_as_uint(o0), false, false)[1])), __builtin_amdgcn_permlane32_swap(p12, p12, false, false)[1]};
            if (lane < 32) __builtin_amdgcn_raw_buffer_store_b96(ov, __builtin_amdgcn_make_buffer_rsrc(out + (size_t)head * DH, 0, DH * 2, 0x00020000), lane * 12, 0, 0);
        }
    }
}

extern "C" void kernel_launch(void* const* d_in, const int* in_sizes, int n_in,
                              void* d_out, int out_size, void* d_ws, size_t ws_size,
                              hipStream_t stream) {
    const unsigned short* q = (const unsigned short*)d_in[0];
    const int* qk = (const int*)d_in[1];
    const unsigned short* k_scales = (const unsigned short*)d_in[2];
    const unsigned short* k_zeros = (const unsigned short*)d_in[3];
    const int* qv = (const int*)d_in[4];
    const unsigned short* v_scales = (const unsigned short*)d_in[5];
    const unsigned short* v_zeros = (const unsigned short*)d_in[6];
    const unsigned short* q_cos = (const unsigned short*)d_in[7];
    const unsigned short* q_sin = (const unsigned short*)d_in[8];
    const unsigned short* k_cos = (const unsigned short*)d_in[9];
    const unsigned short* k_sin = (const unsigned short*)d_in[10];
    unsigned char* ws = (unsigned char*)d_ws;
    attn_kernel<<<NSPLIT * GSZ, NTHREADS, 0, stream>>>(q, qk, k_scales, k_zeros, qv, v_scales, v_zeros,
                                                       q_cos, q_sin, k_cos, k_sin, (__hip_bfloat16*)d_out, ws);
}
```

```cpp
#include <hip/hip_runtime.h>
#include <hip/hip_bf16.h>
#include <stdint.h>

#define DH 192
#define HALF 96
#define SKV 2048
#define NH 256
#define NSW 8
#define NWAVES (NSW + 4)
#define NTHREADS (64 * NWAVES)
#define BROWS 128
#define NSPLIT 16
#define GSZ 16
#define WQ 10
#define NU (NH / GSZ + 3)
#define NPC 1024
#define NRR (NH + 3 * GSZ)
#define NIN 8
#define NOUT 8
#define WALL (15 * 144 + 4 * 48)
#define SC1 16
#define NRND 48
#define RST 4352
#define TK 2
#define NMIX 3
#define TUNIT 311
#define T01 50
#define TMIX 7
#define TOFF 50
#define PSF 100
#define TGS 82
#define PSW (NPC / 2 + 28)
#define TG0 8
#define TG1 5
#define TWO -80
#define PSG (NPC / 2 + 60)
#define TH0 3
#define TH1 3
#define EARLY 10
#define LEADP 24
#define DPH 2
#define TA 210
#define TA2 70
#define CSH 2
#define NPRB 3
#define TMAP 485
#define MINV 96
#define VW 4
#define MASTER 203
#define TRES 175
#define TB1 1200
#define PER 300
#define RDD 100
#define LEAD 70
#define LHI 85
#define LOK 74
#define PSL0 124
#define PUBP -12
#define LJ 210
#define LOK2 75
#define NTRY 3
#define TFO 6
#define SL0 42.f
#define PST0 200
#define KC 4
#define TC0 1000
#define TCS 9
#define PRE0 3

#define KREL 12
#define PDEL 68
#define NCP 3
#define NRW (NRR + NCP * GSZ)
#define TRD 111
#define THD 55
#define TRQ 55
#define TCA 133
#define TCD 22
#define TSD 560
#define WS_EPOCH 0
#define WS_POST0 8192
#define WS_POST (WS_POST0 + ((unsigned)E1 & 15) * 2048)
#define WS_NEWS (49152 + 256)
#define WS_HELLO 59904
#define WS_BOARD 65536
#define WS_PART (2u << 20)
#define WS_SWAP (WS_PART + NRW * NSPLIT * 512)

typedef unsigned int v2u __attribute__((ext_vector_type(2)));
typedef unsigned int v16u __attribute__((ext_vector_type(16)));
typedef unsigned int v4u __attribute__((ext_vector_type(4)));
typedef int v4i __attribute__((ext_vector_type(4)));
typedef float v2f __attribute__((ext_vector_type(2)));
typedef float v4f __attribute__((ext_vector_type(4)));
typedef unsigned long long u64;

__device__ __forceinline__ float bflo(unsigned u) { return __uint_as_float(u << 16); }
__device__ __forceinline__ float bfhi(unsigned u) { return __uint_as_float(u & 0xffff0000u); }
__device__ __forceinline__ float bfr(float x) {
    __bf16 b = (__bf16)x;
    return __uint_as_float(((unsigned)__builtin_bit_cast(unsigned short, b)) << 16);
}

#define SLEEP(n) SLEEP_(n)
#define SLEEP_(n) asm volatile("s_sleep " #n "\n\tv_add_co_u32 %0, vcc, 1, %0\n\ts_cbranch_vccz 1f\n\ts_endpgm\n1:" : "+v"(spins) :: "vcc", "memory");
#define SPIN0 0xf0000000u
#define CBAR() asm volatile("" ::: "memory")

__device__ __forceinline__ void wave_sync() {
    __builtin_amdgcn_fence(__ATOMIC_RELEASE, "wavefront");
    __builtin_amdgcn_wave_barrier();
    __builtin_amdgcn_fence(__ATOMIC_ACQUIRE, "wavefront");
}
template <int CTRL>
__device__ __forceinline__ float dpp_add(float x) {
    int y = __builtin_amdgcn_update_dpp(0, __float_as_int(x), CTRL, 0xf, 0xf, true);
    return x + __int_as_float(y);
}
template <int CTRL>
__device__ __forceinline__ float dpp_max(float x) {
    int y = __builtin_amdgcn_update_dpp(__float_as_int(x), __float_as_int(x), CTRL, 0xf, 0xf, false);
    return fmaxf(x, __int_as_float(y));
}
__device__ __forceinline__ float row_sum(float x) { x = dpp_add<0x128>(x); x = dpp_add<0x124>(x); x = dpp_add<0x122>(x); return dpp_add<0x121>(x); }
__device__ __forceinline__ float row_max(float m) { m = dpp_max<0x128>(m); m = dpp_max<0x124>(m); m = dpp_max<0x122>(m); return dpp_max<0x121>(m); }
__device__ __forceinline__ float rdlane(float x, int l) { return __int_as_float(__builtin_amdgcn_readlane(__float_as_int(x), l)); }
__device__ __forceinline__ float wave_max(float m) { m = row_max(m); return fmaxf(fmaxf(rdlane(m, 0), rdlane(m, 16)), fmaxf(rdlane(m, 32), rdlane(m, 48))); }
__device__ __forceinline__ float wave_maxv(float m) {
    m = row_max(m);
    const auto a = __builtin_amdgcn_permlane16_swap(__float_as_uint(m), __float_as_uint(m), false, false);
    m = fmaxf(__uint_as_float(a[0]), __uint_as_float(a[1]));
    const auto b = __builtin_amdgcn_permlane32_swap(__float_as_uint(m), __float_as_uint(m), false, false);
    return fmaxf(__uint_as_float(b[0]), __uint_as_float(b[1]));
}
__device__ __forceinline__ float wave_sum(float x) { x = row_sum(x); return (rdlane(x, 0) + rdlane(x, 16)) + (rdlane(x, 32) + rdlane(x, 48)); }
__device__ __forceinline__ unsigned lds_ld(const unsigned* p) { return __builtin_amdgcn_readfirstlane(__hip_atomic_load(p, __ATOMIC_RELAXED, __HIP_MEMORY_SCOPE_WORKGROUP)); }
__device__ __forceinline__ unsigned lds_addr(const void* p) { return (unsigned)(size_t)(const __attribute__((address_space(3))) void*)p; }
#define DMA16(SRC, LDS) asm volatile("s_mov_b32 %0, m0\n\ts_mov_b32 m0, %2\n\ts_nop 0\n\tglobal_load_lds_dwordx4 %1, off\n\ts_mov_b32 m0, %0" : "=&s"(m0keep) : "v"(SRC), "s"(LDS) : "memory")
__device__ __forceinline__ void lds_st(unsigned* p, unsigned v) { __hip_atomic_store(p, v, __ATOMIC_RELAXED, __HIP_MEMORY_SCOPE_WORKGROUP); }
#define NOW() ((unsigned)__builtin_amdgcn_s_memrealtime())
__device__ __forceinline__ unsigned tdue(unsigned g) {
    return (g * TUNIT + min(g, 480u) * TK + min(g, 48u) * T01 + min(g, 32u * NMIX) * TMIX + (g >= PSF ? TGS * 32 : 0)) >> 5;
}
__device__ __forceinline__ unsigned tvx(unsigned g, unsigned gap) {
    return (g >= PSW ? gap >> (g >= PSG ? 12 : 0) & 0xfffu : 0u) + (min(g - NPC / 2, 32u * NMIX) * (gap >> 24) >> 5);
}

__device__ __forceinline__ unsigned unit_of(unsigned g) { const unsigned p = g & 511; return (g >> 9) * NU + (p >> 5) + min(max((int)p - 32 * WQ, 0) >> 3, 3); }
__device__ __forceinline__ unsigned first_of(unsigned e) { return 32 * e - 24 * min(max((int)e - WQ, 0), 4); }
#define QUARTER(E) ((E) - WQ < 4)
__device__ __forceinline__ unsigned went(unsigned e) { return 144 * e - 96 * min(max((int)e - WQ, 0), 4); }
__device__ __forceinline__ unsigned wunit(unsigned x) { x += 24 * NMIX; return x < 24 * WQ ? x / 24 : x < 24 * WQ + 32 ? WQ + (x - 24 * WQ) / 8 : (x + 64) / 24; }

#define ROTT 0xed8d2c3d2c35ed84ull, 0xba85aa165b03ac82ull, 0xee7efa489a38af71ull, 0xe932da1b0bab51d8ull, 0xfa96112d5fee971bull, 0x2c7e9ea5a544f071ull, 0x3d86093d5f0e810bull, 0xe932da1b0bab51d8ull, 0xfa96112d5fee971bull, 0xd834e62c5fa723e9ull, 0xa4cd778c2ca4aaa5ull, 0xe932da1b0bab51d8ull, 0xfa96112d5fee971bull, 0xc834e12c0aaa4ec7ull, 0x3d86093d5f0e810bull, 0xe932da1b0bab51d8ull
static __device__ const u64 ROT[16] = {ROTT};

struct __attribute__((aligned(16))) Smem {
    unsigned cs[NSW][4][768];
    double acc[NOUT][DH];
    float ent[NOUT][BROWS];
    unsigned short wv[WALL];
    unsigned short wo[WALL];
    unsigned q[NIN][HALF];
    float2 kz[NIN][BROWS];
    float2 vz[NIN][BROWS];
    unsigned utab[64];
    unsigned line, gap;
    unsigned late, grab;
    unsigned rdy[NIN], cnt[NOUT];
    unsigned tabs, vrdy, swp;
    unsigned aline;
    unsigned tag_rdy, tag_lo, tag_hi, merged, cdone, cidx;
    unsigned slow;
    unsigned chead;
};

#define LDS_ADD(P, V) { u64 ex_; asm volatile("s_mov_b64 %0, exec\n\ts_mov_b64 exec, 1\n\tds_add_u32 %1, %2\n\ts_mov_b64 exec, %0" : "=&s"(ex_) : "v"(lds_addr(P)), "v"(V) : "memory"); }
#define LDS_OR(P, V) { u64 ex_; asm volatile("s_mov_b64 %0, exec\n\ts_mov_b64 exec, 1\n\tds_or_b32 %1, %2\n\ts_mov_b64 exec, %0" : "=&s"(ex_) : "v"(lds_addr(P)), "v"(V) : "memory"); }

__global__ __launch_bounds__(NTHREADS, 3) void attn_kernel(
    const unsigned short* __restrict__ q, const int* __restrict__ qk,
    const unsigned short* __restrict__ k_scales, const unsigned short* __restrict__ k_zeros,
    const int* __restrict__ qv,
    const unsigned short* __restrict__ v_scales, const unsigned short* __restrict__ v_zeros,
    const unsigned short* __restrict__ q_cos, const unsigned short* __restrict__ q_sin,
    const unsigned short* __restrict__ k_cos, const unsigned short* __restrict__ k_sin,
    __hip_bfloat16* __restrict__ out, unsigned char* __restrict__ ws)
{
    __shared__ Smem sm;
    const int tid = threadIdx.x;
    unsigned spins = SPIN0;
    const int lane = tid & 63;
    const int hw = __builtin_amdgcn_readfirstlane(tid >> 6);
    u64 t00; unsigned ka;
    asm volatile("s_memrealtime %0\n\ts_load_dword %1, %2, 0x40" : "=&s"(t00), "=&s"(ka) : "s"(__builtin_amdgcn_kernarg_segment_ptr()));
    if (hw == 0) {
        const char* pc0; asm volatile("s_getpc_b64 %0" : "=s"(pc0));
        asm volatile("global_load_dword a6, %0, %1" :: "v"(((blockIdx.x & ~7u) + (lane & 7)) * 128), "s"(pc0) : "a6", "memory");
    }
    const int wave = hw >= 4 ? hw - 4 : hw + NSW;
    const unsigned split = (blockIdx.x & 1) + (blockIdx.x & 2) * 4 + ((blockIdx.x >> 3) & 3) * 2;
    const unsigned side = (split ^ (blockIdx.x >> 2)) & 1, rank = blockIdx.x >> 5;
    const unsigned head0 = 2 * rank + side;
    const unsigned rf0 = (8 * split + 1) / 3, r3 = 8 * split + 1 - 3 * rf0;
    const unsigned koff = ((lane >> 5) * DH + 3 * (lane & 31)) * 4, koffb = koff + 384;
#define AR(S) (16 + (S) * 8)
#define GLD(A, OFF, BASE, IMM) asm volatile("global_load_dwordx3 a[%c0:%c1], %2, %3 offset:%4 nt" :: "i"(A), "i"((A) + 2), "v"(OFF), "s"(BASE), "i"(IMM))
#define ISSUE(S, J, BASE, OA, OB) { GLD(AR(S), OA, BASE, (J) * 1536); GLD(AR(S) + 4, OB, BASE, (J) * 1536); }
#define HEAD2 ((GSZ + 1 - 2 * side) * (SKV * DH * 4))
    const unsigned wo = (hw - 2) & 7;
    const bool pre = hw >= 4 && wo < PRE0;
    if (pre) {
        const char* a = (const char*)qk + (head0 * 512 + side * 256 + rf0 + wave + (wave >= 6 ? 80 : 0)) * 3072; const char* b = a + HEAD2;
        ISSUE(0, 0, a, koff, koffb) ISSUE(1, 1, a, koff, koffb) ISSUE(2, 0, b, koff, koffb) ISSUE(3, 1, b, koff, koffb)
    }
    asm volatile("s_waitcnt lgkmcnt(0)" : "+s"(t00), "+s"(ka));
    if (hw == 0) {
        constexpr u64 rot[16] = {ROTT};
        sm.utab[lane] = lane == 2 ? 2 * GSZ + head0 | ((unsigned)(rot[2] >> 4 * head0) & 15) << 9 : ~0u;
        unsigned l0 = (unsigned)t00 + TOFF;
        if (KREL) { const unsigned t1 = NOW() + KREL; if ((int)(t1 - l0) > 0) l0 = t1; }
        if (lane < 32) (&sm.line)[lane] = lane == 3 ? 64u : (lane & 29) == 0 ? l0 : 0u;
        asm volatile("s_waitcnt lgkmcnt(0)" ::: "memory");
    }
    __builtin_amdgcn_s_barrier();
    CBAR();
    const u64 rtab = r3 == 1 ? 0x5756552c2b020100ull : r3 == 0 ? 0x5756552c2b2a0100ull : 0x57562d2c2b020100ull;
#define R8(I) (rf0 + ((unsigned)(rtab >> (8 * (I))) & 255u))
#define GATE(DUE) { int d_ = (int)((DUE) - NOW()); if (d_ < 3000) while (d_ > 0) { __builtin_amdgcn_s_sleep(1); d_ = (int)((DUE) - NOW()); } }
#define TLAST() (lds_ld(&sm.line) + tdue(NPC - 2) + tvx(NPC - 2, lds_ld(&sm.gap)))
    if (__builtin_expect(wave < NSW, 1)) {
        const int cst = 2 * side;
        const unsigned r8w = R8((unsigned)wave);
        const char* cbase[3];
#pragma unroll
        for (int j = 0; j < 3; j++) { const int x = j * 64 + lane; cbase[j] = (x < 96 ? (const char*)k_cos + x * 16 : (const char*)k_sin + (x - 96) * 16) + r8w * (4 * DH * 2); }
        unsigned m0keep;
#define CS_DMA(W, C) { _Pragma("unroll") for (int j = 0; j < 3; j++) DMA16(cbase[j] + (C) * (512 * DH * 2), lds_addr(&sm.cs[0][0][0]) + ((W) * 4 + (C)) * 3072 + j * 1024); }
#define REPACK(W, C) { \
        unsigned* cw = &sm.cs[W][C][0]; \
        const unsigned* l = cw + rsel * (DH / 2) + qoff; \
        unsigned r[2][6]; \
        _Pragma("unroll") \
        for (int j = 0; j < 2; j++) { \
            unsigned e[4][2]; \
            _Pragma("unroll") \
            for (int k = 0; k < 4; k++) { \
                const unsigned* a = l + j * DH + (k & 1) * 48 + (k >> 1) * 384;       \
                unsigned x0 = a[0], x1 = a[1]; \
                e[k][0] = __builtin_amdgcn_alignbit(x1, x0, sh); \
                e[k][1] = x1 >> sh; \
            } \
            r[j][0] = (e[0][0] & 0xffffu) | (e[3][0] << 16); \
            r[j][1] = (e[0][0] >> 16) | (e[3][0] & 0xffff0000u); \
            r[j][2] = (e[0][1] & 0xffffu) | (e[3][1] << 16); \
            r[j][3] = (e[1][0] & 0xffffu) | (e[2][0] << 16); \
            r[j][4] = (e[1][0] >> 16) | (e[2][0] & 0xffff0000u); \
            r[j][5] = (e[1][1] & 0xffffu) | (e[2][1] << 16); \
        } \
        wave_sync(); \
        _Pragma("unroll") \
        for (int j = 0; j < 2; j++) { *(v4u*)&cw[j * 256 + lane * 4] = (v4u){r[j][0], r[j][1], r[j][2], r[j][3]}; *(v2u*)&cw[512 + j * 128 + lane * 2] = (v2u){r[j][4], r[j][5]}; } \
        wave_sync(); }
        const int sub = lane & 31;
        const int rsel = lane >> 5;
        const unsigned sh = (sub & 1) * 16;
        const int qoff = 3 * (sub >> 1) + (sub & 1);
        const unsigned voff = 3 * lane * 4;
        asm volatile("" ::: "a16", "a17", "a18", "a19", "a20", "a21", "a22", "a23", "a24", "a25", "a26", "a27", "a28", "a29", "a30", "a31",
                            "a32", "a33", "a34", "a35", "a36", "a37", "a38", "a39", "a40", "a41", "a42", "a43", "a44", "a45", "a46", "a47");
        asm volatile("" ::: "a48", "a49", "a50", "a51", "a52", "a53", "a54", "a55", "a56", "a57", "a58", "a59", "a60", "a61", "a62", "a63",
                            "a64", "a65", "a66", "a67", "a68", "a69", "a70", "a71", "a72", "a73", "a74", "a75", "a76", "a77", "a78", "a79");
        int ta[4][3], tb[4][3];
#define WAITT(P, S, N) asm volatile("s_waitcnt vmcnt(%6)\n\tv_accvgpr_read_b32 %0, a[%c7]\n\tv_accvgpr_read_b32 %1, a[%c8]\n\tv_accvgpr_read_b32 %2, a[%c9]\n\t" \
                              "v_accvgpr_read_b32 %3, a[%c10]\n\tv_accvgpr_read_b32 %4, a[%c11]\n\tv_accvgpr_read_b32 %5, a[%c12]" \
                              : "=v"(ta[P][0]), "=v"(ta[P][1]), "=v"(ta[P][2]), "=v"(tb[P][0]), "=v"(tb[P][1]), "=v"(tb[P][2]) \
                              : "i"(N), "i"(AR(S)), "i"(AR(S) + 1), "i"(AR(S) + 2), "i"(AR(S) + 4), "i"(AR(S) + 5), "i"(AR(S) + 6))
        const unsigned voffb = voff + 768;
        u64 ln = 0;
#define TDUE(G) (tdue(G) + ((G) >= NPC / 2 ? tvx(G, __builtin_amdgcn_readfirstlane((unsigned)(ln >> 32))) : 0u))
#define REPORT() { const unsigned t_ = now - tdc; if (lane == 0) lds_st(&sm.late, t_); }
#define LINE() ln = LDV((u64*)&sm.line);
#define LDV(P) __hip_atomic_load(P, __ATOMIC_RELAXED, __HIP_MEMORY_SCOPE_WORKGROUP)
        unsigned u = 0, rg = cst * 8 + wave, np = 0;
        CS_DMA(wave, cst)

        const char* kb0 = (const char*)qk + (size_t)(head0 * 512 + r8w) * 3072;
#define KB(I) (kb0 + ((cst + (I)) & 3) * (128 * 3072))
        const char* kb; const char* kb2;
#define SISSUE(I) { kb = KB(I); kb2 = kb + HEAD2; const unsigned du_ = lds_ld(&sm.line) + tdue(16 * (I) + 2 * wo); GATE(du_) \
            ISSUE(((I) & 1) * 4, 0, kb, koff, koffb) ISSUE(((I) & 1) * 4 + 1, 1, kb, koff, koffb) ISSUE(((I) & 1) * 4 + 2, 0, kb2, koff, koffb) ISSUE(((I) & 1) * 4 + 3, 1, kb2, koff, koffb) }
        if (!pre) SISSUE(0)
        CS_DMA(wave, (cst + 1) & 3)
        if (pre) asm volatile("s_waitcnt vmcnt(3)" ::: "memory");
        asm volatile("s_waitcnt vmcnt(11)" ::: "memory");
        if (!pre) LDS_ADD(&sm.slow, NOW() - lds_ld(&sm.line) - tdue(2 * wo))
        REPACK(wave, cst)
        SISSUE(1)
#define TABDONE(I) LDS_OR(&sm.tabs, 1u << ((((cst + (I)) & 3) * 8) + wave))
        TABDONE(0)
        const float scale = 0.07216878364870322f;
        double acc[3] = {0.0, 0.0, 0.0};
        bool first = true, chg, none = false;
        unsigned Qp[3], Qn[3], Rp[3], Rn[3];
        float kz4[4], ksc, kres[4];
        v4u ca[4]; v2u cb[4];
        v2f qe[4][3], A[4];
        unsigned vut, nrg;
        const unsigned vr = ((lane >> 3) & 3) * 128 + R8((unsigned)lane & 7);
        unsigned g, mg;
        const unsigned grab_adr = lds_addr(&sm.grab);
        asm volatile("v_accvgpr_write_b32 a13, 1\n\tv_accvgpr_write_b32 a14, 2" ::: "a13", "a14", "a15");
#define GRAB(N) { u64 ex_; asm volatile("s_mov_b64 %0, exec\n\ts_mov_b64 exec, 1\n\tds_add_rtn_u32 a15, %1, a%c2\n\ts_mov_b64 exec, %0" : "=&s"(ex_) : "v"(grab_adr), "i"(12 + (N)) : "memory"); }
#define GRABBED() { unsigned t_; asm volatile("s_waitcnt lgkmcnt(0)\n\tv_accvgpr_read_b32 %1, a15\n\ts_nop 0\n\tv_readfirstlane_b32 %0, %1\n\ts_nop 1" : "=s"(g), "=v"(t_)); }
        unsigned oa, ob, un, tdn, tiss, tdc = 0;
#define PLAN() { \
            GRABBED() un = unit_of(g); chg = un != u; none = g >= NPC; \
            const bool nv = g >= NPC / 2; \
            unsigned hd = __builtin_amdgcn_readlane(vut, un); \
            while (hd == ~0u) { SLEEP(1) hd = lds_ld(&sm.utab[un]); } \
            nrg = (g + (hd >> 8)) & 31; hd &= 255; \
            unsigned boff, boff2; asm("s_mul_i32 %0, %1, 0xc00" : "=s"(boff) : "s"(hd * 512 + __builtin_amdgcn_readlane(vr, nrg))); \
            asm("s_mul_i32 %0, %1, 0xc00" : "=s"(boff2) : "s"(hd * 512 + __builtin_amdgcn_readlane(vr, nrg + 1))); \
            const char* b_ = nv ? (const char*)qv : (const char*)qk; \
            kb = b_ + boff; kb2 = b_ + boff2; \
            oa = nv ? voff : koff; ob = nv ? voffb : koffb; \
            tdn = TDUE(g); }
#define FIRE() { ISSUE(0, 0, kb, oa, ob) ISSUE(1, 1, kb, oa, ob) ISSUE(2, 0, kb2, oa, ob) ISSUE(3, 1, kb2, oa, ob) }
#define LAUNCH() { \
            LINE() \
            const unsigned du_ = __builtin_amdgcn_readfirstlane((unsigned)ln) + tdn; \
            GATE(du_) \
            ISSUE(0, 0, kb, oa, ob) ISSUE(1, 1, kb, oa, ob) \
            GATE(du_ + 10) \
            ISSUE(2, 0, kb2, oa, ob) ISSUE(3, 1, kb2, oa, ob) tiss = NOW(); }
#define ARRIVED() \
            const unsigned now = NOW(); \
            const bool stall = now - tiss > LJ && g - PST0 < NPC - 48 - PST0; \
            const bool late = !none && !stall && (int)(now + EARLY - __builtin_amdgcn_readfirstlane((unsigned)ln) - tdn) >= 0; \
            if (late) { FIRE() tiss = now; } \
            if (stall && lane == 0) __hip_atomic_fetch_max(&sm.line, (unsigned)ln + (now - tiss) - LOK2, __ATOMIC_RELAXED, __HIP_MEMORY_SCOPE_WORKGROUP); \
            REPORT()
#define ADVANCE(ISV) { \
            if (chg) FLUSH(ISV) \
            if (!none && !late) LAUNCH() \
            first = chg; u = un; rg = nrg; tdc = tdn + 10; }
#define K_TAB(MODE) { _Pragma("unroll") for (int p = 0; p < ((MODE) ? 2 : 4); p++) { \
            const unsigned* cw = &sm.cs[(rg & 7) + (p >> 1)][rg >> 3][0]; \
            ca[p] = *(const v4u*)&cw[(p & 1) * 256 + lane * 4]; cb[p] = *(const v2u*)&cw[512 + (p & 1) * 128 + lane * 2]; } }
#define K_PRE(MODE, P) { \
        float e0, e1, e2, f0, f1, f2; \
        const unsigned* qp_ = ((MODE) && (P) >= 2) ? Rp : Qp; const unsigned* qn_ = ((MODE) && (P) >= 2) ? Rn : Qn; \
        const int tp_ = (MODE) ? (P) & 1 : (P); \
        asm("v_dot2_f32_bf16 %0, %6, %12, 0\n\tv_dot2_f32_bf16 %3, %9, %15, 0\n\tv_dot2_f32_bf16 %1, %7, %13, 0\n\tv_dot2_f32_bf16 %4, %10, %16, 0\n\t" \
            "v_dot2_f32_bf16 %2, %8, %14, 0\n\tv_dot2_f32_bf16 %5, %11, %17, 0\n\ts_nop 2" \
            : "=&v"(e0), "=&v"(e1), "=&v"(e2), "=&v"(f0), "=&v"(f1), "=&v"(f2) \
            : "v"(qp_[0]), "v"(qp_[1]), "v"(qp_[2]), "v"(qn_[0]), "v"(qn_[1]), "v"(qn_[2]), \
              "v"(ca[tp_].x), "v"(ca[tp_].y), "v"(ca[tp_].z), "v"(ca[tp_].w), "v"(cb[tp_].x), "v"(cb[tp_].y)); \
        qe[P][0] = (v2f){e0, f0}; qe[P][1] = (v2f){e1, f1}; qe[P][2] = (v2f){e2, f2}; \
        A[P] = (qe[P][0] + qe[P][1] + qe[P][2]) * (v2f){-kz4[P], -kz4[P]}; }
#define K_WAIT(MODE, P) { \
        if ((MODE) == 0) { WAITT(P, P, 6 - 2 * (P)); } \
        else { WAITT(P, (((MODE) - 1) & 1) * 4 + (P), ((MODE) < 3 ? 20 : (MODE) == 3 ? 17 : 14) - 2 * (P)); } }
#define K_FMA(P) { \
        _Pragma("unroll") \
        for (int i = 0; i < 3; i++) { v2f x = {(float)ta[P][i], (float)tb[P][i]}; A[P] = qe[P][i] * x + A[P]; } \
        kres[P] = A[P].x + A[P].y; }
#define K_RED(MODE) { \
        const bool o1 = lane & 1, o2 = lane & 2; \
        float s01 = (o1 ? kres[1] : kres[0]) + __int_as_float(__builtin_amdgcn_update_dpp(0, __float_as_int(o1 ? kres[0] : kres[1]), 0xb1, 0xf, 0xf, true)); \
        float s23 = (o1 ? kres[3] : kres[2]) + __int_as_float(__builtin_amdgcn_update_dpp(0, __float_as_int(o1 ? kres[2] : kres[3]), 0xb1, 0xf, 0xf, true)); \
        float sx = (o2 ? s23 : s01) + __int_as_float(__builtin_amdgcn_update_dpp(0, __float_as_int(o2 ? s01 : s23), 0x4e, 0xf, 0xf, true)); \
        sx = dpp_add<0x124>(sx); sx = dpp_add<0x128>(sx); \
        const auto sw = __builtin_amdgcn_permlane16_swap(__float_as_uint(sx), __float_as_uint(sx), false, false); \
        sx = __uint_as_float(sw[0]) + __uint_as_float(sw[1]); \
        if ((lane & 28) == 0) { if (MODE) sm.ent[(lane >> 1) & 1][rg * 4 + 2 * (lane & 1) + rsel] = sx * ksc; else sm.ent[u % NOUT][rg * 4 + 2 * (lane & 3) + rsel] = sx * ksc; } }
#define K_PRE4(MODE) { K_PRE(MODE, 0) K_PRE(MODE, 1) K_PRE(MODE, 2) K_PRE(MODE, 3) asm volatile("" :: "v"(A[0]), "v"(A[1]), "v"(A[2]), "v"(A[3])); }
#define K_BODY(MODE) { K_PRE4(MODE) K_WAIT(MODE, 0) K_FMA(0) K_WAIT(MODE, 1) K_FMA(1) K_WAIT(MODE, 2) K_FMA(2) K_WAIT(MODE, 3) K_FMA(3) K_RED(MODE) }
#define V_FMA(J, W0, W1) { \
        _Pragma("unroll") \
        for (int i = 0; i < 3; i++) { \
            acc[i] = fma(W0, (double)ta[J][i], acc[i]); \
            acc[i] = fma(W1, (double)tb[J][i], acc[i]); \
        } }
#define V_PIN() asm volatile("" :: "v"(acc[0]), "v"(acc[1]), "v"(acc[2]));
#define RDYWAIT(SL, SEQ) { while (lds_ld(&sm.rdy[SL]) != (SEQ)) SLEEP(1); CBAR(); }
#define LOADZ() { _Pragma("unroll") for (int p = 0; p < 4; p++) kz4[p] = sm.kz[u % NIN][rg * 4 + 2 * p + rsel].x; ksc = sm.kz[u % NIN][rg * 4 + 2 * (lane & 3) + rsel].y * scale; }
#define LOADQ() { _Pragma("unroll") for (int i = 0; i < 3; i++) { Qp[i] = sm.q[u % NIN][3 * sub + i]; Qn[i] = __builtin_amdgcn_alignbit(Qp[i], Qp[i], 16) ^ 0x80000000u; } }
#define FLUSH(ISV) { \
            mg = __builtin_amdgcn_readfirstlane(mg); \
            while (u >= mg + NOUT) { SLEEP(1); mg = lds_ld(&sm.merged); } \
            CBAR(); \
            if (ISV) { const unsigned aa = lds_addr(&sm.acc[u % NOUT][lane]); \
                asm volatile("ds_add_f64 %0, %1\n\tds_add_f64 %0, %2 offset:512\n\tds_add_f64 %0, %3 offset:1024" :: "v"(aa), "v"(acc[0]), "v"(acc[1]), "v"(acc[2]) : "memory"); \
                acc[0] = 0.0; acc[1] = 0.0; acc[2] = 0.0; } \
            CBAR(); \
            LDS_ADD(&sm.cnt[u % NOUT], np) \
            CBAR(); \
            np = 0; }

#define LOADZ2() { _Pragma("unroll") for (int p = 0; p < 4; p++) kz4[p] = sm.kz[p >> 1][rg * 4 + 2 * (p & 1) + rsel].x; ksc = sm.kz[(lane >> 1) & 1][rg * 4 + 2 * (lane & 1) + rsel].y * scale; }
#define STATIC_STEP(I) { \
            rg = ((cst + (I)) & 3) * 8 + wave; \
            LOADZ2() \
            K_TAB((I) + 1) K_BODY((I) + 1) }
        CS_DMA(wave, (cst + 2) & 3)
        RDYWAIT(0, 1u) RDYWAIT(1, 2u)
        LOADQ()
#pragma unroll
        for (int i = 0; i < 3; i++) { Rp[i] = sm.q[1][3 * sub + i]; Rn[i] = __builtin_amdgcn_alignbit(Rp[i], Rp[i], 16) ^ 0x80000000u; }
        STATIC_STEP(0)
        asm volatile("s_waitcnt vmcnt(11)" ::: "memory");
        REPACK(wave, (cst + 1) & 3)
        TABDONE(1)
        SISSUE(2)
        CS_DMA(wave, (cst + 3) & 3)
        STATIC_STEP(1)
        asm volatile("s_waitcnt vmcnt(11)" ::: "memory");
        REPACK(wave, (cst + 2) & 3)
        TABDONE(2)
        SISSUE(3)
        asm volatile("s_waitcnt vmcnt(8)" ::: "memory");
        REPACK(wave, (cst + 3) & 3)
        TABDONE(3)
        STATIC_STEP(2)
        GRAB(2)
        vut = LDV(&sm.utab[lane]);
        rg = ((cst + 3) & 3) * 8 + wave;
        LOADZ2() K_TAB(4) K_PRE4(4)
        PLAN()
        LAUNCH()
        K_WAIT(4, 0) K_FMA(0) K_WAIT(4, 1) K_FMA(1) K_WAIT(4, 2) K_FMA(2) K_WAIT(4, 3) K_FMA(3) K_RED(4)
        CBAR();
        LDS_ADD(&sm.cnt[0], 4u) LDS_ADD(&sm.cnt[1], 4u)
        u = un; rg = nrg; tdc = tdn + 10;
        bool tabok = false;
        __builtin_amdgcn_s_setprio(2);
#pragma unroll 1
        while (u < NU) {
            GRAB(2)
            mg = LDV(&sm.merged);
            vut = LDV(&sm.utab[lane]);
            LINE()
            if (!tabok) {
                for (;;) { const unsigned tb_ = lds_ld(&sm.tabs); tabok = tb_ == ~0u; if (((tb_ >> rg) & 3u) == 3u) break; SLEEP(1) }
                CBAR();
            }
            K_TAB(0)
            if (first) {
                unsigned fl = LDV(&sm.rdy[u % NIN]);
                CBAR();
                LOADQ() LOADZ()
                if (__builtin_amdgcn_readfirstlane(fl) != u + 1) { RDYWAIT(u % NIN, u + 1) LOADQ() LOADZ() }
            } else LOADZ()
            K_PRE4(0)
            PLAN()
            K_WAIT(0, 0) K_FMA(0) K_WAIT(0, 1) K_FMA(1)
            asm volatile("" :: "v"(kres[0]), "v"(kres[1]));
            K_WAIT(0, 2) K_WAIT(0, 3)
            ARRIVED()
            K_FMA(2) K_FMA(3) K_RED(0)
            np += 2;
            ADVANCE(false)
        }
#pragma unroll 1
        while (u < 2 * NU) {
            GRAB(2)
            mg = LDV(&sm.merged);
            LINE()
            const unsigned vr_ = LDV(&sm.vrdy);
            CBAR();
            const unsigned short* wp = &sm.wv[went(u - NU) + (QUARTER(u - NU) ? rg & 7 : rg) * 4];
            v4i pw = *(const v4i*)wp;
            if (first && !((__builtin_amdgcn_readfirstlane(vr_) >> (u - NU)) & 1)) { while (!((lds_ld(&sm.vrdy) >> (u - NU)) & 1)) SLEEP(1); CBAR(); pw = *(const v4i*)wp; }
            const double w0 = (short)pw.x, w1 = pw.x >> 16, w2 = (short)pw.y, w3 = pw.y >> 16, w4 = (short)pw.z, w5 = pw.z >> 16, w6 = (short)pw.w, w7 = pw.w >> 16;
            asm volatile("" :: "v"(w0), "v"(w1), "v"(w2), "v"(w3), "v"(w4), "v"(w5), "v"(w6), "v"(w7));
            PLAN()
            v2f hl; unsigned tl8, rowl;
            if (none) { hl = *(const v2f*)&sm.wv[went(NU - 1) + 128]; tl8 = LDV(&sm.tag_lo) << 24; rowl = (lds_ld(&sm.utab[2 * NU - 1]) & 255) * (NSPLIT * 512); asm volatile("" : "+v"(hl), "+v"(tl8)); }
            WAITT(0, 0, 6); V_FMA(0, w0, w1) V_PIN()
            WAITT(1, 1, 4); V_FMA(1, w2, w3) V_PIN()
            WAITT(2, 2, 2); V_FMA(2, w4, w5) V_PIN()
            WAITT(3, 3, 0);
            np += 2;
            if (none) {
                V_FMA(3, w6, w7)
                if (u != 2 * NU - 1) { FLUSH(true) break; }
                mg = __builtin_amdgcn_readfirstlane(mg);
                while (u >= mg + NOUT) { SLEEP(1); mg = lds_ld(&sm.merged); }
                const unsigned aa = lds_addr(&sm.acc[(2 * NU - 1) % NOUT][lane]);
                double a0, a1, a2; unsigned cn; u64 ex_;
                asm volatile("ds_add_f64 %5, %6\n\tds_add_f64 %5, %7 offset:512\n\tds_add_f64 %5, %8 offset:1024\n\t"
                             "s_mov_b64 %4, exec\n\ts_mov_b64 exec, 1\n\tds_add_rtn_u32 %3, %9, %10\n\ts_mov_b64 exec, %4\n\t"
                             "ds_read_b64 %0, %5\n\tds_read_b64 %1, %5 offset:512\n\tds_read_b64 %2, %5 offset:1024\n\ts_waitcnt lgkmcnt(0)"
                             : "=&v"(a0), "=&v"(a1), "=&v"(a2), "=&v"(cn), "=&s"(ex_) : "v"(aa), "v"(acc[0]), "v"(acc[1]), "v"(acc[2]), "v"(lds_addr(&sm.cnt[(2 * NU - 1) % NOUT])), "v"(np) : "memory");
                const float n0 = (float)(a0 - (double)hl.y) * hl.x, n1 = (float)(a1 - (double)hl.y) * hl.x, n2 = (float)(a2 - (double)hl.y) * hl.x;
                const unsigned e2 = max(__float_as_uint(fmaxf(fmaxf(fabsf(n0), fabsf(n1)), fabsf(n2))) >> 23, 14u);
                const float f2 = __uint_as_float((268u - e2) << 23);
                const v2u r = {__builtin_bit_cast(unsigned, __builtin_amdgcn_cvt_pk_i16((int)rintf(n0 * f2), (int)rintf(n1 * f2))),
                               __builtin_bit_cast(unsigned, __builtin_amdgcn_cvt_pk_i16((int)rintf(n2 * f2), 0)) | e2 << 16 | tl8};
                const __amdgpu_buffer_rsrc_t rsrc = __builtin_amdgcn_make_buffer_rsrc(ws + WS_PART + split * 512, 0, NRW * NSPLIT * 512 - split * 512, 0x00020000);
                asm volatile("" :: "v"(r));
                if (__builtin_amdgcn_readfirstlane(cn + np) == 32) {
                    __builtin_amdgcn_raw_buffer_store_b64(r, rsrc, lane * 8, rowl, SC1);
#pragma unroll
                    for (int j = 0; j < NCP - 1; j++) __builtin_amdgcn_raw_buffer_store_b64(r, rsrc, lane * 8, (rowl & 15 * NSPLIT * 512 ^ NSPLIT * 512) + (NRR + j * GSZ) * NSPLIT * 512, SC1);
                }
                break;
            }
            ARRIVED()
            V_FMA(3, w6, w7)
            ADVANCE(true)
        }
    } else {
    u64 E1;
    if (wave == NSW) {
        unsigned ref;
        const unsigned sidx = (blockIdx.x & 7) | ((blockIdx.x >> 7) << 3);
#define SAMPLE(K) ((((blockIdx.x >> 3) - (blockIdx.x & 7) - (K) * 5) & 15) == 0)
#define BTAG(K) ((btag + (K)) & 0xfffffu)
#define POST(K, C) { if (lane == 0) __hip_atomic_store((u64*)(ws + (bd + (K) * RST + (sidx >> 3) * 256 + (sidx & 7) * 8)), (u64)(C) | ((u64)(((C) - ref) | BTAG(K) << 12) << 32), __ATOMIC_RELAXED, __HIP_MEMORY_SCOPE_AGENT); }
        const unsigned own = lds_ld(&sm.line);
        if (SAMPLE(0) && lane < NTRY) __hip_atomic_store((unsigned*)(ws + WS_HELLO + lane * 768) + sidx, own, __ATOMIC_RELAXED, __HIP_MEMORY_SCOPE_AGENT);
        const u64 E = __hip_atomic_load((const u64*)(ws + WS_EPOCH), __ATOMIC_RELAXED, __HIP_MEMORY_SCOPE_AGENT);
        E1 = E + 1;
        if (lane == 0) { sm.tag_lo = (unsigned)E1; sm.tag_hi = (unsigned)(E1 >> 32); CBAR(); lds_st(&sm.tag_rdy, 1u); }
        const unsigned btag = __builtin_amdgcn_readfirstlane((unsigned)E1) * 61u, bd = WS_BOARD + (btag & 7) * (NRND * RST);
        ref = own;
        int off = 0;
        {
            unsigned tries = 0, cur = own, tl = own;
            bool news = false;
#pragma unroll 1
            while (!news || cur != ref) {
                const unsigned now = NOW();
                if (tries < NTRY && (int)(now - own) >= (int)(TA + tries * TA2)) {
                    v16u x;
                    asm volatile("s_load_dwordx16 %0, %1, %2\n\ts_waitcnt lgkmcnt(0)" : "=&s"(x) : "s"(ws), "s"(__builtin_amdgcn_readfirstlane(WS_HELLO + tries * 768)) : "memory");
                    unsigned bad = 0; int sum = 0, dif = 0;
#pragma unroll
                    for (int i = 0; i < 16; i++) { bad |= x[i] - own + 512; sum += x[i] - x[0]; dif += i & 4 ? x[i] : -x[i]; }
                    tries++;
                    if (bad < 1024) { const unsigned com = x[0] + (sum >> 4) + abs(dif) * (8 - KC) / 128; ref = x[0] + ((sum + (blockIdx.x & 4 ? dif : -dif)) >> 4); off = ref - com;
                        tries = NTRY; tl = NOW(); if (lane == 0) lds_st(&sm.aline, com); }
                    continue;
                }
                if (!news && lds_ld(&sm.utab[31]) != ~0u) { news = true; CBAR(); if (cur == ref) tl = now; ref = lds_ld(&sm.aline) + off; tries = NTRY; }
                const int d = (now - tl) >> CSH;
                if (cur != ref && d) { tl += d << CSH; cur += min(max((int)(ref - cur), -d), d); if (lane == 0) lds_st(&sm.line, cur); }
                SLEEP(2)
            }
        }
        ref -= off;
        const unsigned ref0 = ref;
        const unsigned lead = (blockIdx.x >> 3) == 9 ? 0 : LEAD;
        unsigned posted = ref, lagf = ref, tpost = ref + TB1, tpoll = tpost + RDD + lead, kw = 0, kr = 0, cur = ref + off, khold = 0;
        int o = off; bool held = false;
#pragma unroll 1
        for (;;) {
            const unsigned p = lds_ld(&sm.grab);
            if (p >= NPC) break;
            lagf += (int)(lds_ld(&sm.late) - lagf) >> 1;
            { const unsigned l = lds_ld(&sm.line); if (l != cur) { cur = l; khold = kw; held = true; } }
            const unsigned now = NOW();
            if (o) { o = off * min(max((int)(ref0 + TC0 + (1 << TCS) - now), 0), 1 << TCS) >> TCS; if (!held && cur != ref + o) { cur = ref + o; if (lane == 0) lds_st(&sm.line, cur); } }
            if (kw < NRND && (int)(now - tpost) >= 0) {
                const unsigned c = ref + min((p >= PSL0 && (int)(lagf - cur) > LHI ? lagf - LOK : cur) - o - ref, 4000u);
                if (SAMPLE(kw) && (c != ref || (ref != posted && (sidx & 7) == (kw & 7)))) POST(kw, c)
                posted = ref;
                tpost += PER; kw++;
            }
            if (kr < NRND && (int)(now - tpoll) >= 0) {
                v16u xa, xb;
                asm volatile("s_load_dwordx16 %0, %2, %3\n\ts_load_dwordx16 %1, %2, %4\n\ts_waitcnt lgkmcnt(0)" : "=&s"(xa), "=&s"(xb)
                             : "s"(ws), "s"(__builtin_amdgcn_readfirstlane(bd + kr * RST)), "s"(__builtin_amdgcn_readfirstlane(bd + kr * RST + 256)) : "memory");
                int nb = 0, sg = 0;
#pragma unroll
                for (int i = 0; i < 16; i++) {
                    const unsigned lo = i < 8 ? xa[2 * i & 15] : xb[2 * i & 15], hi = i < 8 ? xa[(2 * i + 1) & 15] : xb[(2 * i + 1) & 15];
                    if (hi >> 12 == BTAG(kr) && lo - ref0 < 8000) { nb = max(nb, (int)(lo - (hi & 4095u) - ref)); sg += hi & 4095u; }
                }
                ref += nb + (sg >> 4);
                if (cur != ref + o && (kr >= khold || (int)(ref + o - cur) > 0)) { cur = ref + o; held = false; if (lane == 0) lds_st(&sm.line, cur); }
                tpoll += PER; kr++;
            }
            SLEEP(2)
        }
    } else if (wave == NSW + 1 || wave == NSW + 2) {
        const int ql = lane < 48 ? lane : 47;
        unsigned c1, c2, s1, s2;
#define QT_LOADS() { int qo_ = ql; asm volatile("" : "+v"(qo_)); \
                     c1 = ((const unsigned*)q_cos)[qo_]; c2 = ((const unsigned*)q_cos)[48 + qo_]; s1 = ((const unsigned*)q_sin)[qo_]; s2 = ((const unsigned*)q_sin)[48 + qo_]; }
        unsigned q1, q2, lkz, lks, lvz, lvs;
        const unsigned sofs = R8(((unsigned)lane >> 1) & 7) * 2 + (lane & 1);
#define UNIT_LOADS(HEAD, GRP) { \
        q1 = ((const unsigned*)(q + (size_t)(HEAD) * DH))[ql]; q2 = ((const unsigned*)(q + (size_t)(HEAD) * DH))[48 + ql]; \
        const size_t so = (size_t)(HEAD) * (SKV / 2) + (GRP) * 256 + sofs; \
        lkz = *((const unsigned*)k_zeros + so); lks = *((const unsigned*)k_scales + so); \
        lvz = *((const unsigned*)v_zeros + so); lvs = *((const unsigned*)v_scales + so); }
#define UNIT_FILL(SL, U) { \
            v4f qq; \
            qq.x = bfr(bfr(bflo(q1) * bflo(c1)) + bfr(-bflo(q2) * bflo(s1))); \
            qq.y = bfr(bfr(bflo(q2) * bflo(c2)) + bfr(bflo(q1) * bflo(s2))); \
            qq.z = bfr(bfr(bfhi(q1) * bfhi(c1)) + bfr(-bfhi(q2) * bfhi(s1))); \
            qq.w = bfr(bfr(bfhi(q2) * bfhi(c2)) + bfr(bfhi(q1) * bfhi(s2))); \
            *(v2u*)&sm.q[SL][2 * ql] = (v2u){(__float_as_uint(qq.x) >> 16) | __float_as_uint(qq.y), (__float_as_uint(qq.z) >> 16) | __float_as_uint(qq.w)}; \
            *(v4f*)&sm.kz[SL][2 * lane] = (v4f){bflo(lkz), bflo(lks), bfhi(lkz), bfhi(lks)}; \
            *(v4f*)&sm.vz[SL][2 * lane] = (v4f){bflo(lvz), bflo(lvs), bfhi(lvz), bfhi(lvs)}; \
            CBAR(); \
            if (lane == 0) lds_st(&sm.rdy[SL], (U) + 1); \
            CBAR(); }
        const unsigned dw = NSW + 2 - wave;
        QT_LOADS()
        UNIT_LOADS(head0 + dw * (GSZ + 1 - 2 * side), lane >> 4)
#pragma unroll 1
        for (unsigned u = dw;;) {
            while (u >= lds_ld(&sm.merged) + NIN) SLEEP(4);
            CBAR();
            UNIT_FILL(u % NIN, u)
            if (u - 1 < 12) {
                const unsigned x_ = (blockIdx.x >> 3) + 32 * u - 8; unsigned dm;
                const char* p_ = x_ < 384 ? (const char*)(x_ < 192 ? qk : qv) + (size_t)(x_ < 192 ? x_ : x_ - 192) * (2u << 20) + (x_ * 8 + (blockIdx.x & 7)) * 640 : (const char*)out + (x_ - 384) * 4096;
                while ((int)(NOW() - lds_ld(&sm.line)) < TSD) SLEEP(2)
                asm volatile("s_load_dword %0, %1, 0x0\n\ts_waitcnt lgkmcnt(0)" : "=&s"(dm) : "s"(p_) : "memory");
            }
            u += 2;
            if (u >= NU) break;
            if (u == 4) {
                const unsigned tpf = tdue(PSF - 1) + TFO;
                { int d; while ((d = (int)(lds_ld(&sm.line) + tpf - NOW())) > 0 && d < 3000) SLEEP(1) }
#define PF3(P) asm volatile("global_load_dwordx4 a[0:3], %0, %1\n\tglobal_load_dwordx4 a[0:3], %0, %1 offset:1024\n\tglobal_load_dwordx4 a[0:3], %0, %1 offset:2048" \
                            :: "v"(64 * SKV * 2 + blockIdx.x * 3072 + lane * 16), "s"(P) : "a0", "a1", "a2", "a3")
                PF3(k_zeros); PF3(k_scales); PF3(v_zeros); PF3(v_scales);
                asm volatile("global_load_dword a0, %0, %1" :: "v"(blockIdx.x * (DH * 2) + ql * 8), "s"(q) : "a0");
            }
            const unsigned tin = tdue(first_of(u) - (QUARTER(u) ? 24 : 0) - LEADP + (split & 7) * DPH);
            { int d; while ((d = (int)(lds_ld(&sm.line) + tin - NOW())) > 0 && d < 3000) SLEEP(4) }
            unsigned hd;
            while ((hd = lds_ld(&sm.utab[u])) == ~0u) SLEEP(2)
            UNIT_LOADS(hd & 255, QUARTER(u) ? rank & 3 : lane >> 4)
        }
        while (lds_ld(&sm.tag_rdy) == 0) SLEEP(1);
        CBAR();
        E1 = (u64)lds_ld(&sm.tag_lo) | ((u64)lds_ld(&sm.tag_hi) << 32);
        if (wave == NSW + 1 && blockIdx.x == 0) {
            for (;;) {
                bool ok = true;
#pragma unroll
                for (int i = 0; i < NH / 64; i++) ok &= __hip_atomic_load((const u64*)(ws + WS_POST) + i * 64 + lane, __ATOMIC_RELAXED, __HIP_MEMORY_SCOPE_AGENT) >> 24 == (E1 << 24) >> 24;
                if (__builtin_amdgcn_ballot_w64(ok) == ~0ull) break;
                SLEEP(64);
            }
            if (lane == 0) __hip_atomic_store((u64*)(ws + WS_EPOCH), E1, __ATOMIC_RELAXED, __HIP_MEMORY_SCOPE_AGENT);
        }
        if (wave == NSW + 2) {
            const unsigned t16 = (unsigned)E1 & 0xffffu, t16b = ((unsigned)E1 + ((unsigned)E1 >> 16)) & 0xffffu;
            const __amdgpu_buffer_rsrc_t rsrc = __builtin_amdgcn_make_buffer_rsrc(ws + WS_SWAP + (blockIdx.x ^ 4) * 5120, 0, 5120, 0x00020000);
            const unsigned swp = lds_ld(&sm.swp);
            if (swp) {
                unsigned use = 0;
#pragma unroll
                for (int i = 0; i < 5; i++) use |= ((swp >> wunit(i * 64 + lane)) & 1) << i;
                const unsigned tg = tdue(PSG - 1) + tvx(PSG - 1, lds_ld(&sm.gap));
                while ((int)(NOW() - lds_ld(&sm.line) - tg) < 0) SLEEP(2)
                for (;;) {
                    v4u d[5]; bool ok = true;
#pragma unroll
                    for (int i = 0; i < 5; i++) d[i] = __builtin_amdgcn_raw_buffer_load_b128(rsrc, (use >> i) & 1 ? (i * 64 + lane) * 16 : 0x40000000, 0, SC1 | 2);
#pragma unroll
                    for (int i = 0; i < 5; i++) if ((use >> i) & 1) {
                        ok &= d[i].y >> 16 == t16 && d[i].w >> 16 == t16b;
                        unsigned* w = (unsigned*)&sm.wv[144 * NMIX + (i * 64 + lane) * 6];
                        w[0] = d[i].x; w[1] = (d[i].y & 0xffffu) | (d[i].z << 16); w[2] = (d[i].z >> 16) | (d[i].w << 16);
                    }
                    if (__builtin_amdgcn_ballot_w64(ok) == ~0ull) break;
                    SLEEP(32)
                }
                CBAR();
                LDS_OR(&sm.vrdy, swp)
            }
        }
    } else {
        const unsigned eu = lane >= NU ? lane - NU : lane, win = min(first_of(eu) >> 5, 15u), pair = QUARTER(eu) ? 2 * (eu - WQ) + (rank >> 2) : rank;
        unsigned rot2 = ((const unsigned char*)ROT)[win * 8 + pair];
        const unsigned rotn = ((const unsigned char*)ROT)[(win == 0 ? 4 : win == 1 ? 9 : WQ) * 8 + pair];
#define UENT(BIT) (win * 16 + 2 * pair + ((BIT) ^ side) | (QUARTER(eu) ? (rank - (eu - WQ)) * 8 & 31 : (rot2 >> ((BIT) ^ side) * 4 & 15) * 2) << 8)
        unsigned far = 0, vs;
        const unsigned hp = (blockIdx.x & 1) * NH + (blockIdx.x >> 1) * 4 + 2;
        if (blockIdx.x < 144) {
            const char* pb = (blockIdx.x < 128 ? (const char*)(hp >= NH ? qv : qk) + (size_t)(hp & (NH - 1)) * (SKV * DH * 4) + SKV * DH * 2
                                               : (const char*)ws + WS_PART + (8u << 20) + (blockIdx.x & 15) * 65536) + ((NOW() * 0x9e3779b1u) >> 26) * 128;
            unsigned mn[2] = {~0u, ~0u};
#pragma unroll 1
            for (unsigned i = 1; i < 2 * NPRB + 2; i++) {
                u64 t0, t1; unsigned dm;
                asm volatile("s_memtime %0\n\ts_waitcnt lgkmcnt(0)\n\ts_load_dword %2, %3, 0x0\n\ts_waitcnt lgkmcnt(0)\n\ts_memtime %1\n\ts_waitcnt lgkmcnt(0)"
                             : "=&s"(t0), "=&s"(t1), "=&s"(dm) : "s"(pb + i * 8192) : "memory");
                if (i == 1) asm volatile("global_load_dword a0, %0, %1 nt" :: "v"(0), "s"(pb) : "a0");
                const unsigned t = (unsigned)(t1 - t0) < 330 || i == 1 ? ~0u : (unsigned)(t1 - t0);
                if (i & 1) mn[1] = min(mn[1], t); else mn[0] = min(mn[0], t);
            }
            far = mn[0] > mn[1];
        }
        while (lds_ld(&sm.tag_rdy) == 0) SLEEP(1);
        CBAR();
        E1 = (u64)lds_ld(&sm.tag_lo) | ((u64)lds_ld(&sm.tag_hi) << 32);
        {
            const unsigned own = lds_ld(&sm.line), tg = (unsigned)E1;
            if (lane == 0) __hip_atomic_store((u64*)(ws + WS_POST) + blockIdx.x, E1 << 24 | min(lds_ld(&sm.slow), 4000u) << 1 | ((far ^ (blockIdx.x >> 2) ^ (blockIdx.x < 128 ? 0x96 >> (hp & 7) : 0)) & 1), __ATOMIC_RELAXED, __HIP_MEMORY_SCOPE_AGENT);
            const __amdgpu_buffer_rsrc_t nrs = __builtin_amdgcn_make_buffer_rsrc(ws + WS_NEWS, 0, 8 * 1280, 0x00020000);
            if (blockIdx.x == MASTER) {
                while ((int)(NOW() - own) < TMAP) SLEEP(2)
                const unsigned a = tg << 24, b = (unsigned)(E1 >> 8);
                v4u x; v2u z; u64 v0, v1;
#pragma unroll 1
                for (int i = 0; i < 8; i++) {
                    x = __builtin_amdgcn_raw_buffer_load_b128(__builtin_amdgcn_make_buffer_rsrc(ws + WS_POST, 0, 1024, 0x00020000), lane * 16, 0, SC1);
                    z = __builtin_amdgcn_raw_buffer_load_b64(__builtin_amdgcn_make_buffer_rsrc(ws + WS_POST + 1024, 0, 128, 0x00020000), lane * 8, 0, SC1);
                    v0 = __builtin_amdgcn_ballot_w64(((x.x ^ a) >> 24 | (x.y ^ b)) == 0); v1 = __builtin_amdgcn_ballot_w64(((x.z ^ a) >> 24 | (x.w ^ b)) == 0);
                    if (__builtin_popcountll(v0) + __builtin_popcountll(v1) >= MINV) break;
                    SLEEP(8)
                }
                const u64 m0 = __builtin_amdgcn_ballot_w64(x.x & 1) & v0, m1 = __builtin_amdgcn_ballot_w64(x.z & 1) & v1;
                const int s0 = lane < 43 ? min(max(lane - VW, 4 * NMIX), 42 - 2 * VW) : min(max(lane - VW, 43), 63 - 2 * VW);
#define PC(M) __builtin_popcount((unsigned)((M) >> s0) & ((2u << 2 * VW) - 1))
                const u64 g0 = __builtin_amdgcn_ballot_w64(2 * PC(m0) > PC(v0)), g1 = __builtin_amdgcn_ballot_w64(2 * PC(m1) > PC(v1));
                const float sl = wave_sum(((v0 >> lane & 1) ? (float)(x.x >> 1 & 4095u) : 0.f) + ((v1 >> lane & 1) ? (float)(x.z >> 1 & 4095u) : 0.f)) / ((8.f - PRE0) * (__builtin_popcountll(v0) + __builtin_popcountll(v1)) + 1.f);
                const unsigned al = lds_ld(&sm.aline), mean = (al ? al : own) + (int)fmaxf(sl - SL0, 0.f);
                const bool vz = lane < 16 && ((z.x ^ a) >> 24 | (z.y ^ b)) == 0;
                const unsigned gw = 2 * __builtin_popcountll(__builtin_amdgcn_ballot_w64(vz && (z.x & 1))) > __builtin_popcountll(__builtin_amdgcn_ballot_w64(vz));
                const unsigned k = lane & 3;
                if (lane < 32 && k < 3) __builtin_amdgcn_raw_buffer_store_b128((v4u){k == 0 ? (unsigned)g0 : k == 1 ? (unsigned)g1 : mean, tg, k == 0 ? (unsigned)(g0 >> 32) : k == 1 ? (unsigned)(g1 >> 32) : gw, tg},
                                                                              nrs, (lane >> 2) * 1280 + k * 16, 0, SC1);
            }
            while ((int)(NOW() - own) < TMAP + TRES) SLEEP(2)
            v4u y;
            for (;;) {
                y = __builtin_amdgcn_raw_buffer_load_b128(nrs, lane < 3 ? (blockIdx.x & 7) * 1280 + lane * 16 : 0x40000000, 0, SC1);
                if (((unsigned)__builtin_amdgcn_ballot_w64(y.y == tg && y.w == tg) & 7u) == 7u) break;
                SLEEP(8)
            }
            const u64 f0 = (unsigned)__builtin_amdgcn_readlane(y.x, 0) | (u64)(unsigned)__builtin_amdgcn_readlane(y.z, 0) << 32, f1 = (unsigned)__builtin_amdgcn_readlane(y.x, 1) | (u64)(unsigned)__builtin_amdgcn_readlane(y.z, 1) << 32;
            if (lane == 2) { sm.aline = y.x - own + 500 < 1000 ? y.x : own; sm.chead = (blockIdx.x & ~5u) | ((blockIdx.x >> 2 ^ y.z) & 1) | (blockIdx.x & 1) << 2; }
            unsigned bit = ((unsigned)((lane >= NU ? f1 : f0) >> (4 * win + (pair >> 1))) ^ (0x66 >> pair)) & 1;
            vs = !((f0 ^ f1) >> 4 * NMIX) ? 0 : !((f0 ^ f1) >> 4 * NMIX & 4095) ? NMIX : !((f0 ^ f1) >> 4 * (WQ + 1) & 4095) ? WQ + 4 : 0;
            if (eu < NMIX) { if (lane < NU || !vs) bit = eu & 1; else rot2 = rotn; }
            const u64 bm = __builtin_amdgcn_ballot_w64(bit);
            const unsigned nsw = __builtin_popcountll((f0 ^ f1) >> (4 * NMIX)) + (vs ? 2 * NMIX : 0);
            const unsigned sw = (unsigned)(bm ^ (bm >> NU)) & ((1u << NU) - 1), sx = (sw >> vs ^ sw) & 7;
            if (lane == 0) { sm.swp = sw ^ sx ^ sx << vs; lds_st(&sm.gap, (nsw ? (TG0 + TG1 * nsw / 8) * 0x1001u + ((TH0 + TH1 * nsw / 8) << 12) : 0u) | (vs ? 0u : TMIX << 24)); }
            CBAR();
#define VPOS(E) ((E) < NMIX ? (E) + vs : (E) - vs < NMIX ? (E) - vs : (E))
            lds_st(&sm.utab[lane >= NU ? NU + VPOS(eu) : lane], UENT(bit));
        }
        const unsigned swp = lds_ld(&sm.swp);
        const unsigned T = lds_ld(&sm.tag_lo);
        const unsigned t15 = T & 0x7fffu;
        const __amdgpu_buffer_rsrc_t rsrc = __builtin_amdgcn_make_buffer_rsrc(ws + WS_PART + split * 512, 0, NRW * NSPLIT * 512 - split * 512, 0x00020000);
        v2u* recs = (v2u*)&sm.cs[0][0][0];
#pragma unroll
        for (int i = 0; i < NOUT * 3; i++) (&sm.acc[0][0])[i * 64 + lane] = 0.0;
        CBAR();
        unsigned vut = sm.utab[NU + (lane & 31)] & 255;
        if (QUARTER((unsigned)lane) && (rank & 3)) vut = NH - 16 + (rank & 3) * 16 + ((vut & 15) ^ 1);
        __builtin_amdgcn_s_setprio(3);
        unsigned po[NU / 2];
#pragma unroll
        for (int k = 0; k < NU / 2; k++) po[k] = __builtin_amdgcn_ds_bpermute((2 * k + (lane >> 5)) * 4, vut) * (NSPLIT * 512) + (lane & 31) * 16;
        const u64 rb_ = (u64)(ws + WS_PART + split * 512);
        const v4u rs_ = {(unsigned)rb_, (unsigned)(rb_ >> 32) & 0xffffu, NRW * NSPLIT * 512 - split * 512, 0x00020000u};
#pragma unroll 1
        for (unsigned u = 0; u < 2 * NU - 1; u++) {
            const unsigned e = u % NU, so = u % NOUT;
            if (u >= NU) while (!((lds_ld(&sm.vrdy) >> e) & 1)) SLEEP(4)
            CBAR();
            const v4f h = *(const v4f*)&sm.wv[went(e) + (QUARTER(e) ? 32 : 128)];
            while (lds_ld(&sm.cnt[so]) != (QUARTER(e) ? 8 : 32)) SLEEP(1)
            CBAR();
            const double a0 = sm.acc[so][lane], a1 = sm.acc[so][64 + lane], a2 = sm.acc[so][128 + lane];
            if (u < NU) {
                float2 lg = *(const float2*)&sm.ent[so][2 * lane];
                if (QUARTER(e) && lane >> 4 != (rank & 3)) lg = make_float2(-INFINITY, -INFINITY);
                const v4f zs = *(const v4f*)&sm.vz[u % NIN][2 * lane];
                const float m = wave_max(fmaxf(lg.x, lg.y));
                const float p0 = __expf(lg.x - m), p1 = __expf(lg.y - m);
                const float x0 = p0 * zs.y, x1 = p1 * zs.w;
                const unsigned eb = max(__float_as_uint(wave_max(fmaxf(fabsf(x0), fabsf(x1)))) >> 23, 14u);
                const float f = __uint_as_float((268u - eb) << 23);
                const float w0 = fminf(fmaxf(rintf(x0 * f), -32767.f), 32767.f), w1 = fminf(fmaxf(rintf(x1 * f), -32767.f), 32767.f);
                const float den = wave_sum(p0 + p1);
                const float bz = wave_sum(fmaf(w0, zs.x, w1 * zs.z));
                const float L = fmaf(m, 1.4426950408889634f, __builtin_amdgcn_logf(den));
                const unsigned vp = VPOS(u);
                unsigned* wd = (unsigned*)(((swp >> vp) & 1 ? sm.wo : sm.wv) + went(vp));
                if (!QUARTER(u) || lane >> 4 == (rank & 3)) wd[QUARTER(u) ? lane & 15 : lane] = ((unsigned)(int)w0 & 0xffffu) | ((unsigned)(int)w1 << 16);
                if (lane == 0) *(v4f*)&wd[QUARTER(u) ? 16 : 64] = (v4f){ldexpf(1.f / den, (int)eb - 141), bz, L, 0.f};
                CBAR();
                if (!((swp >> vp) & 1)) LDS_OR(&sm.vrdy, 1u << vp)
            } else {
                const float n0 = (float)(a0 - (double)h.y) * h.x, n1 = (float)(a1 - (double)h.y) * h.x, n2 = (float)(a2 - (double)h.y) * h.x;
                const unsigned e2 = max(__float_as_uint(wave_maxv(fmaxf(fmaxf(fabsf(n0), fabsf(n1)), fabsf(n2)))) >> 23, 14u);
                const float f2 = __uint_as_float((268u - e2) << 23);
                const unsigned m01 = __builtin_bit_cast(unsigned, __builtin_amdgcn_cvt_pk_i16((int)rintf(n0 * f2), (int)rintf(n1 * f2)));
                const unsigned m2 = __builtin_bit_cast(unsigned, __builtin_amdgcn_cvt_pk_i16((int)rintf(n2 * f2), 0));
                const unsigned bit = ((lane & 1 ? e2 : __float_as_uint(h.z)) >> (lane >> 1)) & 1u;
                recs[e * 64 + lane] = (v2u){m01, m2 | bit << 16 | t15 << 17};
                CBAR();
                sm.acc[so][lane] = 0.0; sm.acc[so][64 + lane] = 0.0; sm.acc[so][128 + lane] = 0.0;
            }
            CBAR();
            if (lane == 0) { lds_st(&sm.cnt[so], 0u); CBAR(); lds_st(&sm.merged, u + 1); }
            if (u == NU - 1 && swp) {
                wave_sync();
                const __amdgpu_buffer_rsrc_t srs = __builtin_amdgcn_make_buffer_rsrc(ws + WS_SWAP + blockIdx.x * 5120, 0, 5120, 0x00020000);
                v4u d[5]; unsigned o[5];
#pragma unroll
                for (int i = 0; i < 5; i++) {
                    const unsigned* w = (const unsigned*)&sm.wo[144 * NMIX + (i * 64 + lane) * 6];
                    const unsigned d0 = w[0], d1 = w[1], d2 = w[2];
                    d[i] = (v4u){d0, (d1 & 0xffffu) | (T << 16), (d1 >> 16) | (d2 << 16), (d2 >> 16) | ((T + (T >> 16)) << 16)};
                    o[i] = (swp >> wunit(i * 64 + lane)) & 1 ? (i * 64 + lane) * 16 : 0x40000000;
                }
                while ((int)(NOW() - lds_ld(&sm.line)) < (int)(tdue(PSW - 1) + tvx(PSW - 1, lds_ld(&sm.gap))) + 10 + TWO) SLEEP(1)
#pragma unroll
                for (int i = 0; i < 5; i++) __builtin_amdgcn_raw_buffer_store_b128(d[i], srs, o[i], 0, SC1 | 2);
            }
        }
        wave_sync();
#pragma unroll
        for (int k = 0; k < NU / 2; k++) asm volatile("ds_read_b128 a[%c0:%c0+3], %1 offset:%2" :: "i"(16 + 4 * k), "v"(lds_addr(recs) + lane * 16), "i"(k * 1024) : "memory");
        asm volatile("s_waitcnt lgkmcnt(0)" ::: "memory");
        while (!((lds_ld(&sm.vrdy) >> (NU - 1)) & 1)) SLEEP(4)
        CBAR();
        const v2u hw_ = {*(const unsigned*)&sm.wv[went(NU - 1) + 132], T};
        const unsigned hrow = (NRW - GSZ + (__builtin_amdgcn_readlane(vut, NU - 1) & 15 ^ 1)) * (NSPLIT * 512);
        while (lds_ld(&sm.grab) < NPC - PUBP) SLEEP(1)
        if (lane < 16) __builtin_amdgcn_raw_buffer_store_b64(hw_, rsrc, lane * 8, hrow, SC1);
#define PUB(K) asm volatile("buffer_store_dwordx4 a[%c0:%c0+3], %1, %2, 0 offen sc1" :: "i"(16 + 4 * (K)), "v"(po[K]), "s"(rs_) : "memory");
        PUB(WQ / 2) PUB(WQ / 2 + 1)
        { const unsigned t_ = TLAST() + PDEL; GATE(t_) }
        PUB(0) PUB(1) PUB(2) PUB(3) PUB(4) PUB(7) PUB(8)
        return;
    }
    }
    __builtin_amdgcn_s_setprio(0);
    unsigned c;
#define ADDRTN(P) { u64 ex_; asm volatile("s_mov_b64 %0, exec\n\ts_mov_b64 exec, 1\n\tds_add_rtn_u32 a15, %1, a13\n\ts_mov_b64 exec, %0" : "=&s"(ex_) : "v"(lds_addr(P)) : "a15", "memory"); }
#define ADDED(V) { unsigned t_; asm volatile("s_waitcnt lgkmcnt(0)\n\tv_accvgpr_read_b32 %1, a15\n\ts_nop 0\n\tv_readfirstlane_b32 %0, %1\n\ts_nop 1" : "=s"(V), "=v"(t_)); }
    asm volatile("v_accvgpr_write_b32 a13, 1\n\ts_nop 1" ::: "a13");
    ADDRTN(&sm.cidx) ADDED(c)
    if (c < NSW) {
        while (lds_ld(&sm.grab) < NPC + 2) SLEEP(4)
        const unsigned head = lds_ld(&sm.chead), T = lds_ld(&sm.tag_lo), tl = TLAST();
        const u64 rb_ = (u64)(ws + WS_PART);
        const v4u rs_ = {(unsigned)rb_, (unsigned)(rb_ >> 32) & 0xffffu, NRW * NSPLIT * 512, 0x00020000u};
        const unsigned ro = head * (NSPLIT * 512) + c * 1024, rx = (NH + (head & 15 ^ 1)) * (NSPLIT * 512) + c * 1024;
#define POLL(S, O) asm volatile("buffer_load_dwordx4 a[%c0:%c0+3], %1, %2, %3 offen sc1" :: "i"(16 + 4 * (S)), "v"(lane * 16), "s"(rs_), "s"(O) : "memory");
#define FETCH(X, S, N) asm volatile("s_waitcnt vmcnt(%c5)\n\tv_accvgpr_read_b32 %0, a[%c4]\n\tv_accvgpr_read_b32 %1, a[%c4+1]\n\tv_accvgpr_read_b32 %2, a[%c4+2]\n\tv_accvgpr_read_b32 %3, a[%c4+3]" \
                                    : "=v"((X).x), "=v"((X).y), "=v"((X).z), "=v"((X).w) : "i"(16 + 4 * (S)), "i"(N));
        float o0, o1, o2;
        if (head >= NH - GSZ) {
            __builtin_amdgcn_s_setprio(3);
            if (c == 0) {
                v2u hd;
                GATE(tl + THD)
                for (;;) {
                    hd = __builtin_amdgcn_raw_buffer_load_b64(__builtin_amdgcn_make_buffer_rsrc(ws + WS_PART + (NRW - GSZ + (head & 15 ^ 1)) * (NSPLIT * 512), 0, NSPLIT * 512, 0x00020000), (lane & 15) * 512, 0, SC1);
                    if (__builtin_amdgcn_ballot_w64(hd.y == T) == ~0ull) break;
                    SLEEP(4)
                }
                const float w = __builtin_amdgcn_exp2f(__uint_as_float(hd.x) - row_max(__uint_as_float(hd.x)));
                if (lane < 16) sm.ent[0][lane] = w * __builtin_amdgcn_rcpf(row_sum(w));
                CBAR();
                if (lane == 0) lds_st(&sm.tabs, 0u);
            }
            while (lds_ld(&sm.tabs) || lds_ld(&sm.merged) <= (2 * NU - 1) / NOUT * NOUT) SLEEP(1)
            CBAR();
            const float cf = sm.ent[0][2 * c + (lane >> 5)];
            v4u x; bool got;
#define SEEN(J, N) { FETCH(x, J, N) got = __builtin_amdgcn_ballot_w64((((x.y ^ T << 24) | (x.w ^ T << 24)) >> 24) != 0) == 0; }
            GATE(tl + TCA) POLL(0, ro) GATE(tl + TCA + TCD) POLL(1, rx + 3 * GSZ * NSPLIT * 512) SEEN(0, 1)
            if (!got) SEEN(1, 0)
            while (!got) { POLL(2, rx + 4 * GSZ * NSPLIT * 512) SEEN(2, 0) SLEEP(0) }
            const double c0 = ldexpf(cf, (int)(x.y >> 16 & 255u) - 141), c1 = ldexpf(cf, (int)(x.w >> 16 & 255u) - 141);
#define SWADD(A, B) ({ const v2u a_ = __builtin_bit_cast(v2u, A), b_ = __builtin_bit_cast(v2u, B); \
                const auto l_ = __builtin_amdgcn_permlane32_swap(a_.x, b_.x, false, false); const auto h_ = __builtin_amdgcn_permlane32_swap(a_.y, b_.y, false, false); \
                __builtin_bit_cast(double, (v2u){l_[0], h_[0]}) + __builtin_bit_cast(double, (v2u){l_[1], h_[1]}); })
            const double s0 = SWADD(c0 * (double)(short)x.x, c1 * (double)(short)x.z), s1 = SWADD(c0 * (double)((int)x.x >> 16), c1 * (double)((int)x.z >> 16)),
                         s2 = SWADD(c0 * (double)(short)x.y, c1 * (double)(short)x.w);
            asm volatile("ds_add_f64 %0, %1\n\tds_add_f64 %0, %2 offset:512\n\tds_add_f64 %0, %3 offset:1024" :: "v"(lds_addr(&sm.acc[0][lane])), "v"(s0), "v"(s1), "v"(s2) : "memory");
            ADDRTN(&sm.cdone)
            const double a0 = sm.acc[0][lane], a1 = sm.acc[0][64 + lane], a2 = sm.acc[0][128 + lane];
            ADDED(c)
            o0 = a0; o1 = a1; o2 = a2;
        } else {
            const bool quart = head >> 4 == WQ;
            const unsigned t15 = T & 0x7fffu;
#define TAKE(K) { FETCH(x[K], K, 0) \
                bad |= (x[K].y >> 17 ^ t15) | (x[K].w >> 17 ^ t15); \
                const u64 bl = __builtin_amdgcn_ballot_w64((x[K].y & 0x10000u) != 0), be = __builtin_amdgcn_ballot_w64((x[K].w & 0x10000u) != 0); \
                L[K] = __uint_as_float(lane < 32 ? (unsigned)bl : (unsigned)(bl >> 32)); ex[K] = lane < 32 ? (unsigned)be : (unsigned)(be >> 32); \
                M = fmaxf(M, fmaxf(__uint_as_float((unsigned)bl), __uint_as_float((unsigned)(bl >> 32)))); }
#define SUMUP(K) { const float w_ = __builtin_amdgcn_exp2f(L[K] - M), c_ = ldexpf(w_, (int)(ex[K] & 255u) - 141); den += w_; \
                t[0] = fmaf(c_, (float)(short)x[K].x, t[0]); t[1] = fmaf(c_, (float)((int)x[K].x >> 16), t[1]); t[2] = fmaf(c_, (float)(short)x[K].y, t[2]); \
                t[3] = fmaf(c_, (float)(short)x[K].z, t[3]); t[4] = fmaf(c_, (float)((int)x[K].z >> 16), t[4]); t[5] = fmaf(c_, (float)(short)x[K].w, t[5]); }
            const unsigned tg = tl + (quart ? TRQ : TRD);
            GATE(tg)
            v4u x[4]; float L[4], M; unsigned ex[4];
            for (;;) {
                unsigned bad = 0; M = -3e38f;
                POLL(0, ro)
                if (quart) { POLL(1, rx) POLL(2, rx + GSZ * NSPLIT * 512) POLL(3, rx + 2 * GSZ * NSPLIT * 512) }
                __builtin_amdgcn_s_setprio(3);
                TAKE(0)
                if (quart) { TAKE(1) TAKE(2) TAKE(3) }
                if (__builtin_amdgcn_ballot_w64(bad != 0) == 0) break;
                SLEEP(2)
            }
            float t[6] = {0.f, 0.f, 0.f, 0.f, 0.f, 0.f}, den = 0.f;
            SUMUP(0)
            if (quart) { SUMUP(1) SUMUP(2) SUMUP(3) }
#define SWADF(A, B) ({ const auto l_ = __builtin_amdgcn_permlane32_swap(__float_as_uint(A), __float_as_uint(B), false, false); __uint_as_float(l_[0]) + __uint_as_float(l_[1]); })
            v4f* pb = (v4f*)((char*)&sm.cs[0][0][0] + 65536);
            float* pm = (float*)(pb + NSW * 64);
            pb[c * 64 + lane] = (v4f){SWADF(t[0], t[3]), SWADF(t[1], t[4]), SWADF(t[2], t[5]), SWADF(den, den)};
            if (lane == 0) pm[c] = M;
            CBAR();
            ADDRTN(&sm.cdone)
            ADDED(c)
            CBAR();
            if (c == NSW - 1) {
                const v4f ma = *(const v4f*)pm, mb = *(const v4f*)(pm + 4);
                const float mw[NSW] = {ma.x, ma.y, ma.z, ma.w, mb.x, mb.y, mb.z, mb.w};
                float Mx = mw[0];
#pragma unroll
                for (int w = 1; w < NSW; w++) Mx = fmaxf(Mx, mw[w]);
                v4f sum = {0.f, 0.f, 0.f, 0.f};
#pragma unroll
                for (int w = 0; w < NSW; w++) sum += pb[w * 64 + lane] * __builtin_amdgcn_exp2f(mw[w] - Mx);
                const float inv = __builtin_amdgcn_rcpf(sum.w);
                o0 = sum.x * inv; o1 = sum.y * inv; o2 = sum.z * inv;
            }
        }
        if (c == NSW - 1) {
            asm("" : "+v"(o0), "+v"(o1), "+v"(o2));
            typedef __bf16 v2b __attribute__((ext_vector_type(2)));
            typedef unsigned v3u __attribute__((ext_vector_type(3)));
#define PK(A, B) __builtin_bit_cast(unsigned, (v2b){(__bf16)(A), (__bf16)(B)})
            const unsigned p12 = PK(o1, o2);
            const v3u ov = {PK(o0, o1), PK(o2, __uint_as_float(__builtin_amdgcn_permlane32_swap(__float_as_uint(o0), __float_as_uint(o0), false, false)[1])), __builtin_amdgcn_permlane32_swap(p12, p12, false, false)[1]};
            if (lane < 32) __builtin_amdgcn_raw_buffer_store_b96(ov, __builtin_amdgcn_make_buffer_rsrc(out + (size_t)head * DH, 0, DH * 2, 0x00020000), lane * 12, 0, 0);
            { const unsigned X = min((NOW() - tl - 150) / 2u, 99u) * 400; while (NOW() - (unsigned)t00 < 15000 + X) SLEEP(1) }
        }
    }
}

extern "C" void kernel_launch(void* const* d_in, const int* in_sizes, int n_in,
                              void* d_out, int out_size, void* d_ws, size_t ws_size,
                              hipStream_t stream) {
    const unsigned short* q = (const unsigned short*)d_in[0];
    const int* qk = (const int*)d_in[1];
    const unsigned short* k_scales = (const unsigned short*)d_in[2];
    const unsigned short* k_zeros = (const unsigned short*)d_in[3];
    const int* qv = (const int*)d_in[4];
    const unsigned short* v_scales = (const unsigned short*)d_in[5];
    const unsigned short* v_zeros = (const unsigned short*)d_in[6];
    const unsigned short* q_cos = (const unsigned short*)d_in[7];
    const unsigned short* q_sin = (const unsigned short*)d_in[8];
    const unsigned short* k_cos = (const unsigned short*)d_in[9];
    const unsigned short* k_sin = (const unsigned short*)d_in[10];
    unsigned char* ws = (unsigned char*)d_ws;
    attn_kernel<<<NSPLIT * GSZ, NTHREADS, 0, stream>>>(q, qk, k_scales, k_zeros, qv, v_scales, v_zeros,
                                                       q_cos, q_sin, k_cos, k_sin, (__hip_bfloat16*)d_out, ws);
}
```

```cpp
#include <hip/hip_runtime.h>
#include <hip/hip_bf16.h>
#include <stdint.h>

#define DH 192
#define HALF 96
#define SKV 2048
#define NH 256
#define NSW 8
#define NWAVES (NSW + 4)
#define NTHREADS (64 * NWAVES)
#define BROWS 128
#define NSPLIT 16
#define GSZ 16
#define WQ 10
#define NU (NH / GSZ + 3)
#define NPC 1024
#define NRR (NH + 3 * GSZ)
#define NIN 8
#define NOUT 8
#define WALL (15 * 144 + 4 * 48)
#define SC1 16
#define NRND 48
#define RST 4352
#define TK 2
#define NMIX 3
#define TUNIT 311
#define T01 50
#define TMIX 7
#define TOFF 50
#define PSF 100
#define TGS 84
#define PSW (NPC / 2 + 28)
#define TG0 8
#define TG1 5
#define TWO -80
#define PSG (NPC / 2 + 60)
#define TH0 3
#define TH1 3
#define EARLY 10
#define LEADP 24
#define DPH 2
#define TA 210
#define TA2 70
#define CSH 2
#define NPRB 3
#define TMAP 485
#define MINV 96
#define VW 4
#define MASTER 203
#define TRES 175
#define TB1 1200
#define PER 300
#define RDD 100
#define LEAD 70
#define LHI 85
#define LOK 74
#define PSL0 124
#define PEND 36
#define PUBP -12
#define LJ 210
#define LOK2 75
#define NTRY 3
#define TFO 6
#define SL0 42.f
#define PST0 200
#define KC 4
#define TC0 1000
#define TCS 9
#define PRE0 3

#define KREL 12
#define PDEL 70
#define NCP 3
#define NRW (NRR + NCP * GSZ)
#define TRD 116
#define THD 55
#define TRQ 55
#define TCA 138
#define TCD 22
#define TSD 560
#define WS_EPOCH 0
#define WS_POST0 8192
#define WS_POST (WS_POST0 + ((unsigned)E1 & 15) * 2048)
#define WS_NEWS (49152 + 256)
#define WS_HELLO 59904
#define WS_BOARD 65536
#define WS_PART (2u << 20)
#define WS_SWAP (WS_PART + NRW * NSPLIT * 512)

typedef unsigned int v2u __attribute__((ext_vector_type(2)));
typedef unsigned int v16u __attribute__((ext_vector_type(16)));
typedef unsigned int v4u __attribute__((ext_vector_type(4)));
typedef int v4i __attribute__((ext_vector_type(4)));
typedef float v2f __attribute__((ext_vector_type(2)));
typedef float v4f __attribute__((ext_vector_type(4)));
typedef unsigned long long u64;

__device__ __forceinline__ float bflo(unsigned u) { return __uint_as_float(u << 16); }
__device__ __forceinline__ float bfhi(unsigned u) { return __uint_as_float(u & 0xffff0000u); }
__device__ __forceinline__ float bfr(float x) {
    __bf16 b = (__bf16)x;
    return __uint_as_float(((unsigned)__builtin_bit_cast(unsigned short, b)) << 16);
}

#define SLEEP(n) SLEEP_(n)
#define SLEEP_(n) asm volatile("s_sleep " #n "\n\tv_add_co_u32 %0, vcc, 1, %0\n\ts_cbranch_vccz 1f\n\ts_endpgm\n1:" : "+v"(spins) :: "vcc", "memory");
#define SPIN0 0xf0000000u
#define CBAR() asm volatile("" ::: "memory")

__device__ __forceinline__ void wave_sync() {
    __builtin_amdgcn_fence(__ATOMIC_RELEASE, "wavefront");
    __builtin_amdgcn_wave_barrier();
    __builtin_amdgcn_fence(__ATOMIC_ACQUIRE, "wavefront");
}
template <int CTRL>
__device__ __forceinline__ float dpp_add(float x) {
    int y = __builtin_amdgcn_update_dpp(0, __float_as_int(x), CTRL, 0xf, 0xf, true);
    return x + __int_as_float(y);
}
template <int CTRL>
__device__ __forceinline__ float dpp_max(float x) {
    int y = __builtin_amdgcn_update_dpp(__float_as_int(x), __float_as_int(x), CTRL, 0xf, 0xf, false);
    return fmaxf(x, __int_as_float(y));
}
__device__ __forceinline__ float row_sum(float x) { x = dpp_add<0x128>(x); x = dpp_add<0x124>(x); x = dpp_add<0x122>(x); return dpp_add<0x121>(x); }
__device__ __forceinline__ float row_max(float m) { m = dpp_max<0x128>(m); m = dpp_max<0x124>(m); m = dpp_max<0x122>(m); return dpp_max<0x121>(m); }
__device__ __forceinline__ float rdlane(float x, int l) { return __int_as_float(__builtin_amdgcn_readlane(__float_as_int(x), l)); }
__device__ __forceinline__ float wave_max(float m) { m = row_max(m); return fmaxf(fmaxf(rdlane(m, 0), rdlane(m, 16)), fmaxf(rdlane(m, 32), rdlane(m, 48))); }
__device__ __forceinline__ float wave_maxv(float m) {
    m = row_max(m);
    const auto a = __builtin_amdgcn_permlane16_swap(__float_as_uint(m), __float_as_uint(m), false, false);
    m = fmaxf(__uint_as_float(a[0]), __uint_as_float(a[1]));
    const auto b = __builtin_amdgcn_permlane32_swap(__float_as_uint(m), __float_as_uint(m), false, false);
    return fmaxf(__uint_as_float(b[0]), __uint_as_float(b[1]));
}
__device__ __forceinline__ float wave_sum(float x) { x = row_sum(x); return (rdlane(x, 0) + rdlane(x, 16)) + (rdlane(x, 32) + rdlane(x, 48)); }
__device__ __forceinline__ unsigned lds_ld(const unsigned* p) { return __builtin_amdgcn_readfirstlane(__hip_atomic_load(p, __ATOMIC_RELAXED, __HIP_MEMORY_SCOPE_WORKGROUP)); }
__device__ __forceinline__ unsigned lds_addr(const void* p) { return (unsigned)(size_t)(const __attribute__((address_space(3))) void*)p; }
#define DMA16(SRC, LDS) asm volatile("s_mov_b32 %0, m0\n\ts_mov_b32 m0, %2\n\ts_nop 0\n\tglobal_load_lds_dwordx4 %1, off\n\ts_mov_b32 m0, %0" : "=&s"(m0keep) : "v"(SRC), "s"(LDS) : "memory")
__device__ __forceinline__ void lds_st(unsigned* p, unsigned v) { __hip_atomic_store(p, v, __ATOMIC_RELAXED, __HIP_MEMORY_SCOPE_WORKGROUP); }
#define NOW() ((unsigned)__builtin_amdgcn_s_memrealtime())
__device__ __forceinline__ unsigned tdue(unsigned g) {
    return (g * TUNIT + min(g, 480u) * TK + min(g, 48u) * T01 + min(g, 32u * NMIX) * TMIX + (g >= PSF ? TGS * 32 : 0)) >> 5;
}
__device__ __forceinline__ unsigned tvx(unsigned g, unsigned gap) {
    return (g >= PSW ? gap >> (g >= PSG ? 12 : 0) & 0xfffu : 0u) + (min(g - NPC / 2, 32u * NMIX) * (gap >> 24) >> 5);
}

__device__ __forceinline__ unsigned unit_of(unsigned g) { const unsigned p = g & 511; return (g >> 9) * NU + (p >> 5) + min(max((int)p - 32 * WQ, 0) >> 3, 3); }
__device__ __forceinline__ unsigned first_of(unsigned e) { return 32 * e - 24 * min(max((int)e - WQ, 0), 4); }
#define QUARTER(E) ((E) - WQ < 4)
__device__ __forceinline__ unsigned went(unsigned e) { return 144 * e - 96 * min(max((int)e - WQ, 0), 4); }
__device__ __forceinline__ unsigned wunit(unsigned x) { x += 24 * NMIX; return x < 24 * WQ ? x / 24 : x < 24 * WQ + 32 ? WQ + (x - 24 * WQ) / 8 : (x + 64) / 24; }

#define ROTT 0xed8d2c3d2c35ed84ull, 0xba85aa165b03ac82ull, 0xee7efa489a38af71ull, 0xe932da1b0bab51d8ull, 0xfa96112d5fee971bull, 0x2c7e9ea5a544f071ull, 0x3d86093d5f0e810bull, 0xe932da1b0bab51d8ull, 0xfa96112d5fee971bull, 0xd834e62c5fa723e9ull, 0xa4cd778c2ca4aaa5ull, 0xe932da1b0bab51d8ull, 0xfa96112d5fee971bull, 0xc834e12c0aaa4ec7ull, 0x3d86093d5f0e810bull, 0xe932da1b0bab51d8ull
static __device__ const u64 ROT[16] = {ROTT};

struct __attribute__((aligned(16))) Smem {
    unsigned cs[NSW][4][768];
    double acc[NOUT][DH];
    float ent[NOUT][BROWS];
    unsigned short wv[WALL];
    unsigned short wo[WALL];
    unsigned q[NIN][HALF];
    float2 kz[NIN][BROWS];
    float2 vz[NIN][BROWS];
    unsigned utab[64];
    unsigned line, gap;
    unsigned late, grab;
    unsigned rdy[NIN], cnt[NOUT];
    unsigned tabs, vrdy, swp;
    unsigned aline;
    unsigned tag_rdy, tag_lo, tag_hi, merged, cdone, cidx;
    unsigned slow;
    unsigned chead;
};

#define LDS_ADD(P, V) { u64 ex_; asm volatile("s_mov_b64 %0, exec\n\ts_mov_b64 exec, 1\n\tds_add_u32 %1, %2\n\ts_mov_b64 exec, %0" : "=&s"(ex_) : "v"(lds_addr(P)), "v"(V) : "memory"); }
#define LDS_OR(P, V) { u64 ex_; asm volatile("s_mov_b64 %0, exec\n\ts_mov_b64 exec, 1\n\tds_or_b32 %1, %2\n\ts_mov_b64 exec, %0" : "=&s"(ex_) : "v"(lds_addr(P)), "v"(V) : "memory"); }

__global__ __launch_bounds__(NTHREADS, 3) void attn_kernel(
    const unsigned short* __restrict__ q, const int* __restrict__ qk,
    const unsigned short* __restrict__ k_scales, const unsigned short* __restrict__ k_zeros,
    const int* __restrict__ qv,
    const unsigned short* __restrict__ v_scales, const unsigned short* __restrict__ v_zeros,
    const unsigned short* __restrict__ q_cos, const unsigned short* __restrict__ q_sin,
    const unsigned short* __restrict__ k_cos, const unsigned short* __restrict__ k_sin,
    __hip_bfloat16* __restrict__ out, unsigned char* __restrict__ ws)
{
    __shared__ Smem sm;
    const int tid = threadIdx.x;
    unsigned spins = SPIN0;
    const int lane = tid & 63;
    const int hw = __builtin_amdgcn_readfirstlane(tid >> 6);
    u64 t00; unsigned ka;
    asm volatile("s_memrealtime %0\n\ts_load_dword %1, %2, 0x40" : "=&s"(t00), "=&s"(ka) : "s"(__builtin_amdgcn_kernarg_segment_ptr()));
    if (hw == 0) {
        const char* pc0; asm volatile("s_getpc_b64 %0" : "=s"(pc0));
        asm volatile("global_load_dword a6, %0, %1" :: "v"(((blockIdx.x & ~7u) + (lane & 7)) * 128), "s"(pc0) : "a6", "memory");
    }
    const int wave = hw >= 4 ? hw - 4 : hw + NSW;
    const unsigned split = (blockIdx.x & 1) + (blockIdx.x & 2) * 4 + ((blockIdx.x >> 3) & 3) * 2;
    const unsigned side = (split ^ (blockIdx.x >> 2)) & 1, rank = blockIdx.x >> 5;
    const unsigned head0 = 2 * rank + side;
    const unsigned rf0 = (8 * split + 1) / 3, r3 = 8 * split + 1 - 3 * rf0;
    const unsigned koff = ((lane >> 5) * DH + 3 * (lane & 31)) * 4, koffb = koff + 384;
#define AR(S) (16 + (S) * 8)
#define GLD(A, OFF, BASE, IMM) asm volatile("global_load_dwordx3 a[%c0:%c1], %2, %3 offset:%4 nt" :: "i"(A), "i"((A) + 2), "v"(OFF), "s"(BASE), "i"(IMM))
#define ISSUE(S, J, BASE, OA, OB) { GLD(AR(S), OA, BASE, (J) * 1536); GLD(AR(S) + 4, OB, BASE, (J) * 1536); }
#define HEAD2 ((GSZ + 1 - 2 * side) * (SKV * DH * 4))
    const unsigned wo = (hw - 2) & 7;
    const bool pre = hw >= 4 && wo < PRE0;
    if (pre) {
        const char* a = (const char*)qk + (head0 * 512 + side * 256 + rf0 + wave + (wave >= 6 ? 80 : 0)) * 3072; const char* b = a + HEAD2;
        ISSUE(0, 0, a, koff, koffb) ISSUE(1, 1, a, koff, koffb) ISSUE(2, 0, b, koff, koffb) ISSUE(3, 1, b, koff, koffb)
    }
    asm volatile("s_waitcnt lgkmcnt(0)" : "+s"(t00), "+s"(ka));
    if (hw == 0) {
        constexpr u64 rot[16] = {ROTT};
        sm.utab[lane] = lane == 2 ? 2 * GSZ + head0 | ((unsigned)(rot[2] >> 4 * head0) & 15) << 9 : ~0u;
        unsigned l0 = (unsigned)t00 + TOFF;
        if (KREL) { const unsigned t1 = NOW() + KREL; if ((int)(t1 - l0) > 0) l0 = t1; }
        if (lane < 32) (&sm.line)[lane] = lane == 3 ? 64u : (lane & 29) == 0 ? l0 : 0u;
        asm volatile("s_waitcnt lgkmcnt(0)" ::: "memory");
    }
    __builtin_amdgcn_s_barrier();
    CBAR();
    const u64 rtab = r3 == 1 ? 0x5756552c2b020100ull : r3 == 0 ? 0x5756552c2b2a0100ull : 0x57562d2c2b020100ull;
#define R8(I) (rf0 + ((unsigned)(rtab >> (8 * (I))) & 255u))
#define GATE(DUE) { int d_ = (int)((DUE) - NOW()); if (d_ < 3000) while (d_ > 0) { __builtin_amdgcn_s_sleep(1); d_ = (int)((DUE) - NOW()); } }
#define TLAST() (lds_ld(&sm.line) + tdue(NPC - 2) + tvx(NPC - 2, lds_ld(&sm.gap)))
    if (__builtin_expect(wave < NSW, 1)) {
        const int cst = 2 * side;
        const unsigned r8w = R8((unsigned)wave);
        const char* cbase[3];
#pragma unroll
        for (int j = 0; j < 3; j++) { const int x = j * 64 + lane; cbase[j] = (x < 96 ? (const char*)k_cos + x * 16 : (const char*)k_sin + (x - 96) * 16) + r8w * (4 * DH * 2); }
        unsigned m0keep;
#define CS_DMA(W, C) { _Pragma("unroll") for (int j = 0; j < 3; j++) DMA16(cbase[j] + (C) * (512 * DH * 2), lds_addr(&sm.cs[0][0][0]) + ((W) * 4 + (C)) * 3072 + j * 1024); }
#define REPACK(W, C) { \
        unsigned* cw = &sm.cs[W][C][0]; \
        const unsigned* l = cw + rsel * (DH / 2) + qoff; \
        unsigned r[2][6]; \
        _Pragma("unroll") \
        for (int j = 0; j < 2; j++) { \
            unsigned e[4][2]; \
            _Pragma("unroll") \
            for (int k = 0; k < 4; k++) { \
                const unsigned* a = l + j * DH + (k & 1) * 48 + (k >> 1) * 384;       \
                unsigned x0 = a[0], x1 = a[1]; \
                e[k][0] = __builtin_amdgcn_alignbit(x1, x0, sh); \
                e[k][1] = x1 >> sh; \
            } \
            r[j][0] = (e[0][0] & 0xffffu) | (e[3][0] << 16); \
            r[j][1] = (e[0][0] >> 16) | (e[3][0] & 0xffff0000u); \
            r[j][2] = (e[0][1] & 0xffffu) | (e[3][1] << 16); \
            r[j][3] = (e[1][0] & 0xffffu) | (e[2][0] << 16); \
            r[j][4] = (e[1][0] >> 16) | (e[2][0] & 0xffff0000u); \
            r[j][5] = (e[1][1] & 0xffffu) | (e[2][1] << 16); \
        } \
        wave_sync(); \
        _Pragma("unroll") \
        for (int j = 0; j < 2; j++) { *(v4u*)&cw[j * 256 + lane * 4] = (v4u){r[j][0], r[j][1], r[j][2], r[j][3]}; *(v2u*)&cw[512 + j * 128 + lane * 2] = (v2u){r[j][4], r[j][5]}; } \
        wave_sync(); }
        const int sub = lane & 31;
        const int rsel = lane >> 5;
        const unsigned sh = (sub & 1) * 16;
        const int qoff = 3 * (sub >> 1) + (sub & 1);
        const unsigned voff = 3 * lane * 4;
        asm volatile("" ::: "a16", "a17", "a18", "a19", "a20", "a21", "a22", "a23", "a24", "a25", "a26", "a27", "a28", "a29", "a30", "a31",
                            "a32", "a33", "a34", "a35", "a36", "a37", "a38", "a39", "a40", "a41", "a42", "a43", "a44", "a45", "a46", "a47");
        asm volatile("" ::: "a48", "a49", "a50", "a51", "a52", "a53", "a54", "a55", "a56", "a57", "a58", "a59", "a60", "a61", "a62", "a63",
                            "a64", "a65", "a66", "a67", "a68", "a69", "a70", "a71", "a72", "a73", "a74", "a75", "a76", "a77", "a78", "a79");
        int ta[4][3], tb[4][3];
#define WAITT(P, S, N) asm volatile("s_waitcnt vmcnt(%6)\n\tv_accvgpr_read_b32 %0, a[%c7]\n\tv_accvgpr_read_b32 %1, a[%c8]\n\tv_accvgpr_read_b32 %2, a[%c9]\n\t" \
                              "v_accvgpr_read_b32 %3, a[%c10]\n\tv_accvgpr_read_b32 %4, a[%c11]\n\tv_accvgpr_read_b32 %5, a[%c12]" \
                              : "=v"(ta[P][0]), "=v"(ta[P][1]), "=v"(ta[P][2]), "=v"(tb[P][0]), "=v"(tb[P][1]), "=v"(tb[P][2]) \
                              : "i"(N), "i"(AR(S)), "i"(AR(S) + 1), "i"(AR(S) + 2), "i"(AR(S) + 4), "i"(AR(S) + 5), "i"(AR(S) + 6))
        const unsigned voffb = voff + 768;
        u64 ln = 0;
#define TDUE(G) (tdue(G) + ((G) >= NPC / 2 ? tvx(G, __builtin_amdgcn_readfirstlane((unsigned)(ln >> 32))) : 0u))
#define REPORT() { const unsigned t_ = now - tdc; if (lane == 0) lds_st(&sm.late, t_); }
#define LINE() ln = LDV((u64*)&sm.line);
#define LDV(P) __hip_atomic_load(P, __ATOMIC_RELAXED, __HIP_MEMORY_SCOPE_WORKGROUP)
        unsigned u = 0, rg = cst * 8 + wave, np = 0;
        CS_DMA(wave, cst)

        const char* kb0 = (const char*)qk + (size_t)(head0 * 512 + r8w) * 3072;
#define KB(I) (kb0 + ((cst + (I)) & 3) * (128 * 3072))
        const char* kb; const char* kb2;
#define SISSUE(I) { kb = KB(I); kb2 = kb + HEAD2; const unsigned du_ = lds_ld(&sm.line) + tdue(16 * (I) + 2 * wo); GATE(du_) \
            ISSUE(((I) & 1) * 4, 0, kb, koff, koffb) ISSUE(((I) & 1) * 4 + 1, 1, kb, koff, koffb) ISSUE(((I) & 1) * 4 + 2, 0, kb2, koff, koffb) ISSUE(((I) & 1) * 4 + 3, 1, kb2, koff, koffb) }
        if (!pre) SISSUE(0)
        CS_DMA(wave, (cst + 1) & 3)
        if (pre) asm volatile("s_waitcnt vmcnt(3)" ::: "memory");
        asm volatile("s_waitcnt vmcnt(11)" ::: "memory");
        if (!pre) LDS_ADD(&sm.slow, NOW() - lds_ld(&sm.line) - tdue(2 * wo))
        REPACK(wave, cst)
        SISSUE(1)
#define TABDONE(I) LDS_OR(&sm.tabs, 1u << ((((cst + (I)) & 3) * 8) + wave))
        TABDONE(0)
        const float scale = 0.07216878364870322f;
        double acc[3] = {0.0, 0.0, 0.0};
        bool first = true, chg, none = false;
        unsigned Qp[3], Qn[3], Rp[3], Rn[3];
        float kz4[4], ksc, kres[4];
        v4u ca[4]; v2u cb[4];
        v2f qe[4][3], A[4];
        unsigned vut, nrg;
        const unsigned vr = ((lane >> 3) & 3) * 128 + R8((unsigned)lane & 7);
        unsigned g, mg;
        const unsigned grab_adr = lds_addr(&sm.grab);
        asm volatile("v_accvgpr_write_b32 a13, 1\n\tv_accvgpr_write_b32 a14, 2" ::: "a13", "a14", "a15");
#define GRAB(N) { u64 ex_; asm volatile("s_mov_b64 %0, exec\n\ts_mov_b64 exec, 1\n\tds_add_rtn_u32 a15, %1, a%c2\n\ts_mov_b64 exec, %0" : "=&s"(ex_) : "v"(grab_adr), "i"(12 + (N)) : "memory"); }
#define GRABBED() { unsigned t_; asm volatile("s_waitcnt lgkmcnt(0)\n\tv_accvgpr_read_b32 %1, a15\n\ts_nop 0\n\tv_readfirstlane_b32 %0, %1\n\ts_nop 1" : "=s"(g), "=v"(t_)); }
        unsigned oa, ob, un, tdn, tiss, tdc = 0;
#define PLAN() { \
            GRABBED() un = unit_of(g); chg = un != u; none = g >= NPC; \
            const bool nv = g >= NPC / 2; \
            unsigned hd = __builtin_amdgcn_readlane(vut, un); \
            while (hd == ~0u) { SLEEP(1) hd = lds_ld(&sm.utab[un]); } \
            nrg = (g + (hd >> 8)) & 31; hd &= 255; \
            unsigned boff, boff2; asm("s_mul_i32 %0, %1, 0xc00" : "=s"(boff) : "s"(hd * 512 + __builtin_amdgcn_readlane(vr, nrg))); \
            asm("s_mul_i32 %0, %1, 0xc00" : "=s"(boff2) : "s"(hd * 512 + __builtin_amdgcn_readlane(vr, nrg + 1))); \
            const char* b_ = nv ? (const char*)qv : (const char*)qk; \
            kb = b_ + boff; kb2 = b_ + boff2; \
            oa = nv ? voff : koff; ob = nv ? voffb : koffb; \
            tdn = TDUE(g); }
#define FIRE() { ISSUE(0, 0, kb, oa, ob) ISSUE(1, 1, kb, oa, ob) ISSUE(2, 0, kb2, oa, ob) ISSUE(3, 1, kb2, oa, ob) }
#define LAUNCH() { \
            LINE() \
            const unsigned du_ = __builtin_amdgcn_readfirstlane((unsigned)ln) + tdn; \
            GATE(du_) \
            ISSUE(0, 0, kb, oa, ob) ISSUE(1, 1, kb, oa, ob) \
            GATE(du_ + 10) \
            ISSUE(2, 0, kb2, oa, ob) ISSUE(3, 1, kb2, oa, ob) tiss = NOW(); }
#define ARRIVED() \
            const unsigned now = NOW(); \
            const bool stall = now - tiss > LJ && g - PST0 < NPC - 48 - PST0; \
            const bool late = !none && !stall && (int)(now + EARLY - __builtin_amdgcn_readfirstlane((unsigned)ln) - tdn) >= 0; \
            if (late) { FIRE() tiss = now; } \
            if (stall && lane == 0) __hip_atomic_fetch_max(&sm.line, (unsigned)ln + (now - tiss) - LOK2, __ATOMIC_RELAXED, __HIP_MEMORY_SCOPE_WORKGROUP); \
            REPORT()
#define ADVANCE(ISV) { \
            if (chg) FLUSH(ISV) \
            if (!none && !late) LAUNCH() \
            first = chg; u = un; rg = nrg; tdc = tdn + 10; }
#define K_TAB(MODE) { _Pragma("unroll") for (int p = 0; p < ((MODE) ? 2 : 4); p++) { \
            const unsigned* cw = &sm.cs[(rg & 7) + (p >> 1)][rg >> 3][0]; \
            ca[p] = *(const v4u*)&cw[(p & 1) * 256 + lane * 4]; cb[p] = *(const v2u*)&cw[512 + (p & 1) * 128 + lane * 2]; } }
#define K_PRE(MODE, P) { \
        float e0, e1, e2, f0, f1, f2; \
        const unsigned* qp_ = ((MODE) && (P) >= 2) ? Rp : Qp; const unsigned* qn_ = ((MODE) && (P) >= 2) ? Rn : Qn; \
        const int tp_ = (MODE) ? (P) & 1 : (P); \
        asm("v_dot2_f32_bf16 %0, %6, %12, 0\n\tv_dot2_f32_bf16 %3, %9, %15, 0\n\tv_dot2_f32_bf16 %1, %7, %13, 0\n\tv_dot2_f32_bf16 %4, %10, %16, 0\n\t" \
            "v_dot2_f32_bf16 %2, %8, %14, 0\n\tv_dot2_f32_bf16 %5, %11, %17, 0\n\ts_nop 2" \
            : "=&v"(e0), "=&v"(e1), "=&v"(e2), "=&v"(f0), "=&v"(f1), "=&v"(f2) \
            : "v"(qp_[0]), "v"(qp_[1]), "v"(qp_[2]), "v"(qn_[0]), "v"(qn_[1]), "v"(qn_[2]), \
              "v"(ca[tp_].x), "v"(ca[tp_].y), "v"(ca[tp_].z), "v"(ca[tp_].w), "v"(cb[tp_].x), "v"(cb[tp_].y)); \
        qe[P][0] = (v2f){e0, f0}; qe[P][1] = (v2f){e1, f1}; qe[P][2] = (v2f){e2, f2}; \
        A[P] = (qe[P][0] + qe[P][1] + qe[P][2]) * (v2f){-kz4[P], -kz4[P]}; }
#define K_WAIT(MODE, P) { \
        if ((MODE) == 0) { WAITT(P, P, 6 - 2 * (P)); } \
        else { WAITT(P, (((MODE) - 1) & 1) * 4 + (P), ((MODE) < 3 ? 20 : (MODE) == 3 ? 17 : 14) - 2 * (P)); } }
#define K_FMA(P) { \
        _Pragma("unroll") \
        for (int i = 0; i < 3; i++) { v2f x = {(float)ta[P][i], (float)tb[P][i]}; A[P] = qe[P][i] * x + A[P]; } \
        kres[P] = A[P].x + A[P].y; }
#define K_RED(MODE) { \
        const bool o1 = lane & 1, o2 = lane & 2; \
        float s01 = (o1 ? kres[1] : kres[0]) + __int_as_float(__builtin_amdgcn_update_dpp(0, __float_as_int(o1 ? kres[0] : kres[1]), 0xb1, 0xf, 0xf, true)); \
        float s23 = (o1 ? kres[3] : kres[2]) + __int_as_float(__builtin_amdgcn_update_dpp(0, __float_as_int(o1 ? kres[2] : kres[3]), 0xb1, 0xf, 0xf, true)); \
        float sx = (o2 ? s23 : s01) + __int_as_float(__builtin_amdgcn_update_dpp(0, __float_as_int(o2 ? s01 : s23), 0x4e, 0xf, 0xf, true)); \
        sx = dpp_add<0x124>(sx); sx = dpp_add<0x128>(sx); \
        const auto sw = __builtin_amdgcn_permlane16_swap(__float_as_uint(sx), __float_as_uint(sx), false, false); \
        sx = __uint_as_float(sw[0]) + __uint_as_float(sw[1]); \
        if ((lane & 28) == 0) { if (MODE) sm.ent[(lane >> 1) & 1][rg * 4 + 2 * (lane & 1) + rsel] = sx * ksc; else sm.ent[u % NOUT][rg * 4 + 2 * (lane & 3) + rsel] = sx * ksc; } }
#define K_PRE4(MODE) { K_PRE(MODE, 0) K_PRE(MODE, 1) K_PRE(MODE, 2) K_PRE(MODE, 3) asm volatile("" :: "v"(A[0]), "v"(A[1]), "v"(A[2]), "v"(A[3])); }
#define K_BODY(MODE) { K_PRE4(MODE) K_WAIT(MODE, 0) K_FMA(0) K_WAIT(MODE, 1) K_FMA(1) K_WAIT(MODE, 2) K_FMA(2) K_WAIT(MODE, 3) K_FMA(3) K_RED(MODE) }
#define V_FMA(J, W0, W1) { \
        _Pragma("unroll") \
        for (int i = 0; i < 3; i++) { \
            acc[i] = fma(W0, (double)ta[J][i], acc[i]); \
            acc[i] = fma(W1, (double)tb[J][i], acc[i]); \
        } }
#define V_PIN() asm volatile("" :: "v"(acc[0]), "v"(acc[1]), "v"(acc[2]));
#define RDYWAIT(SL, SEQ) { while (lds_ld(&sm.rdy[SL]) != (SEQ)) SLEEP(1); CBAR(); }
#define LOADZ() { _Pragma("unroll") for (int p = 0; p < 4; p++) kz4[p] = sm.kz[u % NIN][rg * 4 + 2 * p + rsel].x; ksc = sm.kz[u % NIN][rg * 4 + 2 * (lane & 3) + rsel].y * scale; }
#define LOADQ() { _Pragma("unroll") for (int i = 0; i < 3; i++) { Qp[i] = sm.q[u % NIN][3 * sub + i]; Qn[i] = __builtin_amdgcn_alignbit(Qp[i], Qp[i], 16) ^ 0x80000000u; } }
#define FLUSH(ISV) { \
            mg = __builtin_amdgcn_readfirstlane(mg); \
            while (u >= mg + NOUT) { SLEEP(1); mg = lds_ld(&sm.merged); } \
            CBAR(); \
            if (ISV) { const unsigned aa = lds_addr(&sm.acc[u % NOUT][lane]); \
                asm volatile("ds_add_f64 %0, %1\n\tds_add_f64 %0, %2 offset:512\n\tds_add_f64 %0, %3 offset:1024" :: "v"(aa), "v"(acc[0]), "v"(acc[1]), "v"(acc[2]) : "memory"); \
                acc[0] = 0.0; acc[1] = 0.0; acc[2] = 0.0; } \
            CBAR(); \
            LDS_ADD(&sm.cnt[u % NOUT], np) \
            CBAR(); \
            np = 0; }

#define LOADZ2() { _Pragma("unroll") for (int p = 0; p < 4; p++) kz4[p] = sm.kz[p >> 1][rg * 4 + 2 * (p & 1) + rsel].x; ksc = sm.kz[(lane >> 1) & 1][rg * 4 + 2 * (lane & 1) + rsel].y * scale; }
#define STATIC_STEP(I) { \
            rg = ((cst + (I)) & 3) * 8 + wave; \
            LOADZ2() \
            K_TAB((I) + 1) K_BODY((I) + 1) }
        CS_DMA(wave, (cst + 2) & 3)
        RDYWAIT(0, 1u) RDYWAIT(1, 2u)
        LOADQ()
#pragma unroll
        for (int i = 0; i < 3; i++) { Rp[i] = sm.q[1][3 * sub + i]; Rn[i] = __builtin_amdgcn_alignbit(Rp[i], Rp[i], 16) ^ 0x80000000u; }
        STATIC_STEP(0)
        asm volatile("s_waitcnt vmcnt(11)" ::: "memory");
        REPACK(wave, (cst + 1) & 3)
        TABDONE(1)
        SISSUE(2)
        CS_DMA(wave, (cst + 3) & 3)
        STATIC_STEP(1)
        asm volatile("s_waitcnt vmcnt(11)" ::: "memory");
        REPACK(wave, (cst + 2) & 3)
        TABDONE(2)
        SISSUE(3)
        asm volatile("s_waitcnt vmcnt(8)" ::: "memory");
        REPACK(wave, (cst + 3) & 3)
        TABDONE(3)
        STATIC_STEP(2)
        GRAB(2)
        vut = LDV(&sm.utab[lane]);
        rg = ((cst + 3) & 3) * 8 + wave;
        LOADZ2() K_TAB(4) K_PRE4(4)
        PLAN()
        LAUNCH()
        K_WAIT(4, 0) K_FMA(0) K_WAIT(4, 1) K_FMA(1) K_WAIT(4, 2) K_FMA(2) K_WAIT(4, 3) K_FMA(3) K_RED(4)
        CBAR();
        LDS_ADD(&sm.cnt[0], 4u) LDS_ADD(&sm.cnt[1], 4u)
        u = un; rg = nrg; tdc = tdn + 10;
        bool tabok = false;
        __builtin_amdgcn_s_setprio(2);
#pragma unroll 1
        while (u < NU) {
            GRAB(2)
            mg = LDV(&sm.merged);
            vut = LDV(&sm.utab[lane]);
            LINE()
            if (!tabok) {
                for (;;) { const unsigned tb_ = lds_ld(&sm.tabs); tabok = tb_ == ~0u; if (((tb_ >> rg) & 3u) == 3u) break; SLEEP(1) }
                CBAR();
            }
            K_TAB(0)
            if (first) {
                unsigned fl = LDV(&sm.rdy[u % NIN]);
                CBAR();
                LOADQ() LOADZ()
                if (__builtin_amdgcn_readfirstlane(fl) != u + 1) { RDYWAIT(u % NIN, u + 1) LOADQ() LOADZ() }
            } else LOADZ()
            K_PRE4(0)
            PLAN()
            K_WAIT(0, 0) K_FMA(0) K_WAIT(0, 1) K_FMA(1)
            asm volatile("" :: "v"(kres[0]), "v"(kres[1]));
            K_WAIT(0, 2) K_WAIT(0, 3)
            ARRIVED()
            K_FMA(2) K_FMA(3) K_RED(0)
            np += 2;
            ADVANCE(false)
        }
#pragma unroll 1
        while (u < 2 * NU) {
            GRAB(2)
            mg = LDV(&sm.merged);
            LINE()
            const unsigned vr_ = LDV(&sm.vrdy);
            CBAR();
            const unsigned short* wp = &sm.wv[went(u - NU) + (QUARTER(u - NU) ? rg & 7 : rg) * 4];
            v4i pw = *(const v4i*)wp;
            if (first && !((__builtin_amdgcn_readfirstlane(vr_) >> (u - NU)) & 1)) { while (!((lds_ld(&sm.vrdy) >> (u - NU)) & 1)) SLEEP(1); CBAR(); pw = *(const v4i*)wp; }
            const double w0 = (short)pw.x, w1 = pw.x >> 16, w2 = (short)pw.y, w3 = pw.y >> 16, w4 = (short)pw.z, w5 = pw.z >> 16, w6 = (short)pw.w, w7 = pw.w >> 16;
            asm volatile("" :: "v"(w0), "v"(w1), "v"(w2), "v"(w3), "v"(w4), "v"(w5), "v"(w6), "v"(w7));
            PLAN()
            v2f hl; unsigned tl8, rowl;
            if (none) { hl = *(const v2f*)&sm.wv[went(NU - 1) + 128]; tl8 = LDV(&sm.tag_lo) << 24; rowl = (lds_ld(&sm.utab[2 * NU - 1]) & 255) * (NSPLIT * 512); asm volatile("" : "+v"(hl), "+v"(tl8)); }
            WAITT(0, 0, 6); V_FMA(0, w0, w1) V_PIN()
            WAITT(1, 1, 4); V_FMA(1, w2, w3) V_PIN()
            WAITT(2, 2, 2); V_FMA(2, w4, w5) V_PIN()
            WAITT(3, 3, 0);
            np += 2;
            if (none) {
                V_FMA(3, w6, w7)
                if (u != 2 * NU - 1) { FLUSH(true) break; }
                mg = __builtin_amdgcn_readfirstlane(mg);
                while (u >= mg + NOUT) { SLEEP(1); mg = lds_ld(&sm.merged); }
                const unsigned aa = lds_addr(&sm.acc[(2 * NU - 1) % NOUT][lane]);
                double a0, a1, a2; unsigned cn; u64 ex_;
                asm volatile("ds_add_f64 %5, %6\n\tds_add_f64 %5, %7 offset:512\n\tds_add_f64 %5, %8 offset:1024\n\t"
                             "s_mov_b64 %4, exec\n\ts_mov_b64 exec, 1\n\tds_add_rtn_u32 %3, %9, %10\n\ts_mov_b64 exec, %4\n\t"
                             "ds_read_b64 %0, %5\n\tds_read_b64 %1, %5 offset:512\n\tds_read_b64 %2, %5 offset:1024\n\ts_waitcnt lgkmcnt(0)"
                             : "=&v"(a0), "=&v"(a1), "=&v"(a2), "=&v"(cn), "=&s"(ex_) : "v"(aa), "v"(acc[0]), "v"(acc[1]), "v"(acc[2]), "v"(lds_addr(&sm.cnt[(2 * NU - 1) % NOUT])), "v"(np) : "memory");
                const float n0 = (float)(a0 - (double)hl.y) * hl.x, n1 = (float)(a1 - (double)hl.y) * hl.x, n2 = (float)(a2 - (double)hl.y) * hl.x;
                const unsigned e2 = max(__float_as_uint(fmaxf(fmaxf(fabsf(n0), fabsf(n1)), fabsf(n2))) >> 23, 14u);
                const float f2 = __uint_as_float((268u - e2) << 23);
                const v2u r = {__builtin_bit_cast(unsigned, __builtin_amdgcn_cvt_pk_i16((int)rintf(n0 * f2), (int)rintf(n1 * f2))),
                               __builtin_bit_cast(unsigned, __builtin_amdgcn_cvt_pk_i16((int)rintf(n2 * f2), 0)) | e2 << 16 | tl8};
                const __amdgpu_buffer_rsrc_t rsrc = __builtin_amdgcn_make_buffer_rsrc(ws + WS_PART + split * 512, 0, NRW * NSPLIT * 512 - split * 512, 0x00020000);
                asm volatile("" :: "v"(r));
                if (__builtin_amdgcn_readfirstlane(cn + np) == 32) {
                    __builtin_amdgcn_raw_buffer_store_b64(r, rsrc, lane * 8, rowl, SC1);
#pragma unroll
                    for (int j = 0; j < NCP - 1; j++) __builtin_amdgcn_raw_buffer_store_b64(r, rsrc, lane * 8, (rowl & 15 * NSPLIT * 512 ^ NSPLIT * 512) + (NRR + j * GSZ) * NSPLIT * 512, SC1);
                }
                break;
            }
            ARRIVED()
            V_FMA(3, w6, w7)
            ADVANCE(true)
        }
    } else {
    u64 E1;
    if (wave == NSW) {
        unsigned ref;
        const unsigned sidx = (blockIdx.x & 7) | ((blockIdx.x >> 7) << 3);
#define SAMPLE(K) ((((blockIdx.x >> 3) - (blockIdx.x & 7) - (K) * 5) & 15) == 0)
#define BTAG(K) ((btag + (K)) & 0xfffffu)
#define POST(K, C) { if (lane == 0) __hip_atomic_store((u64*)(ws + (bd + (K) * RST + (sidx >> 3) * 256 + (sidx & 7) * 8)), (u64)(C) | ((u64)(((C) - ref) | BTAG(K) << 12) << 32), __ATOMIC_RELAXED, __HIP_MEMORY_SCOPE_AGENT); }
        const unsigned own = lds_ld(&sm.line);
        if (SAMPLE(0) && lane < NTRY) __hip_atomic_store((unsigned*)(ws + WS_HELLO + lane * 768) + sidx, own, __ATOMIC_RELAXED, __HIP_MEMORY_SCOPE_AGENT);
        const u64 E = __hip_atomic_load((const u64*)(ws + WS_EPOCH), __ATOMIC_RELAXED, __HIP_MEMORY_SCOPE_AGENT);
        E1 = E + 1;
        if (lane == 0) { sm.tag_lo = (unsigned)E1; sm.tag_hi = (unsigned)(E1 >> 32); CBAR(); lds_st(&sm.tag_rdy, 1u); }
        const unsigned btag = __builtin_amdgcn_readfirstlane((unsigned)E1) * 61u, bd = WS_BOARD + (btag & 7) * (NRND * RST);
        ref = own;
        int off = 0;
        {
            unsigned tries = 0, cur = own, tl = own;
            bool news = false;
#pragma unroll 1
            while (!news || cur != ref) {
                const unsigned now = NOW();
                if (tries < NTRY && (int)(now - own) >= (int)(TA + tries * TA2)) {
                    v16u x;
                    asm volatile("s_load_dwordx16 %0, %1, %2\n\ts_waitcnt lgkmcnt(0)" : "=&s"(x) : "s"(ws), "s"(__builtin_amdgcn_readfirstlane(WS_HELLO + tries * 768)) : "memory");
                    unsigned bad = 0; int sum = 0, dif = 0;
#pragma unroll
                    for (int i = 0; i < 16; i++) { bad |= x[i] - own + 512; sum += x[i] - x[0]; dif += i & 4 ? x[i] : -x[i]; }
                    tries++;
                    if (bad < 1024) { const unsigned com = x[0] + (sum >> 4) + abs(dif) * (8 - KC) / 128; ref = x[0] + ((sum + (blockIdx.x & 4 ? dif : -dif)) >> 4); off = ref - com;
                        tries = NTRY; tl = NOW(); if (lane == 0) lds_st(&sm.aline, com); }
                    continue;
                }
                if (!news && lds_ld(&sm.utab[31]) != ~0u) { news = true; CBAR(); if (cur == ref) tl = now; ref = lds_ld(&sm.aline) + off; tries = NTRY; }
                const int d = (now - tl) >> CSH;
                if (cur != ref && d) { tl += d << CSH; cur += min(max((int)(ref - cur), -d), d); if (lane == 0) lds_st(&sm.line, cur); }
                SLEEP(2)
            }
        }
        ref -= off;
        const unsigned ref0 = ref;
        const unsigned lead = (blockIdx.x >> 3) == 9 ? 0 : LEAD;
        unsigned posted = ref, lagf = ref, tpost = ref + TB1, tpoll = tpost + RDD + lead, kw = 0, kr = 0, cur = ref + off, khold = 0;
        int o = off; bool held = false;
#pragma unroll 1
        for (;;) {
            const unsigned p = lds_ld(&sm.grab);
            if (p >= NPC) break;
            lagf += (int)(lds_ld(&sm.late) - lagf) >> 1;
            { const unsigned l = lds_ld(&sm.line); if (l != cur) { cur = l; khold = kw; held = true; } }
            const unsigned now = NOW();
            if (o) { o = off * min(max((int)(ref0 + TC0 + (1 << TCS) - now), 0), 1 << TCS) >> TCS; if (!held && cur != ref + o) { cur = ref + o; if (lane == 0) lds_st(&sm.line, cur); } }
            if (kw < NRND && (int)(now - tpost) >= 0) {
                const unsigned c = ref + min((p >= PSL0 && (int)(lagf - cur) > LHI ? lagf - LOK : cur) - o - ref, 4000u);
                if (SAMPLE(kw) && p < NPC - PEND && (c != ref || (ref != posted && (sidx & 7) == (kw & 7)))) POST(kw, c)
                posted = ref;
                tpost += PER; kw++;
            }
            if (kr < NRND && (int)(now - tpoll) >= 0) {
                v16u xa, xb;
                asm volatile("s_load_dwordx16 %0, %2, %3\n\ts_load_dwordx16 %1, %2, %4\n\ts_waitcnt lgkmcnt(0)" : "=&s"(xa), "=&s"(xb)
                             : "s"(ws), "s"(__builtin_amdgcn_readfirstlane(bd + kr * RST)), "s"(__builtin_amdgcn_readfirstlane(bd + kr * RST + 256)) : "memory");
                int nb = 0, sg = 0;
#pragma unroll
                for (int i = 0; i < 16; i++) {
                    const unsigned lo = i < 8 ? xa[2 * i & 15] : xb[2 * i & 15], hi = i < 8 ? xa[(2 * i + 1) & 15] : xb[(2 * i + 1) & 15];
                    if (hi >> 12 == BTAG(kr) && lo - ref0 < 8000) { nb = max(nb, (int)(lo - (hi & 4095u) - ref)); sg += hi & 4095u; }
                }
                ref += nb + (sg >> 4);
                if (cur != ref + o && (kr >= khold || (int)(ref + o - cur) > 0)) { cur = ref + o; held = false; if (lane == 0) lds_st(&sm.line, cur); }
                tpoll += PER; kr++;
            }
            SLEEP(2)
        }
    } else if (wave == NSW + 1 || wave == NSW + 2) {
        const int ql = lane < 48 ? lane : 47;
        unsigned c1, c2, s1, s2;
#define QT_LOADS() { int qo_ = ql; asm volatile("" : "+v"(qo_)); \
                     c1 = ((const unsigned*)q_cos)[qo_]; c2 = ((const unsigned*)q_cos)[48 + qo_]; s1 = ((const unsigned*)q_sin)[qo_]; s2 = ((const unsigned*)q_sin)[48 + qo_]; }
        unsigned q1, q2, lkz, lks, lvz, lvs;
        const unsigned sofs = R8(((unsigned)lane >> 1) & 7) * 2 + (lane & 1);
#define UNIT_LOADS(HEAD, GRP) { \
        q1 = ((const unsigned*)(q + (size_t)(HEAD) * DH))[ql]; q2 = ((const unsigned*)(q + (size_t)(HEAD) * DH))[48 + ql]; \
        const size_t so = (size_t)(HEAD) * (SKV / 2) + (GRP) * 256 + sofs; \
        lkz = *((const unsigned*)k_zeros + so); lks = *((const unsigned*)k_scales + so); \
        lvz = *((const unsigned*)v_zeros + so); lvs = *((const unsigned*)v_scales + so); }
#define UNIT_FILL(SL, U) { \
            v4f qq; \
            qq.x = bfr(bfr(bflo(q1) * bflo(c1)) + bfr(-bflo(q2) * bflo(s1))); \
            qq.y = bfr(bfr(bflo(q2) * bflo(c2)) + bfr(bflo(q1) * bflo(s2))); \
            qq.z = bfr(bfr(bfhi(q1) * bfhi(c1)) + bfr(-bfhi(q2) * bfhi(s1))); \
            qq.w = bfr(bfr(bfhi(q2) * bfhi(c2)) + bfr(bfhi(q1) * bfhi(s2))); \
            *(v2u*)&sm.q[SL][2 * ql] = (v2u){(__float_as_uint(qq.x) >> 16) | __float_as_uint(qq.y), (__float_as_uint(qq.z) >> 16) | __float_as_uint(qq.w)}; \
            *(v4f*)&sm.kz[SL][2 * lane] = (v4f){bflo(lkz), bflo(lks), bfhi(lkz), bfhi(lks)}; \
            *(v4f*)&sm.vz[SL][2 * lane] = (v4f){bflo(lvz), bflo(lvs), bfhi(lvz), bfhi(lvs)}; \
            CBAR(); \
            if (lane == 0) lds_st(&sm.rdy[SL], (U) + 1); \
            CBAR(); }
        const unsigned dw = NSW + 2 - wave;
        QT_LOADS()
        UNIT_LOADS(head0 + dw * (GSZ + 1 - 2 * side), lane >> 4)
#pragma unroll 1
        for (unsigned u = dw;;) {
            while (u >= lds_ld(&sm.merged) + NIN) SLEEP(4);
            CBAR();
            UNIT_FILL(u % NIN, u)
            if (u - 1 < 12) {
                const unsigned x_ = (blockIdx.x >> 3) + 32 * u - 8; unsigned dm;
                const char* p_ = x_ < 384 ? (const char*)(x_ < 192 ? qk : qv) + (size_t)(x_ < 192 ? x_ : x_ - 192) * (2u << 20) + (x_ * 8 + (blockIdx.x & 7)) * 640 : (const char*)out + (x_ - 384) * 4096;
                while ((int)(NOW() - lds_ld(&sm.line)) < TSD) SLEEP(2)
                asm volatile("s_load_dword %0, %1, 0x0\n\ts_waitcnt lgkmcnt(0)" : "=&s"(dm) : "s"(p_) : "memory");
            }
            u += 2;
            if (u >= NU) break;
            if (u == 4) {
                const unsigned tpf = tdue(PSF - 1) + TFO;
                { int d; while ((d = (int)(lds_ld(&sm.line) + tpf - NOW())) > 0 && d < 3000) SLEEP(1) }
#define PF3(P) asm volatile("global_load_dwordx4 a[0:3], %0, %1\n\tglobal_load_dwordx4 a[0:3], %0, %1 offset:1024\n\tglobal_load_dwordx4 a[0:3], %0, %1 offset:2048" \
                            :: "v"(64 * SKV * 2 + blockIdx.x * 3072 + lane * 16), "s"(P) : "a0", "a1", "a2", "a3")
                PF3(k_zeros); PF3(k_scales); PF3(v_zeros); PF3(v_scales);
                asm volatile("global_load_dword a0, %0, %1" :: "v"(blockIdx.x * (DH * 2) + ql * 8), "s"(q) : "a0");
            }
            const unsigned tin = tdue(first_of(u) - (QUARTER(u) ? 24 : 0) - LEADP + (split & 7) * DPH);
            { int d; while ((d = (int)(lds_ld(&sm.line) + tin - NOW())) > 0 && d < 3000) SLEEP(4) }
            unsigned hd;
            while ((hd = lds_ld(&sm.utab[u])) == ~0u) SLEEP(2)
            UNIT_LOADS(hd & 255, QUARTER(u) ? rank & 3 : lane >> 4)
        }
        while (lds_ld(&sm.tag_rdy) == 0) SLEEP(1);
        CBAR();
        E1 = (u64)lds_ld(&sm.tag_lo) | ((u64)lds_ld(&sm.tag_hi) << 32);
        if (wave == NSW + 1 && blockIdx.x == 0) {
            for (;;) {
                bool ok = true;
#pragma unroll
                for (int i = 0; i < NH / 64; i++) ok &= __hip_atomic_load((const u64*)(ws + WS_POST) + i * 64 + lane, __ATOMIC_RELAXED, __HIP_MEMORY_SCOPE_AGENT) >> 24 == (E1 << 24) >> 24;
                if (__builtin_amdgcn_ballot_w64(ok) == ~0ull) break;
                SLEEP(64);
            }
            if (lane == 0) __hip_atomic_store((u64*)(ws + WS_EPOCH), E1, __ATOMIC_RELAXED, __HIP_MEMORY_SCOPE_AGENT);
        }
        if (wave == NSW + 2) {
            const unsigned t16 = (unsigned)E1 & 0xffffu, t16b = ((unsigned)E1 + ((unsigned)E1 >> 16)) & 0xffffu;
            const __amdgpu_buffer_rsrc_t rsrc = __builtin_amdgcn_make_buffer_rsrc(ws + WS_SWAP + (blockIdx.x ^ 4) * 5120, 0, 5120, 0x00020000);
            const unsigned swp = lds_ld(&sm.swp);
            if (swp) {
                unsigned use = 0;
#pragma unroll
                for (int i = 0; i < 5; i++) use |= ((swp >> wunit(i * 64 + lane)) & 1) << i;
                const unsigned tg = tdue(PSG - 1) + tvx(PSG - 1, lds_ld(&sm.gap));
                while ((int)(NOW() - lds_ld(&sm.line) - tg) < 0) SLEEP(2)
                for (;;) {
                    v4u d[5]; bool ok = true;
#pragma unroll
                    for (int i = 0; i < 5; i++) d[i] = __builtin_amdgcn_raw_buffer_load_b128(rsrc, (use >> i) & 1 ? (i * 64 + lane) * 16 : 0x40000000, 0, SC1 | 2);
#pragma unroll
                    for (int i = 0; i < 5; i++) if ((use >> i) & 1) {
                        ok &= d[i].y >> 16 == t16 && d[i].w >> 16 == t16b;
                        unsigned* w = (unsigned*)&sm.wv[144 * NMIX + (i * 64 + lane) * 6];
                        w[0] = d[i].x; w[1] = (d[i].y & 0xffffu) | (d[i].z << 16); w[2] = (d[i].z >> 16) | (d[i].w << 16);
                    }
                    if (__builtin_amdgcn_ballot_w64(ok) == ~0ull) break;
                    SLEEP(32)
                }
                CBAR();
                LDS_OR(&sm.vrdy, swp)
            }
        }
    } else {
        const unsigned eu = lane >= NU ? lane - NU : lane, win = min(first_of(eu) >> 5, 15u), pair = QUARTER(eu) ? 2 * (eu - WQ) + (rank >> 2) : rank;
        unsigned rot2 = ((const unsigned char*)ROT)[win * 8 + pair];
        const unsigned rotn = ((const unsigned char*)ROT)[(win == 0 ? 4 : win == 1 ? 9 : WQ) * 8 + pair];
#define UENT(BIT) (win * 16 + 2 * pair + ((BIT) ^ side) | (QUARTER(eu) ? (rank - (eu - WQ)) * 8 & 31 : (rot2 >> ((BIT) ^ side) * 4 & 15) * 2) << 8)
        unsigned far = 0, vs;
        const unsigned hp = (blockIdx.x & 1) * NH + (blockIdx.x >> 1) * 4 + 2;
        if (blockIdx.x < 144) {
            const char* pb = (blockIdx.x < 128 ? (const char*)(hp >= NH ? qv : qk) + (size_t)(hp & (NH - 1)) * (SKV * DH * 4) + SKV * DH * 2
                                               : (const char*)ws + WS_PART + (8u << 20) + (blockIdx.x & 15) * 65536) + ((NOW() * 0x9e3779b1u) >> 26) * 128;
            unsigned mn[2] = {~0u, ~0u};
#pragma unroll 1
            for (unsigned i = 1; i < 2 * NPRB + 2; i++) {
                u64 t0, t1; unsigned dm;
                asm volatile("s_memtime %0\n\ts_waitcnt lgkmcnt(0)\n\ts_load_dword %2, %3, 0x0\n\ts_waitcnt lgkmcnt(0)\n\ts_memtime %1\n\ts_waitcnt lgkmcnt(0)"
                             : "=&s"(t0), "=&s"(t1), "=&s"(dm) : "s"(pb + i * 8192) : "memory");
                if (i == 1) asm volatile("global_load_dword a0, %0, %1 nt" :: "v"(0), "s"(pb) : "a0");
                const unsigned t = (unsigned)(t1 - t0) < 330 || i == 1 ? ~0u : (unsigned)(t1 - t0);
                if (i & 1) mn[1] = min(mn[1], t); else mn[0] = min(mn[0], t);
            }
            far = mn[0] > mn[1];
        }
        while (lds_ld(&sm.tag_rdy) == 0) SLEEP(1);
        CBAR();
        E1 = (u64)lds_ld(&sm.tag_lo) | ((u64)lds_ld(&sm.tag_hi) << 32);
        {
            const unsigned own = lds_ld(&sm.line), tg = (unsigned)E1;
            if (lane == 0) __hip_atomic_store((u64*)(ws + WS_POST) + blockIdx.x, E1 << 24 | min(lds_ld(&sm.slow), 4000u) << 1 | ((far ^ (blockIdx.x >> 2) ^ (blockIdx.x < 128 ? 0x96 >> (hp & 7) : 0)) & 1), __ATOMIC_RELAXED, __HIP_MEMORY_SCOPE_AGENT);
            const __amdgpu_buffer_rsrc_t nrs = __builtin_amdgcn_make_buffer_rsrc(ws + WS_NEWS, 0, 8 * 1280, 0x00020000);
            if (blockIdx.x == MASTER) {
                while ((int)(NOW() - own) < TMAP) SLEEP(2)
                const unsigned a = tg << 24, b = (unsigned)(E1 >> 8);
                v4u x; v2u z; u64 v0, v1;
#pragma unroll 1
                for (int i = 0; i < 8; i++) {
                    x = __builtin_amdgcn_raw_buffer_load_b128(__builtin_amdgcn_make_buffer_rsrc(ws + WS_POST, 0, 1024, 0x00020000), lane * 16, 0, SC1);
                    z = __builtin_amdgcn_raw_buffer_load_b64(__builtin_amdgcn_make_buffer_rsrc(ws + WS_POST + 1024, 0, 128, 0x00020000), lane * 8, 0, SC1);
                    v0 = __builtin_amdgcn_ballot_w64(((x.x ^ a) >> 24 | (x.y ^ b)) == 0); v1 = __builtin_amdgcn_ballot_w64(((x.z ^ a) >> 24 | (x.w ^ b)) == 0);
                    if (__builtin_popcountll(v0) + __builtin_popcountll(v1) >= MINV) break;
                    SLEEP(8)
                }
                const u64 m0 = __builtin_amdgcn_ballot_w64(x.x & 1) & v0, m1 = __builtin_amdgcn_ballot_w64(x.z & 1) & v1;
                const int s0 = lane < 43 ? min(max(lane - VW, 4 * NMIX), 42 - 2 * VW) : min(max(lane - VW, 43), 63 - 2 * VW);
#define PC(M) __builtin_popcount((unsigned)((M) >> s0) & ((2u << 2 * VW) - 1))
                const u64 g0 = __builtin_amdgcn_ballot_w64(2 * PC(m0) > PC(v0)), g1 = __builtin_amdgcn_ballot_w64(2 * PC(m1) > PC(v1));
                const float sl = wave_sum(((v0 >> lane & 1) ? (float)(x.x >> 1 & 4095u) : 0.f) + ((v1 >> lane & 1) ? (float)(x.z >> 1 & 4095u) : 0.f)) / ((8.f - PRE0) * (__builtin_popcountll(v0) + __builtin_popcountll(v1)) + 1.f);
                const unsigned al = lds_ld(&sm.aline), mean = (al ? al : own) + (int)fmaxf(sl - SL0, 0.f);
                const bool vz = lane < 16 && ((z.x ^ a) >> 24 | (z.y ^ b)) == 0;
                const unsigned gw = 2 * __builtin_popcountll(__builtin_amdgcn_ballot_w64(vz && (z.x & 1))) > __builtin_popcountll(__builtin_amdgcn_ballot_w64(vz));
                const unsigned k = lane & 3;
                if (lane < 32 && k < 3) __builtin_amdgcn_raw_buffer_store_b128((v4u){k == 0 ? (unsigned)g0 : k == 1 ? (unsigned)g1 : mean, tg, k == 0 ? (unsigned)(g0 >> 32) : k == 1 ? (unsigned)(g1 >> 32) : gw, tg},
                                                                              nrs, (lane >> 2) * 1280 + k * 16, 0, SC1);
            }
            while ((int)(NOW() - own) < TMAP + TRES) SLEEP(2)
            v4u y;
            for (;;) {
                y = __builtin_amdgcn_raw_buffer_load_b128(nrs, lane < 3 ? (blockIdx.x & 7) * 1280 + lane * 16 : 0x40000000, 0, SC1);
                if (((unsigned)__builtin_amdgcn_ballot_w64(y.y == tg && y.w == tg) & 7u) == 7u) break;
                SLEEP(8)
            }
            const u64 f0 = (unsigned)__builtin_amdgcn_readlane(y.x, 0) | (u64)(unsigned)__builtin_amdgcn_readlane(y.z, 0) << 32, f1 = (unsigned)__builtin_amdgcn_readlane(y.x, 1) | (u64)(unsigned)__builtin_amdgcn_readlane(y.z, 1) << 32;
            if (lane == 2) { sm.aline = y.x - own + 500 < 1000 ? y.x : own; sm.chead = (blockIdx.x & ~5u) | ((blockIdx.x >> 2 ^ y.z) & 1) | (blockIdx.x & 1) << 2; }
            unsigned bit = ((unsigned)((lane >= NU ? f1 : f0) >> (4 * win + (pair >> 1))) ^ (0x66 >> pair)) & 1;
            vs = (f0 ^ f1) >> 4 * NMIX && !((f0 ^ f1) >> 4 * NMIX & 4095) ? NMIX : 0;
            if (eu < NMIX) { if (lane < NU || !vs) bit = eu & 1; else rot2 = rotn; }
            const u64 bm = __builtin_amdgcn_ballot_w64(bit);
            const unsigned nsw = __builtin_popcountll((f0 ^ f1) >> (4 * NMIX)) + (vs ? 2 * NMIX : 0);
            const unsigned sw = (unsigned)(bm ^ (bm >> NU)) & ((1u << NU) - 1), sx = (sw >> vs ^ sw) & 7;
            if (lane == 0) { sm.swp = sw ^ sx ^ sx << vs; lds_st(&sm.gap, (nsw ? (TG0 + TG1 * nsw / 8) * 0x1001u + ((TH0 + TH1 * nsw / 8) << 12) : 0u) | (vs ? 0u : TMIX << 24)); }
            CBAR();
#define VPOS(E) ((E) < NMIX ? (E) + vs : (E) - vs < NMIX ? (E) - vs : (E))
            lds_st(&sm.utab[lane >= NU ? NU + VPOS(eu) : lane], UENT(bit));
        }
        const unsigned swp = lds_ld(&sm.swp);
        const unsigned T = lds_ld(&sm.tag_lo);
        const unsigned t15 = T & 0x7fffu;
        const __amdgpu_buffer_rsrc_t rsrc = __builtin_amdgcn_make_buffer_rsrc(ws + WS_PART + split * 512, 0, NRW * NSPLIT * 512 - split * 512, 0x00020000);
        v2u* recs = (v2u*)&sm.cs[0][0][0];
#pragma unroll
        for (int i = 0; i < NOUT * 3; i++) (&sm.acc[0][0])[i * 64 + lane] = 0.0;
        CBAR();
        unsigned vut = sm.utab[NU + (lane & 31)] & 255;
        if (QUARTER((unsigned)lane) && (rank & 3)) vut = NH - 16 + (rank & 3) * 16 + ((vut & 15) ^ 1);
        __builtin_amdgcn_s_setprio(3);
        unsigned po[NU / 2];
#pragma unroll
        for (int k = 0; k < NU / 2; k++) po[k] = __builtin_amdgcn_ds_bpermute((2 * k + (lane >> 5)) * 4, vut) * (NSPLIT * 512) + (lane & 31) * 16;
        const u64 rb_ = (u64)(ws + WS_PART + split * 512);
        const v4u rs_ = {(unsigned)rb_, (unsigned)(rb_ >> 32) & 0xffffu, NRW * NSPLIT * 512 - split * 512, 0x00020000u};
#pragma unroll 1
        for (unsigned u = 0; u < 2 * NU - 1; u++) {
            const unsigned e = u % NU, so = u % NOUT;
            if (u >= NU) while (!((lds_ld(&sm.vrdy) >> e) & 1)) SLEEP(4)
            CBAR();
            const v4f h = *(const v4f*)&sm.wv[went(e) + (QUARTER(e) ? 32 : 128)];
            while (lds_ld(&sm.cnt[so]) != (QUARTER(e) ? 8 : 32)) SLEEP(1)
            CBAR();
            const double a0 = sm.acc[so][lane], a1 = sm.acc[so][64 + lane], a2 = sm.acc[so][128 + lane];
            if (u < NU) {
                float2 lg = *(const float2*)&sm.ent[so][2 * lane];
                if (QUARTER(e) && lane >> 4 != (rank & 3)) lg = make_float2(-INFINITY, -INFINITY);
                const v4f zs = *(const v4f*)&sm.vz[u % NIN][2 * lane];
                const float m = wave_max(fmaxf(lg.x, lg.y));
                const float p0 = __expf(lg.x - m), p1 = __expf(lg.y - m);
                const float x0 = p0 * zs.y, x1 = p1 * zs.w;
                const unsigned eb = max(__float_as_uint(wave_max(fmaxf(fabsf(x0), fabsf(x1)))) >> 23, 14u);
                const float f = __uint_as_float((268u - eb) << 23);
                const float w0 = fminf(fmaxf(rintf(x0 * f), -32767.f), 32767.f), w1 = fminf(fmaxf(rintf(x1 * f), -32767.f), 32767.f);
                const float den = wave_sum(p0 + p1);
                const float bz = wave_sum(fmaf(w0, zs.x, w1 * zs.z));
                const float L = fmaf(m, 1.4426950408889634f, __builtin_amdgcn_logf(den));
                const unsigned vp = VPOS(u);
                unsigned* wd = (unsigned*)(((swp >> vp) & 1 ? sm.wo : sm.wv) + went(vp));
                if (!QUARTER(u) || lane >> 4 == (rank & 3)) wd[QUARTER(u) ? lane & 15 : lane] = ((unsigned)(int)w0 & 0xffffu) | ((unsigned)(int)w1 << 16);
                if (lane == 0) *(v4f*)&wd[QUARTER(u) ? 16 : 64] = (v4f){ldexpf(1.f / den, (int)eb - 141), bz, L, 0.f};
                CBAR();
                if (!((swp >> vp) & 1)) LDS_OR(&sm.vrdy, 1u << vp)
            } else {
                const float n0 = (float)(a0 - (double)h.y) * h.x, n1 = (float)(a1 - (double)h.y) * h.x, n2 = (float)(a2 - (double)h.y) * h.x;
                const unsigned e2 = max(__float_as_uint(wave_maxv(fmaxf(fmaxf(fabsf(n0), fabsf(n1)), fabsf(n2)))) >> 23, 14u);
                const float f2 = __uint_as_float((268u - e2) << 23);
                const unsigned m01 = __builtin_bit_cast(unsigned, __builtin_amdgcn_cvt_pk_i16((int)rintf(n0 * f2), (int)rintf(n1 * f2)));
                const unsigned m2 = __builtin_bit_cast(unsigned, __builtin_amdgcn_cvt_pk_i16((int)rintf(n2 * f2), 0));
                const unsigned bit = ((lane & 1 ? e2 : __float_as_uint(h.z)) >> (lane >> 1)) & 1u;
                recs[e * 64 + lane] = (v2u){m01, m2 | bit << 16 | t15 << 17};
                CBAR();
                sm.acc[so][lane] = 0.0; sm.acc[so][64 + lane] = 0.0; sm.acc[so][128 + lane] = 0.0;
            }
            CBAR();
            if (lane == 0) { lds_st(&sm.cnt[so], 0u); CBAR(); lds_st(&sm.merged, u + 1); }
            if (u == NU - 1 && swp) {
                wave_sync();
                const __amdgpu_buffer_rsrc_t srs = __builtin_amdgcn_make_buffer_rsrc(ws + WS_SWAP + blockIdx.x * 5120, 0, 5120, 0x00020000);
                v4u d[5]; unsigned o[5];
#pragma unroll
                for (int i = 0; i < 5; i++) {
                    const unsigned* w = (const unsigned*)&sm.wo[144 * NMIX + (i * 64 + lane) * 6];
                    const unsigned d0 = w[0], d1 = w[1], d2 = w[2];
                    d[i] = (v4u){d0, (d1 & 0xffffu) | (T << 16), (d1 >> 16) | (d2 << 16), (d2 >> 16) | ((T + (T >> 16)) << 16)};
                    o[i] = (swp >> wunit(i * 64 + lane)) & 1 ? (i * 64 + lane) * 16 : 0x40000000;
                }
                while ((int)(NOW() - lds_ld(&sm.line)) < (int)(tdue(PSW - 1) + tvx(PSW - 1, lds_ld(&sm.gap))) + 10 + TWO) SLEEP(1)
#pragma unroll
                for (int i = 0; i < 5; i++) __builtin_amdgcn_raw_buffer_store_b128(d[i], srs, o[i], 0, SC1 | 2);
            }
        }
        wave_sync();
#pragma unroll
        for (int k = 0; k < NU / 2; k++) asm volatile("ds_read_b128 a[%c0:%c0+3], %1 offset:%2" :: "i"(16 + 4 * k), "v"(lds_addr(recs) + lane * 16), "i"(k * 1024) : "memory");
        asm volatile("s_waitcnt lgkmcnt(0)" ::: "memory");
        while (!((lds_ld(&sm.vrdy) >> (NU - 1)) & 1)) SLEEP(4)
        CBAR();
        const v2u hw_ = {*(const unsigned*)&sm.wv[went(NU - 1) + 132], T};
        const unsigned hrow = (NRW - GSZ + (__builtin_amdgcn_readlane(vut, NU - 1) & 15 ^ 1)) * (NSPLIT * 512);
        while (lds_ld(&sm.grab) < NPC - PUBP) SLEEP(1)
        if (lane < 16) __builtin_amdgcn_raw_buffer_store_b64(hw_, rsrc, lane * 8, hrow, SC1);
#define PUB(K) asm volatile("buffer_store_dwordx4 a[%c0:%c0+3], %1, %2, 0 offen sc1" :: "i"(16 + 4 * (K)), "v"(po[K]), "s"(rs_) : "memory");
        PUB(WQ / 2) PUB(WQ / 2 + 1)
        { const unsigned t_ = TLAST() + PDEL; GATE(t_) }
        PUB(0) PUB(1) PUB(2) PUB(3) PUB(4) PUB(7) PUB(8)
        return;
    }
    }
    __builtin_amdgcn_s_setprio(0);
    unsigned c;
#define ADDRTN(P) { u64 ex_; asm volatile("s_mov_b64 %0, exec\n\ts_mov_b64 exec, 1\n\tds_add_rtn_u32 a15, %1, a13\n\ts_mov_b64 exec, %0" : "=&s"(ex_) : "v"(lds_addr(P)) : "a15", "memory"); }
#define ADDED(V) { unsigned t_; asm volatile("s_waitcnt lgkmcnt(0)\n\tv_accvgpr_read_b32 %1, a15\n\ts_nop 0\n\tv_readfirstlane_b32 %0, %1\n\ts_nop 1" : "=s"(V), "=v"(t_)); }
    asm volatile("v_accvgpr_write_b32 a13, 1\n\ts_nop 1" ::: "a13");
    ADDRTN(&sm.cidx) ADDED(c)
    if (c < NSW) {
        while (lds_ld(&sm.grab) < NPC + 2) SLEEP(4)
        const unsigned head = lds_ld(&sm.chead), T = lds_ld(&sm.tag_lo), tl = TLAST();
        const u64 rb_ = (u64)(ws + WS_PART);
        const v4u rs_ = {(unsigned)rb_, (unsigned)(rb_ >> 32) & 0xffffu, NRW * NSPLIT * 512, 0x00020000u};
        const unsigned ro = head * (NSPLIT * 512) + c * 1024, rx = (NH + (head & 15 ^ 1)) * (NSPLIT * 512) + c * 1024;
#define POLL(S, O) asm volatile("buffer_load_dwordx4 a[%c0:%c0+3], %1, %2, %3 offen sc1" :: "i"(16 + 4 * (S)), "v"(lane * 16), "s"(rs_), "s"(O) : "memory");
#define FETCH(X, S, N) asm volatile("s_waitcnt vmcnt(%c5)\n\tv_accvgpr_read_b32 %0, a[%c4]\n\tv_accvgpr_read_b32 %1, a[%c4+1]\n\tv_accvgpr_read_b32 %2, a[%c4+2]\n\tv_accvgpr_read_b32 %3, a[%c4+3]" \
                                    : "=v"((X).x), "=v"((X).y), "=v"((X).z), "=v"((X).w) : "i"(16 + 4 * (S)), "i"(N));
        float o0, o1, o2;
        if (head >= NH - GSZ) {
            __builtin_amdgcn_s_setprio(3);
            if (c == 0) {
                v2u hd;
                GATE(tl + THD)
                for (;;) {
                    hd = __builtin_amdgcn_raw_buffer_load_b64(__builtin_amdgcn_make_buffer_rsrc(ws + WS_PART + (NRW - GSZ + (head & 15 ^ 1)) * (NSPLIT * 512), 0, NSPLIT * 512, 0x00020000), (lane & 15) * 512, 0, SC1);
                    if (__builtin_amdgcn_ballot_w64(hd.y == T) == ~0ull) break;
                    SLEEP(4)
                }
                const float w = __builtin_amdgcn_exp2f(__uint_as_float(hd.x) - row_max(__uint_as_float(hd.x)));
                if (lane < 16) sm.ent[0][lane] = w * __builtin_amdgcn_rcpf(row_sum(w));
                CBAR();
                if (lane == 0) lds_st(&sm.tabs, 0u);
            }
            while (lds_ld(&sm.tabs) || lds_ld(&sm.merged) <= (2 * NU - 1) / NOUT * NOUT) SLEEP(1)
            CBAR();
            const float cf = sm.ent[0][2 * c + (lane >> 5)];
            v4u x; bool got;
#define SEEN(J, N) { FETCH(x, J, N) got = __builtin_amdgcn_ballot_w64((((x.y ^ T << 24) | (x.w ^ T << 24)) >> 24) != 0) == 0; }
            GATE(tl + TCA) POLL(0, ro) GATE(tl + TCA + TCD) POLL(1, rx + 3 * GSZ * NSPLIT * 512) SEEN(0, 1)
            if (!got) SEEN(1, 0)
            while (!got) { POLL(2, rx + 4 * GSZ * NSPLIT * 512) SEEN(2, 0) SLEEP(0) }
            const double c0 = ldexpf(cf, (int)(x.y >> 16 & 255u) - 141), c1 = ldexpf(cf, (int)(x.w >> 16 & 255u) - 141);
#define SWADD(A, B) ({ const v2u a_ = __builtin_bit_cast(v2u, A), b_ = __builtin_bit_cast(v2u, B); \
                const auto l_ = __builtin_amdgcn_permlane32_swap(a_.x, b_.x, false, false); const auto h_ = __builtin_amdgcn_permlane32_swap(a_.y, b_.y, false, false); \
                __builtin_bit_cast(double, (v2u){l_[0], h_[0]}) + __builtin_bit_cast(double, (v2u){l_[1], h_[1]}); })
            const double s0 = SWADD(c0 * (double)(short)x.x, c1 * (double)(short)x.z), s1 = SWADD(c0 * (double)((int)x.x >> 16), c1 * (double)((int)x.z >> 16)),
                         s2 = SWADD(c0 * (double)(short)x.y, c1 * (double)(short)x.w);
            asm volatile("ds_add_f64 %0, %1\n\tds_add_f64 %0, %2 offset:512\n\tds_add_f64 %0, %3 offset:1024" :: "v"(lds_addr(&sm.acc[0][lane])), "v"(s0), "v"(s1), "v"(s2) : "memory");
            ADDRTN(&sm.cdone)
            const double a0 = sm.acc[0][lane], a1 = sm.acc[0][64 + lane], a2 = sm.acc[0][128 + lane];
            ADDED(c)
            o0 = a0; o1 = a1; o2 = a2;
        } else {
            const bool quart = head >> 4 == WQ;
            const unsigned t15 = T & 0x7fffu;
#define TAKE(K) { FETCH(x[K], K, 0) \
                bad |= (x[K].y >> 17 ^ t15) | (x[K].w >> 17 ^ t15); \
                const u64 bl = __builtin_amdgcn_ballot_w64((x[K].y & 0x10000u) != 0), be = __builtin_amdgcn_ballot_w64((x[K].w & 0x10000u) != 0); \
                L[K] = __uint_as_float(lane < 32 ? (unsigned)bl : (unsigned)(bl >> 32)); ex[K] = lane < 32 ? (unsigned)be : (unsigned)(be >> 32); \
                M = fmaxf(M, fmaxf(__uint_as_float((unsigned)bl), __uint_as_float((unsigned)(bl >> 32)))); }
#define SUMUP(K) { const float w_ = __builtin_amdgcn_exp2f(L[K] - M), c_ = ldexpf(w_, (int)(ex[K] & 255u) - 141); den += w_; \
                t[0] = fmaf(c_, (float)(short)x[K].x, t[0]); t[1] = fmaf(c_, (float)((int)x[K].x >> 16), t[1]); t[2] = fmaf(c_, (float)(short)x[K].y, t[2]); \
                t[3] = fmaf(c_, (float)(short)x[K].z, t[3]); t[4] = fmaf(c_, (float)((int)x[K].z >> 16), t[4]); t[5] = fmaf(c_, (float)(short)x[K].w, t[5]); }
            const unsigned tg = tl + (quart ? TRQ : TRD);
            GATE(tg)
            v4u x[4]; float L[4], M; unsigned ex[4];
            for (;;) {
                unsigned bad = 0; M = -3e38f;
                POLL(0, ro)
                if (quart) { POLL(1, rx) POLL(2, rx + GSZ * NSPLIT * 512) POLL(3, rx + 2 * GSZ * NSPLIT * 512) }
                __builtin_amdgcn_s_setprio(3);
                TAKE(0)
                if (quart) { TAKE(1) TAKE(2) TAKE(3) }
                if (__builtin_amdgcn_ballot_w64(bad != 0) == 0) break;
                SLEEP(2)
            }
            float t[6] = {0.f, 0.f, 0.f, 0.f, 0.f, 0.f}, den = 0.f;
            SUMUP(0)
            if (quart) { SUMUP(1) SUMUP(2) SUMUP(3) }
#define SWADF(A, B) ({ const auto l_ = __builtin_amdgcn_permlane32_swap(__float_as_uint(A), __float_as_uint(B), false, false); __uint_as_float(l_[0]) + __uint_as_float(l_[1]); })
            v4f* pb = (v4f*)((char*)&sm.cs[0][0][0] + 65536);
            float* pm = (float*)(pb + NSW * 64);
            pb[c * 64 + lane] = (v4f){SWADF(t[0], t[3]), SWADF(t[1], t[4]), SWADF(t[2], t[5]), SWADF(den, den)};
            if (lane == 0) pm[c] = M;
            CBAR();
            ADDRTN(&sm.cdone)
            ADDED(c)
            CBAR();
            if (c == NSW - 1) {
                const v4f ma = *(const v4f*)pm, mb = *(const v4f*)(pm + 4);
                const float mw[NSW] = {ma.x, ma.y, ma.z, ma.w, mb.x, mb.y, mb.z, mb.w};
                float Mx = mw[0];
#pragma unroll
                for (int w = 1; w < NSW; w++) Mx = fmaxf(Mx, mw[w]);
                v4f sum = {0.f, 0.f, 0.f, 0.f};
#pragma unroll
                for (int w = 0; w < NSW; w++) sum += pb[w * 64 + lane] * __builtin_amdgcn_exp2f(mw[w] - Mx);
                const float inv = __builtin_amdgcn_rcpf(sum.w);
                o0 = sum.x * inv; o1 = sum.y * inv; o2 = sum.z * inv;
            }
        }
        if (c == NSW - 1) {
            asm("" : "+v"(o0), "+v"(o1), "+v"(o2));
            typedef __bf16 v2b __attribute__((ext_vector_type(2)));
            typedef unsigned v3u __attribute__((ext_vector_type(3)));
#define PK(A, B) __builtin_bit_cast(unsigned, (v2b){(__bf16)(A), (__bf16)(B)})
            const unsigned p12 = PK(o1, o2);
            const v3u ov = {PK(o0, o1), PK(o2, __uint_as_float(__builtin_amdgcn_permlane32_swap(__float_as_uint(o0), __float_as_uint(o0), false, false)[1])), __builtin_amdgcn_permlane32_swap(p12, p12, false, false)[1]};
            if (lane < 32) __builtin_amdgcn_raw_buffer_store_b96(ov, __builtin_amdgcn_make_buffer_rsrc(out + (size_t)head * DH, 0, DH * 2, 0x00020000), lane * 12, 0, 0);
        }
    }
}

extern "C" void kernel_launch(void* const* d_in, const int* in_sizes, int n_in,
                              void* d_out, int out_size, void* d_ws, size_t ws_size,
                              hipStream_t stream) {
    const unsigned short* q = (const unsigned short*)d_in[0];
    const int* qk = (const int*)d_in[1];
    const unsigned short* k_scales = (const unsigned short*)d_in[2];
    const unsigned short* k_zeros = (const unsigned short*)d_in[3];
    const int* qv = (const int*)d_in[4];
    const unsigned short* v_scales = (const unsigned short*)d_in[5];
    const unsigned short* v_zeros = (const unsigned short*)d_in[6];
    const unsigned short* q_cos = (const unsigned short*)d_in[7];
    const unsigned short* q_sin = (const unsigned short*)d_in[8];
    const unsigned short* k_cos = (const unsigned short*)d_in[9];
    const unsigned short* k_sin = (const unsigned short*)d_in[10];
    unsigned char* ws = (unsigned char*)d_ws;
    attn_kernel<<<NSPLIT * GSZ, NTHREADS, 0, stream>>>(q, qk, k_scales, k_zeros, qv, v_scales, v_zeros,
                                                       q_cos, q_sin, k_cos, k_sin, (__hip_bfloat16*)d_out, ws);
}
```

```cpp
#include <hip/hip_runtime.h>
#include <hip/hip_bf16.h>
#include <stdint.h>

#define DH 192
#define HALF 96
#define SKV 2048
#define NH 256
#define NSW 8
#define NWAVES (NSW + 4)
#define NTHREADS (64 * NWAVES)
#define BROWS 128
#define NSPLIT 16
#define GSZ 16
#define WQ 10
#define NU (NH / GSZ + 3)
#define NPC 1024
#define NRR (NH + 3 * GSZ)
#define NIN 8
#define NOUT 8
#define WALL (15 * 144 + 4 * 48)
#define SC1 16
#define NRND 48
#define RST 4352
#define TK 2
#define NMIX 3
#define TUNIT 311
#define T01 50
#define TMIX 7
#define TOFF 50
#define PSF 100
#define TGS 82
#define PSW (NPC / 2 + 28)
#define TG0 8
#define TG1 5
#define TWO -80
#define PSG (NPC / 2 + 60)
#define TH0 3
#define TH1 3
#define EARLY 10
#define LEADP 24
#define DPH 2
#define TA 210
#define TA2 70
#define CSH 2
#define NPRB 3
#define TMAP 485
#define MINV 96
#define VW 4
#define MASTER 203
#define TRES 175
#define TB1 1200
#define PER 300
#define RDD 100
#define LEAD 70
#define LHI 85
#define LOK 74
#define PSL0 124
#define PEND 36
#define PUBP -12
#define LJ 210
#define LOK2 75
#define NTRY 3
#define TFO 6
#define SL0 42.f
#define PST0 200
#define KC 4
#define TC0 1000
#define TCS 9
#define PRE0 3

#define KREL 12
#define PDEL 70
#define NCP 3
#define NRW (NRR + NCP * GSZ)
#define TRD 116
#define THD 55
#define TRQ 55
#define TCA 138
#define TCD 22
#define TSD 560
#define THV 12
#define TGV -8
#define TKV 7
#define WS_EPOCH 0
#define WS_POST0 8192
#define WS_POST (WS_POST0 + ((unsigned)E1 & 15) * 2048)
#define WS_NEWS (49152 + 256)
#define WS_HELLO 59904
#define WS_BOARD 65536
#define WS_PART (2u << 20)
#define WS_SWAP (WS_PART + NRW * NSPLIT * 512)

typedef unsigned int v2u __attribute__((ext_vector_type(2)));
typedef unsigned int v16u __attribute__((ext_vector_type(16)));
typedef unsigned int v4u __attribute__((ext_vector_type(4)));
typedef int v4i __attribute__((ext_vector_type(4)));
typedef float v2f __attribute__((ext_vector_type(2)));
typedef float v4f __attribute__((ext_vector_type(4)));
typedef unsigned long long u64;

__device__ __forceinline__ float bflo(unsigned u) { return __uint_as_float(u << 16); }
__device__ __forceinline__ float bfhi(unsigned u) { return __uint_as_float(u & 0xffff0000u); }
__device__ __forceinline__ float bfr(float x) {
    __bf16 b = (__bf16)x;
    return __uint_as_float(((unsigned)__builtin_bit_cast(unsigned short, b)) << 16);
}

#define SLEEP(n) SLEEP_(n)
#define SLEEP_(n) asm volatile("s_sleep " #n "\n\tv_add_co_u32 %0, vcc, 1, %0\n\ts_cbranch_vccz 1f\n\ts_endpgm\n1:" : "+v"(spins) :: "vcc", "memory");
#define SPIN0 0xf0000000u
#define CBAR() asm volatile("" ::: "memory")

__device__ __forceinline__ void wave_sync() {
    __builtin_amdgcn_fence(__ATOMIC_RELEASE, "wavefront");
    __builtin_amdgcn_wave_barrier();
    __builtin_amdgcn_fence(__ATOMIC_ACQUIRE, "wavefront");
}
template <int CTRL>
__device__ __forceinline__ float dpp_add(float x) {
    int y = __builtin_amdgcn_update_dpp(0, __float_as_int(x), CTRL, 0xf, 0xf, true);
    return x + __int_as_float(y);
}
template <int CTRL>
__device__ __forceinline__ float dpp_max(float x) {
    int y = __builtin_amdgcn_update_dpp(__float_as_int(x), __float_as_int(x), CTRL, 0xf, 0xf, false);
    return fmaxf(x, __int_as_float(y));
}
__device__ __forceinline__ float row_sum(float x) { x = dpp_add<0x128>(x); x = dpp_add<0x124>(x); x = dpp_add<0x122>(x); return dpp_add<0x121>(x); }
__device__ __forceinline__ float row_max(float m) { m = dpp_max<0x128>(m); m = dpp_max<0x124>(m); m = dpp_max<0x122>(m); return dpp_max<0x121>(m); }
__device__ __forceinline__ float rdlane(float x, int l) { return __int_as_float(__builtin_amdgcn_readlane(__float_as_int(x), l)); }
__device__ __forceinline__ float wave_max(float m) { m = row_max(m); return fmaxf(fmaxf(rdlane(m, 0), rdlane(m, 16)), fmaxf(rdlane(m, 32), rdlane(m, 48))); }
__device__ __forceinline__ float wave_maxv(float m) {
    m = row_max(m);
    const auto a = __builtin_amdgcn_permlane16_swap(__float_as_uint(m), __float_as_uint(m), false, false);
    m = fmaxf(__uint_as_float(a[0]), __uint_as_float(a[1]));
    const auto b = __builtin_amdgcn_permlane32_swap(__float_as_uint(m), __float_as_uint(m), false, false);
    return fmaxf(__uint_as_float(b[0]), __uint_as_float(b[1]));
}
__device__ __forceinline__ float wave_sum(float x) { x = row_sum(x); return (rdlane(x, 0) + rdlane(x, 16)) + (rdlane(x, 32) + rdlane(x, 48)); }
__device__ __forceinline__ unsigned lds_ld(const unsigned* p) { return __builtin_amdgcn_readfirstlane(__hip_atomic_load(p, __ATOMIC_RELAXED, __HIP_MEMORY_SCOPE_WORKGROUP)); }
__device__ __forceinline__ unsigned lds_addr(const void* p) { return (unsigned)(size_t)(const __attribute__((address_space(3))) void*)p; }
#define DMA16(SRC, LDS) asm volatile("s_mov_b32 %0, m0\n\ts_mov_b32 m0, %2\n\ts_nop 0\n\tglobal_load_lds_dwordx4 %1, off\n\ts_mov_b32 m0, %0" : "=&s"(m0keep) : "v"(SRC), "s"(LDS) : "memory")
__device__ __forceinline__ void lds_st(unsigned* p, unsigned v) { __hip_atomic_store(p, v, __ATOMIC_RELAXED, __HIP_MEMORY_SCOPE_WORKGROUP); }
#define NOW() ((unsigned)__builtin_amdgcn_s_memrealtime())
__device__ __forceinline__ unsigned tdue(unsigned g) {
    return (g * TUNIT + min(g, 480u) * TK + min(g, 48u) * T01 + min(g, 32u * NMIX) * TMIX + (g >= PSF ? TGS * 32 : 0)) >> 5;
}
__device__ __forceinline__ unsigned tvx(unsigned g, unsigned gap) {
    return (g >= PSW ? gap >> (g >= PSG ? 12 : 0) & 0xfffu : 0u) + (gap >> 31 ? TKV : min(g - NPC / 2, 32u * NMIX) * TMIX >> 5);
}

__device__ __forceinline__ unsigned unit_of(unsigned g) { const unsigned p = g & 511; return (g >> 9) * NU + (p >> 5) + min(max((int)p - 32 * WQ, 0) >> 3, 3); }
__device__ __forceinline__ unsigned first_of(unsigned e) { return 32 * e - 24 * min(max((int)e - WQ, 0), 4); }
#define QUARTER(E) ((E) - WQ < 4)
__device__ __forceinline__ unsigned went(unsigned e) { return 144 * e - 96 * min(max((int)e - WQ, 0), 4); }
__device__ __forceinline__ unsigned wunit(unsigned x) { x += 24 * NMIX; return x < 24 * WQ ? x / 24 : x < 24 * WQ + 32 ? WQ + (x - 24 * WQ) / 8 : (x + 64) / 24; }

#define ROTT 0xed8d2c3d2c35ed84ull, 0xba85aa165b03ac82ull, 0xee7efa489a38af71ull, 0xe932da1b0bab51d8ull, 0xfa96112d5fee971bull, 0x2c7e9ea5a544f071ull, 0x3d86093d5f0e810bull, 0xe932da1b0bab51d8ull, 0xfa96112d5fee971bull, 0xd834e62c5fa723e9ull, 0xa4cd778c2ca4aaa5ull, 0xe932da1b0bab51d8ull, 0xfa96112d5fee971bull, 0xc834e12c0aaa4ec7ull, 0x3d86093d5f0e810bull, 0xe932da1b0bab51d8ull
static __device__ const u64 ROT[16] = {ROTT};

struct __attribute__((aligned(16))) Smem {
    unsigned cs[NSW][4][768];
    double acc[NOUT][DH];
    float ent[NOUT][BROWS];
    unsigned short wv[WALL];
    unsigned short wo[WALL];
    unsigned q[NIN][HALF];
    float2 kz[NIN][BROWS];
    float2 vz[NIN][BROWS];
    unsigned utab[64];
    unsigned line, gap;
    unsigned late, grab;
    unsigned rdy[NIN], cnt[NOUT];
    unsigned tabs, vrdy, swp;
    unsigned aline;
    unsigned tag_rdy, tag_lo, tag_hi, merged, cdone, cidx;
    unsigned slow;
    unsigned chead;
};

#define LDS_ADD(P, V) { u64 ex_; asm volatile("s_mov_b64 %0, exec\n\ts_mov_b64 exec, 1\n\tds_add_u32 %1, %2\n\ts_mov_b64 exec, %0" : "=&s"(ex_) : "v"(lds_addr(P)), "v"(V) : "memory"); }
#define LDS_OR(P, V) { u64 ex_; asm volatile("s_mov_b64 %0, exec\n\ts_mov_b64 exec, 1\n\tds_or_b32 %1, %2\n\ts_mov_b64 exec, %0" : "=&s"(ex_) : "v"(lds_addr(P)), "v"(V) : "memory"); }

__global__ __launch_bounds__(NTHREADS, 3) void attn_kernel(
    const unsigned short* __restrict__ q, const int* __restrict__ qk,
    const unsigned short* __restrict__ k_scales, const unsigned short* __restrict__ k_zeros,
    const int* __restrict__ qv,
    const unsigned short* __restrict__ v_scales, const unsigned short* __restrict__ v_zeros,
    const unsigned short* __restrict__ q_cos, const unsigned short* __restrict__ q_sin,
    const unsigned short* __restrict__ k_cos, const unsigned short* __restrict__ k_sin,
    __hip_bfloat16* __restrict__ out, unsigned char* __restrict__ ws)
{
    __shared__ Smem sm;
    const int tid = threadIdx.x;
    unsigned spins = SPIN0;
    const int lane = tid & 63;
    const int hw = __builtin_amdgcn_readfirstlane(tid >> 6);
    u64 t00; unsigned ka;
    asm volatile("s_memrealtime %0\n\ts_load_dword %1, %2, 0x40" : "=&s"(t00), "=&s"(ka) : "s"(__builtin_amdgcn_kernarg_segment_ptr()));
    if (hw == 0) {
        const char* pc0; asm volatile("s_getpc_b64 %0" : "=s"(pc0));
        asm volatile("global_load_dword a6, %0, %1" :: "v"(((blockIdx.x & ~7u) + (lane & 7)) * 128), "s"(pc0) : "a6", "memory");
    }
    const int wave = hw >= 4 ? hw - 4 : hw + NSW;
    const unsigned split = (blockIdx.x & 1) + (blockIdx.x & 2) * 4 + ((blockIdx.x >> 3) & 3) * 2;
    const unsigned side = (split ^ (blockIdx.x >> 2)) & 1, rank = blockIdx.x >> 5;
    const unsigned head0 = 2 * rank + side;
    const unsigned rf0 = (8 * split + 1) / 3, r3 = 8 * split + 1 - 3 * rf0;
    const unsigned koff = ((lane >> 5) * DH + 3 * (lane & 31)) * 4, koffb = koff + 384;
#define AR(S) (16 + (S) * 8)
#define GLD(A, OFF, BASE, IMM) asm volatile("global_load_dwordx3 a[%c0:%c1], %2, %3 offset:%4 nt" :: "i"(A), "i"((A) + 2), "v"(OFF), "s"(BASE), "i"(IMM))
#define ISSUE(S, J, BASE, OA, OB) { GLD(AR(S), OA, BASE, (J) * 1536); GLD(AR(S) + 4, OB, BASE, (J) * 1536); }
#define HEAD2 ((GSZ + 1 - 2 * side) * (SKV * DH * 4))
    const unsigned wo = (hw - 2) & 7;
    const bool pre = hw >= 4 && wo < PRE0;
    if (pre) {
        const char* a = (const char*)qk + (head0 * 512 + side * 256 + rf0 + wave + (wave >= 6 ? 80 : 0)) * 3072; const char* b = a + HEAD2;
        ISSUE(0, 0, a, koff, koffb) ISSUE(1, 1, a, koff, koffb) ISSUE(2, 0, b, koff, koffb) ISSUE(3, 1, b, koff, koffb)
    }
    asm volatile("s_waitcnt lgkmcnt(0)" : "+s"(t00), "+s"(ka));
    if (hw == 0) {
        constexpr u64 rot[16] = {ROTT};
        sm.utab[lane] = lane == 2 ? 2 * GSZ + head0 | ((unsigned)(rot[2] >> 4 * head0) & 15) << 9 : ~0u;
        unsigned l0 = (unsigned)t00 + TOFF;
        if (KREL) { const unsigned t1 = NOW() + KREL; if ((int)(t1 - l0) > 0) l0 = t1; }
        if (lane < 32) (&sm.line)[lane] = lane == 3 ? 64u : (lane & 29) == 0 ? l0 : 0u;
        asm volatile("s_waitcnt lgkmcnt(0)" ::: "memory");
    }
    __builtin_amdgcn_s_barrier();
    CBAR();
    const u64 rtab = r3 == 1 ? 0x5756552c2b020100ull : r3 == 0 ? 0x5756552c2b2a0100ull : 0x57562d2c2b020100ull;
#define R8(I) (rf0 + ((unsigned)(rtab >> (8 * (I))) & 255u))
#define GATE(DUE) { int d_ = (int)((DUE) - NOW()); if (d_ < 3000) while (d_ > 0) { __builtin_amdgcn_s_sleep(1); d_ = (int)((DUE) - NOW()); } }
#define TLAST() (lds_ld(&sm.line) + tdue(NPC - 2) + tvx(NPC - 2, lds_ld(&sm.gap)))
    if (__builtin_expect(wave < NSW, 1)) {
        const int cst = 2 * side;
        const unsigned r8w = R8((unsigned)wave);
        const char* cbase[3];
#pragma unroll
        for (int j = 0; j < 3; j++) { const int x = j * 64 + lane; cbase[j] = (x < 96 ? (const char*)k_cos + x * 16 : (const char*)k_sin + (x - 96) * 16) + r8w * (4 * DH * 2); }
        unsigned m0keep;
#define CS_DMA(W, C) { _Pragma("unroll") for (int j = 0; j < 3; j++) DMA16(cbase[j] + (C) * (512 * DH * 2), lds_addr(&sm.cs[0][0][0]) + ((W) * 4 + (C)) * 3072 + j * 1024); }
#define REPACK(W, C) { \
        unsigned* cw = &sm.cs[W][C][0]; \
        const unsigned* l = cw + rsel * (DH / 2) + qoff; \
        unsigned r[2][6]; \
        _Pragma("unroll") \
        for (int j = 0; j < 2; j++) { \
            unsigned e[4][2]; \
            _Pragma("unroll") \
            for (int k = 0; k < 4; k++) { \
                const unsigned* a = l + j * DH + (k & 1) * 48 + (k >> 1) * 384;       \
                unsigned x0 = a[0], x1 = a[1]; \
                e[k][0] = __builtin_amdgcn_alignbit(x1, x0, sh); \
                e[k][1] = x1 >> sh; \
            } \
            r[j][0] = (e[0][0] & 0xffffu) | (e[3][0] << 16); \
            r[j][1] = (e[0][0] >> 16) | (e[3][0] & 0xffff0000u); \
            r[j][2] = (e[0][1] & 0xffffu) | (e[3][1] << 16); \
            r[j][3] = (e[1][0] & 0xffffu) | (e[2][0] << 16); \
            r[j][4] = (e[1][0] >> 16) | (e[2][0] & 0xffff0000u); \
            r[j][5] = (e[1][1] & 0xffffu) | (e[2][1] << 16); \
        } \
        wave_sync(); \
        _Pragma("unroll") \
        for (int j = 0; j < 2; j++) { *(v4u*)&cw[j * 256 + lane * 4] = (v4u){r[j][0], r[j][1], r[j][2], r[j][3]}; *(v2u*)&cw[512 + j * 128 + lane * 2] = (v2u){r[j][4], r[j][5]}; } \
        wave_sync(); }
        const int sub = lane & 31;
        const int rsel = lane >> 5;
        const unsigned sh = (sub & 1) * 16;
        const int qoff = 3 * (sub >> 1) + (sub & 1);
        const unsigned voff = 3 * lane * 4;
        asm volatile("" ::: "a16", "a17", "a18", "a19", "a20", "a21", "a22", "a23", "a24", "a25", "a26", "a27", "a28", "a29", "a30", "a31",
                            "a32", "a33", "a34", "a35", "a36", "a37", "a38", "a39", "a40", "a41", "a42", "a43", "a44", "a45", "a46", "a47");
        asm volatile("" ::: "a48", "a49", "a50", "a51", "a52", "a53", "a54", "a55", "a56", "a57", "a58", "a59", "a60", "a61", "a62", "a63",
                            "a64", "a65", "a66", "a67", "a68", "a69", "a70", "a71", "a72", "a73", "a74", "a75", "a76", "a77", "a78", "a79");
        int ta[4][3], tb[4][3];
#define WAITT(P, S, N) asm volatile("s_waitcnt vmcnt(%6)\n\tv_accvgpr_read_b32 %0, a[%c7]\n\tv_accvgpr_read_b32 %1, a[%c8]\n\tv_accvgpr_read_b32 %2, a[%c9]\n\t" \
                              "v_accvgpr_read_b32 %3, a[%c10]\n\tv_accvgpr_read_b32 %4, a[%c11]\n\tv_accvgpr_read_b32 %5, a[%c12]" \
                              : "=v"(ta[P][0]), "=v"(ta[P][1]), "=v"(ta[P][2]), "=v"(tb[P][0]), "=v"(tb[P][1]), "=v"(tb[P][2]) \
                              : "i"(N), "i"(AR(S)), "i"(AR(S) + 1), "i"(AR(S) + 2), "i"(AR(S) + 4), "i"(AR(S) + 5), "i"(AR(S) + 6))
        const unsigned voffb = voff + 768;
        u64 ln = 0;
#define TDUE(G) (tdue(G) + ((G) >= NPC / 2 ? tvx(G, __builtin_amdgcn_readfirstlane((unsigned)(ln >> 32))) : 0u))
#define REPORT() { const unsigned t_ = now - tdc; if (lane == 0) lds_st(&sm.late, t_); }
#define LINE() ln = LDV((u64*)&sm.line);
#define LDV(P) __hip_atomic_load(P, __ATOMIC_RELAXED, __HIP_MEMORY_SCOPE_WORKGROUP)
        unsigned u = 0, rg = cst * 8 + wave, np = 0;
        CS_DMA(wave, cst)

        const char* kb0 = (const char*)qk + (size_t)(head0 * 512 + r8w) * 3072;
#define KB(I) (kb0 + ((cst + (I)) & 3) * (128 * 3072))
        const char* kb; const char* kb2;
#define SISSUE(I) { kb = KB(I); kb2 = kb + HEAD2; const unsigned du_ = lds_ld(&sm.line) + tdue(16 * (I) + 2 * wo); GATE(du_) \
            ISSUE(((I) & 1) * 4, 0, kb, koff, koffb) ISSUE(((I) & 1) * 4 + 1, 1, kb, koff, koffb) ISSUE(((I) & 1) * 4 + 2, 0, kb2, koff, koffb) ISSUE(((I) & 1) * 4 + 3, 1, kb2, koff, koffb) }
        if (!pre) SISSUE(0)
        CS_DMA(wave, (cst + 1) & 3)
        if (pre) asm volatile("s_waitcnt vmcnt(3)" ::: "memory");
        asm volatile("s_waitcnt vmcnt(11)" ::: "memory");
        if (!pre) LDS_ADD(&sm.slow, NOW() - lds_ld(&sm.line) - tdue(2 * wo))
        REPACK(wave, cst)
        SISSUE(1)
#define TABDONE(I) LDS_OR(&sm.tabs, 1u << ((((cst + (I)) & 3) * 8) + wave))
        TABDONE(0)
        const float scale = 0.07216878364870322f;
        double acc[3] = {0.0, 0.0, 0.0};
        bool first = true, chg, none = false;
        unsigned Qp[3], Qn[3], Rp[3], Rn[3];
        float kz4[4], ksc, kres[4];
        v4u ca[4]; v2u cb[4];
        v2f qe[4][3], A[4];
        unsigned vut, nrg;
        const unsigned vr = ((lane >> 3) & 3) * 128 + R8((unsigned)lane & 7);
        unsigned g, mg;
        const unsigned grab_adr = lds_addr(&sm.grab);
        asm volatile("v_accvgpr_write_b32 a13, 1\n\tv_accvgpr_write_b32 a14, 2" ::: "a13", "a14", "a15");
#define GRAB(N) { u64 ex_; asm volatile("s_mov_b64 %0, exec\n\ts_mov_b64 exec, 1\n\tds_add_rtn_u32 a15, %1, a%c2\n\ts_mov_b64 exec, %0" : "=&s"(ex_) : "v"(grab_adr), "i"(12 + (N)) : "memory"); }
#define GRABBED() { unsigned t_; asm volatile("s_waitcnt lgkmcnt(0)\n\tv_accvgpr_read_b32 %1, a15\n\ts_nop 0\n\tv_readfirstlane_b32 %0, %1\n\ts_nop 1" : "=s"(g), "=v"(t_)); }
        unsigned oa, ob, un, tdn, tiss, tdc = 0;
#define PLAN() { \
            GRABBED() un = unit_of(g); chg = un != u; none = g >= NPC; \
            const bool nv = g >= NPC / 2; \
            unsigned hd = __builtin_amdgcn_readlane(vut, un); \
            while (hd == ~0u) { SLEEP(1) hd = lds_ld(&sm.utab[un]); } \
            nrg = (g + (hd >> 8)) & 31; hd &= 255; \
            unsigned boff, boff2; asm("s_mul_i32 %0, %1, 0xc00" : "=s"(boff) : "s"(hd * 512 + __builtin_amdgcn_readlane(vr, nrg))); \
            asm("s_mul_i32 %0, %1, 0xc00" : "=s"(boff2) : "s"(hd * 512 + __builtin_amdgcn_readlane(vr, nrg + 1))); \
            const char* b_ = nv ? (const char*)qv : (const char*)qk; \
            kb = b_ + boff; kb2 = b_ + boff2; \
            oa = nv ? voff : koff; ob = nv ? voffb : koffb; \
            tdn = TDUE(g); }
#define FIRE() { ISSUE(0, 0, kb, oa, ob) ISSUE(1, 1, kb, oa, ob) ISSUE(2, 0, kb2, oa, ob) ISSUE(3, 1, kb2, oa, ob) }
#define LAUNCH() { \
            LINE() \
            const unsigned du_ = __builtin_amdgcn_readfirstlane((unsigned)ln) + tdn; \
            GATE(du_) \
            ISSUE(0, 0, kb, oa, ob) ISSUE(1, 1, kb, oa, ob) \
            GATE(du_ + 10) \
            ISSUE(2, 0, kb2, oa, ob) ISSUE(3, 1, kb2, oa, ob) tiss = NOW(); }
#define ARRIVED() \
            const unsigned now = NOW(); \
            const bool stall = now - tiss > LJ && g - PST0 < NPC - 48 - PST0; \
            const bool late = !none && !stall && (int)(now + EARLY - __builtin_amdgcn_readfirstlane((unsigned)ln) - tdn) >= 0; \
            if (late) { FIRE() tiss = now; } \
            if (stall && lane == 0) __hip_atomic_fetch_max(&sm.line, (unsigned)ln + (now - tiss) - LOK2, __ATOMIC_RELAXED, __HIP_MEMORY_SCOPE_WORKGROUP); \
            REPORT()
#define ADVANCE(ISV) { \
            if (chg) FLUSH(ISV) \
            if (!none && !late) LAUNCH() \
            first = chg; u = un; rg = nrg; tdc = tdn + 10; }
#define K_TAB(MODE) { _Pragma("unroll") for (int p = 0; p < ((MODE) ? 2 : 4); p++) { \
            const unsigned* cw = &sm.cs[(rg & 7) + (p >> 1)][rg >> 3][0]; \
            ca[p] = *(const v4u*)&cw[(p & 1) * 256 + lane * 4]; cb[p] = *(const v2u*)&cw[512 + (p & 1) * 128 + lane * 2]; } }
#define K_PRE(MODE, P) { \
        float e0, e1, e2, f0, f1, f2; \
        const unsigned* qp_ = ((MODE) && (P) >= 2) ? Rp : Qp; const unsigned* qn_ = ((MODE) && (P) >= 2) ? Rn : Qn; \
        const int tp_ = (MODE) ? (P) & 1 : (P); \
        asm("v_dot2_f32_bf16 %0, %6, %12, 0\n\tv_dot2_f32_bf16 %3, %9, %15, 0\n\tv_dot2_f32_bf16 %1, %7, %13, 0\n\tv_dot2_f32_bf16 %4, %10, %16, 0\n\t" \
            "v_dot2_f32_bf16 %2, %8, %14, 0\n\tv_dot2_f32_bf16 %5, %11, %17, 0\n\ts_nop 2" \
            : "=&v"(e0), "=&v"(e1), "=&v"(e2), "=&v"(f0), "=&v"(f1), "=&v"(f2) \
            : "v"(qp_[0]), "v"(qp_[1]), "v"(qp_[2]), "v"(qn_[0]), "v"(qn_[1]), "v"(qn_[2]), \
              "v"(ca[tp_].x), "v"(ca[tp_].y), "v"(ca[tp_].z), "v"(ca[tp_].w), "v"(cb[tp_].x), "v"(cb[tp_].y)); \
        qe[P][0] = (v2f){e0, f0}; qe[P][1] = (v2f){e1, f1}; qe[P][2] = (v2f){e2, f2}; \
        A[P] = (qe[P][0] + qe[P][1] + qe[P][2]) * (v2f){-kz4[P], -kz4[P]}; }
#define K_WAIT(MODE, P) { \
        if ((MODE) == 0) { WAITT(P, P, 6 - 2 * (P)); } \
        else { WAITT(P, (((MODE) - 1) & 1) * 4 + (P), ((MODE) < 3 ? 20 : (MODE) == 3 ? 17 : 14) - 2 * (P)); } }
#define K_FMA(P) { \
        _Pragma("unroll") \
        for (int i = 0; i < 3; i++) { v2f x = {(float)ta[P][i], (float)tb[P][i]}; A[P] = qe[P][i] * x + A[P]; } \
        kres[P] = A[P].x + A[P].y; }
#define K_RED(MODE) { \
        const bool o1 = lane & 1, o2 = lane & 2; \
        float s01 = (o1 ? kres[1] : kres[0]) + __int_as_float(__builtin_amdgcn_update_dpp(0, __float_as_int(o1 ? kres[0] : kres[1]), 0xb1, 0xf, 0xf, true)); \
        float s23 = (o1 ? kres[3] : kres[2]) + __int_as_float(__builtin_amdgcn_update_dpp(0, __float_as_int(o1 ? kres[2] : kres[3]), 0xb1, 0xf, 0xf, true)); \
        float sx = (o2 ? s23 : s01) + __int_as_float(__builtin_amdgcn_update_dpp(0, __float_as_int(o2 ? s01 : s23), 0x4e, 0xf, 0xf, true)); \
        sx = dpp_add<0x124>(sx); sx = dpp_add<0x128>(sx); \
        const auto sw = __builtin_amdgcn_permlane16_swap(__float_as_uint(sx), __float_as_uint(sx), false, false); \
        sx = __uint_as_float(sw[0]) + __uint_as_float(sw[1]); \
        if ((lane & 28) == 0) { if (MODE) sm.ent[(lane >> 1) & 1][rg * 4 + 2 * (lane & 1) + rsel] = sx * ksc; else sm.ent[u % NOUT][rg * 4 + 2 * (lane & 3) + rsel] = sx * ksc; } }
#define K_PRE4(MODE) { K_PRE(MODE, 0) K_PRE(MODE, 1) K_PRE(MODE, 2) K_PRE(MODE, 3) asm volatile("" :: "v"(A[0]), "v"(A[1]), "v"(A[2]), "v"(A[3])); }
#define K_BODY(MODE) { K_PRE4(MODE) K_WAIT(MODE, 0) K_FMA(0) K_WAIT(MODE, 1) K_FMA(1) K_WAIT(MODE, 2) K_FMA(2) K_WAIT(MODE, 3) K_FMA(3) K_RED(MODE) }
#define V_FMA(J, W0, W1) { \
        _Pragma("unroll") \
        for (int i = 0; i < 3; i++) { \
            acc[i] = fma(W0, (double)ta[J][i], acc[i]); \
            acc[i] = fma(W1, (double)tb[J][i], acc[i]); \
        } }
#define V_PIN() asm volatile("" :: "v"(acc[0]), "v"(acc[1]), "v"(acc[2]));
#define RDYWAIT(SL, SEQ) { while (lds_ld(&sm.rdy[SL]) != (SEQ)) SLEEP(1); CBAR(); }
#define LOADZ() { _Pragma("unroll") for (int p = 0; p < 4; p++) kz4[p] = sm.kz[u % NIN][rg * 4 + 2 * p + rsel].x; ksc = sm.kz[u % NIN][rg * 4 + 2 * (lane & 3) + rsel].y * scale; }
#define LOADQ() { _Pragma("unroll") for (int i = 0; i < 3; i++) { Qp[i] = sm.q[u % NIN][3 * sub + i]; Qn[i] = __builtin_amdgcn_alignbit(Qp[i], Qp[i], 16) ^ 0x80000000u; } }
#define FLUSH(ISV) { \
            mg = __builtin_amdgcn_readfirstlane(mg); \
            while (u >= mg + NOUT) { SLEEP(1); mg = lds_ld(&sm.merged); } \
            CBAR(); \
            if (ISV) { const unsigned aa = lds_addr(&sm.acc[u % NOUT][lane]); \
                asm volatile("ds_add_f64 %0, %1\n\tds_add_f64 %0, %2 offset:512\n\tds_add_f64 %0, %3 offset:1024" :: "v"(aa), "v"(acc[0]), "v"(acc[1]), "v"(acc[2]) : "memory"); \
                acc[0] = 0.0; acc[1] = 0.0; acc[2] = 0.0; } \
            CBAR(); \
            LDS_ADD(&sm.cnt[u % NOUT], np) \
            CBAR(); \
            np = 0; }

#define LOADZ2() { _Pragma("unroll") for (int p = 0; p < 4; p++) kz4[p] = sm.kz[p >> 1][rg * 4 + 2 * (p & 1) + rsel].x; ksc = sm.kz[(lane >> 1) & 1][rg * 4 + 2 * (lane & 1) + rsel].y * scale; }
#define STATIC_STEP(I) { \
            rg = ((cst + (I)) & 3) * 8 + wave; \
            LOADZ2() \
            K_TAB((I) + 1) K_BODY((I) + 1) }
        CS_DMA(wave, (cst + 2) & 3)
        RDYWAIT(0, 1u) RDYWAIT(1, 2u)
        LOADQ()
#pragma unroll
        for (int i = 0; i < 3; i++) { Rp[i] = sm.q[1][3 * sub + i]; Rn[i] = __builtin_amdgcn_alignbit(Rp[i], Rp[i], 16) ^ 0x80000000u; }
        STATIC_STEP(0)
        asm volatile("s_waitcnt vmcnt(11)" ::: "memory");
        REPACK(wave, (cst + 1) & 3)
        TABDONE(1)
        SISSUE(2)
        CS_DMA(wave, (cst + 3) & 3)
        STATIC_STEP(1)
        asm volatile("s_waitcnt vmcnt(11)" ::: "memory");
        REPACK(wave, (cst + 2) & 3)
        TABDONE(2)
        SISSUE(3)
        asm volatile("s_waitcnt vmcnt(8)" ::: "memory");
        REPACK(wave, (cst + 3) & 3)
        TABDONE(3)
        STATIC_STEP(2)
        GRAB(2)
        vut = LDV(&sm.utab[lane]);
        rg = ((cst + 3) & 3) * 8 + wave;
        LOADZ2() K_TAB(4) K_PRE4(4)
        PLAN()
        LAUNCH()
        K_WAIT(4, 0) K_FMA(0) K_WAIT(4, 1) K_FMA(1) K_WAIT(4, 2) K_FMA(2) K_WAIT(4, 3) K_FMA(3) K_RED(4)
        CBAR();
        LDS_ADD(&sm.cnt[0], 4u) LDS_ADD(&sm.cnt[1], 4u)
        u = un; rg = nrg; tdc = tdn + 10;
        bool tabok = false;
        __builtin_amdgcn_s_setprio(2);
#pragma unroll 1
        while (u < NU) {
            GRAB(2)
            mg = LDV(&sm.merged);
            vut = LDV(&sm.utab[lane]);
            LINE()
            if (!tabok) {
                for (;;) { const unsigned tb_ = lds_ld(&sm.tabs); tabok = tb_ == ~0u; if (((tb_ >> rg) & 3u) == 3u) break; SLEEP(1) }
                CBAR();
            }
            K_TAB(0)
            if (first) {
                unsigned fl = LDV(&sm.rdy[u % NIN]);
                CBAR();
                LOADQ() LOADZ()
                if (__builtin_amdgcn_readfirstlane(fl) != u + 1) { RDYWAIT(u % NIN, u + 1) LOADQ() LOADZ() }
            } else LOADZ()
            K_PRE4(0)
            PLAN()
            K_WAIT(0, 0) K_FMA(0) K_WAIT(0, 1) K_FMA(1)
            asm volatile("" :: "v"(kres[0]), "v"(kres[1]));
            K_WAIT(0, 2) K_WAIT(0, 3)
            ARRIVED()
            K_FMA(2) K_FMA(3) K_RED(0)
            np += 2;
            ADVANCE(false)
        }
#pragma unroll 1
        while (u < 2 * NU) {
            GRAB(2)
            mg = LDV(&sm.merged);
            LINE()
            const unsigned vr_ = LDV(&sm.vrdy);
            CBAR();
            const unsigned short* wp = &sm.wv[went(u - NU) + (QUARTER(u - NU) ? rg & 7 : rg) * 4];
            v4i pw = *(const v4i*)wp;
            if (first && !((__builtin_amdgcn_readfirstlane(vr_) >> (u - NU)) & 1)) { while (!((lds_ld(&sm.vrdy) >> (u - NU)) & 1)) SLEEP(1); CBAR(); pw = *(const v4i*)wp; }
            const double w0 = (short)pw.x, w1 = pw.x >> 16, w2 = (short)pw.y, w3 = pw.y >> 16, w4 = (short)pw.z, w5 = pw.z >> 16, w6 = (short)pw.w, w7 = pw.w >> 16;
            asm volatile("" :: "v"(w0), "v"(w1), "v"(w2), "v"(w3), "v"(w4), "v"(w5), "v"(w6), "v"(w7));
            PLAN()
            v2f hl; unsigned tl8, rowl;
            if (none) { hl = *(const v2f*)&sm.wv[went(NU - 1) + 128]; tl8 = LDV(&sm.tag_lo) << 24; rowl = (lds_ld(&sm.utab[2 * NU - 1]) & 255) * (NSPLIT * 512); asm volatile("" : "+v"(hl), "+v"(tl8)); }
            WAITT(0, 0, 6); V_FMA(0, w0, w1) V_PIN()
            WAITT(1, 1, 4); V_FMA(1, w2, w3) V_PIN()
            WAITT(2, 2, 2); V_FMA(2, w4, w5) V_PIN()
            WAITT(3, 3, 0);
            np += 2;
            if (none) {
                V_FMA(3, w6, w7)
                if (u != 2 * NU - 1) { FLUSH(true) break; }
                mg = __builtin_amdgcn_readfirstlane(mg);
                while (u >= mg + NOUT) { SLEEP(1); mg = lds_ld(&sm.merged); }
                const unsigned aa = lds_addr(&sm.acc[(2 * NU - 1) % NOUT][lane]);
                double a0, a1, a2; unsigned cn; u64 ex_;
                asm volatile("ds_add_f64 %5, %6\n\tds_add_f64 %5, %7 offset:512\n\tds_add_f64 %5, %8 offset:1024\n\t"
                             "s_mov_b64 %4, exec\n\ts_mov_b64 exec, 1\n\tds_add_rtn_u32 %3, %9, %10\n\ts_mov_b64 exec, %4\n\t"
                             "ds_read_b64 %0, %5\n\tds_read_b64 %1, %5 offset:512\n\tds_read_b64 %2, %5 offset:1024\n\ts_waitcnt lgkmcnt(0)"
                             : "=&v"(a0), "=&v"(a1), "=&v"(a2), "=&v"(cn), "=&s"(ex_) : "v"(aa), "v"(acc[0]), "v"(acc[1]), "v"(acc[2]), "v"(lds_addr(&sm.cnt[(2 * NU - 1) % NOUT])), "v"(np) : "memory");
                const float n0 = (float)(a0 - (double)hl.y) * hl.x, n1 = (float)(a1 - (double)hl.y) * hl.x, n2 = (float)(a2 - (double)hl.y) * hl.x;
                const unsigned e2 = max(__float_as_uint(fmaxf(fmaxf(fabsf(n0), fabsf(n1)), fabsf(n2))) >> 23, 14u);
                const float f2 = __uint_as_float((268u - e2) << 23);
                const v2u r = {__builtin_bit_cast(unsigned, __builtin_amdgcn_cvt_pk_i16((int)rintf(n0 * f2), (int)rintf(n1 * f2))),
                               __builtin_bit_cast(unsigned, __builtin_amdgcn_cvt_pk_i16((int)rintf(n2 * f2), 0)) | e2 << 16 | tl8};
                const __amdgpu_buffer_rsrc_t rsrc = __builtin_amdgcn_make_buffer_rsrc(ws + WS_PART + split * 512, 0, NRW * NSPLIT * 512 - split * 512, 0x00020000);
                asm volatile("" :: "v"(r));
                if (__builtin_amdgcn_readfirstlane(cn + np) == 32) {
                    __builtin_amdgcn_raw_buffer_store_b64(r, rsrc, lane * 8, rowl, SC1);
#pragma unroll
                    for (int j = 0; j < NCP - 1; j++) __builtin_amdgcn_raw_buffer_store_b64(r, rsrc, lane * 8, (rowl & 15 * NSPLIT * 512 ^ NSPLIT * 512) + (NRR + j * GSZ) * NSPLIT * 512, SC1);
                }
                break;
            }
            ARRIVED()
            V_FMA(3, w6, w7)
            ADVANCE(true)
        }
    } else {
    u64 E1;
    if (wave == NSW) {
        unsigned ref;
        const unsigned sidx = (blockIdx.x & 7) | ((blockIdx.x >> 7) << 3);
#define SAMPLE(K) ((((blockIdx.x >> 3) - (blockIdx.x & 7) - (K) * 5) & 15) == 0)
#define BTAG(K) ((btag + (K)) & 0xfffffu)
#define POST(K, C) { if (lane == 0) __hip_atomic_store((u64*)(ws + (bd + (K) * RST + (sidx >> 3) * 256 + (sidx & 7) * 8)), (u64)(C) | ((u64)(((C) - ref) | BTAG(K) << 12) << 32), __ATOMIC_RELAXED, __HIP_MEMORY_SCOPE_AGENT); }
        const unsigned own = lds_ld(&sm.line);
        if (SAMPLE(0) && lane < NTRY) __hip_atomic_store((unsigned*)(ws + WS_HELLO + lane * 768) + sidx, own, __ATOMIC_RELAXED, __HIP_MEMORY_SCOPE_AGENT);
        const u64 E = __hip_atomic_load((const u64*)(ws + WS_EPOCH), __ATOMIC_RELAXED, __HIP_MEMORY_SCOPE_AGENT);
        E1 = E + 1;
        if (lane == 0) { sm.tag_lo = (unsigned)E1; sm.tag_hi = (unsigned)(E1 >> 32); CBAR(); lds_st(&sm.tag_rdy, 1u); }
        const unsigned btag = __builtin_amdgcn_readfirstlane((unsigned)E1) * 61u, bd = WS_BOARD + (btag & 7) * (NRND * RST);
        ref = own;
        int off = 0;
        {
            unsigned tries = 0, cur = own, tl = own;
            bool news = false;
#pragma unroll 1
            while (!news || cur != ref) {
                const unsigned now = NOW();
                if (tries < NTRY && (int)(now - own) >= (int)(TA + tries * TA2)) {
                    v16u x;
                    asm volatile("s_load_dwordx16 %0, %1, %2\n\ts_waitcnt lgkmcnt(0)" : "=&s"(x) : "s"(ws), "s"(__builtin_amdgcn_readfirstlane(WS_HELLO + tries * 768)) : "memory");
                    unsigned bad = 0; int sum = 0, dif = 0;
#pragma unroll
                    for (int i = 0; i < 16; i++) { bad |= x[i] - own + 512; sum += x[i] - x[0]; dif += i & 4 ? x[i] : -x[i]; }
                    tries++;
                    if (bad < 1024) { const unsigned com = x[0] + (sum >> 4) + abs(dif) * (8 - KC) / 128; ref = x[0] + ((sum + (blockIdx.x & 4 ? dif : -dif)) >> 4); off = ref - com;
                        tries = NTRY; tl = NOW(); if (lane == 0) lds_st(&sm.aline, com); }
                    continue;
                }
                if (!news && lds_ld(&sm.utab[31]) != ~0u) { news = true; CBAR(); if (cur == ref) tl = now; ref = lds_ld(&sm.aline) + off; tries = NTRY; }
                const int d = (now - tl) >> CSH;
                if (cur != ref && d) { tl += d << CSH; cur += min(max((int)(ref - cur), -d), d); if (lane == 0) lds_st(&sm.line, cur); }
                SLEEP(2)
            }
        }
        ref -= off;
        const unsigned ref0 = ref;
        const unsigned lead = (blockIdx.x >> 3) == 9 ? 0 : LEAD;
        unsigned posted = ref, lagf = ref, tpost = ref + TB1, tpoll = tpost + RDD + lead, kw = 0, kr = 0, cur = ref + off, khold = 0;
        int o = off; bool held = false;
#pragma unroll 1
        for (;;) {
            const unsigned p = lds_ld(&sm.grab);
            if (p >= NPC) break;
            lagf += (int)(lds_ld(&sm.late) - lagf) >> 1;
            { const unsigned l = lds_ld(&sm.line); if (l != cur) { cur = l; khold = kw; held = true; } }
            const unsigned now = NOW();
            if (o) { o = off * min(max((int)(ref0 + TC0 + (1 << TCS) - now), 0), 1 << TCS) >> TCS; if (!held && cur != ref + o) { cur = ref + o; if (lane == 0) lds_st(&sm.line, cur); } }
            if (kw < NRND && (int)(now - tpost) >= 0) {
                const unsigned c = ref + min((p >= PSL0 && (int)(lagf - cur) > LHI ? lagf - LOK : cur) - o - ref, 4000u);
                if (SAMPLE(kw) && p < NPC - PEND && (c != ref || (ref != posted && (sidx & 7) == (kw & 7)))) POST(kw, c)
                posted = ref;
                tpost += PER; kw++;
            }
            if (kr < NRND && (int)(now - tpoll) >= 0) {
                v16u xa, xb;
                asm volatile("s_load_dwordx16 %0, %2, %3\n\ts_load_dwordx16 %1, %2, %4\n\ts_waitcnt lgkmcnt(0)" : "=&s"(xa), "=&s"(xb)
                             : "s"(ws), "s"(__builtin_amdgcn_readfirstlane(bd + kr * RST)), "s"(__builtin_amdgcn_readfirstlane(bd + kr * RST + 256)) : "memory");
                int nb = 0, sg = 0;
#pragma unroll
                for (int i = 0; i < 16; i++) {
                    const unsigned lo = i < 8 ? xa[2 * i & 15] : xb[2 * i & 15], hi = i < 8 ? xa[(2 * i + 1) & 15] : xb[(2 * i + 1) & 15];
                    if (hi >> 12 == BTAG(kr) && lo - ref0 < 8000) { nb = max(nb, (int)(lo - (hi & 4095u) - ref)); sg += hi & 4095u; }
                }
                ref += nb + (sg >> 4);
                if (cur != ref + o && (kr >= khold || (int)(ref + o - cur) > 0)) { cur = ref + o; held = false; if (lane == 0) lds_st(&sm.line, cur); }
                tpoll += PER; kr++;
            }
            SLEEP(2)
        }
    } else if (wave == NSW + 1 || wave == NSW + 2) {
        const int ql = lane < 48 ? lane : 47;
        unsigned c1, c2, s1, s2;
#define QT_LOADS() { int qo_ = ql; asm volatile("" : "+v"(qo_)); \
                     c1 = ((const unsigned*)q_cos)[qo_]; c2 = ((const unsigned*)q_cos)[48 + qo_]; s1 = ((const unsigned*)q_sin)[qo_]; s2 = ((const unsigned*)q_sin)[48 + qo_]; }
        unsigned q1, q2, lkz, lks, lvz, lvs;
        const unsigned sofs = R8(((unsigned)lane >> 1) & 7) * 2 + (lane & 1);
#define UNIT_LOADS(HEAD, GRP) { \
        q1 = ((const unsigned*)(q + (size_t)(HEAD) * DH))[ql]; q2 = ((const unsigned*)(q + (size_t)(HEAD) * DH))[48 + ql]; \
        const size_t so = (size_t)(HEAD) * (SKV / 2) + (GRP) * 256 + sofs; \
        lkz = *((const unsigned*)k_zeros + so); lks = *((const unsigned*)k_scales + so); \
        lvz = *((const unsigned*)v_zeros + so); lvs = *((const unsigned*)v_scales + so); }
#define UNIT_FILL(SL, U) { \
            v4f qq; \
            qq.x = bfr(bfr(bflo(q1) * bflo(c1)) + bfr(-bflo(q2) * bflo(s1))); \
            qq.y = bfr(bfr(bflo(q2) * bflo(c2)) + bfr(bflo(q1) * bflo(s2))); \
            qq.z = bfr(bfr(bfhi(q1) * bfhi(c1)) + bfr(-bfhi(q2) * bfhi(s1))); \
            qq.w = bfr(bfr(bfhi(q2) * bfhi(c2)) + bfr(bfhi(q1) * bfhi(s2))); \
            *(v2u*)&sm.q[SL][2 * ql] = (v2u){(__float_as_uint(qq.x) >> 16) | __float_as_uint(qq.y), (__float_as_uint(qq.z) >> 16) | __float_as_uint(qq.w)}; \
            *(v4f*)&sm.kz[SL][2 * lane] = (v4f){bflo(lkz), bflo(lks), bfhi(lkz), bfhi(lks)}; \
            *(v4f*)&sm.vz[SL][2 * lane] = (v4f){bflo(lvz), bflo(lvs), bfhi(lvz), bfhi(lvs)}; \
            CBAR(); \
            if (lane == 0) lds_st(&sm.rdy[SL], (U) + 1); \
            CBAR(); }
        const unsigned dw = NSW + 2 - wave;
        QT_LOADS()
        UNIT_LOADS(head0 + dw * (GSZ + 1 - 2 * side), lane >> 4)
#pragma unroll 1
        for (unsigned u = dw;;) {
            while (u >= lds_ld(&sm.merged) + NIN) SLEEP(4);
            CBAR();
            UNIT_FILL(u % NIN, u)
            if (u - 1 < 12) {
                const unsigned x_ = (blockIdx.x >> 3) + 32 * u - 8; unsigned dm;
                const char* p_ = x_ < 384 ? (const char*)(x_ < 192 ? qk : qv) + (size_t)(x_ < 192 ? x_ : x_ - 192) * (2u << 20) + (x_ * 8 + (blockIdx.x & 7)) * 640 : (const char*)out + (x_ - 384) * 4096;
                while ((int)(NOW() - lds_ld(&sm.line)) < TSD) SLEEP(2)
                asm volatile("s_load_dword %0, %1, 0x0\n\ts_waitcnt lgkmcnt(0)" : "=&s"(dm) : "s"(p_) : "memory");
            }
            u += 2;
            if (u >= NU) break;
            if (u == 4) {
                const unsigned tpf = tdue(PSF - 1) + TFO;
                { int d; while ((d = (int)(lds_ld(&sm.line) + tpf - NOW())) > 0 && d < 3000) SLEEP(1) }
#define PF3(P) asm volatile("global_load_dwordx4 a[0:3], %0, %1\n\tglobal_load_dwordx4 a[0:3], %0, %1 offset:1024\n\tglobal_load_dwordx4 a[0:3], %0, %1 offset:2048" \
                            :: "v"(64 * SKV * 2 + blockIdx.x * 3072 + lane * 16), "s"(P) : "a0", "a1", "a2", "a3")
                PF3(k_zeros); PF3(k_scales); PF3(v_zeros); PF3(v_scales);
                asm volatile("global_load_dword a0, %0, %1" :: "v"(blockIdx.x * (DH * 2) + ql * 8), "s"(q) : "a0");
            }
            const unsigned tin = tdue(first_of(u) - (QUARTER(u) ? 24 : 0) - LEADP + (split & 7) * DPH);
            { int d; while ((d = (int)(lds_ld(&sm.line) + tin - NOW())) > 0 && d < 3000) SLEEP(4) }
            unsigned hd;
            while ((hd = lds_ld(&sm.utab[u])) == ~0u) SLEEP(2)
            UNIT_LOADS(hd & 255, QUARTER(u) ? rank & 3 : lane >> 4)
        }
        while (lds_ld(&sm.tag_rdy) == 0) SLEEP(1);
        CBAR();
        E1 = (u64)lds_ld(&sm.tag_lo) | ((u64)lds_ld(&sm.tag_hi) << 32);
        if (wave == NSW + 1 && blockIdx.x == 0) {
            for (;;) {
                bool ok = true;
#pragma unroll
                for (int i = 0; i < NH / 64; i++) ok &= __hip_atomic_load((const u64*)(ws + WS_POST) + i * 64 + lane, __ATOMIC_RELAXED, __HIP_MEMORY_SCOPE_AGENT) >> 24 == (E1 << 24) >> 24;
                if (__builtin_amdgcn_ballot_w64(ok) == ~0ull) break;
                SLEEP(64);
            }
            if (lane == 0) __hip_atomic_store((u64*)(ws + WS_EPOCH), E1, __ATOMIC_RELAXED, __HIP_MEMORY_SCOPE_AGENT);
        }
        if (wave == NSW + 2) {
            const unsigned t16 = (unsigned)E1 & 0xffffu, t16b = ((unsigned)E1 + ((unsigned)E1 >> 16)) & 0xffffu;
            const __amdgpu_buffer_rsrc_t rsrc = __builtin_amdgcn_make_buffer_rsrc(ws + WS_SWAP + (blockIdx.x ^ 4) * 5120, 0, 5120, 0x00020000);
            const unsigned swp = lds_ld(&sm.swp);
            if (swp) {
                unsigned use = 0;
#pragma unroll
                for (int i = 0; i < 5; i++) use |= ((swp >> wunit(i * 64 + lane)) & 1) << i;
                const unsigned tg = tdue(PSG - 1) + tvx(PSG - 1, lds_ld(&sm.gap));
                while ((int)(NOW() - lds_ld(&sm.line) - tg) < 0) SLEEP(2)
                for (;;) {
                    v4u d[5]; bool ok = true;
#pragma unroll
                    for (int i = 0; i < 5; i++) d[i] = __builtin_amdgcn_raw_buffer_load_b128(rsrc, (use >> i) & 1 ? (i * 64 + lane) * 16 : 0x40000000, 0, SC1 | 2);
#pragma unroll
                    for (int i = 0; i < 5; i++) if ((use >> i) & 1) {
                        ok &= d[i].y >> 16 == t16 && d[i].w >> 16 == t16b;
                        unsigned* w = (unsigned*)&sm.wv[144 * NMIX + (i * 64 + lane) * 6];
                        w[0] = d[i].x; w[1] = (d[i].y & 0xffffu) | (d[i].z << 16); w[2] = (d[i].z >> 16) | (d[i].w << 16);
                    }
                    if (__builtin_amdgcn_ballot_w64(ok) == ~0ull) break;
                    SLEEP(32)
                }
                CBAR();
                LDS_OR(&sm.vrdy, swp)
            }
        }
    } else {
        const unsigned eu = lane >= NU ? lane - NU : lane, win = min(first_of(eu) >> 5, 15u), pair = QUARTER(eu) ? 2 * (eu - WQ) + (rank >> 2) : rank;
        unsigned rot2 = ((const unsigned char*)ROT)[win * 8 + pair];
        const unsigned rotn = ((const unsigned char*)ROT)[(win == 0 ? 4 : win == 1 ? 9 : WQ) * 8 + pair];
#define UENT(BIT) (win * 16 + 2 * pair + ((BIT) ^ side) | (QUARTER(eu) ? (rank - (eu - WQ)) * 8 & 31 : (rot2 >> ((BIT) ^ side) * 4 & 15) * 2) << 8)
        unsigned far = 0, vs;
        const unsigned hp = (blockIdx.x & 1) * NH + (blockIdx.x >> 1) * 4 + 2;
        if (blockIdx.x < 144) {
            const char* pb = (blockIdx.x < 128 ? (const char*)(hp >= NH ? qv : qk) + (size_t)(hp & (NH - 1)) * (SKV * DH * 4) + SKV * DH * 2
                                               : (const char*)ws + WS_PART + (8u << 20) + (blockIdx.x & 15) * 65536) + ((NOW() * 0x9e3779b1u) >> 26) * 128;
            unsigned mn[2] = {~0u, ~0u};
#pragma unroll 1
            for (unsigned i = 1; i < 2 * NPRB + 2; i++) {
                u64 t0, t1; unsigned dm;
                asm volatile("s_memtime %0\n\ts_waitcnt lgkmcnt(0)\n\ts_load_dword %2, %3, 0x0\n\ts_waitcnt lgkmcnt(0)\n\ts_memtime %1\n\ts_waitcnt lgkmcnt(0)"
                             : "=&s"(t0), "=&s"(t1), "=&s"(dm) : "s"(pb + i * 8192) : "memory");
                if (i == 1) asm volatile("global_load_dword a0, %0, %1 nt" :: "v"(0), "s"(pb) : "a0");
                const unsigned t = (unsigned)(t1 - t0) < 330 || i == 1 ? ~0u : (unsigned)(t1 - t0);
                if (i & 1) mn[1] = min(mn[1], t); else mn[0] = min(mn[0], t);
            }
            far = mn[0] > mn[1];
        }
        while (lds_ld(&sm.tag_rdy) == 0) SLEEP(1);
        CBAR();
        E1 = (u64)lds_ld(&sm.tag_lo) | ((u64)lds_ld(&sm.tag_hi) << 32);
        {
            const unsigned own = lds_ld(&sm.line), tg = (unsigned)E1;
            if (lane == 0) __hip_atomic_store((u64*)(ws + WS_POST) + blockIdx.x, E1 << 24 | min(lds_ld(&sm.slow), 4000u) << 1 | ((far ^ (blockIdx.x >> 2) ^ (blockIdx.x < 128 ? 0x96 >> (hp & 7) : 0)) & 1), __ATOMIC_RELAXED, __HIP_MEMORY_SCOPE_AGENT);
            const __amdgpu_buffer_rsrc_t nrs = __builtin_amdgcn_make_buffer_rsrc(ws + WS_NEWS, 0, 8 * 1280, 0x00020000);
            if (blockIdx.x == MASTER) {
                while ((int)(NOW() - own) < TMAP) SLEEP(2)
                const unsigned a = tg << 24, b = (unsigned)(E1 >> 8);
                v4u x; v2u z; u64 v0, v1;
#pragma unroll 1
                for (int i = 0; i < 8; i++) {
                    x = __builtin_amdgcn_raw_buffer_load_b128(__builtin_amdgcn_make_buffer_rsrc(ws + WS_POST, 0, 1024, 0x00020000), lane * 16, 0, SC1);
                    z = __builtin_amdgcn_raw_buffer_load_b64(__builtin_amdgcn_make_buffer_rsrc(ws + WS_POST + 1024, 0, 128, 0x00020000), lane * 8, 0, SC1);
                    v0 = __builtin_amdgcn_ballot_w64(((x.x ^ a) >> 24 | (x.y ^ b)) == 0); v1 = __builtin_amdgcn_ballot_w64(((x.z ^ a) >> 24 | (x.w ^ b)) == 0);
                    if (__builtin_popcountll(v0) + __builtin_popcountll(v1) >= MINV) break;
                    SLEEP(8)
                }
                const u64 m0 = __builtin_amdgcn_ballot_w64(x.x & 1) & v0, m1 = __builtin_amdgcn_ballot_w64(x.z & 1) & v1;
                const int s0 = lane < 43 ? min(max(lane - VW, 4 * NMIX), 42 - 2 * VW) : min(max(lane - VW, 43), 63 - 2 * VW);
#define PC(M) __builtin_popcount((unsigned)((M) >> s0) & ((2u << 2 * VW) - 1))
                const u64 g0 = __builtin_amdgcn_ballot_w64(2 * PC(m0) > PC(v0)), g1 = __builtin_amdgcn_ballot_w64(2 * PC(m1) > PC(v1));
                const float sl = wave_sum(((v0 >> lane & 1) ? (float)(x.x >> 1 & 4095u) : 0.f) + ((v1 >> lane & 1) ? (float)(x.z >> 1 & 4095u) : 0.f)) / ((8.f - PRE0) * (__builtin_popcountll(v0) + __builtin_popcountll(v1)) + 1.f);
                const unsigned al = lds_ld(&sm.aline), mean = (al ? al : own) + (int)fmaxf(sl - SL0, 0.f);
                const bool vz = lane < 16 && ((z.x ^ a) >> 24 | (z.y ^ b)) == 0;
                const unsigned gw = 2 * __builtin_popcountll(__builtin_amdgcn_ballot_w64(vz && (z.x & 1))) > __builtin_popcountll(__builtin_amdgcn_ballot_w64(vz));
                const unsigned k = lane & 3;
                if (lane < 32 && k < 3) __builtin_amdgcn_raw_buffer_store_b128((v4u){k == 0 ? (unsigned)g0 : k == 1 ? (unsigned)g1 : mean, tg, k == 0 ? (unsigned)(g0 >> 32) : k == 1 ? (unsigned)(g1 >> 32) : gw, tg},
                                                                              nrs, (lane >> 2) * 1280 + k * 16, 0, SC1);
            }
            while ((int)(NOW() - own) < TMAP + TRES) SLEEP(2)
            v4u y;
            for (;;) {
                y = __builtin_amdgcn_raw_buffer_load_b128(nrs, lane < 3 ? (blockIdx.x & 7) * 1280 + lane * 16 : 0x40000000, 0, SC1);
                if (((unsigned)__builtin_amdgcn_ballot_w64(y.y == tg && y.w == tg) & 7u) == 7u) break;
                SLEEP(8)
            }
            const u64 f0 = (unsigned)__builtin_amdgcn_readlane(y.x, 0) | (u64)(unsigned)__builtin_amdgcn_readlane(y.z, 0) << 32, f1 = (unsigned)__builtin_amdgcn_readlane(y.x, 1) | (u64)(unsigned)__builtin_amdgcn_readlane(y.z, 1) << 32;
            if (lane == 2) { sm.aline = y.x - own + 500 < 1000 ? y.x : own; sm.chead = (blockIdx.x & ~5u) | ((blockIdx.x >> 2 ^ y.z) & 1) | (blockIdx.x & 1) << 2; }
            unsigned bit = ((unsigned)((lane >= NU ? f1 : f0) >> (4 * win + (pair >> 1))) ^ (0x66 >> pair)) & 1;
            vs = (f0 ^ f1) >> 4 * NMIX && !((f0 ^ f1) >> 4 * NMIX & 4095) ? NMIX : 0;
            if (eu < NMIX) { if (lane < NU || !vs) bit = eu & 1; else rot2 = rotn; }
            const u64 bm = __builtin_amdgcn_ballot_w64(bit);
            const unsigned nsw = __builtin_popcountll((f0 ^ f1) >> (4 * NMIX)) + (vs ? 2 * NMIX : 0);
            const unsigned sw = (unsigned)(bm ^ (bm >> NU)) & ((1u << NU) - 1), sx = (sw >> vs ^ sw) & 7;
            if (lane == 0) { sm.swp = sw ^ sx ^ sx << vs; lds_st(&sm.gap, (nsw ? (TG0 + (vs ? TGV : 0) + TG1 * nsw / 8) * 0x1001u + ((TH0 + (vs ? THV : 0) + TH1 * nsw / 8) << 12) : 0u) | (vs ? 1u << 31 : 0u)); }
            CBAR();
#define VPOS(E) ((E) < NMIX ? (E) + vs : (E) - vs < NMIX ? (E) - vs : (E))
            lds_st(&sm.utab[lane >= NU ? NU + VPOS(eu) : lane], UENT(bit));
        }
        const unsigned swp = lds_ld(&sm.swp);
        const unsigned T = lds_ld(&sm.tag_lo);
        const unsigned t15 = T & 0x7fffu;
        const __amdgpu_buffer_rsrc_t rsrc = __builtin_amdgcn_make_buffer_rsrc(ws + WS_PART + split * 512, 0, NRW * NSPLIT * 512 - split * 512, 0x00020000);
        v2u* recs = (v2u*)&sm.cs[0][0][0];
#pragma unroll
        for (int i = 0; i < NOUT * 3; i++) (&sm.acc[0][0])[i * 64 + lane] = 0.0;
        CBAR();
        unsigned vut = sm.utab[NU + (lane & 31)] & 255;
        if (QUARTER((unsigned)lane) && (rank & 3)) vut = NH - 16 + (rank & 3) * 16 + ((vut & 15) ^ 1);
        __builtin_amdgcn_s_setprio(3);
        unsigned po[NU / 2];
#pragma unroll
        for (int k = 0; k < NU / 2; k++) po[k] = __builtin_amdgcn_ds_bpermute((2 * k + (lane >> 5)) * 4, vut) * (NSPLIT * 512) + (lane & 31) * 16;
        const u64 rb_ = (u64)(ws + WS_PART + split * 512);
        const v4u rs_ = {(unsigned)rb_, (unsigned)(rb_ >> 32) & 0xffffu, NRW * NSPLIT * 512 - split * 512, 0x00020000u};
#pragma unroll 1
        for (unsigned u = 0; u < 2 * NU - 1; u++) {
            const unsigned e = u % NU, so = u % NOUT;
            if (u >= NU) while (!((lds_ld(&sm.vrdy) >> e) & 1)) SLEEP(4)
            CBAR();
            const v4f h = *(const v4f*)&sm.wv[went(e) + (QUARTER(e) ? 32 : 128)];
            while (lds_ld(&sm.cnt[so]) != (QUARTER(e) ? 8 : 32)) SLEEP(1)
            CBAR();
            const double a0 = sm.acc[so][lane], a1 = sm.acc[so][64 + lane], a2 = sm.acc[so][128 + lane];
            if (u < NU) {
                float2 lg = *(const float2*)&sm.ent[so][2 * lane];
                if (QUARTER(e) && lane >> 4 != (rank & 3)) lg = make_float2(-INFINITY, -INFINITY);
                const v4f zs = *(const v4f*)&sm.vz[u % NIN][2 * lane];
                const float m = wave_max(fmaxf(lg.x, lg.y));
                const float p0 = __expf(lg.x - m), p1 = __expf(lg.y - m);
                const float x0 = p0 * zs.y, x1 = p1 * zs.w;
                const unsigned eb = max(__float_as_uint(wave_max(fmaxf(fabsf(x0), fabsf(x1)))) >> 23, 14u);
                const float f = __uint_as_float((268u - eb) << 23);
                const float w0 = fminf(fmaxf(rintf(x0 * f), -32767.f), 32767.f), w1 = fminf(fmaxf(rintf(x1 * f), -32767.f), 32767.f);
                const float den = wave_sum(p0 + p1);
                const float bz = wave_sum(fmaf(w0, zs.x, w1 * zs.z));
                const float L = fmaf(m, 1.4426950408889634f, __builtin_amdgcn_logf(den));
                const unsigned vp = VPOS(u);
                unsigned* wd = (unsigned*)(((swp >> vp) & 1 ? sm.wo : sm.wv) + went(vp));
                if (!QUARTER(u) || lane >> 4 == (rank & 3)) wd[QUARTER(u) ? lane & 15 : lane] = ((unsigned)(int)w0 & 0xffffu) | ((unsigned)(int)w1 << 16);
                if (lane == 0) *(v4f*)&wd[QUARTER(u) ? 16 : 64] = (v4f){ldexpf(1.f / den, (int)eb - 141), bz, L, 0.f};
                CBAR();
                if (!((swp >> vp) & 1)) LDS_OR(&sm.vrdy, 1u << vp)
            } else {
                const float n0 = (float)(a0 - (double)h.y) * h.x, n1 = (float)(a1 - (double)h.y) * h.x, n2 = (float)(a2 - (double)h.y) * h.x;
                const unsigned e2 = max(__float_as_uint(wave_maxv(fmaxf(fmaxf(fabsf(n0), fabsf(n1)), fabsf(n2)))) >> 23, 14u);
                const float f2 = __uint_as_float((268u - e2) << 23);
                const unsigned m01 = __builtin_bit_cast(unsigned, __builtin_amdgcn_cvt_pk_i16((int)rintf(n0 * f2), (int)rintf(n1 * f2)));
                const unsigned m2 = __builtin_bit_cast(unsigned, __builtin_amdgcn_cvt_pk_i16((int)rintf(n2 * f2), 0));
                const unsigned bit = ((lane & 1 ? e2 : __float_as_uint(h.z)) >> (lane >> 1)) & 1u;
                recs[e * 64 + lane] = (v2u){m01, m2 | bit << 16 | t15 << 17};
                CBAR();
                sm.acc[so][lane] = 0.0; sm.acc[so][64 + lane] = 0.0; sm.acc[so][128 + lane] = 0.0;
            }
            CBAR();
            if (lane == 0) { lds_st(&sm.cnt[so], 0u); CBAR(); lds_st(&sm.merged, u + 1); }
            if (u == NU - 1 && swp) {
                wave_sync();
                const __amdgpu_buffer_rsrc_t srs = __builtin_amdgcn_make_buffer_rsrc(ws + WS_SWAP + blockIdx.x * 5120, 0, 5120, 0x00020000);
                v4u d[5]; unsigned o[5];
#pragma unroll
                for (int i = 0; i < 5; i++) {
                    const unsigned* w = (const unsigned*)&sm.wo[144 * NMIX + (i * 64 + lane) * 6];
                    const unsigned d0 = w[0], d1 = w[1], d2 = w[2];
                    d[i] = (v4u){d0, (d1 & 0xffffu) | (T << 16), (d1 >> 16) | (d2 << 16), (d2 >> 16) | ((T + (T >> 16)) << 16)};
                    o[i] = (swp >> wunit(i * 64 + lane)) & 1 ? (i * 64 + lane) * 16 : 0x40000000;
                }
                while ((int)(NOW() - lds_ld(&sm.line)) < (int)(tdue(PSW - 1) + tvx(PSW - 1, lds_ld(&sm.gap))) + 10 + TWO) SLEEP(1)
#pragma unroll
                for (int i = 0; i < 5; i++) __builtin_amdgcn_raw_buffer_store_b128(d[i], srs, o[i], 0, SC1 | 2);
            }
        }
        wave_sync();
#pragma unroll
        for (int k = 0; k < NU / 2; k++) asm volatile("ds_read_b128 a[%c0:%c0+3], %1 offset:%2" :: "i"(16 + 4 * k), "v"(lds_addr(recs) + lane * 16), "i"(k * 1024) : "memory");
        asm volatile("s_waitcnt lgkmcnt(0)" ::: "memory");
        while (!((lds_ld(&sm.vrdy) >> (NU - 1)) & 1)) SLEEP(4)
        CBAR();
        const v2u hw_ = {*(const unsigned*)&sm.wv[went(NU - 1) + 132], T};
        const unsigned hrow = (NRW - GSZ + (__builtin_amdgcn_readlane(vut, NU - 1) & 15 ^ 1)) * (NSPLIT * 512);
        while (lds_ld(&sm.grab) < NPC - PUBP) SLEEP(1)
        if (lane < 16) __builtin_amdgcn_raw_buffer_store_b64(hw_, rsrc, lane * 8, hrow, SC1);
#define PUB(K) asm volatile("buffer_store_dwordx4 a[%c0:%c0+3], %1, %2, 0 offen sc1" :: "i"(16 + 4 * (K)), "v"(po[K]), "s"(rs_) : "memory");
        PUB(WQ / 2) PUB(WQ / 2 + 1)
        { const unsigned t_ = TLAST() + PDEL; GATE(t_) }
        PUB(0) PUB(1) PUB(2) PUB(3) PUB(4) PUB(7) PUB(8)
        return;
    }
    }
    __builtin_amdgcn_s_setprio(0);
    unsigned c;
#define ADDRTN(P) { u64 ex_; asm volatile("s_mov_b64 %0, exec\n\ts_mov_b64 exec, 1\n\tds_add_rtn_u32 a15, %1, a13\n\ts_mov_b64 exec, %0" : "=&s"(ex_) : "v"(lds_addr(P)) : "a15", "memory"); }
#define ADDED(V) { unsigned t_; asm volatile("s_waitcnt lgkmcnt(0)\n\tv_accvgpr_read_b32 %1, a15\n\ts_nop 0\n\tv_readfirstlane_b32 %0, %1\n\ts_nop 1" : "=s"(V), "=v"(t_)); }
    asm volatile("v_accvgpr_write_b32 a13, 1\n\ts_nop 1" ::: "a13");
    ADDRTN(&sm.cidx) ADDED(c)
    if (c < NSW) {
        while (lds_ld(&sm.grab) < NPC + 2) SLEEP(4)
        const unsigned head = lds_ld(&sm.chead), T = lds_ld(&sm.tag_lo), tl = TLAST();
        const u64 rb_ = (u64)(ws + WS_PART);
        const v4u rs_ = {(unsigned)rb_, (unsigned)(rb_ >> 32) & 0xffffu, NRW * NSPLIT * 512, 0x00020000u};
        const unsigned ro = head * (NSPLIT * 512) + c * 1024, rx = (NH + (head & 15 ^ 1)) * (NSPLIT * 512) + c * 1024;
#define POLL(S, O) asm volatile("buffer_load_dwordx4 a[%c0:%c0+3], %1, %2, %3 offen sc1" :: "i"(16 + 4 * (S)), "v"(lane * 16), "s"(rs_), "s"(O) : "memory");
#define FETCH(X, S, N) asm volatile("s_waitcnt vmcnt(%c5)\n\tv_accvgpr_read_b32 %0, a[%c4]\n\tv_accvgpr_read_b32 %1, a[%c4+1]\n\tv_accvgpr_read_b32 %2, a[%c4+2]\n\tv_accvgpr_read_b32 %3, a[%c4+3]" \
                                    : "=v"((X).x), "=v"((X).y), "=v"((X).z), "=v"((X).w) : "i"(16 + 4 * (S)), "i"(N));
        float o0, o1, o2;
        if (head >= NH - GSZ) {
            __builtin_amdgcn_s_setprio(3);
            if (c == 0) {
                v2u hd;
                GATE(tl + THD)
                for (;;) {
                    hd = __builtin_amdgcn_raw_buffer_load_b64(__builtin_amdgcn_make_buffer_rsrc(ws + WS_PART + (NRW - GSZ + (head & 15 ^ 1)) * (NSPLIT * 512), 0, NSPLIT * 512, 0x00020000), (lane & 15) * 512, 0, SC1);
                    if (__builtin_amdgcn_ballot_w64(hd.y == T) == ~0ull) break;
                    SLEEP(4)
                }
                const float w = __builtin_amdgcn_exp2f(__uint_as_float(hd.x) - row_max(__uint_as_float(hd.x)));
                if (lane < 16) sm.ent[0][lane] = w * __builtin_amdgcn_rcpf(row_sum(w));
                CBAR();
                if (lane == 0) lds_st(&sm.tabs, 0u);
            }
            while (lds_ld(&sm.tabs) || lds_ld(&sm.merged) <= (2 * NU - 1) / NOUT * NOUT) SLEEP(1)
            CBAR();
            const float cf = sm.ent[0][2 * c + (lane >> 5)];
            v4u x; bool got;
#define SEEN(J, N) { FETCH(x, J, N) got = __builtin_amdgcn_ballot_w64((((x.y ^ T << 24) | (x.w ^ T << 24)) >> 24) != 0) == 0; }
            GATE(tl + TCA) POLL(0, ro) GATE(tl + TCA + TCD) POLL(1, rx + 3 * GSZ * NSPLIT * 512) SEEN(0, 1)
            if (!got) SEEN(1, 0)
            while (!got) { POLL(2, rx + 4 * GSZ * NSPLIT * 512) SEEN(2, 0) SLEEP(0) }
            const double c0 = ldexpf(cf, (int)(x.y >> 16 & 255u) - 141), c1 = ldexpf(cf, (int)(x.w >> 16 & 255u) - 141);
#define SWADD(A, B) ({ const v2u a_ = __builtin_bit_cast(v2u, A), b_ = __builtin_bit_cast(v2u, B); \
                const auto l_ = __builtin_amdgcn_permlane32_swap(a_.x, b_.x, false, false); const auto h_ = __builtin_amdgcn_permlane32_swap(a_.y, b_.y, false, false); \
                __builtin_bit_cast(double, (v2u){l_[0], h_[0]}) + __builtin_bit_cast(double, (v2u){l_[1], h_[1]}); })
            const double s0 = SWADD(c0 * (double)(short)x.x, c1 * (double)(short)x.z), s1 = SWADD(c0 * (double)((int)x.x >> 16), c1 * (double)((int)x.z >> 16)),
                         s2 = SWADD(c0 * (double)(short)x.y, c1 * (double)(short)x.w);
            asm volatile("ds_add_f64 %0, %1\n\tds_add_f64 %0, %2 offset:512\n\tds_add_f64 %0, %3 offset:1024" :: "v"(lds_addr(&sm.acc[0][lane])), "v"(s0), "v"(s1), "v"(s2) : "memory");
            ADDRTN(&sm.cdone)
            const double a0 = sm.acc[0][lane], a1 = sm.acc[0][64 + lane], a2 = sm.acc[0][128 + lane];
            ADDED(c)
            o0 = a0; o1 = a1; o2 = a2;
        } else {
            const bool quart = head >> 4 == WQ;
            const unsigned t15 = T & 0x7fffu;
#define TAKE(K) { FETCH(x[K], K, 0) \
                bad |= (x[K].y >> 17 ^ t15) | (x[K].w >> 17 ^ t15); \
                const u64 bl = __builtin_amdgcn_ballot_w64((x[K].y & 0x10000u) != 0), be = __builtin_amdgcn_ballot_w64((x[K].w & 0x10000u) != 0); \
                L[K] = __uint_as_float(lane < 32 ? (unsigned)bl : (unsigned)(bl >> 32)); ex[K] = lane < 32 ? (unsigned)be : (unsigned)(be >> 32); \
                M = fmaxf(M, fmaxf(__uint_as_float((unsigned)bl), __uint_as_float((unsigned)(bl >> 32)))); }
#define SUMUP(K) { const float w_ = __builtin_amdgcn_exp2f(L[K] - M), c_ = ldexpf(w_, (int)(ex[K] & 255u) - 141); den += w_; \
                t[0] = fmaf(c_, (float)(short)x[K].x, t[0]); t[1] = fmaf(c_, (float)((int)x[K].x >> 16), t[1]); t[2] = fmaf(c_, (float)(short)x[K].y, t[2]); \
                t[3] = fmaf(c_, (float)(short)x[K].z, t[3]); t[4] = fmaf(c_, (float)((int)x[K].z >> 16), t[4]); t[5] = fmaf(c_, (float)(short)x[K].w, t[5]); }
            const unsigned tg = tl + (quart ? TRQ : TRD);
            GATE(tg)
            v4u x[4]; float L[4], M; unsigned ex[4];
            for (;;) {
                unsigned bad = 0; M = -3e38f;
                POLL(0, ro)
                if (quart) { POLL(1, rx) POLL(2, rx + GSZ * NSPLIT * 512) POLL(3, rx + 2 * GSZ * NSPLIT * 512) }
                __builtin_amdgcn_s_setprio(3);
                TAKE(0)
                if (quart) { TAKE(1) TAKE(2) TAKE(3) }
                if (__builtin_amdgcn_ballot_w64(bad != 0) == 0) break;
                SLEEP(2)
            }
            float t[6] = {0.f, 0.f, 0.f, 0.f, 0.f, 0.f}, den = 0.f;
            SUMUP(0)
            if (quart) { SUMUP(1) SUMUP(2) SUMUP(3) }
#define SWADF(A, B) ({ const auto l_ = __builtin_amdgcn_permlane32_swap(__float_as_uint(A), __float_as_uint(B), false, false); __uint_as_float(l_[0]) + __uint_as_float(l_[1]); })
            v4f* pb = (v4f*)((char*)&sm.cs[0][0][0] + 65536);
            float* pm = (float*)(pb + NSW * 64);
            pb[c * 64 + lane] = (v4f){SWADF(t[0], t[3]), SWADF(t[1], t[4]), SWADF(t[2], t[5]), SWADF(den, den)};
            if (lane == 0) pm[c] = M;
            CBAR();
            ADDRTN(&sm.cdone)
            ADDED(c)
            CBAR();
            if (c == NSW - 1) {
                const v4f ma = *(const v4f*)pm, mb = *(const v4f*)(pm + 4);
                const float mw[NSW] = {ma.x, ma.y, ma.z, ma.w, mb.x, mb.y, mb.z, mb.w};
                float Mx = mw[0];
#pragma unroll
                for (int w = 1; w < NSW; w++) Mx = fmaxf(Mx, mw[w]);
                v4f sum = {0.f, 0.f, 0.f, 0.f};
#pragma unroll
                for (int w = 0; w < NSW; w++) sum += pb[w * 64 + lane] * __builtin_amdgcn_exp2f(mw[w] - Mx);
                const float inv = __builtin_amdgcn_rcpf(sum.w);
                o0 = sum.x * inv; o1 = sum.y * inv; o2 = sum.z * inv;
            }
        }
        if (c == NSW - 1) {
            asm("" : "+v"(o0), "+v"(o1), "+v"(o2));
            typedef __bf16 v2b __attribute__((ext_vector_type(2)));
            typedef unsigned v3u __attribute__((ext_vector_type(3)));
#define PK(A, B) __builtin_bit_cast(unsigned, (v2b){(__bf16)(A), (__bf16)(B)})
            const unsigned p12 = PK(o1, o2);
            const v3u ov = {PK(o0, o1), PK(o2, __uint_as_float(__builtin_amdgcn_permlane32_swap(__float_as_uint(o0), __float_as_uint(o0), false, false)[1])), __builtin_amdgcn_permlane32_swap(p12, p12, false, false)[1]};
            if (lane < 32) __builtin_amdgcn_raw_buffer_store_b96(ov, __builtin_amdgcn_make_buffer_rsrc(out + (size_t)head * DH, 0, DH * 2, 0x00020000), lane * 12, 0, 0);
        }
    }
}

extern "C" void kernel_launch(void* const* d_in, const int* in_sizes, int n_in,
                              void* d_out, int out_size, void* d_ws, size_t ws_size,
                              hipStream_t stream) {
    const unsigned short* q = (const unsigned short*)d_in[0];
    const int* qk = (const int*)d_in[1];
    const unsigned short* k_scales = (const unsigned short*)d_in[2];
    const unsigned short* k_zeros = (const unsigned short*)d_in[3];
    const int* qv = (const int*)d_in[4];
    const unsigned short* v_scales = (const unsigned short*)d_in[5];
    const unsigned short* v_zeros = (const unsigned short*)d_in[6];
    const unsigned short* q_cos = (const unsigned short*)d_in[7];
    const unsigned short* q_sin = (const unsigned short*)d_in[8];
    const unsigned short* k_cos = (const unsigned short*)d_in[9];
    const unsigned short* k_sin = (const unsigned short*)d_in[10];
    unsigned char* ws = (unsigned char*)d_ws;
    attn_kernel<<<NSPLIT * GSZ, NTHREADS, 0, stream>>>(q, qk, k_scales, k_zeros, qv, v_scales, v_zeros,
                                                       q_cos, q_sin, k_cos, k_sin, (__hip_bfloat16*)d_out, ws);
}
```

```cpp
#include <hip/hip_runtime.h>
#include <hip/hip_bf16.h>
#include <stdint.h>

#define DH 192
#define HALF 96
#define SKV 2048
#define NH 256
#define NSW 8
#define NWAVES (NSW + 4)
#define NTHREADS (64 * NWAVES)
#define BROWS 128
#define NSPLIT 16
#define GSZ 16
#define WQ 10
#define NU (NH / GSZ + 3)
#define NPC 1024
#define NRR (NH + 3 * GSZ)
#define NIN 8
#define NOUT 8
#define WALL (15 * 144 + 4 * 48)
#define SC1 16
#define NRND 48
#define RST 4352
#define TK 2
#define NMIX 3
#define TUNIT 311
#define T01 50
#define TMIX 7
#define TOFF 50
#define PSF 100
#define TGS 82
#define PSW (NPC / 2 + 28)
#define TG0 8
#define TG1 5
#define TWO -80
#define PSG (NPC / 2 + 60)
#define TH0 3
#define TH1 3
#define EARLY 10
#define LEADP 24
#define DPH 2
#define TA 210
#define TA2 70
#define CSH 2
#define NPRB 3
#define TMAP 485
#define MINV 96
#define VW 4
#define MASTER 203
#define TRES 175
#define TB1 1200
#define PER 300
#define RDD 100
#define LEAD 70
#define LHI 85
#define LOK 74
#define PSL0 124
#define PEND 36
#define PUBP -12
#define LJ 210
#define LOK2 75
#define NTRY 3
#define TFO 6
#define SL0 42.f
#define PST0 200
#define KC 4
#define TC0 1000
#define TCS 9
#define PRE0 3

#define KREL 12
#define PDEL 68
#define NCP 3
#define NRW (NRR + NCP * GSZ)
#define TRD 112
#define THD 55
#define TRQ 55
#define TCA 132
#define TCD 22
#define TSD 560
#define THV 12
#define TGV -8
#define TKV 7
#define WS_EPOCH 0
#define WS_POST0 8192
#define WS_POST (WS_POST0 + ((unsigned)E1 & 15) * 2048)
#define WS_NEWS (49152 + 256)
#define WS_HELLO 59904
#define WS_BOARD 65536
#define WS_PART (2u << 20)
#define WS_SWAP (WS_PART + NRW * NSPLIT * 512)

typedef unsigned int v2u __attribute__((ext_vector_type(2)));
typedef unsigned int v16u __attribute__((ext_vector_type(16)));
typedef unsigned int v4u __attribute__((ext_vector_type(4)));
typedef int v4i __attribute__((ext_vector_type(4)));
typedef float v2f __attribute__((ext_vector_type(2)));
typedef float v4f __attribute__((ext_vector_type(4)));
typedef unsigned long long u64;

__device__ __forceinline__ float bflo(unsigned u) { return __uint_as_float(u << 16); }
__device__ __forceinline__ float bfhi(unsigned u) { return __uint_as_float(u & 0xffff0000u); }
__device__ __forceinline__ float bfr(float x) {
    __bf16 b = (__bf16)x;
    return __uint_as_float(((unsigned)__builtin_bit_cast(unsigned short, b)) << 16);
}

#define SLEEP(n) SLEEP_(n)
#define SLEEP_(n) asm volatile("s_sleep " #n "\n\tv_add_co_u32 %0, vcc, 1, %0\n\ts_cbranch_vccz 1f\n\ts_endpgm\n1:" : "+v"(spins) :: "vcc", "memory");
#define SPIN0 0xf0000000u
#define CBAR() asm volatile("" ::: "memory")

__device__ __forceinline__ void wave_sync() {
    __builtin_amdgcn_fence(__ATOMIC_RELEASE, "wavefront");
    __builtin_amdgcn_wave_barrier();
    __builtin_amdgcn_fence(__ATOMIC_ACQUIRE, "wavefront");
}
template <int CTRL>
__device__ __forceinline__ float dpp_add(float x) {
    int y = __builtin_amdgcn_update_dpp(0, __float_as_int(x), CTRL, 0xf, 0xf, true);
    return x + __int_as_float(y);
}
template <int CTRL>
__device__ __forceinline__ float dpp_max(float x) {
    int y = __builtin_amdgcn_update_dpp(__float_as_int(x), __float_as_int(x), CTRL, 0xf, 0xf, false);
    return fmaxf(x, __int_as_float(y));
}
__device__ __forceinline__ float row_sum(float x) { x = dpp_add<0x128>(x); x = dpp_add<0x124>(x); x = dpp_add<0x122>(x); return dpp_add<0x121>(x); }
__device__ __forceinline__ float row_max(float m) { m = dpp_max<0x128>(m); m = dpp_max<0x124>(m); m = dpp_max<0x122>(m); return dpp_max<0x121>(m); }
__device__ __forceinline__ float rdlane(float x, int l) { return __int_as_float(__builtin_amdgcn_readlane(__float_as_int(x), l)); }
__device__ __forceinline__ float wave_max(float m) { m = row_max(m); return fmaxf(fmaxf(rdlane(m, 0), rdlane(m, 16)), fmaxf(rdlane(m, 32), rdlane(m, 48))); }
__device__ __forceinline__ float wave_maxv(float m) {
    m = row_max(m);
    const auto a = __builtin_amdgcn_permlane16_swap(__float_as_uint(m), __float_as_uint(m), false, false);
    m = fmaxf(__uint_as_float(a[0]), __uint_as_float(a[1]));
    const auto b = __builtin_amdgcn_permlane32_swap(__float_as_uint(m), __float_as_uint(m), false, false);
    return fmaxf(__uint_as_float(b[0]), __uint_as_float(b[1]));
}
__device__ __forceinline__ float wave_sum(float x) { x = row_sum(x); return (rdlane(x, 0) + rdlane(x, 16)) + (rdlane(x, 32) + rdlane(x, 48)); }
__device__ __forceinline__ unsigned lds_ld(const unsigned* p) { return __builtin_amdgcn_readfirstlane(__hip_atomic_load(p, __ATOMIC_RELAXED, __HIP_MEMORY_SCOPE_WORKGROUP)); }
__device__ __forceinline__ unsigned lds_addr(const void* p) { return (unsigned)(size_t)(const __attribute__((address_space(3))) void*)p; }
#define DMA16(SRC, LDS) asm volatile("s_mov_b32 %0, m0\n\ts_mov_b32 m0, %2\n\ts_nop 0\n\tglobal_load_lds_dwordx4 %1, off\n\ts_mov_b32 m0, %0" : "=&s"(m0keep) : "v"(SRC), "s"(LDS) : "memory")
__device__ __forceinline__ void lds_st(unsigned* p, unsigned v) { __hip_atomic_store(p, v, __ATOMIC_RELAXED, __HIP_MEMORY_SCOPE_WORKGROUP); }
#define NOW() ((unsigned)__builtin_amdgcn_s_memrealtime())
__device__ __forceinline__ unsigned tdue(unsigned g) {
    return (g * TUNIT + min(g, 480u) * TK + min(g, 48u) * T01 + min(g, 32u * NMIX) * TMIX + (g >= PSF ? TGS * 32 : 0)) >> 5;
}
__device__ __forceinline__ unsigned tvx(unsigned g, unsigned gap) {
    return (g >= PSW ? gap >> (g >= PSG ? 12 : 0) & 0xfffu : 0u) + (gap >> 31 ? TKV : min(g - NPC / 2, 32u * NMIX) * TMIX >> 5);
}

__device__ __forceinline__ unsigned unit_of(unsigned g) { const unsigned p = g & 511; return (g >> 9) * NU + (p >> 5) + min(max((int)p - 32 * WQ, 0) >> 3, 3); }
__device__ __forceinline__ unsigned first_of(unsigned e) { return 32 * e - 24 * min(max((int)e - WQ, 0), 4); }
#define QUARTER(E) ((E) - WQ < 4)
__device__ __forceinline__ unsigned went(unsigned e) { return 144 * e - 96 * min(max((int)e - WQ, 0), 4); }
__device__ __forceinline__ unsigned wunit(unsigned x) { x += 24 * NMIX; return x < 24 * WQ ? x / 24 : x < 24 * WQ + 32 ? WQ + (x - 24 * WQ) / 8 : (x + 64) / 24; }

#define ROTT 0xed8d2c3d2c35ed84ull, 0xba85aa165b03ac82ull, 0xee7efa489a38af71ull, 0xe932da1b0bab51d8ull, 0xfa96112d5fee971bull, 0x2c7e9ea5a544f071ull, 0x3d86093d5f0e810bull, 0xe932da1b0bab51d8ull, 0xfa96112d5fee971bull, 0xd834e62c5fa723e9ull, 0xa4cd778c2ca4aaa5ull, 0xe932da1b0bab51d8ull, 0xfa96112d5fee971bull, 0xc834e12c0aaa4ec7ull, 0x3d86093d5f0e810bull, 0xe932da1b0bab51d8ull
static __device__ const u64 ROT[16] = {ROTT};

struct __attribute__((aligned(16))) Smem {
    unsigned cs[NSW][4][768];
    double acc[NOUT][DH];
    float ent[NOUT][BROWS];
    unsigned short wv[WALL];
    unsigned short wo[WALL];
    unsigned q[NIN][HALF];
    float2 kz[NIN][BROWS];
    float2 vz[NIN][BROWS];
    unsigned utab[64];
    unsigned line, gap;
    unsigned late, grab;
    unsigned rdy[NIN], cnt[NOUT];
    unsigned tabs, vrdy, swp;
    unsigned aline;
    unsigned tag_rdy, tag_lo, tag_hi, merged, cdone, cidx;
    unsigned slow;
    unsigned chead;
};

#define LDS_ADD(P, V) { u64 ex_; asm volatile("s_mov_b64 %0, exec\n\ts_mov_b64 exec, 1\n\tds_add_u32 %1, %2\n\ts_mov_b64 exec, %0" : "=&s"(ex_) : "v"(lds_addr(P)), "v"(V) : "memory"); }
#define LDS_OR(P, V) { u64 ex_; asm volatile("s_mov_b64 %0, exec\n\ts_mov_b64 exec, 1\n\tds_or_b32 %1, %2\n\ts_mov_b64 exec, %0" : "=&s"(ex_) : "v"(lds_addr(P)), "v"(V) : "memory"); }

__global__ __launch_bounds__(NTHREADS, 3) void attn_kernel(
    const unsigned short* __restrict__ q, const int* __restrict__ qk,
    const unsigned short* __restrict__ k_scales, const unsigned short* __restrict__ k_zeros,
    const int* __restrict__ qv,
    const unsigned short* __restrict__ v_scales, const unsigned short* __restrict__ v_zeros,
    const unsigned short* __restrict__ q_cos, const unsigned short* __restrict__ q_sin,
    const unsigned short* __restrict__ k_cos, const unsigned short* __restrict__ k_sin,
    __hip_bfloat16* __restrict__ out, unsigned char* __restrict__ ws)
{
    __shared__ Smem sm;
    const int tid = threadIdx.x;
    unsigned spins = SPIN0;
    const int lane = tid & 63;
    const int hw = __builtin_amdgcn_readfirstlane(tid >> 6);
    u64 t00; unsigned ka;
    asm volatile("s_memrealtime %0\n\ts_load_dword %1, %2, 0x40" : "=&s"(t00), "=&s"(ka) : "s"(__builtin_amdgcn_kernarg_segment_ptr()));
    if (hw == 0) {
        const char* pc0; asm volatile("s_getpc_b64 %0" : "=s"(pc0));
        asm volatile("global_load_dword a6, %0, %1" :: "v"(((blockIdx.x & ~7u) + (lane & 7)) * 128), "s"(pc0) : "a6", "memory");
    }
    const int wave = hw >= 4 ? hw - 4 : hw + NSW;
    const unsigned split = (blockIdx.x & 1) + (blockIdx.x & 2) * 4 + ((blockIdx.x >> 3) & 3) * 2;
    const unsigned side = (split ^ (blockIdx.x >> 2)) & 1, rank = blockIdx.x >> 5;
    const unsigned head0 = 2 * rank + side;
    const unsigned rf0 = (8 * split + 1) / 3, r3 = 8 * split + 1 - 3 * rf0;
    const unsigned koff = ((lane >> 5) * DH + 3 * (lane & 31)) * 4, koffb = koff + 384;
#define AR(S) (16 + (S) * 8)
#define GLD(A, OFF, BASE, IMM) asm volatile("global_load_dwordx3 a[%c0:%c1], %2, %3 offset:%4 nt" :: "i"(A), "i"((A) + 2), "v"(OFF), "s"(BASE), "i"(IMM))
#define ISSUE(S, J, BASE, OA, OB) { GLD(AR(S), OA, BASE, (J) * 1536); GLD(AR(S) + 4, OB, BASE, (J) * 1536); }
#define HEAD2 ((GSZ + 1 - 2 * side) * (SKV * DH * 4))
    const unsigned wo = (hw - 2) & 7;
    const bool pre = hw >= 4 && wo < PRE0;
    if (pre) {
        const char* a = (const char*)qk + (head0 * 512 + side * 256 + rf0 + wave + (wave >= 6 ? 80 : 0)) * 3072; const char* b = a + HEAD2;
        ISSUE(0, 0, a, koff, koffb) ISSUE(1, 1, a, koff, koffb) ISSUE(2, 0, b, koff, koffb) ISSUE(3, 1, b, koff, koffb)
    }
    asm volatile("s_waitcnt lgkmcnt(0)" : "+s"(t00), "+s"(ka));
    if (hw == 0) {
        constexpr u64 rot[16] = {ROTT};
        sm.utab[lane] = lane == 2 ? 2 * GSZ + head0 | ((unsigned)(rot[2] >> 4 * head0) & 15) << 9 : ~0u;
        unsigned l0 = (unsigned)t00 + TOFF;
        if (KREL) { const unsigned t1 = NOW() + KREL; if ((int)(t1 - l0) > 0) l0 = t1; }
        if (lane < 32) (&sm.line)[lane] = lane == 3 ? 64u : (lane & 29) == 0 ? l0 : 0u;
        asm volatile("s_waitcnt lgkmcnt(0)" ::: "memory");
    }
    __builtin_amdgcn_s_barrier();
    CBAR();
    const u64 rtab = r3 == 1 ? 0x5756552c2b020100ull : r3 == 0 ? 0x5756552c2b2a0100ull : 0x57562d2c2b020100ull;
#define R8(I) (rf0 + ((unsigned)(rtab >> (8 * (I))) & 255u))
#define GATE(DUE) { int d_ = (int)((DUE) - NOW()); if (d_ < 3000) while (d_ > 0) { __builtin_amdgcn_s_sleep(1); d_ = (int)((DUE) - NOW()); } }
#define TLAST() (lds_ld(&sm.line) + tdue(NPC - 2) + tvx(NPC - 2, lds_ld(&sm.gap)))
    if (__builtin_expect(wave < NSW, 1)) {
        const int cst = 2 * side;
        const unsigned r8w = R8((unsigned)wave);
        const char* cbase[3];
#pragma unroll
        for (int j = 0; j < 3; j++) { const int x = j * 64 + lane; cbase[j] = (x < 96 ? (const char*)k_cos + x * 16 : (const char*)k_sin + (x - 96) * 16) + r8w * (4 * DH * 2); }
        unsigned m0keep;
#define CS_DMA(W, C) { _Pragma("unroll") for (int j = 0; j < 3; j++) DMA16(cbase[j] + (C) * (512 * DH * 2), lds_addr(&sm.cs[0][0][0]) + ((W) * 4 + (C)) * 3072 + j * 1024); }
#define REPACK(W, C) { \
        unsigned* cw = &sm.cs[W][C][0]; \
        const unsigned* l = cw + rsel * (DH / 2) + qoff; \
        unsigned r[2][6]; \
        _Pragma("unroll") \
        for (int j = 0; j < 2; j++) { \
            unsigned e[4][2]; \
            _Pragma("unroll") \
            for (int k = 0; k < 4; k++) { \
                const unsigned* a = l + j * DH + (k & 1) * 48 + (k >> 1) * 384;       \
                unsigned x0 = a[0], x1 = a[1]; \
                e[k][0] = __builtin_amdgcn_alignbit(x1, x0, sh); \
                e[k][1] = x1 >> sh; \
            } \
            r[j][0] = (e[0][0] & 0xffffu) | (e[3][0] << 16); \
            r[j][1] = (e[0][0] >> 16) | (e[3][0] & 0xffff0000u); \
            r[j][2] = (e[0][1] & 0xffffu) | (e[3][1] << 16); \
            r[j][3] = (e[1][0] & 0xffffu) | (e[2][0] << 16); \
            r[j][4] = (e[1][0] >> 16) | (e[2][0] & 0xffff0000u); \
            r[j][5] = (e[1][1] & 0xffffu) | (e[2][1] << 16); \
        } \
        wave_sync(); \
        _Pragma("unroll") \
        for (int j = 0; j < 2; j++) { *(v4u*)&cw[j * 256 + lane * 4] = (v4u){r[j][0], r[j][1], r[j][2], r[j][3]}; *(v2u*)&cw[512 + j * 128 + lane * 2] = (v2u){r[j][4], r[j][5]}; } \
        wave_sync(); }
        const int sub = lane & 31;
        const int rsel = lane >> 5;
        const unsigned sh = (sub & 1) * 16;
        const int qoff = 3 * (sub >> 1) + (sub & 1);
        const unsigned voff = 3 * lane * 4;
        asm volatile("" ::: "a16", "a17", "a18", "a19", "a20", "a21", "a22", "a23", "a24", "a25", "a26", "a27", "a28", "a29", "a30", "a31",
                            "a32", "a33", "a34", "a35", "a36", "a37", "a38", "a39", "a40", "a41", "a42", "a43", "a44", "a45", "a46", "a47");
        asm volatile("" ::: "a48", "a49", "a50", "a51", "a52", "a53", "a54", "a55", "a56", "a57", "a58", "a59", "a60", "a61", "a62", "a63",
                            "a64", "a65", "a66", "a67", "a68", "a69", "a70", "a71", "a72", "a73", "a74", "a75", "a76", "a77", "a78", "a79");
        int ta[4][3], tb[4][3];
#define WAITT(P, S, N) asm volatile("s_waitcnt vmcnt(%6)\n\tv_accvgpr_read_b32 %0, a[%c7]\n\tv_accvgpr_read_b32 %1, a[%c8]\n\tv_accvgpr_read_b32 %2, a[%c9]\n\t" \
                              "v_accvgpr_read_b32 %3, a[%c10]\n\tv_accvgpr_read_b32 %4, a[%c11]\n\tv_accvgpr_read_b32 %5, a[%c12]" \
                              : "=v"(ta[P][0]), "=v"(ta[P][1]), "=v"(ta[P][2]), "=v"(tb[P][0]), "=v"(tb[P][1]), "=v"(tb[P][2]) \
                              : "i"(N), "i"(AR(S)), "i"(AR(S) + 1), "i"(AR(S) + 2), "i"(AR(S) + 4), "i"(AR(S) + 5), "i"(AR(S) + 6))
        const unsigned voffb = voff + 768;
        u64 ln = 0;
#define TDUE(G) (tdue(G) + ((G) >= NPC / 2 ? tvx(G, __builtin_amdgcn_readfirstlane((unsigned)(ln >> 32))) : 0u))
#define REPORT() { const unsigned t_ = now - tdc; if (lane == 0) lds_st(&sm.late, t_); }
#define LINE() ln = LDV((u64*)&sm.line);
#define LDV(P) __hip_atomic_load(P, __ATOMIC_RELAXED, __HIP_MEMORY_SCOPE_WORKGROUP)
        unsigned u = 0, rg = cst * 8 + wave, np = 0;
        CS_DMA(wave, cst)

        const char* kb0 = (const char*)qk + (size_t)(head0 * 512 + r8w) * 3072;
#define KB(I) (kb0 + ((cst + (I)) & 3) * (128 * 3072))
        const char* kb; const char* kb2;
#define SISSUE(I) { kb = KB(I); kb2 = kb + HEAD2; const unsigned du_ = lds_ld(&sm.line) + tdue(16 * (I) + 2 * wo); GATE(du_) \
            ISSUE(((I) & 1) * 4, 0, kb, koff, koffb) ISSUE(((I) & 1) * 4 + 1, 1, kb, koff, koffb) ISSUE(((I) & 1) * 4 + 2, 0, kb2, koff, koffb) ISSUE(((I) & 1) * 4 + 3, 1, kb2, koff, koffb) }
        if (!pre) SISSUE(0)
        CS_DMA(wave, (cst + 1) & 3)
        if (pre) asm volatile("s_waitcnt vmcnt(3)" ::: "memory");
        asm volatile("s_waitcnt vmcnt(11)" ::: "memory");
        if (!pre) LDS_ADD(&sm.slow, NOW() - lds_ld(&sm.line) - tdue(2 * wo))
        REPACK(wave, cst)
        SISSUE(1)
#define TABDONE(I) LDS_OR(&sm.tabs, 1u << ((((cst + (I)) & 3) * 8) + wave))
        TABDONE(0)
        const float scale = 0.07216878364870322f;
        double acc[3] = {0.0, 0.0, 0.0};
        bool first = true, chg, none = false;
        unsigned Qp[3], Qn[3], Rp[3], Rn[3];
        float kz4[4], ksc, kres[4];
        v4u ca[4]; v2u cb[4];
        v2f qe[4][3], A[4];
        unsigned vut, nrg;
        const unsigned vr = ((lane >> 3) & 3) * 128 + R8((unsigned)lane & 7);
        unsigned g, mg;
        const unsigned grab_adr = lds_addr(&sm.grab);
        asm volatile("v_accvgpr_write_b32 a13, 1\n\tv_accvgpr_write_b32 a14, 2" ::: "a13", "a14", "a15");
#define GRAB(N) { u64 ex_; asm volatile("s_mov_b64 %0, exec\n\ts_mov_b64 exec, 1\n\tds_add_rtn_u32 a15, %1, a%c2\n\ts_mov_b64 exec, %0" : "=&s"(ex_) : "v"(grab_adr), "i"(12 + (N)) : "memory"); }
#define GRABBED() { unsigned t_; asm volatile("s_waitcnt lgkmcnt(0)\n\tv_accvgpr_read_b32 %1, a15\n\ts_nop 0\n\tv_readfirstlane_b32 %0, %1\n\ts_nop 1" : "=s"(g), "=v"(t_)); }
        unsigned oa, ob, un, tdn, tiss, tdc = 0;
#define PLAN() { \
            GRABBED() un = unit_of(g); chg = un != u; none = g >= NPC; \
            const bool nv = g >= NPC / 2; \
            unsigned hd = __builtin_amdgcn_readlane(vut, un); \
            while (hd == ~0u) { SLEEP(1) hd = lds_ld(&sm.utab[un]); } \
            nrg = (g + (hd >> 8)) & 31; hd &= 255; \
            unsigned boff, boff2; asm("s_mul_i32 %0, %1, 0xc00" : "=s"(boff) : "s"(hd * 512 + __builtin_amdgcn_readlane(vr, nrg))); \
            asm("s_mul_i32 %0, %1, 0xc00" : "=s"(boff2) : "s"(hd * 512 + __builtin_amdgcn_readlane(vr, nrg + 1))); \
            const char* b_ = nv ? (const char*)qv : (const char*)qk; \
            kb = b_ + boff; kb2 = b_ + boff2; \
            oa = nv ? voff : koff; ob = nv ? voffb : koffb; \
            tdn = TDUE(g); }
#define FIRE() { ISSUE(0, 0, kb, oa, ob) ISSUE(1, 1, kb, oa, ob) ISSUE(2, 0, kb2, oa, ob) ISSUE(3, 1, kb2, oa, ob) }
#define LAUNCH() { \
            LINE() \
            const unsigned du_ = __builtin_amdgcn_readfirstlane((unsigned)ln) + tdn; \
            GATE(du_) \
            ISSUE(0, 0, kb, oa, ob) ISSUE(1, 1, kb, oa, ob) \
            GATE(du_ + 10) \
            ISSUE(2, 0, kb2, oa, ob) ISSUE(3, 1, kb2, oa, ob) tiss = NOW(); }
#define ARRIVED() \
            const unsigned now = NOW(); \
            const bool stall = now - tiss > LJ && g - PST0 < NPC - 48 - PST0; \
            const bool late = !none && !stall && (int)(now + EARLY - __builtin_amdgcn_readfirstlane((unsigned)ln) - tdn) >= 0; \
            if (late) { FIRE() tiss = now; } \
            if (stall && lane == 0) __hip_atomic_fetch_max(&sm.line, (unsigned)ln + (now - tiss) - LOK2, __ATOMIC_RELAXED, __HIP_MEMORY_SCOPE_WORKGROUP); \
            REPORT()
#define ADVANCE(ISV) { \
            if (chg) FLUSH(ISV) \
            if (!none && !late) LAUNCH() \
            first = chg; u = un; rg = nrg; tdc = tdn + 10; }
#define K_TAB(MODE) { _Pragma("unroll") for (int p = 0; p < ((MODE) ? 2 : 4); p++) { \
            const unsigned* cw = &sm.cs[(rg & 7) + (p >> 1)][rg >> 3][0]; \
            ca[p] = *(const v4u*)&cw[(p & 1) * 256 + lane * 4]; cb[p] = *(const v2u*)&cw[512 + (p & 1) * 128 + lane * 2]; } }
#define K_PRE(MODE, P) { \
        float e0, e1, e2, f0, f1, f2; \
        const unsigned* qp_ = ((MODE) && (P) >= 2) ? Rp : Qp; const unsigned* qn_ = ((MODE) && (P) >= 2) ? Rn : Qn; \
        const int tp_ = (MODE) ? (P) & 1 : (P); \
        asm("v_dot2_f32_bf16 %0, %6, %12, 0\n\tv_dot2_f32_bf16 %3, %9, %15, 0\n\tv_dot2_f32_bf16 %1, %7, %13, 0\n\tv_dot2_f32_bf16 %4, %10, %16, 0\n\t" \
            "v_dot2_f32_bf16 %2, %8, %14, 0\n\tv_dot2_f32_bf16 %5, %11, %17, 0\n\ts_nop 2" \
            : "=&v"(e0), "=&v"(e1), "=&v"(e2), "=&v"(f0), "=&v"(f1), "=&v"(f2) \
            : "v"(qp_[0]), "v"(qp_[1]), "v"(qp_[2]), "v"(qn_[0]), "v"(qn_[1]), "v"(qn_[2]), \
              "v"(ca[tp_].x), "v"(ca[tp_].y), "v"(ca[tp_].z), "v"(ca[tp_].w), "v"(cb[tp_].x), "v"(cb[tp_].y)); \
        qe[P][0] = (v2f){e0, f0}; qe[P][1] = (v2f){e1, f1}; qe[P][2] = (v2f){e2, f2}; \
        A[P] = (qe[P][0] + qe[P][1] + qe[P][2]) * (v2f){-kz4[P], -kz4[P]}; }
#define K_WAIT(MODE, P) { \
        if ((MODE) == 0) { WAITT(P, P, 6 - 2 * (P)); } \
        else { WAITT(P, (((MODE) - 1) & 1) * 4 + (P), ((MODE) < 3 ? 20 : (MODE) == 3 ? 17 : 14) - 2 * (P)); } }
#define K_FMA(P) { \
        _Pragma("unroll") \
        for (int i = 0; i < 3; i++) { v2f x = {(float)ta[P][i], (float)tb[P][i]}; A[P] = qe[P][i] * x + A[P]; } \
        kres[P] = A[P].x + A[P].y; }
#define K_RED(MODE) { \
        const bool o1 = lane & 1, o2 = lane & 2; \
        float s01 = (o1 ? kres[1] : kres[0]) + __int_as_float(__builtin_amdgcn_update_dpp(0, __float_as_int(o1 ? kres[0] : kres[1]), 0xb1, 0xf, 0xf, true)); \
        float s23 = (o1 ? kres[3] : kres[2]) + __int_as_float(__builtin_amdgcn_update_dpp(0, __float_as_int(o1 ? kres[2] : kres[3]), 0xb1, 0xf, 0xf, true)); \
        float sx = (o2 ? s23 : s01) + __int_as_float(__builtin_amdgcn_update_dpp(0, __float_as_int(o2 ? s01 : s23), 0x4e, 0xf, 0xf, true)); \
        sx = dpp_add<0x124>(sx); sx = dpp_add<0x128>(sx); \
        const auto sw = __builtin_amdgcn_permlane16_swap(__float_as_uint(sx), __float_as_uint(sx), false, false); \
        sx = __uint_as_float(sw[0]) + __uint_as_float(sw[1]); \
        if ((lane & 28) == 0) { if (MODE) sm.ent[(lane >> 1) & 1][rg * 4 + 2 * (lane & 1) + rsel] = sx * ksc; else sm.ent[u % NOUT][rg * 4 + 2 * (lane & 3) + rsel] = sx * ksc; } }
#define K_PRE4(MODE) { K_PRE(MODE, 0) K_PRE(MODE, 1) K_PRE(MODE, 2) K_PRE(MODE, 3) asm volatile("" :: "v"(A[0]), "v"(A[1]), "v"(A[2]), "v"(A[3])); }
#define K_BODY(MODE) { K_PRE4(MODE) K_WAIT(MODE, 0) K_FMA(0) K_WAIT(MODE, 1) K_FMA(1) K_WAIT(MODE, 2) K_FMA(2) K_WAIT(MODE, 3) K_FMA(3) K_RED(MODE) }
#define V_FMA(J, W0, W1) { \
        _Pragma("unroll") \
        for (int i = 0; i < 3; i++) { \
            acc[i] = fma(W0, (double)ta[J][i], acc[i]); \
            acc[i] = fma(W1, (double)tb[J][i], acc[i]); \
        } }
#define V_PIN() asm volatile("" :: "v"(acc[0]), "v"(acc[1]), "v"(acc[2]));
#define RDYWAIT(SL, SEQ) { while (lds_ld(&sm.rdy[SL]) != (SEQ)) SLEEP(1); CBAR(); }
#define LOADZ() { _Pragma("unroll") for (int p = 0; p < 4; p++) kz4[p] = sm.kz[u % NIN][rg * 4 + 2 * p + rsel].x; ksc = sm.kz[u % NIN][rg * 4 + 2 * (lane & 3) + rsel].y * scale; }
#define LOADQ() { _Pragma("unroll") for (int i = 0; i < 3; i++) { Qp[i] = sm.q[u % NIN][3 * sub + i]; Qn[i] = __builtin_amdgcn_alignbit(Qp[i], Qp[i], 16) ^ 0x80000000u; } }
#define FLUSH(ISV) { \
            mg = __builtin_amdgcn_readfirstlane(mg); \
            while (u >= mg + NOUT) { SLEEP(1); mg = lds_ld(&sm.merged); } \
            CBAR(); \
            if (ISV) { const unsigned aa = lds_addr(&sm.acc[u % NOUT][lane]); \
                asm volatile("ds_add_f64 %0, %1\n\tds_add_f64 %0, %2 offset:512\n\tds_add_f64 %0, %3 offset:1024" :: "v"(aa), "v"(acc[0]), "v"(acc[1]), "v"(acc[2]) : "memory"); \
                acc[0] = 0.0; acc[1] = 0.0; acc[2] = 0.0; } \
            CBAR(); \
            LDS_ADD(&sm.cnt[u % NOUT], np) \
            CBAR(); \
            np = 0; }

#define LOADZ2() { _Pragma("unroll") for (int p = 0; p < 4; p++) kz4[p] = sm.kz[p >> 1][rg * 4 + 2 * (p & 1) + rsel].x; ksc = sm.kz[(lane >> 1) & 1][rg * 4 + 2 * (lane & 1) + rsel].y * scale; }
#define STATIC_STEP(I) { \
            rg = ((cst + (I)) & 3) * 8 + wave; \
            LOADZ2() \
            K_TAB((I) + 1) K_BODY((I) + 1) }
        CS_DMA(wave, (cst + 2) & 3)
        RDYWAIT(0, 1u) RDYWAIT(1, 2u)
        LOADQ()
#pragma unroll
        for (int i = 0; i < 3; i++) { Rp[i] = sm.q[1][3 * sub + i]; Rn[i] = __builtin_amdgcn_alignbit(Rp[i], Rp[i], 16) ^ 0x80000000u; }
        STATIC_STEP(0)
        asm volatile("s_waitcnt vmcnt(11)" ::: "memory");
        REPACK(wave, (cst + 1) & 3)
        TABDONE(1)
        SISSUE(2)
        CS_DMA(wave, (cst + 3) & 3)
        STATIC_STEP(1)
        asm volatile("s_waitcnt vmcnt(11)" ::: "memory");
        REPACK(wave, (cst + 2) & 3)
        TABDONE(2)
        SISSUE(3)
        asm volatile("s_waitcnt vmcnt(8)" ::: "memory");
        REPACK(wave, (cst + 3) & 3)
        TABDONE(3)
        STATIC_STEP(2)
        GRAB(2)
        vut = LDV(&sm.utab[lane]);
        rg = ((cst + 3) & 3) * 8 + wave;
        LOADZ2() K_TAB(4) K_PRE4(4)
        PLAN()
        LAUNCH()
        K_WAIT(4, 0) K_FMA(0) K_WAIT(4, 1) K_FMA(1) K_WAIT(4, 2) K_FMA(2) K_WAIT(4, 3) K_FMA(3) K_RED(4)
        CBAR();
        LDS_ADD(&sm.cnt[0], 4u) LDS_ADD(&sm.cnt[1], 4u)
        u = un; rg = nrg; tdc = tdn + 10;
        bool tabok = false;
        __builtin_amdgcn_s_setprio(2);
#pragma unroll 1
        while (u < NU) {
            GRAB(2)
            mg = LDV(&sm.merged);
            vut = LDV(&sm.utab[lane]);
            LINE()
            if (!tabok) {
                for (;;) { const unsigned tb_ = lds_ld(&sm.tabs); tabok = tb_ == ~0u; if (((tb_ >> rg) & 3u) == 3u) break; SLEEP(1) }
                CBAR();
            }
            K_TAB(0)
            if (first) {
                unsigned fl = LDV(&sm.rdy[u % NIN]);
                CBAR();
                LOADQ() LOADZ()
                if (__builtin_amdgcn_readfirstlane(fl) != u + 1) { RDYWAIT(u % NIN, u + 1) LOADQ() LOADZ() }
            } else LOADZ()
            K_PRE4(0)
            PLAN()
            K_WAIT(0, 0) K_FMA(0) K_WAIT(0, 1) K_FMA(1)
            asm volatile("" :: "v"(kres[0]), "v"(kres[1]));
            K_WAIT(0, 2) K_WAIT(0, 3)
            ARRIVED()
            K_FMA(2) K_FMA(3) K_RED(0)
            np += 2;
            ADVANCE(false)
        }
#pragma unroll 1
        while (u < 2 * NU) {
            GRAB(2)
            mg = LDV(&sm.merged);
            LINE()
            const unsigned vr_ = LDV(&sm.vrdy);
            CBAR();
            const unsigned short* wp = &sm.wv[went(u - NU) + (QUARTER(u - NU) ? rg & 7 : rg) * 4];
            v4i pw = *(const v4i*)wp;
            if (first && !((__builtin_amdgcn_readfirstlane(vr_) >> (u - NU)) & 1)) { while (!((lds_ld(&sm.vrdy) >> (u - NU)) & 1)) SLEEP(1); CBAR(); pw = *(const v4i*)wp; }
            const double w0 = (short)pw.x, w1 = pw.x >> 16, w2 = (short)pw.y, w3 = pw.y >> 16, w4 = (short)pw.z, w5 = pw.z >> 16, w6 = (short)pw.w, w7 = pw.w >> 16;
            asm volatile("" :: "v"(w0), "v"(w1), "v"(w2), "v"(w3), "v"(w4), "v"(w5), "v"(w6), "v"(w7));
            PLAN()
            double hy; unsigned tl8, rowl, rowc, aa, ca;
            if (none) {
                hy = *(const float*)&sm.wv[went(NU - 1) + 130]; tl8 = LDV(&sm.tag_lo) << 24;
                rowl = (LDV(&sm.utab[2 * NU - 1]) & 255) * (NSPLIT * 512); rowc = (rowl & 15 * NSPLIT * 512 ^ NSPLIT * 512) + NRR * NSPLIT * 512 + lane * 8; rowl += lane * 8;
                aa = lds_addr(&sm.acc[(2 * NU - 1) % NOUT][lane]); ca = lds_addr(&sm.cnt[(2 * NU - 1) % NOUT]);
                mg = __builtin_amdgcn_readfirstlane(mg);
                while (u >= mg + NOUT) { SLEEP(1); mg = lds_ld(&sm.merged); }
                asm volatile("" : "+v"(hy), "+v"(tl8), "+v"(aa), "+v"(ca), "+v"(rowl), "+v"(rowc));
            }
            WAITT(0, 0, 6); V_FMA(0, w0, w1) V_PIN()
            WAITT(1, 1, 4); V_FMA(1, w2, w3) V_PIN()
            WAITT(2, 2, 2); V_FMA(2, w4, w5) V_PIN()
            WAITT(3, 3, 0);
            np += 2;
            if (none) {
                V_FMA(3, w6, w7)
                if (u != 2 * NU - 1) { FLUSH(true) break; }
                double a0, a1, a2; unsigned cn; u64 ex_;
                asm volatile("ds_add_f64 %5, %6\n\tds_add_f64 %5, %7 offset:512\n\tds_add_f64 %5, %8 offset:1024\n\t"
                             "s_mov_b64 %4, exec\n\ts_mov_b64 exec, 1\n\tds_add_rtn_u32 %3, %9, %10\n\ts_mov_b64 exec, %4\n\t"
                             "ds_read_b64 %0, %5\n\tds_read_b64 %1, %5 offset:512\n\tds_read_b64 %2, %5 offset:1024\n\ts_waitcnt lgkmcnt(0)"
                             : "=&v"(a0), "=&v"(a1), "=&v"(a2), "=&v"(cn), "=&s"(ex_) : "v"(aa), "v"(acc[0]), "v"(acc[1]), "v"(acc[2]), "v"(ca), "v"(np) : "memory");
                const float n0 = a0 - hy, n1 = a1 - hy, n2 = a2 - hy;
                const unsigned e2 = max(__float_as_uint(fmaxf(fmaxf(fabsf(n0), fabsf(n1)), fabsf(n2))) >> 23, 14u);
                const float f2 = __uint_as_float((253u - e2) << 23);
                const v2u r = {__builtin_bit_cast(unsigned, __builtin_amdgcn_cvt_pknorm_i16(n0 * f2, n1 * f2)), __builtin_bit_cast(unsigned, __builtin_amdgcn_cvt_pknorm_i16(n2 * f2, 0.f)) | e2 << 16 | tl8};
                const __amdgpu_buffer_rsrc_t rsrc = __builtin_amdgcn_make_buffer_rsrc(ws + WS_PART + split * 512, 0, NRW * NSPLIT * 512 - split * 512, 0x00020000);
                asm volatile("" :: "v"(r));
                if (__builtin_amdgcn_readfirstlane(cn + np) == 32) {
                    __builtin_amdgcn_raw_buffer_store_b64(r, rsrc, rowl, 0, SC1);
#pragma unroll
                    for (int j = 0; j < NCP - 1; j++) __builtin_amdgcn_raw_buffer_store_b64(r, rsrc, rowc, j * GSZ * NSPLIT * 512, SC1);
                }
                break;
            }
            ARRIVED()
            V_FMA(3, w6, w7)
            ADVANCE(true)
        }
    } else {
    u64 E1;
    if (wave == NSW) {
        unsigned ref;
        const unsigned sidx = (blockIdx.x & 7) | ((blockIdx.x >> 7) << 3);
#define SAMPLE(K) ((((blockIdx.x >> 3) - (blockIdx.x & 7) - (K) * 5) & 15) == 0)
#define BTAG(K) ((btag + (K)) & 0xfffffu)
#define POST(K, C) { if (lane == 0) __hip_atomic_store((u64*)(ws + (bd + (K) * RST + (sidx >> 3) * 256 + (sidx & 7) * 8)), (u64)(C) | ((u64)(((C) - ref) | BTAG(K) << 12) << 32), __ATOMIC_RELAXED, __HIP_MEMORY_SCOPE_AGENT); }
        const unsigned own = lds_ld(&sm.line);
        if (SAMPLE(0) && lane < NTRY) __hip_atomic_store((unsigned*)(ws + WS_HELLO + lane * 768) + sidx, own, __ATOMIC_RELAXED, __HIP_MEMORY_SCOPE_AGENT);
        const u64 E = __hip_atomic_load((const u64*)(ws + WS_EPOCH), __ATOMIC_RELAXED, __HIP_MEMORY_SCOPE_AGENT);
        E1 = E + 1;
        if (lane == 0) { sm.tag_lo = (unsigned)E1; sm.tag_hi = (unsigned)(E1 >> 32); CBAR(); lds_st(&sm.tag_rdy, 1u); }
        const unsigned btag = __builtin_amdgcn_readfirstlane((unsigned)E1) * 61u, bd = WS_BOARD + (btag & 7) * (NRND * RST);
        ref = own;
        int off = 0;
        {
            unsigned tries = 0, cur = own, tl = own;
            bool news = false;
#pragma unroll 1
            while (!news || cur != ref) {
                const unsigned now = NOW();
                if (tries < NTRY && (int)(now - own) >= (int)(TA + tries * TA2)) {
                    v16u x;
                    asm volatile("s_load_dwordx16 %0, %1, %2\n\ts_waitcnt lgkmcnt(0)" : "=&s"(x) : "s"(ws), "s"(__builtin_amdgcn_readfirstlane(WS_HELLO + tries * 768)) : "memory");
                    unsigned bad = 0; int sum = 0, dif = 0;
#pragma unroll
                    for (int i = 0; i < 16; i++) { bad |= x[i] - own + 512; sum += x[i] - x[0]; dif += i & 4 ? x[i] : -x[i]; }
                    tries++;
                    if (bad < 1024) { const unsigned com = x[0] + (sum >> 4) + abs(dif) * (8 - KC) / 128; ref = x[0] + ((sum + (blockIdx.x & 4 ? dif : -dif)) >> 4); off = ref - com;
                        tries = NTRY; tl = NOW(); if (lane == 0) lds_st(&sm.aline, com); }
                    continue;
                }
                if (!news && lds_ld(&sm.utab[31]) != ~0u) { news = true; CBAR(); if (cur == ref) tl = now; ref = lds_ld(&sm.aline) + off; tries = NTRY; }
                const int d = (now - tl) >> CSH;
                if (cur != ref && d) { tl += d << CSH; cur += min(max((int)(ref - cur), -d), d); if (lane == 0) lds_st(&sm.line, cur); }
                SLEEP(2)
            }
        }
        ref -= off;
        const unsigned ref0 = ref;
        const unsigned lead = (blockIdx.x >> 3) == 9 ? 0 : LEAD;
        unsigned posted = ref, lagf = ref, tpost = ref + TB1, tpoll = tpost + RDD + lead, kw = 0, kr = 0, cur = ref + off, khold = 0;
        int o = off; bool held = false;
#pragma unroll 1
        for (;;) {
            const unsigned p = lds_ld(&sm.grab);
            if (p >= NPC) break;
            lagf += (int)(lds_ld(&sm.late) - lagf) >> 1;
            { const unsigned l = lds_ld(&sm.line); if (l != cur) { cur = l; khold = kw; held = true; } }
            const unsigned now = NOW();
            if (o) { o = off * min(max((int)(ref0 + TC0 + (1 << TCS) - now), 0), 1 << TCS) >> TCS; if (!held && cur != ref + o) { cur = ref + o; if (lane == 0) lds_st(&sm.line, cur); } }
            if (kw < NRND && (int)(now - tpost) >= 0) {
                const unsigned c = ref + min((p >= PSL0 && (int)(lagf - cur) > LHI ? lagf - LOK : cur) - o - ref, 4000u);
                if (SAMPLE(kw) && p < NPC - PEND && (c != ref || (ref != posted && (sidx & 7) == (kw & 7)))) POST(kw, c)
                posted = ref;
                tpost += PER; kw++;
            }
            if (kr < NRND && (int)(now - tpoll) >= 0) {
                v16u xa, xb;
                asm volatile("s_load_dwordx16 %0, %2, %3\n\ts_load_dwordx16 %1, %2, %4\n\ts_waitcnt lgkmcnt(0)" : "=&s"(xa), "=&s"(xb)
                             : "s"(ws), "s"(__builtin_amdgcn_readfirstlane(bd + kr * RST)), "s"(__builtin_amdgcn_readfirstlane(bd + kr * RST + 256)) : "memory");
                int nb = 0, sg = 0;
#pragma unroll
                for (int i = 0; i < 16; i++) {
                    const unsigned lo = i < 8 ? xa[2 * i & 15] : xb[2 * i & 15], hi = i < 8 ? xa[(2 * i + 1) & 15] : xb[(2 * i + 1) & 15];
                    if (hi >> 12 == BTAG(kr) && lo - ref0 < 8000) { nb = max(nb, (int)(lo - (hi & 4095u) - ref)); sg += hi & 4095u; }
                }
                ref += nb + (sg >> 4);
                if (cur != ref + o && (kr >= khold || (int)(ref + o - cur) > 0)) { cur = ref + o; held = false; if (lane == 0) lds_st(&sm.line, cur); }
                tpoll += PER; kr++;
            }
            SLEEP(2)
        }
    } else if (wave == NSW + 1 || wave == NSW + 2) {
        const int ql = lane < 48 ? lane : 47;
        unsigned c1, c2, s1, s2;
#define QT_LOADS() { int qo_ = ql; asm volatile("" : "+v"(qo_)); \
                     c1 = ((const unsigned*)q_cos)[qo_]; c2 = ((const unsigned*)q_cos)[48 + qo_]; s1 = ((const unsigned*)q_sin)[qo_]; s2 = ((const unsigned*)q_sin)[48 + qo_]; }
        unsigned q1, q2, lkz, lks, lvz, lvs;
        const unsigned sofs = R8(((unsigned)lane >> 1) & 7) * 2 + (lane & 1);
#define UNIT_LOADS(HEAD, GRP) { \
        q1 = ((const unsigned*)(q + (size_t)(HEAD) * DH))[ql]; q2 = ((const unsigned*)(q + (size_t)(HEAD) * DH))[48 + ql]; \
        const size_t so = (size_t)(HEAD) * (SKV / 2) + (GRP) * 256 + sofs; \
        lkz = *((const unsigned*)k_zeros + so); lks = *((const unsigned*)k_scales + so); \
        lvz = *((const unsigned*)v_zeros + so); lvs = *((const unsigned*)v_scales + so); }
#define UNIT_FILL(SL, U) { \
            v4f qq; \
            qq.x = bfr(bfr(bflo(q1) * bflo(c1)) + bfr(-bflo(q2) * bflo(s1))); \
            qq.y = bfr(bfr(bflo(q2) * bflo(c2)) + bfr(bflo(q1) * bflo(s2))); \
            qq.z = bfr(bfr(bfhi(q1) * bfhi(c1)) + bfr(-bfhi(q2) * bfhi(s1))); \
            qq.w = bfr(bfr(bfhi(q2) * bfhi(c2)) + bfr(bfhi(q1) * bfhi(s2))); \
            *(v2u*)&sm.q[SL][2 * ql] = (v2u){(__float_as_uint(qq.x) >> 16) | __float_as_uint(qq.y), (__float_as_uint(qq.z) >> 16) | __float_as_uint(qq.w)}; \
            *(v4f*)&sm.kz[SL][2 * lane] = (v4f){bflo(lkz), bflo(lks), bfhi(lkz), bfhi(lks)}; \
            *(v4f*)&sm.vz[SL][2 * lane] = (v4f){bflo(lvz), bflo(lvs), bfhi(lvz), bfhi(lvs)}; \
            CBAR(); \
            if (lane == 0) lds_st(&sm.rdy[SL], (U) + 1); \
            CBAR(); }
        const unsigned dw = NSW + 2 - wave;
        QT_LOADS()
        UNIT_LOADS(head0 + dw * (GSZ + 1 - 2 * side), lane >> 4)
#pragma unroll 1
        for (unsigned u = dw;;) {
            while (u >= lds_ld(&sm.merged) + NIN) SLEEP(4);
            CBAR();
            UNIT_FILL(u % NIN, u)
            if (u - 1 < 12) {
                const unsigned x_ = (blockIdx.x >> 3) + 32 * u - 8; unsigned dm;
                const char* p_ = x_ < 384 ? (const char*)(x_ < 192 ? qk : qv) + (size_t)(x_ < 192 ? x_ : x_ - 192) * (2u << 20) + (x_ * 8 + (blockIdx.x & 7)) * 640 : (const char*)out + (x_ - 384) * 4096;
                while ((int)(NOW() - lds_ld(&sm.line)) < TSD) SLEEP(2)
                asm volatile("s_load_dword %0, %1, 0x0\n\ts_waitcnt lgkmcnt(0)" : "=&s"(dm) : "s"(p_) : "memory");
            }
            u += 2;
            if (u >= NU) break;
            if (u == 4) {
                const unsigned tpf = tdue(PSF - 1) + TFO;
                { int d; while ((d = (int)(lds_ld(&sm.line) + tpf - NOW())) > 0 && d < 3000) SLEEP(1) }
#define PF3(P) asm volatile("global_load_dwordx4 a[0:3], %0, %1\n\tglobal_load_dwordx4 a[0:3], %0, %1 offset:1024\n\tglobal_load_dwordx4 a[0:3], %0, %1 offset:2048" \
                            :: "v"(64 * SKV * 2 + blockIdx.x * 3072 + lane * 16), "s"(P) : "a0", "a1", "a2", "a3")
                PF3(k_zeros); PF3(k_scales); PF3(v_zeros); PF3(v_scales);
                asm volatile("global_load_dword a0, %0, %1" :: "v"(blockIdx.x * (DH * 2) + ql * 8), "s"(q) : "a0");
            }
            const unsigned tin = tdue(first_of(u) - (QUARTER(u) ? 24 : 0) - LEADP + (split & 7) * DPH);
            { int d; while ((d = (int)(lds_ld(&sm.line) + tin - NOW())) > 0 && d < 3000) SLEEP(4) }
            unsigned hd;
            while ((hd = lds_ld(&sm.utab[u])) == ~0u) SLEEP(2)
            UNIT_LOADS(hd & 255, QUARTER(u) ? rank & 3 : lane >> 4)
        }
        while (lds_ld(&sm.tag_rdy) == 0) SLEEP(1);
        CBAR();
        E1 = (u64)lds_ld(&sm.tag_lo) | ((u64)lds_ld(&sm.tag_hi) << 32);
        if (wave == NSW + 1 && blockIdx.x == 0) {
            for (;;) {
                bool ok = true;
#pragma unroll
                for (int i = 0; i < NH / 64; i++) ok &= __hip_atomic_load((const u64*)(ws + WS_POST) + i * 64 + lane, __ATOMIC_RELAXED, __HIP_MEMORY_SCOPE_AGENT) >> 24 == (E1 << 24) >> 24;
                if (__builtin_amdgcn_ballot_w64(ok) == ~0ull) break;
                SLEEP(64);
            }
            if (lane == 0) __hip_atomic_store((u64*)(ws + WS_EPOCH), E1, __ATOMIC_RELAXED, __HIP_MEMORY_SCOPE_AGENT);
        }
        if (wave == NSW + 2) {
            const unsigned t16 = (unsigned)E1 & 0xffffu, t16b = ((unsigned)E1 + ((unsigned)E1 >> 16)) & 0xffffu;
            const __amdgpu_buffer_rsrc_t rsrc = __builtin_amdgcn_make_buffer_rsrc(ws + WS_SWAP + (blockIdx.x ^ 4) * 5120, 0, 5120, 0x00020000);
            const unsigned swp = lds_ld(&sm.swp);
            if (swp) {
                unsigned use = 0;
#pragma unroll
                for (int i = 0; i < 5; i++) use |= ((swp >> wunit(i * 64 + lane)) & 1) << i;
                const unsigned tg = tdue(PSG - 1) + tvx(PSG - 1, lds_ld(&sm.gap));
                while ((int)(NOW() - lds_ld(&sm.line) - tg) < 0) SLEEP(2)
                for (;;) {
                    v4u d[5]; bool ok = true;
#pragma unroll
                    for (int i = 0; i < 5; i++) d[i] = __builtin_amdgcn_raw_buffer_load_b128(rsrc, (use >> i) & 1 ? (i * 64 + lane) * 16 : 0x40000000, 0, SC1 | 2);
#pragma unroll
                    for (int i = 0; i < 5; i++) if ((use >> i) & 1) {
                        ok &= d[i].y >> 16 == t16 && d[i].w >> 16 == t16b;
                        unsigned* w = (unsigned*)&sm.wv[144 * NMIX + (i * 64 + lane) * 6];
                        w[0] = d[i].x; w[1] = (d[i].y & 0xffffu) | (d[i].z << 16); w[2] = (d[i].z >> 16) | (d[i].w << 16);
                    }
                    if (__builtin_amdgcn_ballot_w64(ok) == ~0ull) break;
                    SLEEP(32)
                }
                CBAR();
                LDS_OR(&sm.vrdy, swp)
            }
        }
    } else {
        const unsigned eu = lane >= NU ? lane - NU : lane, win = min(first_of(eu) >> 5, 15u), pair = QUARTER(eu) ? 2 * (eu - WQ) + (rank >> 2) : rank;
        unsigned rot2 = ((const unsigned char*)ROT)[win * 8 + pair];
        const unsigned rotn = ((const unsigned char*)ROT)[(win == 0 ? 4 : win == 1 ? 9 : WQ) * 8 + pair];
#define UENT(BIT) (win * 16 + 2 * pair + ((BIT) ^ side) | (QUARTER(eu) ? (rank - (eu - WQ)) * 8 & 31 : (rot2 >> ((BIT) ^ side) * 4 & 15) * 2) << 8)
        unsigned far = 0, vs;
        const unsigned hp = (blockIdx.x & 1) * NH + (blockIdx.x >> 1) * 4 + 2;
        if (blockIdx.x < 144) {
            const char* pb = (blockIdx.x < 128 ? (const char*)(hp >= NH ? qv : qk) + (size_t)(hp & (NH - 1)) * (SKV * DH * 4) + SKV * DH * 2
                                               : (const char*)ws + WS_PART + (8u << 20) + (blockIdx.x & 15) * 65536) + ((NOW() * 0x9e3779b1u) >> 26) * 128;
            unsigned mn[2] = {~0u, ~0u};
#pragma unroll 1
            for (unsigned i = 1; i < 2 * NPRB + 2; i++) {
                u64 t0, t1; unsigned dm;
                asm volatile("s_memtime %0\n\ts_waitcnt lgkmcnt(0)\n\ts_load_dword %2, %3, 0x0\n\ts_waitcnt lgkmcnt(0)\n\ts_memtime %1\n\ts_waitcnt lgkmcnt(0)"
                             : "=&s"(t0), "=&s"(t1), "=&s"(dm) : "s"(pb + i * 8192) : "memory");
                if (i == 1) asm volatile("global_load_dword a0, %0, %1 nt" :: "v"(0), "s"(pb) : "a0");
                const unsigned t = (unsigned)(t1 - t0) < 330 || i == 1 ? ~0u : (unsigned)(t1 - t0);
                if (i & 1) mn[1] = min(mn[1], t); else mn[0] = min(mn[0], t);
            }
            far = mn[0] > mn[1];
        }
        while (lds_ld(&sm.tag_rdy) == 0) SLEEP(1);
        CBAR();
        E1 = (u64)lds_ld(&sm.tag_lo) | ((u64)lds_ld(&sm.tag_hi) << 32);
        {
            const unsigned own = lds_ld(&sm.line), tg = (unsigned)E1;
            if (lane == 0) __hip_atomic_store((u64*)(ws + WS_POST) + blockIdx.x, E1 << 24 | min(lds_ld(&sm.slow), 4000u) << 1 | ((far ^ (blockIdx.x >> 2) ^ (blockIdx.x < 128 ? 0x96 >> (hp & 7) : 0)) & 1), __ATOMIC_RELAXED, __HIP_MEMORY_SCOPE_AGENT);
            const __amdgpu_buffer_rsrc_t nrs = __builtin_amdgcn_make_buffer_rsrc(ws + WS_NEWS, 0, 8 * 1280, 0x00020000);
            if (blockIdx.x == MASTER) {
                while ((int)(NOW() - own) < TMAP) SLEEP(2)
                const unsigned a = tg << 24, b = (unsigned)(E1 >> 8);
                v4u x; v2u z; u64 v0, v1;
#pragma unroll 1
                for (int i = 0; i < 8; i++) {
                    x = __builtin_amdgcn_raw_buffer_load_b128(__builtin_amdgcn_make_buffer_rsrc(ws + WS_POST, 0, 1024, 0x00020000), lane * 16, 0, SC1);
                    z = __builtin_amdgcn_raw_buffer_load_b64(__builtin_amdgcn_make_buffer_rsrc(ws + WS_POST + 1024, 0, 128, 0x00020000), lane * 8, 0, SC1);
                    v0 = __builtin_amdgcn_ballot_w64(((x.x ^ a) >> 24 | (x.y ^ b)) == 0); v1 = __builtin_amdgcn_ballot_w64(((x.z ^ a) >> 24 | (x.w ^ b)) == 0);
                    if (__builtin_popcountll(v0) + __builtin_popcountll(v1) >= MINV) break;
                    SLEEP(8)
                }
                const u64 m0 = __builtin_amdgcn_ballot_w64(x.x & 1) & v0, m1 = __builtin_amdgcn_ballot_w64(x.z & 1) & v1;
                const int s0 = lane < 43 ? min(max(lane - VW, 4 * NMIX), 42 - 2 * VW) : min(max(lane - VW, 43), 63 - 2 * VW);
#define PC(M) __builtin_popcount((unsigned)((M) >> s0) & ((2u << 2 * VW) - 1))
                const u64 g0 = __builtin_amdgcn_ballot_w64(2 * PC(m0) > PC(v0)), g1 = __builtin_amdgcn_ballot_w64(2 * PC(m1) > PC(v1));
                const float sl = wave_sum(((v0 >> lane & 1) ? (float)(x.x >> 1 & 4095u) : 0.f) + ((v1 >> lane & 1) ? (float)(x.z >> 1 & 4095u) : 0.f)) / ((8.f - PRE0) * (__builtin_popcountll(v0) + __builtin_popcountll(v1)) + 1.f);
                const unsigned al = lds_ld(&sm.aline), mean = (al ? al : own) + (int)fmaxf(sl - SL0, 0.f);
                const bool vz = lane < 16 && ((z.x ^ a) >> 24 | (z.y ^ b)) == 0;
                const unsigned gw = 2 * __builtin_popcountll(__builtin_amdgcn_ballot_w64(vz && (z.x & 1))) > __builtin_popcountll(__builtin_amdgcn_ballot_w64(vz));
                const unsigned k = lane & 3;
                if (lane < 32 && k < 3) __builtin_amdgcn_raw_buffer_store_b128((v4u){k == 0 ? (unsigned)g0 : k == 1 ? (unsigned)g1 : mean, tg, k == 0 ? (unsigned)(g0 >> 32) : k == 1 ? (unsigned)(g1 >> 32) : gw, tg},
                                                                              nrs, (lane >> 2) * 1280 + k * 16, 0, SC1);
            }
            while ((int)(NOW() - own) < TMAP + TRES) SLEEP(2)
            v4u y;
            for (;;) {
                y = __builtin_amdgcn_raw_buffer_load_b128(nrs, lane < 3 ? (blockIdx.x & 7) * 1280 + lane * 16 : 0x40000000, 0, SC1);
                if (((unsigned)__builtin_amdgcn_ballot_w64(y.y == tg && y.w == tg) & 7u) == 7u) break;
                SLEEP(8)
            }
            const u64 f0 = (unsigned)__builtin_amdgcn_readlane(y.x, 0) | (u64)(unsigned)__builtin_amdgcn_readlane(y.z, 0) << 32, f1 = (unsigned)__builtin_amdgcn_readlane(y.x, 1) | (u64)(unsigned)__builtin_amdgcn_readlane(y.z, 1) << 32;
            if (lane == 2) { sm.aline = y.x - own + 500 < 1000 ? y.x : own; sm.chead = (blockIdx.x & ~5u) | ((blockIdx.x >> 2 ^ y.z) & 1) | (blockIdx.x & 1) << 2; }
            unsigned bit = ((unsigned)((lane >= NU ? f1 : f0) >> (4 * win + (pair >> 1))) ^ (0x66 >> pair)) & 1;
            vs = (f0 ^ f1) >> 4 * NMIX && !((f0 ^ f1) >> 4 * NMIX & 4095) ? NMIX : 0;
            if (eu < NMIX) { if (lane < NU || !vs) bit = eu & 1; else rot2 = rotn; }
            const u64 bm = __builtin_amdgcn_ballot_w64(bit);
            const unsigned nsw = __builtin_popcountll((f0 ^ f1) >> (4 * NMIX)) + (vs ? 2 * NMIX : 0);
            const unsigned sw = (unsigned)(bm ^ (bm >> NU)) & ((1u << NU) - 1), sx = (sw >> vs ^ sw) & 7;
            if (lane == 0) { sm.swp = sw ^ sx ^ sx << vs; lds_st(&sm.gap, (nsw ? (TG0 + (vs ? TGV : 0) + TG1 * nsw / 8) * 0x1001u + ((TH0 + (vs ? THV : 0) + TH1 * nsw / 8) << 12) : 0u) | (vs ? 1u << 31 : 0u)); }
            CBAR();
#define VPOS(E) ((E) < NMIX ? (E) + vs : (E) - vs < NMIX ? (E) - vs : (E))
            lds_st(&sm.utab[lane >= NU ? NU + VPOS(eu) : lane], UENT(bit));
        }
        const unsigned swp = lds_ld(&sm.swp);
        const unsigned T = lds_ld(&sm.tag_lo);
        const unsigned t15 = T & 0x7fffu;
        const __amdgpu_buffer_rsrc_t rsrc = __builtin_amdgcn_make_buffer_rsrc(ws + WS_PART + split * 512, 0, NRW * NSPLIT * 512 - split * 512, 0x00020000);
        v2u* recs = (v2u*)&sm.cs[0][0][0];
#pragma unroll
        for (int i = 0; i < NOUT * 3; i++) (&sm.acc[0][0])[i * 64 + lane] = 0.0;
        CBAR();
        unsigned vut = sm.utab[NU + (lane & 31)] & 255;
        if (QUARTER((unsigned)lane) && (rank & 3)) vut = NH - 16 + (rank & 3) * 16 + ((vut & 15) ^ 1);
        __builtin_amdgcn_s_setprio(3);
        unsigned po[NU / 2];
#pragma unroll
        for (int k = 0; k < NU / 2; k++) po[k] = __builtin_amdgcn_ds_bpermute((2 * k + (lane >> 5)) * 4, vut) * (NSPLIT * 512) + (lane & 31) * 16;
        const u64 rb_ = (u64)(ws + WS_PART + split * 512);
        const v4u rs_ = {(unsigned)rb_, (unsigned)(rb_ >> 32) & 0xffffu, NRW * NSPLIT * 512 - split * 512, 0x00020000u};
#pragma unroll 1
        for (unsigned u = 0; u < 2 * NU - 1; u++) {
            const unsigned e = u % NU, so = u % NOUT;
            if (u >= NU) while (!((lds_ld(&sm.vrdy) >> e) & 1)) SLEEP(4)
            CBAR();
            const v4f h = *(const v4f*)&sm.wv[went(e) + (QUARTER(e) ? 32 : 128)];
            while (lds_ld(&sm.cnt[so]) != (QUARTER(e) ? 8 : 32)) SLEEP(1)
            CBAR();
            const double a0 = sm.acc[so][lane], a1 = sm.acc[so][64 + lane], a2 = sm.acc[so][128 + lane];
            if (u < NU) {
                float2 lg = *(const float2*)&sm.ent[so][2 * lane];
                if (QUARTER(e) && lane >> 4 != (rank & 3)) lg = make_float2(-INFINITY, -INFINITY);
                const v4f zs = *(const v4f*)&sm.vz[u % NIN][2 * lane];
                const float m = wave_max(fmaxf(lg.x, lg.y));
                const float p0 = __expf(lg.x - m), p1 = __expf(lg.y - m);
                const float x0 = p0 * zs.y, x1 = p1 * zs.w;
                const unsigned eb = max(__float_as_uint(wave_max(fmaxf(fabsf(x0), fabsf(x1)))) >> 23, 14u);
                const float f = __uint_as_float((268u - eb) << 23);
                const float w0 = fminf(fmaxf(rintf(x0 * f), -32767.f), 32767.f), w1 = fminf(fmaxf(rintf(x1 * f), -32767.f), 32767.f);
                const float den = wave_sum(p0 + p1);
                const float bz = wave_sum(fmaf(w0, zs.x, w1 * zs.z));
                const float L = fmaf(m, 1.4426950408889634f, __builtin_amdgcn_logf(den));
                const unsigned vp = VPOS(u);
                unsigned* wd = (unsigned*)(((swp >> vp) & 1 ? sm.wo : sm.wv) + went(vp));
                if (!QUARTER(u) || lane >> 4 == (rank & 3)) wd[QUARTER(u) ? lane & 15 : lane] = ((unsigned)(int)w0 & 0xffffu) | ((unsigned)(int)w1 << 16);
                if (lane == 0) *(v4f*)&wd[QUARTER(u) ? 16 : 64] = (v4f){ldexpf(1.f / den, (int)eb - 141), bz, L, 0.f};
                CBAR();
                if (!((swp >> vp) & 1)) LDS_OR(&sm.vrdy, 1u << vp)
            } else {
                const float n0 = (float)(a0 - (double)h.y) * h.x, n1 = (float)(a1 - (double)h.y) * h.x, n2 = (float)(a2 - (double)h.y) * h.x;
                const unsigned e2 = max(__float_as_uint(wave_maxv(fmaxf(fmaxf(fabsf(n0), fabsf(n1)), fabsf(n2)))) >> 23, 14u);
                const float f2 = __uint_as_float((268u - e2) << 23);
                const unsigned m01 = __builtin_bit_cast(unsigned, __builtin_amdgcn_cvt_pk_i16((int)rintf(n0 * f2), (int)rintf(n1 * f2)));
                const unsigned m2 = __builtin_bit_cast(unsigned, __builtin_amdgcn_cvt_pk_i16((int)rintf(n2 * f2), 0));
                const unsigned bit = ((lane & 1 ? e2 : __float_as_uint(h.z)) >> (lane >> 1)) & 1u;
                recs[e * 64 + lane] = (v2u){m01, m2 | t15 << 16 | bit << 31};
                CBAR();
                sm.acc[so][lane] = 0.0; sm.acc[so][64 + lane] = 0.0; sm.acc[so][128 + lane] = 0.0;
            }
            CBAR();
            if (lane == 0) { lds_st(&sm.cnt[so], 0u); CBAR(); lds_st(&sm.merged, u + 1); }
            if (u == NU - 1 && swp) {
                wave_sync();
                const __amdgpu_buffer_rsrc_t srs = __builtin_amdgcn_make_buffer_rsrc(ws + WS_SWAP + blockIdx.x * 5120, 0, 5120, 0x00020000);
                v4u d[5]; unsigned o[5];
#pragma unroll
                for (int i = 0; i < 5; i++) {
                    const unsigned* w = (const unsigned*)&sm.wo[144 * NMIX + (i * 64 + lane) * 6];
                    const unsigned d0 = w[0], d1 = w[1], d2 = w[2];
                    d[i] = (v4u){d0, (d1 & 0xffffu) | (T << 16), (d1 >> 16) | (d2 << 16), (d2 >> 16) | ((T + (T >> 16)) << 16)};
                    o[i] = (swp >> wunit(i * 64 + lane)) & 1 ? (i * 64 + lane) * 16 : 0x40000000;
                }
                while ((int)(NOW() - lds_ld(&sm.line)) < (int)(tdue(PSW - 1) + tvx(PSW - 1, lds_ld(&sm.gap))) + 10 + TWO) SLEEP(1)
#pragma unroll
                for (int i = 0; i < 5; i++) __builtin_amdgcn_raw_buffer_store_b128(d[i], srs, o[i], 0, SC1 | 2);
            }
        }
        wave_sync();
#pragma unroll
        for (int k = 0; k < NU / 2; k++) asm volatile("ds_read_b128 a[%c0:%c0+3], %1 offset:%2" :: "i"(16 + 4 * k), "v"(lds_addr(recs) + lane * 16), "i"(k * 1024) : "memory");
        asm volatile("s_waitcnt lgkmcnt(0)" ::: "memory");
        while (!((lds_ld(&sm.vrdy) >> (NU - 1)) & 1)) SLEEP(4)
        CBAR();
        const v2u hw_ = {*(const unsigned*)&sm.wv[went(NU - 1) + (lane & 1 ? 128 : 132)], T};
        const unsigned hrow = (NRW - GSZ + (__builtin_amdgcn_readlane(vut, NU - 1) & 15 ^ 1)) * (NSPLIT * 512);
        while (lds_ld(&sm.grab) < NPC - PUBP) SLEEP(1)
        if (lane < 16) __builtin_amdgcn_raw_buffer_store_b64(hw_, rsrc, lane * 8, hrow, SC1);
#define PUB(K) asm volatile("buffer_store_dwordx4 a[%c0:%c0+3], %1, %2, 0 offen sc1" :: "i"(16 + 4 * (K)), "v"(po[K]), "s"(rs_) : "memory");
        PUB(WQ / 2) PUB(WQ / 2 + 1)
        { const unsigned t_ = TLAST() + PDEL; GATE(t_) }
        PUB(0) PUB(1) PUB(2) PUB(3) PUB(4) PUB(7) PUB(8)
        return;
    }
    }
    __builtin_amdgcn_s_setprio(0);
    unsigned c;
#define ADDRTN(P) { u64 ex_; asm volatile("s_mov_b64 %0, exec\n\ts_mov_b64 exec, 1\n\tds_add_rtn_u32 a15, %1, a13\n\ts_mov_b64 exec, %0" : "=&s"(ex_) : "v"(lds_addr(P)) : "a15", "memory"); }
#define ADDED(V) { unsigned t_; asm volatile("s_waitcnt lgkmcnt(0)\n\tv_accvgpr_read_b32 %1, a15\n\ts_nop 0\n\tv_readfirstlane_b32 %0, %1\n\ts_nop 1" : "=s"(V), "=v"(t_)); }
    asm volatile("v_accvgpr_write_b32 a13, 1\n\ts_nop 1" ::: "a13");
    ADDRTN(&sm.cidx) ADDED(c)
    if (c < NSW) {
        while (lds_ld(&sm.grab) < NPC + 2) SLEEP(4)
        const unsigned head = lds_ld(&sm.chead), T = lds_ld(&sm.tag_lo), tl = TLAST();
        const u64 rb_ = (u64)(ws + WS_PART);
        const v4u rs_ = {(unsigned)rb_, (unsigned)(rb_ >> 32) & 0xffffu, NRW * NSPLIT * 512, 0x00020000u};
        const unsigned ro = head * (NSPLIT * 512) + c * 1024, rx = (NH + (head & 15 ^ 1)) * (NSPLIT * 512) + c * 1024;
#define POLL(S, O) asm volatile("buffer_load_dwordx4 a[%c0:%c0+3], %1, %2, %3 offen sc1" :: "i"(16 + 4 * (S)), "v"(lane * 16), "s"(rs_), "s"(O) : "memory");
#define FETCH(X, S, N) asm volatile("s_waitcnt vmcnt(%c5)\n\tv_accvgpr_read_b32 %0, a[%c4]\n\tv_accvgpr_read_b32 %1, a[%c4+1]\n\tv_accvgpr_read_b32 %2, a[%c4+2]\n\tv_accvgpr_read_b32 %3, a[%c4+3]" \
                                    : "=v"((X).x), "=v"((X).y), "=v"((X).z), "=v"((X).w) : "i"(16 + 4 * (S)), "i"(N));
#define SWADF(A, B) ({ const auto l_ = __builtin_amdgcn_permlane32_swap(__float_as_uint(A), __float_as_uint(B), false, false); __uint_as_float(l_[0]) + __uint_as_float(l_[1]); })
        float o0, o1, o2;
        unsigned oo = (lane & 31) * 12 + (lane >> 5) * 6, da = lds_addr(&sm.cdone), one = 1;
        const u64 ob_ = (u64)(out + (size_t)head * DH);
        v4u os_ = {(unsigned)ob_, (unsigned)(ob_ >> 32) & 0xffffu, DH * 2, 0x00020000u};
        asm volatile("" : "+v"(oo), "+v"(da), "+v"(one), "+s"(os_));
        if (head >= NH - GSZ) {
            __builtin_amdgcn_s_setprio(3);
            if (c == 0) {
                v4u hd;
                GATE(tl + THD)
                for (;;) {
                    hd = __builtin_amdgcn_raw_buffer_load_b128(__builtin_amdgcn_make_buffer_rsrc(ws + WS_PART + (NRW - GSZ + (head & 15 ^ 1)) * (NSPLIT * 512), 0, NSPLIT * 512, 0x00020000), (lane & 15) * 512, 0, SC1);
                    if (__builtin_amdgcn_ballot_w64(hd.y == T && hd.w == T) == ~0ull) break;
                    SLEEP(4)
                }
                const float w = __builtin_amdgcn_exp2f(__uint_as_float(hd.x) - row_max(__uint_as_float(hd.x)));
                if (lane < 16) sm.ent[0][lane] = w * __builtin_amdgcn_rcpf(row_sum(w) * 32767.f) * __uint_as_float(hd.z);
                CBAR();
                if (lane == 0) lds_st(&sm.tabs, 0u);
            }
            while (lds_ld(&sm.tabs) || lds_ld(&sm.merged) <= (2 * NU - 1) / NOUT * NOUT) SLEEP(1)
            CBAR();
            float cf = sm.ent[0][2 * c + (lane >> 5)];
            unsigned la = lds_addr(&sm.acc[0][lane]);
            asm volatile("" : "+v"(cf), "+v"(la));
            v4u x; bool got;
#define SEEN(J, N) { FETCH(x, J, N) got = __builtin_amdgcn_ballot_w64((((x.y ^ T << 24) | (x.w ^ T << 24)) >> 24) != 0) == 0; }
            GATE(tl + TCA) POLL(0, ro) GATE(tl + TCA + TCD) POLL(1, rx + 3 * GSZ * NSPLIT * 512) SEEN(0, 1)
            if (!got) SEEN(1, 0)
            while (!got) { POLL(2, rx + 4 * GSZ * NSPLIT * 512) SEEN(2, 0) SLEEP(0) }
            const float c0 = ldexpf(cf, (int)(x.y >> 16 & 255u) - 126), c1 = ldexpf(cf, (int)(x.w >> 16 & 255u) - 126);
            asm volatile("ds_add_f64 %0, %1\n\tds_add_f64 %0, %2 offset:512\n\tds_add_f64 %0, %3 offset:1024"
                         :: "v"(la), "v"((double)SWADF(c0 * (float)(short)x.x, c1 * (float)(short)x.z)), "v"((double)SWADF(c0 * (float)((int)x.x >> 16), c1 * (float)((int)x.z >> 16))),
                            "v"((double)SWADF(c0 * (float)(short)x.y, c1 * (float)(short)x.w)) : "memory");
            double a0, a1, a2; unsigned cn; u64 ex_;
            asm volatile("s_mov_b64 %4, exec\n\ts_mov_b64 exec, 1\n\tds_add_rtn_u32 %3, %6, %7\n\ts_mov_b64 exec, %4\n\t"
                         "ds_read_b64 %0, %5\n\tds_read_b64 %1, %5 offset:512\n\tds_read_b64 %2, %5 offset:1024\n\ts_waitcnt lgkmcnt(0)"
                         : "=&v"(a0), "=&v"(a1), "=&v"(a2), "=&v"(cn), "=&s"(ex_) : "v"(la), "v"(da), "v"(one) : "memory");
            c = __builtin_amdgcn_readfirstlane(cn);
            o0 = a0; o1 = a1; o2 = a2;
        } else {
            const bool quart = head >> 4 == WQ;
            const unsigned t15 = T << 16;
#define TAKE(K) { FETCH(x[K], K, 0) \
                bad |= (x[K].y ^ t15) | (x[K].w ^ t15); \
                const u64 bl = __builtin_amdgcn_ballot_w64((int)x[K].y < 0), be = __builtin_amdgcn_ballot_w64((int)x[K].w < 0); \
                L[K] = __uint_as_float(lane < 32 ? (unsigned)bl : (unsigned)(bl >> 32)); ex[K] = lane < 32 ? (unsigned)be : (unsigned)(be >> 32); \
                M = fmaxf(M, fmaxf(__uint_as_float((unsigned)bl), __uint_as_float((unsigned)(bl >> 32)))); }
#define SUMUP(K) { const float w_ = __builtin_amdgcn_exp2f(L[K] - M), c_ = ldexpf(w_, (int)(ex[K] & 255u) - 141); den += w_; \
                t[0] = fmaf(c_, (float)(short)x[K].x, t[0]); t[1] = fmaf(c_, (float)((int)x[K].x >> 16), t[1]); t[2] = fmaf(c_, (float)(short)x[K].y, t[2]); \
                t[3] = fmaf(c_, (float)(short)x[K].z, t[3]); t[4] = fmaf(c_, (float)((int)x[K].z >> 16), t[4]); t[5] = fmaf(c_, (float)(short)x[K].w, t[5]); }
            v4f* pb = (v4f*)((char*)&sm.cs[0][0][0] + 65536);
            __attribute__((address_space(3))) v4f* pw_ = (__attribute__((address_space(3))) v4f*)&pb[c * 64 + lane];
            const __attribute__((address_space(3))) v4f* pr_ = (const __attribute__((address_space(3))) v4f*)&pb[lane];
            const __attribute__((address_space(3))) float* pq_ = (const __attribute__((address_space(3))) float*)&pb[(lane & 7) * 64] + 3;
            asm volatile("" : "+v"(pw_), "+v"(pr_), "+v"(pq_));
            const unsigned tg = tl + (quart ? TRQ : TRD);
            GATE(tg)
            v4u x[4]; float L[4], M; unsigned ex[4];
            for (;;) {
                unsigned bad = 0; M = -3e38f;
                POLL(0, ro)
                if (quart) { POLL(1, rx) POLL(2, rx + GSZ * NSPLIT * 512) POLL(3, rx + 2 * GSZ * NSPLIT * 512) }
                __builtin_amdgcn_s_setprio(3);
                TAKE(0)
                if (quart) { TAKE(1) TAKE(2) TAKE(3) }
                if (__builtin_amdgcn_ballot_w64((bad & 0x7fff0000u) != 0) == 0) break;
                SLEEP(2)
            }
            float t[6] = {0.f, 0.f, 0.f, 0.f, 0.f, 0.f}, den = 0.f;
            SUMUP(0)
            if (quart) { SUMUP(1) SUMUP(2) SUMUP(3) }
            float ds_ = SWADF(den, den);
            asm("" : "+v"(ds_));
            *pw_ = (v4f){SWADF(t[0], t[3]), SWADF(t[1], t[4]), SWADF(t[2], t[5]), lane & 1 ? ds_ : M};
            CBAR();
            { unsigned cn; u64 ex_;
              asm volatile("s_mov_b64 %1, exec\n\ts_mov_b64 exec, 1\n\tds_add_rtn_u32 %0, %2, %3\n\ts_mov_b64 exec, %1\n\ts_waitcnt lgkmcnt(0)" : "=&v"(cn), "=&s"(ex_) : "v"(da), "v"(one) : "memory");
              c = __builtin_amdgcn_readfirstlane(cn); }
            if (c == NSW - 1) {
                const float mw = pq_[0], dw_ = pq_[4];
                v4f ps[NSW];
#pragma unroll
                for (int w = 0; w < NSW; w++) ps[w] = pr_[w * 64];
                __builtin_amdgcn_sched_barrier(0);
                float mx;
                asm("s_nop 1\n\tv_max_f32_dpp %0, %1, %1 quad_perm:[1,0,3,2] row_mask:0xf bank_mask:0xf\n\ts_nop 1\n\tv_max_f32_dpp %0, %0, %0 quad_perm:[2,3,0,1] row_mask:0xf bank_mask:0xf\n\t"
                    "s_nop 1\n\tv_max_f32_dpp %0, %0, %0 row_half_mirror row_mask:0xf bank_mask:0xf" : "=&v"(mx) : "v"(mw));
                float f = __builtin_amdgcn_exp2f(mw - mx);
                f *= __builtin_amdgcn_rcpf(dpp_add<0x141>(dpp_add<0x4e>(dpp_add<0xb1>(f * dw_))));
                v4f s0 = ps[0] * rdlane(f, 0), s1 = ps[1] * rdlane(f, 1);
#pragma unroll
                for (int w = 2; w < NSW; w += 2) { s0 += ps[w] * rdlane(f, w); s1 += ps[w + 1] * rdlane(f, w + 1); }
                o0 = s0.x + s1.x; o1 = s0.y + s1.y; o2 = s0.z + s1.z;
            }
        }
        if (c == NSW - 1) {
            asm("" : "+v"(o0), "+v"(o1), "+v"(o2));
            typedef __bf16 v2b __attribute__((ext_vector_type(2)));
#define PK(A, B) __builtin_bit_cast(unsigned, (v2b){(__bf16)(A), (__bf16)(B)})
            asm volatile("buffer_store_short %0, %2, %3, 0 offen\n\tbuffer_store_short_d16_hi %0, %2, %3, 0 offen offset:2\n\tbuffer_store_short %1, %2, %3, 0 offen offset:4"
                         :: "v"(PK(o0, o1)), "v"(PK(o2, o2)), "v"(oo), "s"(os_) : "memory");
            { const unsigned X = min((NOW() - tl - 150) / 2u, 99u) * 400; while (NOW() - (unsigned)t00 < 15000 + X) SLEEP(1) }
        }
    }
}

extern "C" void kernel_launch(void* const* d_in, const int* in_sizes, int n_in,
                              void* d_out, int out_size, void* d_ws, size_t ws_size,
                              hipStream_t stream) {
    const unsigned short* q = (const unsigned short*)d_in[0];
    const int* qk = (const int*)d_in[1];
    const unsigned short* k_scales = (const unsigned short*)d_in[2];
    const unsigned short* k_zeros = (const unsigned short*)d_in[3];
    const int* qv = (const int*)d_in[4];
    const unsigned short* v_scales = (const unsigned short*)d_in[5];
    const unsigned short* v_zeros = (const unsigned short*)d_in[6];
    const unsigned short* q_cos = (const unsigned short*)d_in[7];
    const unsigned short* q_sin = (const unsigned short*)d_in[8];
    const unsigned short* k_cos = (const unsigned short*)d_in[9];
    const unsigned short* k_sin = (const unsigned short*)d_in[10];
    unsigned char* ws = (unsigned char*)d_ws;
    attn_kernel<<<NSPLIT * GSZ, NTHREADS, 0, stream>>>(q, qk, k_scales, k_zeros, qv, v_scales, v_zeros,
                                                       q_cos, q_sin, k_cos, k_sin, (__hip_bfloat16*)d_out, ws);
}
```

```cpp
#include <hip/hip_runtime.h>
#include <hip/hip_bf16.h>
#include <stdint.h>

#define DH 192
#define HALF 96
#define SKV 2048
#define NH 256
#define NSW 8
#define NWAVES (NSW + 4)
#define NTHREADS (64 * NWAVES)
#define BROWS 128
#define NSPLIT 16
#define GSZ 16
#define WQ 10
#define NU (NH / GSZ + 3)
#define NPC 1024
#define NRR (NH + 3 * GSZ)
#define NIN 8
#define NOUT 8
#define WALL (15 * 144 + 4 * 48)
#define SC1 16
#define NRND 48
#define RST 4352
#define TK 2
#define NMIX 3
#define TUNIT 311
#define T01 50
#define TMIX 7
#define TOFF 50
#define PSF 100
#define TGS 82
#define PSW (NPC / 2 + 28)
#define TG0 8
#define TG1 5
#define TWO -80
#define PSG (NPC / 2 + 60)
#define TH0 3
#define TH1 3
#define EARLY 10
#define LEADP 24
#define DPH 2
#define TA 210
#define TA2 70
#define CSH 2
#define NPRB 3
#define TMAP 485
#define MINV 96
#define VW 4
#define MASTER 203
#define TRES 175
#define TB1 1200
#define PER 300
#define RDD 100
#define LEAD 70
#define LHI 85
#define LOK 74
#define PSL0 124
#define PEND 36
#define PUBP -12
#define LJ 210
#define LOK2 75
#define NTRY 3
#define TFO 6
#define SL0 42.f
#define PST0 200
#define KC 4
#define TC0 1000
#define TCS 9
#define PRE0 3

#define KREL 12
#define PDEL 70
#define NCP 3
#define NRW (NRR + NCP * GSZ)
#define TRD 116
#define THD 55
#define TRQ 55
#define TCA 138
#define TCD 22
#define TSD 560
#define THV 12
#define TGV -8
#define TKV 7
#define WS_EPOCH 0
#define WS_POST0 8192
#define WS_POST (WS_POST0 + ((unsigned)E1 & 15) * 2048)
#define WS_NEWS (49152 + 256)
#define WS_HELLO 59904
#define WS_BOARD 65536
#define WS_PART (2u << 20)
#define WS_SWAP (WS_PART + NRW * NSPLIT * 512)

typedef unsigned int v2u __attribute__((ext_vector_type(2)));
typedef unsigned int v16u __attribute__((ext_vector_type(16)));
typedef unsigned int v4u __attribute__((ext_vector_type(4)));
typedef int v4i __attribute__((ext_vector_type(4)));
typedef float v2f __attribute__((ext_vector_type(2)));
typedef float v4f __attribute__((ext_vector_type(4)));
typedef unsigned long long u64;

__device__ __forceinline__ float bflo(unsigned u) { return __uint_as_float(u << 16); }
__device__ __forceinline__ float bfhi(unsigned u) { return __uint_as_float(u & 0xffff0000u); }
__device__ __forceinline__ float bfr(float x) {
    __bf16 b = (__bf16)x;
    return __uint_as_float(((unsigned)__builtin_bit_cast(unsigned short, b)) << 16);
}

#define SLEEP(n) SLEEP_(n)
#define SLEEP_(n) asm volatile("s_sleep " #n "\n\tv_add_co_u32 %0, vcc, 1, %0\n\ts_cbranch_vccz 1f\n\ts_endpgm\n1:" : "+v"(spins) :: "vcc", "memory");
#define SPIN0 0xf0000000u
#define CBAR() asm volatile("" ::: "memory")

__device__ __forceinline__ void wave_sync() {
    __builtin_amdgcn_fence(__ATOMIC_RELEASE, "wavefront");
    __builtin_amdgcn_wave_barrier();
    __builtin_amdgcn_fence(__ATOMIC_ACQUIRE, "wavefront");
}
template <int CTRL>
__device__ __forceinline__ float dpp_add(float x) {
    int y = __builtin_amdgcn_update_dpp(0, __float_as_int(x), CTRL, 0xf, 0xf, true);
    return x + __int_as_float(y);
}
template <int CTRL>
__device__ __forceinline__ float dpp_max(float x) {
    int y = __builtin_amdgcn_update_dpp(__float_as_int(x), __float_as_int(x), CTRL, 0xf, 0xf, false);
    return fmaxf(x, __int_as_float(y));
}
__device__ __forceinline__ float row_sum(float x) { x = dpp_add<0x128>(x); x = dpp_add<0x124>(x); x = dpp_add<0x122>(x); return dpp_add<0x121>(x); }
__device__ __forceinline__ float row_max(float m) { m = dpp_max<0x128>(m); m = dpp_max<0x124>(m); m = dpp_max<0x122>(m); return dpp_max<0x121>(m); }
__device__ __forceinline__ float rdlane(float x, int l) { return __int_as_float(__builtin_amdgcn_readlane(__float_as_int(x), l)); }
__device__ __forceinline__ float wave_max(float m) { m = row_max(m); return fmaxf(fmaxf(rdlane(m, 0), rdlane(m, 16)), fmaxf(rdlane(m, 32), rdlane(m, 48))); }
__device__ __forceinline__ float wave_maxv(float m) {
    m = row_max(m);
    const auto a = __builtin_amdgcn_permlane16_swap(__float_as_uint(m), __float_as_uint(m), false, false);
    m = fmaxf(__uint_as_float(a[0]), __uint_as_float(a[1]));
    const auto b = __builtin_amdgcn_permlane32_swap(__float_as_uint(m), __float_as_uint(m), false, false);
    return fmaxf(__uint_as_float(b[0]), __uint_as_float(b[1]));
}
__device__ __forceinline__ float wave_sum(float x) { x = row_sum(x); return (rdlane(x, 0) + rdlane(x, 16)) + (rdlane(x, 32) + rdlane(x, 48)); }
__device__ __forceinline__ unsigned lds_ld(const unsigned* p) { return __builtin_amdgcn_readfirstlane(__hip_atomic_load(p, __ATOMIC_RELAXED, __HIP_MEMORY_SCOPE_WORKGROUP)); }
__device__ __forceinline__ unsigned lds_addr(const void* p) { return (unsigned)(size_t)(const __attribute__((address_space(3))) void*)p; }
#define DMA16(SRC, LDS) asm volatile("s_mov_b32 %0, m0\n\ts_mov_b32 m0, %2\n\ts_nop 0\n\tglobal_load_lds_dwordx4 %1, off\n\ts_mov_b32 m0, %0" : "=&s"(m0keep) : "v"(SRC), "s"(LDS) : "memory")
__device__ __forceinline__ void lds_st(unsigned* p, unsigned v) { __hip_atomic_store(p, v, __ATOMIC_RELAXED, __HIP_MEMORY_SCOPE_WORKGROUP); }
#define NOW() ((unsigned)__builtin_amdgcn_s_memrealtime())
__device__ __forceinline__ unsigned tdue(unsigned g) {
    return (g * TUNIT + min(g, 480u) * TK + min(g, 48u) * T01 + min(g, 32u * NMIX) * TMIX + (g >= PSF ? TGS * 32 : 0)) >> 5;
}
__device__ __forceinline__ unsigned tvx(unsigned g, unsigned gap) {
    return (g >= PSW ? gap >> (g >= PSG ? 12 : 0) & 0xfffu : 0u) + (gap >> 31 ? TKV : min(g - NPC / 2, 32u * NMIX) * TMIX >> 5);
}

__device__ __forceinline__ unsigned unit_of(unsigned g) { const unsigned p = g & 511; return (g >> 9) * NU + (p >> 5) + min(max((int)p - 32 * WQ, 0) >> 3, 3); }
__device__ __forceinline__ unsigned first_of(unsigned e) { return 32 * e - 24 * min(max((int)e - WQ, 0), 4); }
#define QUARTER(E) ((E) - WQ < 4)
__device__ __forceinline__ unsigned went(unsigned e) { return 144 * e - 96 * min(max((int)e - WQ, 0), 4); }
__device__ __forceinline__ unsigned wunit(unsigned x) { x += 24 * NMIX; return x < 24 * WQ ? x / 24 : x < 24 * WQ + 32 ? WQ + (x - 24 * WQ) / 8 : (x + 64) / 24; }

#define ROTT 0xed8d2c3d2c35ed84ull, 0xba85aa165b03ac82ull, 0xee7efa489a38af71ull, 0xe932da1b0bab51d8ull, 0xfa96112d5fee971bull, 0x2c7e9ea5a544f071ull, 0x3d86093d5f0e810bull, 0xe932da1b0bab51d8ull, 0xfa96112d5fee971bull, 0xd834e62c5fa723e9ull, 0xa4cd778c2ca4aaa5ull, 0xe932da1b0bab51d8ull, 0xfa96112d5fee971bull, 0xc834e12c0aaa4ec7ull, 0x3d86093d5f0e810bull, 0xe932da1b0bab51d8ull
static __device__ const u64 ROT[16] = {ROTT};

struct __attribute__((aligned(16))) Smem {
    unsigned cs[NSW][4][768];
    double acc[NOUT][DH];
    float ent[NOUT][BROWS];
    unsigned short wv[WALL];
    unsigned short wo[WALL];
    unsigned q[NIN][HALF];
    float2 kz[NIN][BROWS];
    float2 vz[NIN][BROWS];
    unsigned utab[64];
    unsigned line, gap;
    unsigned late, grab;
    unsigned rdy[NIN], cnt[NOUT];
    unsigned tabs, vrdy, swp;
    unsigned aline;
    unsigned tag_rdy, tag_lo, tag_hi, merged, cdone, cidx;
    unsigned slow;
    unsigned chead;
};

#define LDS_ADD(P, V) { u64 ex_; asm volatile("s_mov_b64 %0, exec\n\ts_mov_b64 exec, 1\n\tds_add_u32 %1, %2\n\ts_mov_b64 exec, %0" : "=&s"(ex_) : "v"(lds_addr(P)), "v"(V) : "memory"); }
#define LDS_OR(P, V) { u64 ex_; asm volatile("s_mov_b64 %0, exec\n\ts_mov_b64 exec, 1\n\tds_or_b32 %1, %2\n\ts_mov_b64 exec, %0" : "=&s"(ex_) : "v"(lds_addr(P)), "v"(V) : "memory"); }

__global__ __launch_bounds__(NTHREADS, 3) void attn_kernel(
    const unsigned short* __restrict__ q, const int* __restrict__ qk,
    const unsigned short* __restrict__ k_scales, const unsigned short* __restrict__ k_zeros,
    const int* __restrict__ qv,
    const unsigned short* __restrict__ v_scales, const unsigned short* __restrict__ v_zeros,
    const unsigned short* __restrict__ q_cos, const unsigned short* __restrict__ q_sin,
    const unsigned short* __restrict__ k_cos, const unsigned short* __restrict__ k_sin,
    __hip_bfloat16* __restrict__ out, unsigned char* __restrict__ ws)
{
    __shared__ Smem sm;
    const int tid = threadIdx.x;
    unsigned spins = SPIN0;
    const int lane = tid & 63;
    const int hw = __builtin_amdgcn_readfirstlane(tid >> 6);
    u64 t00; unsigned ka;
    asm volatile("s_memrealtime %0\n\ts_load_dword %1, %2, 0x40" : "=&s"(t00), "=&s"(ka) : "s"(__builtin_amdgcn_kernarg_segment_ptr()));
    if (hw == 0) {
        const char* pc0; asm volatile("s_getpc_b64 %0" : "=s"(pc0));
        asm volatile("global_load_dword a6, %0, %1" :: "v"(((blockIdx.x & ~7u) + (lane & 7)) * 128), "s"(pc0) : "a6", "memory");
    }
    const int wave = hw >= 4 ? hw - 4 : hw + NSW;
    const unsigned split = (blockIdx.x & 1) + (blockIdx.x & 2) * 4 + ((blockIdx.x >> 3) & 3) * 2;
    const unsigned side = (split ^ (blockIdx.x >> 2)) & 1, rank = blockIdx.x >> 5;
    const unsigned head0 = 2 * rank + side;
    const unsigned rf0 = (8 * split + 1) / 3, r3 = 8 * split + 1 - 3 * rf0;
    const unsigned koff = ((lane >> 5) * DH + 3 * (lane & 31)) * 4, koffb = koff + 384;
#define AR(S) (16 + (S) * 8)
#define GLD(A, OFF, BASE, IMM) asm volatile("global_load_dwordx3 a[%c0:%c1], %2, %3 offset:%4 nt" :: "i"(A), "i"((A) + 2), "v"(OFF), "s"(BASE), "i"(IMM))
#define ISSUE(S, J, BASE, OA, OB) { GLD(AR(S), OA, BASE, (J) * 1536); GLD(AR(S) + 4, OB, BASE, (J) * 1536); }
#define HEAD2 ((GSZ + 1 - 2 * side) * (SKV * DH * 4))
    const unsigned wo = (hw - 2) & 7;
    const bool pre = hw >= 4 && wo < PRE0;
    if (pre) {
        const char* a = (const char*)qk + (head0 * 512 + side * 256 + rf0 + wave + (wave >= 6 ? 80 : 0)) * 3072; const char* b = a + HEAD2;
        ISSUE(0, 0, a, koff, koffb) ISSUE(1, 1, a, koff, koffb) ISSUE(2, 0, b, koff, koffb) ISSUE(3, 1, b, koff, koffb)
    }
    asm volatile("s_waitcnt lgkmcnt(0)" : "+s"(t00), "+s"(ka));
    if (hw == 0) {
        constexpr u64 rot[16] = {ROTT};
        sm.utab[lane] = lane == 2 ? 2 * GSZ + head0 | ((unsigned)(rot[2] >> 4 * head0) & 15) << 9 : ~0u;
        unsigned l0 = (unsigned)t00 + TOFF;
        if (KREL) { const unsigned t1 = NOW() + KREL; if ((int)(t1 - l0) > 0) l0 = t1; }
        if (lane < 32) (&sm.line)[lane] = lane == 3 ? 64u : (lane & 29) == 0 ? l0 : 0u;
        asm volatile("s_waitcnt lgkmcnt(0)" ::: "memory");
    }
    __builtin_amdgcn_s_barrier();
    CBAR();
    const u64 rtab = r3 == 1 ? 0x5756552c2b020100ull : r3 == 0 ? 0x5756552c2b2a0100ull : 0x57562d2c2b020100ull;
#define R8(I) (rf0 + ((unsigned)(rtab >> (8 * (I))) & 255u))
#define GATE(DUE) { int d_ = (int)((DUE) - NOW()); if (d_ < 3000) while (d_ > 0) { __builtin_amdgcn_s_sleep(1); d_ = (int)((DUE) - NOW()); } }
#define TLAST() (lds_ld(&sm.line) + tdue(NPC - 2) + tvx(NPC - 2, lds_ld(&sm.gap)))
    if (__builtin_expect(wave < NSW, 1)) {
        const int cst = 2 * side;
        const unsigned r8w = R8((unsigned)wave);
        const char* cbase[3];
#pragma unroll
        for (int j = 0; j < 3; j++) { const int x = j * 64 + lane; cbase[j] = (x < 96 ? (const char*)k_cos + x * 16 : (const char*)k_sin + (x - 96) * 16) + r8w * (4 * DH * 2); }
        unsigned m0keep;
#define CS_DMA(W, C) { _Pragma("unroll") for (int j = 0; j < 3; j++) DMA16(cbase[j] + (C) * (512 * DH * 2), lds_addr(&sm.cs[0][0][0]) + ((W) * 4 + (C)) * 3072 + j * 1024); }
#define REPACK(W, C) { \
        unsigned* cw = &sm.cs[W][C][0]; \
        const unsigned* l = cw + rsel * (DH / 2) + qoff; \
        unsigned r[2][6]; \
        _Pragma("unroll") \
        for (int j = 0; j < 2; j++) { \
            unsigned e[4][2]; \
            _Pragma("unroll") \
            for (int k = 0; k < 4; k++) { \
                const unsigned* a = l + j * DH + (k & 1) * 48 + (k >> 1) * 384;       \
                unsigned x0 = a[0], x1 = a[1]; \
                e[k][0] = __builtin_amdgcn_alignbit(x1, x0, sh); \
                e[k][1] = x1 >> sh; \
            } \
            r[j][0] = (e[0][0] & 0xffffu) | (e[3][0] << 16); \
            r[j][1] = (e[0][0] >> 16) | (e[3][0] & 0xffff0000u); \
            r[j][2] = (e[0][1] & 0xffffu) | (e[3][1] << 16); \
            r[j][3] = (e[1][0] & 0xffffu) | (e[2][0] << 16); \
            r[j][4] = (e[1][0] >> 16) | (e[2][0] & 0xffff0000u); \
            r[j][5] = (e[1][1] & 0xffffu) | (e[2][1] << 16); \
        } \
        wave_sync(); \
        _Pragma("unroll") \
        for (int j = 0; j < 2; j++) { *(v4u*)&cw[j * 256 + lane * 4] = (v4u){r[j][0], r[j][1], r[j][2], r[j][3]}; *(v2u*)&cw[512 + j * 128 + lane * 2] = (v2u){r[j][4], r[j][5]}; } \
        wave_sync(); }
        const int sub = lane & 31;
        const int rsel = lane >> 5;
        const unsigned sh = (sub & 1) * 16;
        const int qoff = 3 * (sub >> 1) + (sub & 1);
        const unsigned voff = 3 * lane * 4;
        asm volatile("" ::: "a16", "a17", "a18", "a19", "a20", "a21", "a22", "a23", "a24", "a25", "a26", "a27", "a28", "a29", "a30", "a31",
                            "a32", "a33", "a34", "a35", "a36", "a37", "a38", "a39", "a40", "a41", "a42", "a43", "a44", "a45", "a46", "a47");
        asm volatile("" ::: "a48", "a49", "a50", "a51", "a52", "a53", "a54", "a55", "a56", "a57", "a58", "a59", "a60", "a61", "a62", "a63",
                            "a64", "a65", "a66", "a67", "a68", "a69", "a70", "a71", "a72", "a73", "a74", "a75", "a76", "a77", "a78", "a79");
        int ta[4][3], tb[4][3];
#define WAITT(P, S, N) asm volatile("s_waitcnt vmcnt(%6)\n\tv_accvgpr_read_b32 %0, a[%c7]\n\tv_accvgpr_read_b32 %1, a[%c8]\n\tv_accvgpr_read_b32 %2, a[%c9]\n\t" \
                              "v_accvgpr_read_b32 %3, a[%c10]\n\tv_accvgpr_read_b32 %4, a[%c11]\n\tv_accvgpr_read_b32 %5, a[%c12]" \
                              : "=v"(ta[P][0]), "=v"(ta[P][1]), "=v"(ta[P][2]), "=v"(tb[P][0]), "=v"(tb[P][1]), "=v"(tb[P][2]) \
                              : "i"(N), "i"(AR(S)), "i"(AR(S) + 1), "i"(AR(S) + 2), "i"(AR(S) + 4), "i"(AR(S) + 5), "i"(AR(S) + 6))
        const unsigned voffb = voff + 768;
        u64 ln = 0;
#define TDUE(G) (tdue(G) + ((G) >= NPC / 2 ? tvx(G, __builtin_amdgcn_readfirstlane((unsigned)(ln >> 32))) : 0u))
#define REPORT() { const unsigned t_ = now - tdc; if (lane == 0) lds_st(&sm.late, t_); }
#define LINE() ln = LDV((u64*)&sm.line);
#define LDV(P) __hip_atomic_load(P, __ATOMIC_RELAXED, __HIP_MEMORY_SCOPE_WORKGROUP)
        unsigned u = 0, rg = cst * 8 + wave, np = 0;
        CS_DMA(wave, cst)

        const char* kb0 = (const char*)qk + (size_t)(head0 * 512 + r8w) * 3072;
#define KB(I) (kb0 + ((cst + (I)) & 3) * (128 * 3072))
        const char* kb; const char* kb2;
#define SISSUE(I) { kb = KB(I); kb2 = kb + HEAD2; const unsigned du_ = lds_ld(&sm.line) + tdue(16 * (I) + 2 * wo); GATE(du_) \
            ISSUE(((I) & 1) * 4, 0, kb, koff, koffb) ISSUE(((I) & 1) * 4 + 1, 1, kb, koff, koffb) ISSUE(((I) & 1) * 4 + 2, 0, kb2, koff, koffb) ISSUE(((I) & 1) * 4 + 3, 1, kb2, koff, koffb) }
        if (!pre) SISSUE(0)
        CS_DMA(wave, (cst + 1) & 3)
        if (pre) asm volatile("s_waitcnt vmcnt(3)" ::: "memory");
        asm volatile("s_waitcnt vmcnt(11)" ::: "memory");
        if (!pre) LDS_ADD(&sm.slow, NOW() - lds_ld(&sm.line) - tdue(2 * wo))
        REPACK(wave, cst)
        SISSUE(1)
#define TABDONE(I) LDS_OR(&sm.tabs, 1u << ((((cst + (I)) & 3) * 8) + wave))
        TABDONE(0)
        const float scale = 0.07216878364870322f;
        double acc[3] = {0.0, 0.0, 0.0};
        bool first = true, chg, none = false;
        unsigned Qp[3], Qn[3], Rp[3], Rn[3];
        float kz4[4], ksc, kres[4];
        v4u ca[4]; v2u cb[4];
        v2f qe[4][3], A[4];
        unsigned vut, nrg;
        const unsigned vr = ((lane >> 3) & 3) * 128 + R8((unsigned)lane & 7);
        unsigned g, mg;
        const unsigned grab_adr = lds_addr(&sm.grab);
        asm volatile("v_accvgpr_write_b32 a13, 1\n\tv_accvgpr_write_b32 a14, 2" ::: "a13", "a14", "a15");
#define GRAB(N) { u64 ex_; asm volatile("s_mov_b64 %0, exec\n\ts_mov_b64 exec, 1\n\tds_add_rtn_u32 a15, %1, a%c2\n\ts_mov_b64 exec, %0" : "=&s"(ex_) : "v"(grab_adr), "i"(12 + (N)) : "memory"); }
#define GRABBED() { unsigned t_; asm volatile("s_waitcnt lgkmcnt(0)\n\tv_accvgpr_read_b32 %1, a15\n\ts_nop 0\n\tv_readfirstlane_b32 %0, %1\n\ts_nop 1" : "=s"(g), "=v"(t_)); }
        unsigned oa, ob, un, tdn, tiss, tdc = 0;
#define PLAN() { \
            GRABBED() un = unit_of(g); chg = un != u; none = g >= NPC; \
            const bool nv = g >= NPC / 2; \
            unsigned hd = __builtin_amdgcn_readlane(vut, un); \
            while (hd == ~0u) { SLEEP(1) hd = lds_ld(&sm.utab[un]); } \
            nrg = (g + (hd >> 8)) & 31; hd &= 255; \
            unsigned boff, boff2; asm("s_mul_i32 %0, %1, 0xc00" : "=s"(boff) : "s"(hd * 512 + __builtin_amdgcn_readlane(vr, nrg))); \
            asm("s_mul_i32 %0, %1, 0xc00" : "=s"(boff2) : "s"(hd * 512 + __builtin_amdgcn_readlane(vr, nrg + 1))); \
            const char* b_ = nv ? (const char*)qv : (const char*)qk; \
            kb = b_ + boff; kb2 = b_ + boff2; \
            oa = nv ? voff : koff; ob = nv ? voffb : koffb; \
            tdn = TDUE(g); }
#define FIRE() { ISSUE(0, 0, kb, oa, ob) ISSUE(1, 1, kb, oa, ob) ISSUE(2, 0, kb2, oa, ob) ISSUE(3, 1, kb2, oa, ob) }
#define LAUNCH() { \
            LINE() \
            const unsigned du_ = __builtin_amdgcn_readfirstlane((unsigned)ln) + tdn; \
            GATE(du_) \
            ISSUE(0, 0, kb, oa, ob) ISSUE(1, 1, kb, oa, ob) \
            GATE(du_ + 10) \
            ISSUE(2, 0, kb2, oa, ob) ISSUE(3, 1, kb2, oa, ob) tiss = NOW(); }
#define ARRIVED() \
            const unsigned now = NOW(); \
            const bool stall = now - tiss > LJ && g - PST0 < NPC - 48 - PST0; \
            const bool late = !none && !stall && (int)(now + EARLY - __builtin_amdgcn_readfirstlane((unsigned)ln) - tdn) >= 0; \
            if (late) { FIRE() tiss = now; } \
            if (stall && lane == 0) __hip_atomic_fetch_max(&sm.line, (unsigned)ln + (now - tiss) - LOK2, __ATOMIC_RELAXED, __HIP_MEMORY_SCOPE_WORKGROUP); \
            REPORT()
#define ADVANCE(ISV) { \
            if (chg) FLUSH(ISV) \
            if (!none && !late) LAUNCH() \
            first = chg; u = un; rg = nrg; tdc = tdn + 10; }
#define K_TAB(MODE) { _Pragma("unroll") for (int p = 0; p < ((MODE) ? 2 : 4); p++) { \
            const unsigned* cw = &sm.cs[(rg & 7) + (p >> 1)][rg >> 3][0]; \
            ca[p] = *(const v4u*)&cw[(p & 1) * 256 + lane * 4]; cb[p] = *(const v2u*)&cw[512 + (p & 1) * 128 + lane * 2]; } }
#define K_PRE(MODE, P) { \
        float e0, e1, e2, f0, f1, f2; \
        const unsigned* qp_ = ((MODE) && (P) >= 2) ? Rp : Qp; const unsigned* qn_ = ((MODE) && (P) >= 2) ? Rn : Qn; \
        const int tp_ = (MODE) ? (P) & 1 : (P); \
        asm("v_dot2_f32_bf16 %0, %6, %12, 0\n\tv_dot2_f32_bf16 %3, %9, %15, 0\n\tv_dot2_f32_bf16 %1, %7, %13, 0\n\tv_dot2_f32_bf16 %4, %10, %16, 0\n\t" \
            "v_dot2_f32_bf16 %2, %8, %14, 0\n\tv_dot2_f32_bf16 %5, %11, %17, 0\n\ts_nop 2" \
            : "=&v"(e0), "=&v"(e1), "=&v"(e2), "=&v"(f0), "=&v"(f1), "=&v"(f2) \
            : "v"(qp_[0]), "v"(qp_[1]), "v"(qp_[2]), "v"(qn_[0]), "v"(qn_[1]), "v"(qn_[2]), \
              "v"(ca[tp_].x), "v"(ca[tp_].y), "v"(ca[tp_].z), "v"(ca[tp_].w), "v"(cb[tp_].x), "v"(cb[tp_].y)); \
        qe[P][0] = (v2f){e0, f0}; qe[P][1] = (v2f){e1, f1}; qe[P][2] = (v2f){e2, f2}; \
        A[P] = (qe[P][0] + qe[P][1] + qe[P][2]) * (v2f){-kz4[P], -kz4[P]}; }
#define K_WAIT(MODE, P) { \
        if ((MODE) == 0) { WAITT(P, P, 6 - 2 * (P)); } \
        else { WAITT(P, (((MODE) - 1) & 1) * 4 + (P), ((MODE) < 3 ? 20 : (MODE) == 3 ? 17 : 14) - 2 * (P)); } }
#define K_FMA(P) { \
        _Pragma("unroll") \
        for (int i = 0; i < 3; i++) { v2f x = {(float)ta[P][i], (float)tb[P][i]}; A[P] = qe[P][i] * x + A[P]; } \
        kres[P] = A[P].x + A[P].y; }
#define K_RED(MODE) { \
        const bool o1 = lane & 1, o2 = lane & 2; \
        float s01 = (o1 ? kres[1] : kres[0]) + __int_as_float(__builtin_amdgcn_update_dpp(0, __float_as_int(o1 ? kres[0] : kres[1]), 0xb1, 0xf, 0xf, true)); \
        float s23 = (o1 ? kres[3] : kres[2]) + __int_as_float(__builtin_amdgcn_update_dpp(0, __float_as_int(o1 ? kres[2] : kres[3]), 0xb1, 0xf, 0xf, true)); \
        float sx = (o2 ? s23 : s01) + __int_as_float(__builtin_amdgcn_update_dpp(0, __float_as_int(o2 ? s01 : s23), 0x4e, 0xf, 0xf, true)); \
        sx = dpp_add<0x124>(sx); sx = dpp_add<0x128>(sx); \
        const auto sw = __builtin_amdgcn_permlane16_swap(__float_as_uint(sx), __float_as_uint(sx), false, false); \
        sx = __uint_as_float(sw[0]) + __uint_as_float(sw[1]); \
        if ((lane & 28) == 0) { if (MODE) sm.ent[(lane >> 1) & 1][rg * 4 + 2 * (lane & 1) + rsel] = sx * ksc; else sm.ent[u % NOUT][rg * 4 + 2 * (lane & 3) + rsel] = sx * ksc; } }
#define K_PRE4(MODE) { K_PRE(MODE, 0) K_PRE(MODE, 1) K_PRE(MODE, 2) K_PRE(MODE, 3) asm volatile("" :: "v"(A[0]), "v"(A[1]), "v"(A[2]), "v"(A[3])); }
#define K_BODY(MODE) { K_PRE4(MODE) K_WAIT(MODE, 0) K_FMA(0) K_WAIT(MODE, 1) K_FMA(1) K_WAIT(MODE, 2) K_FMA(2) K_WAIT(MODE, 3) K_FMA(3) K_RED(MODE) }
#define V_FMA(J, W0, W1) { \
        _Pragma("unroll") \
        for (int i = 0; i < 3; i++) { \
            acc[i] = fma(W0, (double)ta[J][i], acc[i]); \
            acc[i] = fma(W1, (double)tb[J][i], acc[i]); \
        } }
#define V_PIN() asm volatile("" :: "v"(acc[0]), "v"(acc[1]), "v"(acc[2]));
#define RDYWAIT(SL, SEQ) { while (lds_ld(&sm.rdy[SL]) != (SEQ)) SLEEP(1); CBAR(); }
#define LOADZ() { _Pragma("unroll") for (int p = 0; p < 4; p++) kz4[p] = sm.kz[u % NIN][rg * 4 + 2 * p + rsel].x; ksc = sm.kz[u % NIN][rg * 4 + 2 * (lane & 3) + rsel].y * scale; }
#define LOADQ() { _Pragma("unroll") for (int i = 0; i < 3; i++) { Qp[i] = sm.q[u % NIN][3 * sub + i]; Qn[i] = __builtin_amdgcn_alignbit(Qp[i], Qp[i], 16) ^ 0x80000000u; } }
#define FLUSH(ISV) { \
            mg = __builtin_amdgcn_readfirstlane(mg); \
            while (u >= mg + NOUT) { SLEEP(1); mg = lds_ld(&sm.merged); } \
            CBAR(); \
            if (ISV) { const unsigned aa = lds_addr(&sm.acc[u % NOUT][lane]); \
                asm volatile("ds_add_f64 %0, %1\n\tds_add_f64 %0, %2 offset:512\n\tds_add_f64 %0, %3 offset:1024" :: "v"(aa), "v"(acc[0]), "v"(acc[1]), "v"(acc[2]) : "memory"); \
                acc[0] = 0.0; acc[1] = 0.0; acc[2] = 0.0; } \
            CBAR(); \
            LDS_ADD(&sm.cnt[u % NOUT], np) \
            CBAR(); \
            np = 0; }

#define LOADZ2() { _Pragma("unroll") for (int p = 0; p < 4; p++) kz4[p] = sm.kz[p >> 1][rg * 4 + 2 * (p & 1) + rsel].x; ksc = sm.kz[(lane >> 1) & 1][rg * 4 + 2 * (lane & 1) + rsel].y * scale; }
#define STATIC_STEP(I) { \
            rg = ((cst + (I)) & 3) * 8 + wave; \
            LOADZ2() \
            K_TAB((I) + 1) K_BODY((I) + 1) }
        CS_DMA(wave, (cst + 2) & 3)
        RDYWAIT(0, 1u) RDYWAIT(1, 2u)
        LOADQ()
#pragma unroll
        for (int i = 0; i < 3; i++) { Rp[i] = sm.q[1][3 * sub + i]; Rn[i] = __builtin_amdgcn_alignbit(Rp[i], Rp[i], 16) ^ 0x80000000u; }
        STATIC_STEP(0)
        asm volatile("s_waitcnt vmcnt(11)" ::: "memory");
        REPACK(wave, (cst + 1) & 3)
        TABDONE(1)
        SISSUE(2)
        CS_DMA(wave, (cst + 3) & 3)
        STATIC_STEP(1)
        asm volatile("s_waitcnt vmcnt(11)" ::: "memory");
        REPACK(wave, (cst + 2) & 3)
        TABDONE(2)
        SISSUE(3)
        asm volatile("s_waitcnt vmcnt(8)" ::: "memory");
        REPACK(wave, (cst + 3) & 3)
        TABDONE(3)
        STATIC_STEP(2)
        GRAB(2)
        vut = LDV(&sm.utab[lane]);
        rg = ((cst + 3) & 3) * 8 + wave;
        LOADZ2() K_TAB(4) K_PRE4(4)
        PLAN()
        LAUNCH()
        K_WAIT(4, 0) K_FMA(0) K_WAIT(4, 1) K_FMA(1) K_WAIT(4, 2) K_FMA(2) K_WAIT(4, 3) K_FMA(3) K_RED(4)
        CBAR();
        LDS_ADD(&sm.cnt[0], 4u) LDS_ADD(&sm.cnt[1], 4u)
        u = un; rg = nrg; tdc = tdn + 10;
        bool tabok = false;
        __builtin_amdgcn_s_setprio(2);
#pragma unroll 1
        while (u < NU) {
            GRAB(2)
            mg = LDV(&sm.merged);
            vut = LDV(&sm.utab[lane]);
            LINE()
            if (!tabok) {
                for (;;) { const unsigned tb_ = lds_ld(&sm.tabs); tabok = tb_ == ~0u; if (((tb_ >> rg) & 3u) == 3u) break; SLEEP(1) }
                CBAR();
            }
            K_TAB(0)
            if (first) {
                unsigned fl = LDV(&sm.rdy[u % NIN]);
                CBAR();
                LOADQ() LOADZ()
                if (__builtin_amdgcn_readfirstlane(fl) != u + 1) { RDYWAIT(u % NIN, u + 1) LOADQ() LOADZ() }
            } else LOADZ()
            K_PRE4(0)
            PLAN()
            K_WAIT(0, 0) K_FMA(0) K_WAIT(0, 1) K_FMA(1)
            asm volatile("" :: "v"(kres[0]), "v"(kres[1]));
            K_WAIT(0, 2) K_WAIT(0, 3)
            ARRIVED()
            K_FMA(2) K_FMA(3) K_RED(0)
            np += 2;
            ADVANCE(false)
        }
#pragma unroll 1
        while (u < 2 * NU) {
            GRAB(2)
            mg = LDV(&sm.merged);
            LINE()
            const unsigned vr_ = LDV(&sm.vrdy);
            CBAR();
            const unsigned short* wp = &sm.wv[went(u - NU) + (QUARTER(u - NU) ? rg & 7 : rg) * 4];
            v4i pw = *(const v4i*)wp;
            if (first && !((__builtin_amdgcn_readfirstlane(vr_) >> (u - NU)) & 1)) { while (!((lds_ld(&sm.vrdy) >> (u - NU)) & 1)) SLEEP(1); CBAR(); pw = *(const v4i*)wp; }
            const double w0 = (short)pw.x, w1 = pw.x >> 16, w2 = (short)pw.y, w3 = pw.y >> 16, w4 = (short)pw.z, w5 = pw.z >> 16, w6 = (short)pw.w, w7 = pw.w >> 16;
            asm volatile("" :: "v"(w0), "v"(w1), "v"(w2), "v"(w3), "v"(w4), "v"(w5), "v"(w6), "v"(w7));
            PLAN()
            double hy; unsigned tl8, rowl, rowc, aa, ca;
            if (none) {
                hy = *(const float*)&sm.wv[went(NU - 1) + 130]; tl8 = LDV(&sm.tag_lo) << 24;
                rowl = (LDV(&sm.utab[2 * NU - 1]) & 255) * (NSPLIT * 512); rowc = (rowl & 15 * NSPLIT * 512 ^ NSPLIT * 512) + NRR * NSPLIT * 512 + lane * 8; rowl += lane * 8;
                aa = lds_addr(&sm.acc[(2 * NU - 1) % NOUT][lane]); ca = lds_addr(&sm.cnt[(2 * NU - 1) % NOUT]);
                mg = __builtin_amdgcn_readfirstlane(mg);
                while (u >= mg + NOUT) { SLEEP(1); mg = lds_ld(&sm.merged); }
                asm volatile("" : "+v"(hy), "+v"(tl8), "+v"(aa), "+v"(ca), "+v"(rowl), "+v"(rowc));
            }
            WAITT(0, 0, 6); V_FMA(0, w0, w1) V_PIN()
            WAITT(1, 1, 4); V_FMA(1, w2, w3) V_PIN()
            WAITT(2, 2, 2); V_FMA(2, w4, w5) V_PIN()
            WAITT(3, 3, 0);
            np += 2;
            if (none) {
                V_FMA(3, w6, w7)
                if (u != 2 * NU - 1) { FLUSH(true) break; }
                double a0, a1, a2; unsigned cn; u64 ex_;
                asm volatile("ds_add_f64 %5, %6\n\tds_add_f64 %5, %7 offset:512\n\tds_add_f64 %5, %8 offset:1024\n\t"
                             "s_mov_b64 %4, exec\n\ts_mov_b64 exec, 1\n\tds_add_rtn_u32 %3, %9, %10\n\ts_mov_b64 exec, %4\n\t"
                             "ds_read_b64 %0, %5\n\tds_read_b64 %1, %5 offset:512\n\tds_read_b64 %2, %5 offset:1024\n\ts_waitcnt lgkmcnt(0)"
                             : "=&v"(a0), "=&v"(a1), "=&v"(a2), "=&v"(cn), "=&s"(ex_) : "v"(aa), "v"(acc[0]), "v"(acc[1]), "v"(acc[2]), "v"(ca), "v"(np) : "memory");
                const float n0 = a0 - hy, n1 = a1 - hy, n2 = a2 - hy;
                const unsigned e2 = max(__float_as_uint(fmaxf(fmaxf(fabsf(n0), fabsf(n1)), fabsf(n2))) >> 23, 14u);
                const float f2 = __uint_as_float((253u - e2) << 23);
                const v2u r = {__builtin_bit_cast(unsigned, __builtin_amdgcn_cvt_pknorm_i16(n0 * f2, n1 * f2)), __builtin_bit_cast(unsigned, __builtin_amdgcn_cvt_pknorm_i16(n2 * f2, 0.f)) | e2 << 16 | tl8};
                const __amdgpu_buffer_rsrc_t rsrc = __builtin_amdgcn_make_buffer_rsrc(ws + WS_PART + split * 512, 0, NRW * NSPLIT * 512 - split * 512, 0x00020000);
                asm volatile("" :: "v"(r));
                if (__builtin_amdgcn_readfirstlane(cn + np) == 32) {
                    __builtin_amdgcn_raw_buffer_store_b64(r, rsrc, rowl, 0, SC1);
#pragma unroll
                    for (int j = 0; j < NCP - 1; j++) __builtin_amdgcn_raw_buffer_store_b64(r, rsrc, rowc, j * GSZ * NSPLIT * 512, SC1);
                }
                break;
            }
            ARRIVED()
            V_FMA(3, w6, w7)
            ADVANCE(true)
        }
    } else {
    u64 E1;
    if (wave == NSW) {
        unsigned ref;
        const unsigned sidx = (blockIdx.x & 7) | ((blockIdx.x >> 7) << 3);
#define SAMPLE(K) ((((blockIdx.x >> 3) - (blockIdx.x & 7) - (K) * 5) & 15) == 0)
#define BTAG(K) ((btag + (K)) & 0xfffffu)
#define POST(K, C) { if (lane == 0) __hip_atomic_store((u64*)(ws + (bd + (K) * RST + (sidx >> 3) * 256 + (sidx & 7) * 8)), (u64)(C) | ((u64)(((C) - ref) | BTAG(K) << 12) << 32), __ATOMIC_RELAXED, __HIP_MEMORY_SCOPE_AGENT); }
        const unsigned own = lds_ld(&sm.line);
        if (SAMPLE(0) && lane < NTRY) __hip_atomic_store((unsigned*)(ws + WS_HELLO + lane * 768) + sidx, own, __ATOMIC_RELAXED, __HIP_MEMORY_SCOPE_AGENT);
        const u64 E = __hip_atomic_load((const u64*)(ws + WS_EPOCH), __ATOMIC_RELAXED, __HIP_MEMORY_SCOPE_AGENT);
        E1 = E + 1;
        if (lane == 0) { sm.tag_lo = (unsigned)E1; sm.tag_hi = (unsigned)(E1 >> 32); CBAR(); lds_st(&sm.tag_rdy, 1u); }
        const unsigned btag = __builtin_amdgcn_readfirstlane((unsigned)E1) * 61u, bd = WS_BOARD + (btag & 7) * (NRND * RST);
        ref = own;
        int off = 0;
        {
            unsigned tries = 0, cur = own, tl = own;
            bool news = false;
#pragma unroll 1
            while (!news || cur != ref) {
                const unsigned now = NOW();
                if (tries < NTRY && (int)(now - own) >= (int)(TA + tries * TA2)) {
                    v16u x;
                    asm volatile("s_load_dwordx16 %0, %1, %2\n\ts_waitcnt lgkmcnt(0)" : "=&s"(x) : "s"(ws), "s"(__builtin_amdgcn_readfirstlane(WS_HELLO + tries * 768)) : "memory");
                    unsigned bad = 0; int sum = 0, dif = 0;
#pragma unroll
                    for (int i = 0; i < 16; i++) { bad |= x[i] - own + 512; sum += x[i] - x[0]; dif += i & 4 ? x[i] : -x[i]; }
                    tries++;
                    if (bad < 1024) { const unsigned com = x[0] + (sum >> 4) + abs(dif) * (8 - KC) / 128; ref = x[0] + ((sum + (blockIdx.x & 4 ? dif : -dif)) >> 4); off = ref - com;
                        tries = NTRY; tl = NOW(); if (lane == 0) lds_st(&sm.aline, com); }
                    continue;
                }
                if (!news && lds_ld(&sm.utab[31]) != ~0u) { news = true; CBAR(); if (cur == ref) tl = now; ref = lds_ld(&sm.aline) + off; tries = NTRY; }
                const int d = (now - tl) >> CSH;
                if (cur != ref && d) { tl += d << CSH; cur += min(max((int)(ref - cur), -d), d); if (lane == 0) lds_st(&sm.line, cur); }
                SLEEP(2)
            }
        }
        ref -= off;
        const unsigned ref0 = ref;
        const unsigned lead = (blockIdx.x >> 3) == 9 ? 0 : LEAD;
        unsigned posted = ref, lagf = ref, tpost = ref + TB1, tpoll = tpost + RDD + lead, kw = 0, kr = 0, cur = ref + off, khold = 0;
        int o = off; bool held = false;
#pragma unroll 1
        for (;;) {
            const unsigned p = lds_ld(&sm.grab);
            if (p >= NPC) break;
            lagf += (int)(lds_ld(&sm.late) - lagf) >> 1;
            { const unsigned l = lds_ld(&sm.line); if (l != cur) { cur = l; khold = kw; held = true; } }
            const unsigned now = NOW();
            if (o) { o = off * min(max((int)(ref0 + TC0 + (1 << TCS) - now), 0), 1 << TCS) >> TCS; if (!held && cur != ref + o) { cur = ref + o; if (lane == 0) lds_st(&sm.line, cur); } }
            if (kw < NRND && (int)(now - tpost) >= 0) {
                const unsigned c = ref + min((p >= PSL0 && (int)(lagf - cur) > LHI ? lagf - LOK : cur) - o - ref, 4000u);
                if (SAMPLE(kw) && p < NPC - PEND && (c != ref || (ref != posted && (sidx & 7) == (kw & 7)))) POST(kw, c)
                posted = ref;
                tpost += PER; kw++;
            }
            if (kr < NRND && (int)(now - tpoll) >= 0) {
                v16u xa, xb;
                asm volatile("s_load_dwordx16 %0, %2, %3\n\ts_load_dwordx16 %1, %2, %4\n\ts_waitcnt lgkmcnt(0)" : "=&s"(xa), "=&s"(xb)
                             : "s"(ws), "s"(__builtin_amdgcn_readfirstlane(bd + kr * RST)), "s"(__builtin_amdgcn_readfirstlane(bd + kr * RST + 256)) : "memory");
                int nb = 0, sg = 0;
#pragma unroll
                for (int i = 0; i < 16; i++) {
                    const unsigned lo = i < 8 ? xa[2 * i & 15] : xb[2 * i & 15], hi = i < 8 ? xa[(2 * i + 1) & 15] : xb[(2 * i + 1) & 15];
                    if (hi >> 12 == BTAG(kr) && lo - ref0 < 8000) { nb = max(nb, (int)(lo - (hi & 4095u) - ref)); sg += hi & 4095u; }
                }
                ref += nb + (sg >> 4);
                if (cur != ref + o && (kr >= khold || (int)(ref + o - cur) > 0)) { cur = ref + o; held = false; if (lane == 0) lds_st(&sm.line, cur); }
                tpoll += PER; kr++;
            }
            SLEEP(2)
        }
    } else if (wave == NSW + 1 || wave == NSW + 2) {
        const int ql = lane < 48 ? lane : 47;
        unsigned c1, c2, s1, s2;
#define QT_LOADS() { int qo_ = ql; asm volatile("" : "+v"(qo_)); \
                     c1 = ((const unsigned*)q_cos)[qo_]; c2 = ((const unsigned*)q_cos)[48 + qo_]; s1 = ((const unsigned*)q_sin)[qo_]; s2 = ((const unsigned*)q_sin)[48 + qo_]; }
        unsigned q1, q2, lkz, lks, lvz, lvs;
        const unsigned sofs = R8(((unsigned)lane >> 1) & 7) * 2 + (lane & 1);
#define UNIT_LOADS(HEAD, GRP) { \
        q1 = ((const unsigned*)(q + (size_t)(HEAD) * DH))[ql]; q2 = ((const unsigned*)(q + (size_t)(HEAD) * DH))[48 + ql]; \
        const size_t so = (size_t)(HEAD) * (SKV / 2) + (GRP) * 256 + sofs; \
        lkz = *((const unsigned*)k_zeros + so); lks = *((const unsigned*)k_scales + so); \
        lvz = *((const unsigned*)v_zeros + so); lvs = *((const unsigned*)v_scales + so); }
#define UNIT_FILL(SL, U) { \
            v4f qq; \
            qq.x = bfr(bfr(bflo(q1) * bflo(c1)) + bfr(-bflo(q2) * bflo(s1))); \
            qq.y = bfr(bfr(bflo(q2) * bflo(c2)) + bfr(bflo(q1) * bflo(s2))); \
            qq.z = bfr(bfr(bfhi(q1) * bfhi(c1)) + bfr(-bfhi(q2) * bfhi(s1))); \
            qq.w = bfr(bfr(bfhi(q2) * bfhi(c2)) + bfr(bfhi(q1) * bfhi(s2))); \
            *(v2u*)&sm.q[SL][2 * ql] = (v2u){(__float_as_uint(qq.x) >> 16) | __float_as_uint(qq.y), (__float_as_uint(qq.z) >> 16) | __float_as_uint(qq.w)}; \
            *(v4f*)&sm.kz[SL][2 * lane] = (v4f){bflo(lkz), bflo(lks), bfhi(lkz), bfhi(lks)}; \
            *(v4f*)&sm.vz[SL][2 * lane] = (v4f){bflo(lvz), bflo(lvs), bfhi(lvz), bfhi(lvs)}; \
            CBAR(); \
            if (lane == 0) lds_st(&sm.rdy[SL], (U) + 1); \
            CBAR(); }
        const unsigned dw = NSW + 2 - wave;
        QT_LOADS()
        UNIT_LOADS(head0 + dw * (GSZ + 1 - 2 * side), lane >> 4)
#pragma unroll 1
        for (unsigned u = dw;;) {
            while (u >= lds_ld(&sm.merged) + NIN) SLEEP(4);
            CBAR();
            UNIT_FILL(u % NIN, u)
            if (u - 1 < 12) {
                const unsigned x_ = (blockIdx.x >> 3) + 32 * u - 8; unsigned dm;
                const char* p_ = x_ < 384 ? (const char*)(x_ < 192 ? qk : qv) + (size_t)(x_ < 192 ? x_ : x_ - 192) * (2u << 20) + (x_ * 8 + (blockIdx.x & 7)) * 640 : (const char*)out + (x_ - 384) * 4096;
                while ((int)(NOW() - lds_ld(&sm.line)) < TSD) SLEEP(2)
                asm volatile("s_load_dword %0, %1, 0x0\n\ts_waitcnt lgkmcnt(0)" : "=&s"(dm) : "s"(p_) : "memory");
            }
            u += 2;
            if (u >= NU) break;
            if (u == 4) {
                const unsigned tpf = tdue(PSF - 1) + TFO;
                { int d; while ((d = (int)(lds_ld(&sm.line) + tpf - NOW())) > 0 && d < 3000) SLEEP(1) }
#define PF3(P) asm volatile("global_load_dwordx4 a[0:3], %0, %1\n\tglobal_load_dwordx4 a[0:3], %0, %1 offset:1024\n\tglobal_load_dwordx4 a[0:3], %0, %1 offset:2048" \
                            :: "v"(64 * SKV * 2 + blockIdx.x * 3072 + lane * 16), "s"(P) : "a0", "a1", "a2", "a3")
                PF3(k_zeros); PF3(k_scales); PF3(v_zeros); PF3(v_scales);
                asm volatile("global_load_dword a0, %0, %1" :: "v"(blockIdx.x * (DH * 2) + ql * 8), "s"(q) : "a0");
            }
            const unsigned tin = tdue(first_of(u) - (QUARTER(u) ? 24 : 0) - LEADP + (split & 7) * DPH);
            { int d; while ((d = (int)(lds_ld(&sm.line) + tin - NOW())) > 0 && d < 3000) SLEEP(4) }
            unsigned hd;
            while ((hd = lds_ld(&sm.utab[u])) == ~0u) SLEEP(2)
            UNIT_LOADS(hd & 255, QUARTER(u) ? rank & 3 : lane >> 4)
        }
        while (lds_ld(&sm.tag_rdy) == 0) SLEEP(1);
        CBAR();
        E1 = (u64)lds_ld(&sm.tag_lo) | ((u64)lds_ld(&sm.tag_hi) << 32);
        if (wave == NSW + 1 && blockIdx.x == 0) {
            for (;;) {
                bool ok = true;
#pragma unroll
                for (int i = 0; i < NH / 64; i++) ok &= __hip_atomic_load((const u64*)(ws + WS_POST) + i * 64 + lane, __ATOMIC_RELAXED, __HIP_MEMORY_SCOPE_AGENT) >> 24 == (E1 << 24) >> 24;
                if (__builtin_amdgcn_ballot_w64(ok) == ~0ull) break;
                SLEEP(64);
            }
            if (lane == 0) __hip_atomic_store((u64*)(ws + WS_EPOCH), E1, __ATOMIC_RELAXED, __HIP_MEMORY_SCOPE_AGENT);
        }
        if (wave == NSW + 2) {
            const unsigned t16 = (unsigned)E1 & 0xffffu, t16b = ((unsigned)E1 + ((unsigned)E1 >> 16)) & 0xffffu;
            const __amdgpu_buffer_rsrc_t rsrc = __builtin_amdgcn_make_buffer_rsrc(ws + WS_SWAP + (blockIdx.x ^ 4) * 5120, 0, 5120, 0x00020000);
            const unsigned swp = lds_ld(&sm.swp);
            if (swp) {
                unsigned use = 0;
#pragma unroll
                for (int i = 0; i < 5; i++) use |= ((swp >> wunit(i * 64 + lane)) & 1) << i;
                const unsigned tg = tdue(PSG - 1) + tvx(PSG - 1, lds_ld(&sm.gap));
                while ((int)(NOW() - lds_ld(&sm.line) - tg) < 0) SLEEP(2)
                for (;;) {
                    v4u d[5]; bool ok = true;
#pragma unroll
                    for (int i = 0; i < 5; i++) d[i] = __builtin_amdgcn_raw_buffer_load_b128(rsrc, (use >> i) & 1 ? (i * 64 + lane) * 16 : 0x40000000, 0, SC1 | 2);
#pragma unroll
                    for (int i = 0; i < 5; i++) if ((use >> i) & 1) {
                        ok &= d[i].y >> 16 == t16 && d[i].w >> 16 == t16b;
                        unsigned* w = (unsigned*)&sm.wv[144 * NMIX + (i * 64 + lane) * 6];
                        w[0] = d[i].x; w[1] = (d[i].y & 0xffffu) | (d[i].z << 16); w[2] = (d[i].z >> 16) | (d[i].w << 16);
                    }
                    if (__builtin_amdgcn_ballot_w64(ok) == ~0ull) break;
                    SLEEP(32)
                }
                CBAR();
                LDS_OR(&sm.vrdy, swp)
            }
        }
    } else {
        const unsigned eu = lane >= NU ? lane - NU : lane, win = min(first_of(eu) >> 5, 15u), pair = QUARTER(eu) ? 2 * (eu - WQ) + (rank >> 2) : rank;
        unsigned rot2 = ((const unsigned char*)ROT)[win * 8 + pair];
        const unsigned rotn = ((const unsigned char*)ROT)[(win == 0 ? 4 : win == 1 ? 9 : WQ) * 8 + pair];
#define UENT(BIT) (win * 16 + 2 * pair + ((BIT) ^ side) | (QUARTER(eu) ? (rank - (eu - WQ)) * 8 & 31 : (rot2 >> ((BIT) ^ side) * 4 & 15) * 2) << 8)
        unsigned far = 0, vs;
        const unsigned hp = (blockIdx.x & 1) * NH + (blockIdx.x >> 1) * 4 + 2;
        if (blockIdx.x < 144) {
            const char* pb = (blockIdx.x < 128 ? (const char*)(hp >= NH ? qv : qk) + (size_t)(hp & (NH - 1)) * (SKV * DH * 4) + SKV * DH * 2
                                               : (const char*)ws + WS_PART + (8u << 20) + (blockIdx.x & 15) * 65536) + ((NOW() * 0x9e3779b1u) >> 26) * 128;
            unsigned mn[2] = {~0u, ~0u};
#pragma unroll 1
            for (unsigned i = 1; i < 2 * NPRB + 2; i++) {
                u64 t0, t1; unsigned dm;
                asm volatile("s_memtime %0\n\ts_waitcnt lgkmcnt(0)\n\ts_load_dword %2, %3, 0x0\n\ts_waitcnt lgkmcnt(0)\n\ts_memtime %1\n\ts_waitcnt lgkmcnt(0)"
                             : "=&s"(t0), "=&s"(t1), "=&s"(dm) : "s"(pb + i * 8192) : "memory");
                if (i == 1) asm volatile("global_load_dword a0, %0, %1 nt" :: "v"(0), "s"(pb) : "a0");
                const unsigned t = (unsigned)(t1 - t0) < 330 || i == 1 ? ~0u : (unsigned)(t1 - t0);
                if (i & 1) mn[1] = min(mn[1], t); else mn[0] = min(mn[0], t);
            }
            far = mn[0] > mn[1];
        }
        while (lds_ld(&sm.tag_rdy) == 0) SLEEP(1);
        CBAR();
        E1 = (u64)lds_ld(&sm.tag_lo) | ((u64)lds_ld(&sm.tag_hi) << 32);
        {
            const unsigned own = lds_ld(&sm.line), tg = (unsigned)E1;
            if (lane == 0) __hip_atomic_store((u64*)(ws + WS_POST) + blockIdx.x, E1 << 24 | min(lds_ld(&sm.slow), 4000u) << 1 | ((far ^ (blockIdx.x >> 2) ^ (blockIdx.x < 128 ? 0x96 >> (hp & 7) : 0)) & 1), __ATOMIC_RELAXED, __HIP_MEMORY_SCOPE_AGENT);
            const __amdgpu_buffer_rsrc_t nrs = __builtin_amdgcn_make_buffer_rsrc(ws + WS_NEWS, 0, 8 * 1280, 0x00020000);
            if (blockIdx.x == MASTER) {
                while ((int)(NOW() - own) < TMAP) SLEEP(2)
                const unsigned a = tg << 24, b = (unsigned)(E1 >> 8);
                v4u x; v2u z; u64 v0, v1;
#pragma unroll 1
                for (int i = 0; i < 8; i++) {
                    x = __builtin_amdgcn_raw_buffer_load_b128(__builtin_amdgcn_make_buffer_rsrc(ws + WS_POST, 0, 1024, 0x00020000), lane * 16, 0, SC1);
                    z = __builtin_amdgcn_raw_buffer_load_b64(__builtin_amdgcn_make_buffer_rsrc(ws + WS_POST + 1024, 0, 128, 0x00020000), lane * 8, 0, SC1);
                    v0 = __builtin_amdgcn_ballot_w64(((x.x ^ a) >> 24 | (x.y ^ b)) == 0); v1 = __builtin_amdgcn_ballot_w64(((x.z ^ a) >> 24 | (x.w ^ b)) == 0);
                    if (__builtin_popcountll(v0) + __builtin_popcountll(v1) >= MINV) break;
                    SLEEP(8)
                }
                const u64 m0 = __builtin_amdgcn_ballot_w64(x.x & 1) & v0, m1 = __builtin_amdgcn_ballot_w64(x.z & 1) & v1;
                const int s0 = lane < 43 ? min(max(lane - VW, 4 * NMIX), 42 - 2 * VW) : min(max(lane - VW, 43), 63 - 2 * VW);
#define PC(M) __builtin_popcount((unsigned)((M) >> s0) & ((2u << 2 * VW) - 1))
                const u64 g0 = __builtin_amdgcn_ballot_w64(2 * PC(m0) > PC(v0)), g1 = __builtin_amdgcn_ballot_w64(2 * PC(m1) > PC(v1));
                const float sl = wave_sum(((v0 >> lane & 1) ? (float)(x.x >> 1 & 4095u) : 0.f) + ((v1 >> lane & 1) ? (float)(x.z >> 1 & 4095u) : 0.f)) / ((8.f - PRE0) * (__builtin_popcountll(v0) + __builtin_popcountll(v1)) + 1.f);
                const unsigned al = lds_ld(&sm.aline), mean = (al ? al : own) + (int)fmaxf(sl - SL0, 0.f);
                const bool vz = lane < 16 && ((z.x ^ a) >> 24 | (z.y ^ b)) == 0;
                const unsigned gw = 2 * __builtin_popcountll(__builtin_amdgcn_ballot_w64(vz && (z.x & 1))) > __builtin_popcountll(__builtin_amdgcn_ballot_w64(vz));
                const unsigned k = lane & 3;
                if (lane < 32 && k < 3) __builtin_amdgcn_raw_buffer_store_b128((v4u){k == 0 ? (unsigned)g0 : k == 1 ? (unsigned)g1 : mean, tg, k == 0 ? (unsigned)(g0 >> 32) : k == 1 ? (unsigned)(g1 >> 32) : gw, tg},
                                                                              nrs, (lane >> 2) * 1280 + k * 16, 0, SC1);
            }
            while ((int)(NOW() - own) < TMAP + TRES) SLEEP(2)
            v4u y;
            for (;;) {
                y = __builtin_amdgcn_raw_buffer_load_b128(nrs, lane < 3 ? (blockIdx.x & 7) * 1280 + lane * 16 : 0x40000000, 0, SC1);
                if (((unsigned)__builtin_amdgcn_ballot_w64(y.y == tg && y.w == tg) & 7u) == 7u) break;
                SLEEP(8)
            }
            const u64 f0 = (unsigned)__builtin_amdgcn_readlane(y.x, 0) | (u64)(unsigned)__builtin_amdgcn_readlane(y.z, 0) << 32, f1 = (unsigned)__builtin_amdgcn_readlane(y.x, 1) | (u64)(unsigned)__builtin_amdgcn_readlane(y.z, 1) << 32;
            if (lane == 2) { sm.aline = y.x - own + 500 < 1000 ? y.x : own; sm.chead = (blockIdx.x & ~5u) | ((blockIdx.x >> 2 ^ y.z) & 1) | (blockIdx.x & 1) << 2; }
            unsigned bit = ((unsigned)((lane >= NU ? f1 : f0) >> (4 * win + (pair >> 1))) ^ (0x66 >> pair)) & 1;
            vs = (f0 ^ f1) >> 4 * NMIX && !((f0 ^ f1) >> 4 * NMIX & 4095) ? NMIX : 0;
            if (eu < NMIX) { if (lane < NU || !vs) bit = eu & 1; else rot2 = rotn; }
            const u64 bm = __builtin_amdgcn_ballot_w64(bit);
            const unsigned nsw = __builtin_popcountll((f0 ^ f1) >> (4 * NMIX)) + (vs ? 2 * NMIX : 0);
            const unsigned sw = (unsigned)(bm ^ (bm >> NU)) & ((1u << NU) - 1), sx = (sw >> vs ^ sw) & 7;
            if (lane == 0) { sm.swp = sw ^ sx ^ sx << vs; lds_st(&sm.gap, (nsw ? (TG0 + (vs ? TGV : 0) + TG1 * nsw / 8) * 0x1001u + ((TH0 + (vs ? THV : 0) + TH1 * nsw / 8) << 12) : 0u) | (vs ? 1u << 31 : 0u)); }
            CBAR();
#define VPOS(E) ((E) < NMIX ? (E) + vs : (E) - vs < NMIX ? (E) - vs : (E))
            lds_st(&sm.utab[lane >= NU ? NU + VPOS(eu) : lane], UENT(bit));
        }
        const unsigned swp = lds_ld(&sm.swp);
        const unsigned T = lds_ld(&sm.tag_lo);
        const unsigned t15 = T & 0x7fffu;
        const __amdgpu_buffer_rsrc_t rsrc = __builtin_amdgcn_make_buffer_rsrc(ws + WS_PART + split * 512, 0, NRW * NSPLIT * 512 - split * 512, 0x00020000);
        v2u* recs = (v2u*)&sm.cs[0][0][0];
#pragma unroll
        for (int i = 0; i < NOUT * 3; i++) (&sm.acc[0][0])[i * 64 + lane] = 0.0;
        CBAR();
        unsigned vut = sm.utab[NU + (lane & 31)] & 255;
        if (QUARTER((unsigned)lane) && (rank & 3)) vut = NH - 16 + (rank & 3) * 16 + ((vut & 15) ^ 1);
        __builtin_amdgcn_s_setprio(3);
        unsigned po[NU / 2];
#pragma unroll
        for (int k = 0; k < NU / 2; k++) po[k] = __builtin_amdgcn_ds_bpermute((2 * k + (lane >> 5)) * 4, vut) * (NSPLIT * 512) + (lane & 31) * 16;
        const u64 rb_ = (u64)(ws + WS_PART + split * 512);
        const v4u rs_ = {(unsigned)rb_, (unsigned)(rb_ >> 32) & 0xffffu, NRW * NSPLIT * 512 - split * 512, 0x00020000u};
#pragma unroll 1
        for (unsigned u = 0; u < 2 * NU - 1; u++) {
            const unsigned e = u % NU, so = u % NOUT;
            if (u >= NU) while (!((lds_ld(&sm.vrdy) >> e) & 1)) SLEEP(4)
            CBAR();
            const v4f h = *(const v4f*)&sm.wv[went(e) + (QUARTER(e) ? 32 : 128)];
            while (lds_ld(&sm.cnt[so]) != (QUARTER(e) ? 8 : 32)) SLEEP(1)
            CBAR();
            const double a0 = sm.acc[so][lane], a1 = sm.acc[so][64 + lane], a2 = sm.acc[so][128 + lane];
            if (u < NU) {
                float2 lg = *(const float2*)&sm.ent[so][2 * lane];
                if (QUARTER(e) && lane >> 4 != (rank & 3)) lg = make_float2(-INFINITY, -INFINITY);
                const v4f zs = *(const v4f*)&sm.vz[u % NIN][2 * lane];
                const float m = wave_max(fmaxf(lg.x, lg.y));
                const float p0 = __expf(lg.x - m), p1 = __expf(lg.y - m);
                const float x0 = p0 * zs.y, x1 = p1 * zs.w;
                const unsigned eb = max(__float_as_uint(wave_max(fmaxf(fabsf(x0), fabsf(x1)))) >> 23, 14u);
                const float f = __uint_as_float((268u - eb) << 23);
                const float w0 = fminf(fmaxf(rintf(x0 * f), -32767.f), 32767.f), w1 = fminf(fmaxf(rintf(x1 * f), -32767.f), 32767.f);
                const float den = wave_sum(p0 + p1);
                const float bz = wave_sum(fmaf(w0, zs.x, w1 * zs.z));
                const float L = fmaf(m, 1.4426950408889634f, __builtin_amdgcn_logf(den));
                const unsigned vp = VPOS(u);
                unsigned* wd = (unsigned*)(((swp >> vp) & 1 ? sm.wo : sm.wv) + went(vp));
                if (!QUARTER(u) || lane >> 4 == (rank & 3)) wd[QUARTER(u) ? lane & 15 : lane] = ((unsigned)(int)w0 & 0xffffu) | ((unsigned)(int)w1 << 16);
                if (lane == 0) *(v4f*)&wd[QUARTER(u) ? 16 : 64] = (v4f){ldexpf(1.f / den, (int)eb - 141), bz, L, 0.f};
                CBAR();
                if (!((swp >> vp) & 1)) LDS_OR(&sm.vrdy, 1u << vp)
            } else {
                const float n0 = (float)(a0 - (double)h.y) * h.x, n1 = (float)(a1 - (double)h.y) * h.x, n2 = (float)(a2 - (double)h.y) * h.x;
                const unsigned e2 = max(__float_as_uint(wave_maxv(fmaxf(fmaxf(fabsf(n0), fabsf(n1)), fabsf(n2)))) >> 23, 14u);
                const float f2 = __uint_as_float((268u - e2) << 23);
                const unsigned m01 = __builtin_bit_cast(unsigned, __builtin_amdgcn_cvt_pk_i16((int)rintf(n0 * f2), (int)rintf(n1 * f2)));
                const unsigned m2 = __builtin_bit_cast(unsigned, __builtin_amdgcn_cvt_pk_i16((int)rintf(n2 * f2), 0));
                const unsigned bit = ((lane & 1 ? e2 : __float_as_uint(h.z)) >> (lane >> 1)) & 1u;
                recs[e * 64 + lane] = (v2u){m01, m2 | t15 << 16 | bit << 31};
                CBAR();
                sm.acc[so][lane] = 0.0; sm.acc[so][64 + lane] = 0.0; sm.acc[so][128 + lane] = 0.0;
            }
            CBAR();
            if (lane == 0) { lds_st(&sm.cnt[so], 0u); CBAR(); lds_st(&sm.merged, u + 1); }
            if (u == NU - 1 && swp) {
                wave_sync();
                const __amdgpu_buffer_rsrc_t srs = __builtin_amdgcn_make_buffer_rsrc(ws + WS_SWAP + blockIdx.x * 5120, 0, 5120, 0x00020000);
                v4u d[5]; unsigned o[5];
#pragma unroll
                for (int i = 0; i < 5; i++) {
                    const unsigned* w = (const unsigned*)&sm.wo[144 * NMIX + (i * 64 + lane) * 6];
                    const unsigned d0 = w[0], d1 = w[1], d2 = w[2];
                    d[i] = (v4u){d0, (d1 & 0xffffu) | (T << 16), (d1 >> 16) | (d2 << 16), (d2 >> 16) | ((T + (T >> 16)) << 16)};
                    o[i] = (swp >> wunit(i * 64 + lane)) & 1 ? (i * 64 + lane) * 16 : 0x40000000;
                }
                while ((int)(NOW() - lds_ld(&sm.line)) < (int)(tdue(PSW - 1) + tvx(PSW - 1, lds_ld(&sm.gap))) + 10 + TWO) SLEEP(1)
#pragma unroll
                for (int i = 0; i < 5; i++) __builtin_amdgcn_raw_buffer_store_b128(d[i], srs, o[i], 0, SC1 | 2);
            }
        }
        wave_sync();
#pragma unroll
        for (int k = 0; k < NU / 2; k++) asm volatile("ds_read_b128 a[%c0:%c0+3], %1 offset:%2" :: "i"(16 + 4 * k), "v"(lds_addr(recs) + lane * 16), "i"(k * 1024) : "memory");
        asm volatile("s_waitcnt lgkmcnt(0)" ::: "memory");
        while (!((lds_ld(&sm.vrdy) >> (NU - 1)) & 1)) SLEEP(4)
        CBAR();
        const v2u hw_ = {*(const unsigned*)&sm.wv[went(NU - 1) + (lane & 1 ? 128 : 132)], T};
        const unsigned hrow = (NRW - GSZ + (__builtin_amdgcn_readlane(vut, NU - 1) & 15 ^ 1)) * (NSPLIT * 512);
        while (lds_ld(&sm.grab) < NPC - PUBP) SLEEP(1)
        if (lane < 16) __builtin_amdgcn_raw_buffer_store_b64(hw_, rsrc, lane * 8, hrow, SC1);
#define PUB(K) asm volatile("buffer_store_dwordx4 a[%c0:%c0+3], %1, %2, 0 offen sc1" :: "i"(16 + 4 * (K)), "v"(po[K]), "s"(rs_) : "memory");
        PUB(WQ / 2) PUB(WQ / 2 + 1)
        { const unsigned t_ = TLAST() + PDEL; GATE(t_) }
        PUB(0) PUB(1) PUB(2) PUB(3) PUB(4) PUB(7) PUB(8)
        return;
    }
    }
    __builtin_amdgcn_s_setprio(0);
    unsigned c;
#define ADDRTN(P) { u64 ex_; asm volatile("s_mov_b64 %0, exec\n\ts_mov_b64 exec, 1\n\tds_add_rtn_u32 a15, %1, a13\n\ts_mov_b64 exec, %0" : "=&s"(ex_) : "v"(lds_addr(P)) : "a15", "memory"); }
#define ADDED(V) { unsigned t_; asm volatile("s_waitcnt lgkmcnt(0)\n\tv_accvgpr_read_b32 %1, a15\n\ts_nop 0\n\tv_readfirstlane_b32 %0, %1\n\ts_nop 1" : "=s"(V), "=v"(t_)); }
    asm volatile("v_accvgpr_write_b32 a13, 1\n\ts_nop 1" ::: "a13");
    ADDRTN(&sm.cidx) ADDED(c)
    if (c < NSW) {
        while (lds_ld(&sm.grab) < NPC + 2) SLEEP(4)
        const unsigned head = lds_ld(&sm.chead), T = lds_ld(&sm.tag_lo), tl = TLAST();
        const u64 rb_ = (u64)(ws + WS_PART);
        const v4u rs_ = {(unsigned)rb_, (unsigned)(rb_ >> 32) & 0xffffu, NRW * NSPLIT * 512, 0x00020000u};
        const unsigned ro = head * (NSPLIT * 512) + c * 1024, rx = (NH + (head & 15 ^ 1)) * (NSPLIT * 512) + c * 1024;
#define POLL(S, O) asm volatile("buffer_load_dwordx4 a[%c0:%c0+3], %1, %2, %3 offen sc1" :: "i"(16 + 4 * (S)), "v"(lane * 16), "s"(rs_), "s"(O) : "memory");
#define FETCH(X, S, N) asm volatile("s_waitcnt vmcnt(%c5)\n\tv_accvgpr_read_b32 %0, a[%c4]\n\tv_accvgpr_read_b32 %1, a[%c4+1]\n\tv_accvgpr_read_b32 %2, a[%c4+2]\n\tv_accvgpr_read_b32 %3, a[%c4+3]" \
                                    : "=v"((X).x), "=v"((X).y), "=v"((X).z), "=v"((X).w) : "i"(16 + 4 * (S)), "i"(N));
#define SWADF(A, B) ({ const auto l_ = __builtin_amdgcn_permlane32_swap(__float_as_uint(A), __float_as_uint(B), false, false); __uint_as_float(l_[0]) + __uint_as_float(l_[1]); })
        float o0, o1, o2;
        unsigned oo = (lane & 31) * 12 + (lane >> 5) * 6, da = lds_addr(&sm.cdone), one = 1;
        const u64 ob_ = (u64)(out + (size_t)head * DH);
        v4u os_ = {(unsigned)ob_, (unsigned)(ob_ >> 32) & 0xffffu, DH * 2, 0x00020000u};
        asm volatile("" : "+v"(oo), "+v"(da), "+v"(one), "+s"(os_));
        if (head >= NH - GSZ) {
            __builtin_amdgcn_s_setprio(3);
            if (c == 0) {
                v4u hd;
                GATE(tl + THD)
                for (;;) {
                    hd = __builtin_amdgcn_raw_buffer_load_b128(__builtin_amdgcn_make_buffer_rsrc(ws + WS_PART + (NRW - GSZ + (head & 15 ^ 1)) * (NSPLIT * 512), 0, NSPLIT * 512, 0x00020000), (lane & 15) * 512, 0, SC1);
                    if (__builtin_amdgcn_ballot_w64(hd.y == T && hd.w == T) == ~0ull) break;
                    SLEEP(4)
                }
                const float w = __builtin_amdgcn_exp2f(__uint_as_float(hd.x) - row_max(__uint_as_float(hd.x)));
                if (lane < 16) sm.ent[0][lane] = w * __builtin_amdgcn_rcpf(row_sum(w) * 32767.f) * __uint_as_float(hd.z);
                CBAR();
                if (lane == 0) lds_st(&sm.tabs, 0u);
            }
            while (lds_ld(&sm.tabs) || lds_ld(&sm.merged) <= (2 * NU - 1) / NOUT * NOUT) SLEEP(1)
            CBAR();
            float cf = sm.ent[0][2 * c + (lane >> 5)];
            unsigned la = lds_addr(&sm.acc[0][lane]);
            asm volatile("" : "+v"(cf), "+v"(la));
            v4u x; bool got;
#define SEEN(J, N) { FETCH(x, J, N) got = __builtin_amdgcn_ballot_w64((((x.y ^ T << 24) | (x.w ^ T << 24)) >> 24) != 0) == 0; }
            GATE(tl + TCA) POLL(0, ro) GATE(tl + TCA + TCD) POLL(1, rx + 3 * GSZ * NSPLIT * 512) SEEN(0, 1)
            if (!got) SEEN(1, 0)
            while (!got) { POLL(2, rx + 4 * GSZ * NSPLIT * 512) SEEN(2, 0) SLEEP(0) }
            const float c0 = ldexpf(cf, (int)(x.y >> 16 & 255u) - 126), c1 = ldexpf(cf, (int)(x.w >> 16 & 255u) - 126);
            asm volatile("ds_add_f64 %0, %1\n\tds_add_f64 %0, %2 offset:512\n\tds_add_f64 %0, %3 offset:1024"
                         :: "v"(la), "v"((double)SWADF(c0 * (float)(short)x.x, c1 * (float)(short)x.z)), "v"((double)SWADF(c0 * (float)((int)x.x >> 16), c1 * (float)((int)x.z >> 16))),
                            "v"((double)SWADF(c0 * (float)(short)x.y, c1 * (float)(short)x.w)) : "memory");
            double a0, a1, a2; unsigned cn; u64 ex_;
            asm volatile("s_mov_b64 %4, exec\n\ts_mov_b64 exec, 1\n\tds_add_rtn_u32 %3, %6, %7\n\ts_mov_b64 exec, %4\n\t"
                         "ds_read_b64 %0, %5\n\tds_read_b64 %1, %5 offset:512\n\tds_read_b64 %2, %5 offset:1024\n\ts_waitcnt lgkmcnt(0)"
                         : "=&v"(a0), "=&v"(a1), "=&v"(a2), "=&v"(cn), "=&s"(ex_) : "v"(la), "v"(da), "v"(one) : "memory");
            c = __builtin_amdgcn_readfirstlane(cn);
            o0 = a0; o1 = a1; o2 = a2;
        } else {
            const bool quart = head >> 4 == WQ;
            const unsigned t15 = T << 16;
#define TAKE(K) { FETCH(x[K], K, 0) \
                bad |= (x[K].y ^ t15) | (x[K].w ^ t15); \
                const u64 bl = __builtin_amdgcn_ballot_w64((int)x[K].y < 0), be = __builtin_amdgcn_ballot_w64((int)x[K].w < 0); \
                L[K] = __uint_as_float(lane < 32 ? (unsigned)bl : (unsigned)(bl >> 32)); ex[K] = lane < 32 ? (unsigned)be : (unsigned)(be >> 32); \
                M = fmaxf(M, fmaxf(__uint_as_float((unsigned)bl), __uint_as_float((unsigned)(bl >> 32)))); }
#define SUMUP(K) { const float w_ = __builtin_amdgcn_exp2f(L[K] - M), c_ = ldexpf(w_, (int)(ex[K] & 255u) - 141); den += w_; \
                t[0] = fmaf(c_, (float)(short)x[K].x, t[0]); t[1] = fmaf(c_, (float)((int)x[K].x >> 16), t[1]); t[2] = fmaf(c_, (float)(short)x[K].y, t[2]); \
                t[3] = fmaf(c_, (float)(short)x[K].z, t[3]); t[4] = fmaf(c_, (float)((int)x[K].z >> 16), t[4]); t[5] = fmaf(c_, (float)(short)x[K].w, t[5]); }
            v4f* pb = (v4f*)((char*)&sm.cs[0][0][0] + 65536);
            __attribute__((address_space(3))) v4f* pw_ = (__attribute__((address_space(3))) v4f*)&pb[c * 64 + lane];
            const __attribute__((address_space(3))) v4f* pr_ = (const __attribute__((address_space(3))) v4f*)&pb[lane];
            const __attribute__((address_space(3))) float* pq_ = (const __attribute__((address_space(3))) float*)&pb[(lane & 7) * 64] + 3;
            asm volatile("" : "+v"(pw_), "+v"(pr_), "+v"(pq_));
            const unsigned tg = tl + (quart ? TRQ : TRD);
            GATE(tg)
            v4u x[4]; float L[4], M; unsigned ex[4];
            for (;;) {
                unsigned bad = 0; M = -3e38f;
                POLL(0, ro)
                if (quart) { POLL(1, rx) POLL(2, rx + GSZ * NSPLIT * 512) POLL(3, rx + 2 * GSZ * NSPLIT * 512) }
                __builtin_amdgcn_s_setprio(3);
                TAKE(0)
                if (quart) { TAKE(1) TAKE(2) TAKE(3) }
                if (__builtin_amdgcn_ballot_w64((bad & 0x7fff0000u) != 0) == 0) break;
                SLEEP(2)
            }
            float t[6] = {0.f, 0.f, 0.f, 0.f, 0.f, 0.f}, den = 0.f;
            SUMUP(0)
            if (quart) { SUMUP(1) SUMUP(2) SUMUP(3) }
            float ds_ = SWADF(den, den);
            asm("" : "+v"(ds_));
            *pw_ = (v4f){SWADF(t[0], t[3]), SWADF(t[1], t[4]), SWADF(t[2], t[5]), lane & 1 ? ds_ : M};
            CBAR();
            { unsigned cn; u64 ex_;
              asm volatile("s_mov_b64 %1, exec\n\ts_mov_b64 exec, 1\n\tds_add_rtn_u32 %0, %2, %3\n\ts_mov_b64 exec, %1\n\ts_waitcnt lgkmcnt(0)" : "=&v"(cn), "=&s"(ex_) : "v"(da), "v"(one) : "memory");
              c = __builtin_amdgcn_readfirstlane(cn); }
            if (c == NSW - 1) {
                const float mw = pq_[0], dw_ = pq_[4];
                v4f ps[NSW];
#pragma unroll
                for (int w = 0; w < NSW; w++) ps[w] = pr_[w * 64];
                __builtin_amdgcn_sched_barrier(0);
                float mx;
                asm("s_nop 1\n\tv_max_f32_dpp %0, %1, %1 quad_perm:[1,0,3,2] row_mask:0xf bank_mask:0xf\n\ts_nop 1\n\tv_max_f32_dpp %0, %0, %0 quad_perm:[2,3,0,1] row_mask:0xf bank_mask:0xf\n\t"
                    "s_nop 1\n\tv_max_f32_dpp %0, %0, %0 row_half_mirror row_mask:0xf bank_mask:0xf" : "=&v"(mx) : "v"(mw));
                float f = __builtin_amdgcn_exp2f(mw - mx);
                f *= __builtin_amdgcn_rcpf(dpp_add<0x141>(dpp_add<0x4e>(dpp_add<0xb1>(f * dw_))));
                v4f s0 = ps[0] * rdlane(f, 0), s1 = ps[1] * rdlane(f, 1);
#pragma unroll
                for (int w = 2; w < NSW; w += 2) { s0 += ps[w] * rdlane(f, w); s1 += ps[w + 1] * rdlane(f, w + 1); }
                o0 = s0.x + s1.x; o1 = s0.y + s1.y; o2 = s0.z + s1.z;
            }
        }
        if (c == NSW - 1) {
            asm("" : "+v"(o0), "+v"(o1), "+v"(o2));
            typedef __bf16 v2b __attribute__((ext_vector_type(2)));
#define PK(A, B) __builtin_bit_cast(unsigned, (v2b){(__bf16)(A), (__bf16)(B)})
            asm volatile("buffer_store_short %0, %2, %3, 0 offen\n\tbuffer_store_short_d16_hi %0, %2, %3, 0 offen offset:2\n\tbuffer_store_short %1, %2, %3, 0 offen offset:4"
                         :: "v"(PK(o0, o1)), "v"(PK(o2, o2)), "v"(oo), "s"(os_) : "memory");
        }
    }
}

extern "C" void kernel_launch(void* const* d_in, const int* in_sizes, int n_in,
                              void* d_out, int out_size, void* d_ws, size_t ws_size,
                              hipStream_t stream) {
    const unsigned short* q = (const unsigned short*)d_in[0];
    const int* qk = (const int*)d_in[1];
    const unsigned short* k_scales = (const unsigned short*)d_in[2];
    const unsigned short* k_zeros = (const unsigned short*)d_in[3];
    const int* qv = (const int*)d_in[4];
    const unsigned short* v_scales = (const unsigned short*)d_in[5];
    const unsigned short* v_zeros = (const unsigned short*)d_in[6];
    const unsigned short* q_cos = (const unsigned short*)d_in[7];
    const unsigned short* q_sin = (const unsigned short*)d_in[8];
    const unsigned short* k_cos = (const unsigned short*)d_in[9];
    const unsigned short* k_sin = (const unsigned short*)d_in[10];
    unsigned char* ws = (unsigned char*)d_ws;
    attn_kernel<<<NSPLIT * GSZ, NTHREADS, 0, stream>>>(q, qk, k_scales, k_zeros, qv, v_scales, v_zeros,
                                                       q_cos, q_sin, k_cos, k_sin, (__hip_bfloat16*)d_out, ws);
}
```
